```python
import torch

def _softmax(x, axis=-1):
    return torch.softmax(x, dim=axis)
def _gelu(x):
    return torch.nn.functional.gelu(x, approximate="tanh")
def _silu(x):
    return torch.nn.functional.silu(x)
def _sigmoid(x):
    return torch.sigmoid(x)
def _layernorm(x, axis=-1, eps=1e-5):
    m = x.mean(dim=axis, keepdim=True)
    v = x.var(dim=axis, keepdim=True, unbiased=False)
    return (x - m) / torch.sqrt(v + eps)
def _scatter_add(tgt, idx, src, axis=0):
    return tgt.clone().index_add_(axis, idx, src)
def _segment_sum(src, seg, n):
    out = torch.zeros((n,) + src.shape[1:], dtype=src.dtype)
    return out.index_add_(0, seg, src)
def _pad(x, pw):
    flat = tuple(v for pair in reversed(pw) for v in pair)
    return torch.nn.functional.pad(x, flat)

kernel_name = 'recurrent_scan__sliding_block_cached__glu_gated_swiglu__decode'

def setup_inputs():

    _w = (lambda a: (lambda t: t.to(torch.bfloat16) if t.is_floating_point() else t)(torch.as_tensor(a)))
    def _A_setup():
        B, Sq, D = 1, 1, 640
        Hq, Hkv, Sk, Dh = 32, 32, 32768, 192
        assert Hq % Hkv == 0, (Hq, Hkv)
        return {
            "x": _w((lambda *s: torch.randn(*s, dtype=torch.bfloat16) * 0.05)(B, Sq, D)),
            "k_cache": _w((lambda *s: torch.randn(*s, dtype=torch.bfloat16) * 3.2)(B, Hkv, Sk, Dh)),
            "v_cache": _w((lambda *s: torch.randn(*s, dtype=torch.bfloat16) * 3.2)(B, Hkv, Sk, Dh)),
            "wq": _w((lambda *s: torch.randn(*s, dtype=torch.bfloat16) * 3.2)(D, Hq * Dh) * (D ** -0.5)),
            "wo": _w((lambda *s: torch.randn(*s, dtype=torch.bfloat16) * 3.2)(Hq * Dh, D) * ((Hq * Dh) ** -0.5)),
        }
    _A_in = _A_setup()

    def _B_setup():
        B, S, D, F = 1, 1, 640, 1536
        return {
            "x": _w((lambda *s: torch.randn(*s, dtype=torch.bfloat16) * 0.05)(B, S, D)),
            "w1": _w((lambda *s: torch.randn(*s, dtype=torch.bfloat16) * 0.4)(D, F) * (2.0 / D) ** 0.5),
            "w3": _w((lambda *s: torch.randn(*s, dtype=torch.bfloat16) * 0.4)(D, F) * (2.0 / D) ** 0.5),
            "w2": _w((lambda *s: torch.randn(*s, dtype=torch.bfloat16) * 0.4)(F, D) * (2.0 / F) ** 0.5),
        }
    _B_in = _B_setup()

    return {
        "A_x": _A_in["x"],
        "A_k_cache": _A_in["k_cache"],
        "A_v_cache": _A_in["v_cache"],
        "A_wq": _A_in["wq"],
        "A_wo": _A_in["wo"],
        "B_x": _B_in["x"],
        "B_w1": _B_in["w1"],
        "B_w3": _B_in["w3"],
        "B_w2": _B_in["w2"],
    }

def reference(A_x, A_k_cache, A_v_cache, A_wq, A_wo, B_x, B_w1, B_w3, B_w2):
    def _A_ref(x, k_cache, v_cache, wq, wo):
        B, Sq, D = x.shape
        Hq, Dh = 32, 192
        Hkv, Sk = k_cache.shape[1], k_cache.shape[2]
        W = min(4096, max(256, Sk // 4))
        rep = Hq // Hkv
        q = (lambda x, *d: x.permute(*d))((x @ wq).reshape(B, Sq, Hq, Dh), 0, 2, 1, 3)
        k = (lambda x, n, axis: torch.repeat_interleave(x, n, dim=axis))(k_cache[:, :, Sk - W:, :], rep, axis=1)
        v = (lambda x, n, axis: torch.repeat_interleave(x, n, dim=axis))(v_cache[:, :, Sk - W:, :], rep, axis=1)
        scores = torch.einsum("bhqd,bhkd->bhqk", q, k) * (Dh ** -0.5)
        p = _softmax(scores, axis=-1)
        o = torch.einsum("bhqk,bhkd->bhqd", p, v)
        return (lambda x, *d: x.permute(*d))(o, 0, 2, 1, 3).reshape(B, Sq, Hq * Dh) @ wo

    def _B_ref(x, w1, w3, w2):
        gate = torch.einsum("bsd,df->bsf", x, w1)
        up = torch.einsum("bsd,df->bsf", x, w3)
        h = _silu(gate) * up
        out = torch.einsum("bsf,fd->bsd", h, w2)
        return out

    x = A_x
    xn = _layernorm(x, axis=-1)
    s = _A_ref(xn, A_k_cache, A_v_cache, A_wq, A_wo)
    g = _silu(_B_ref(xn, B_w1, B_w3, B_w2))
    return x + s * g
```

```cpp
#define LEAK 0
#define LEAKCOND (w!=0)
#include <hip/hip_runtime.h>
#include <hip/hip_bf16.h>
#include <stdint.h>

typedef __hip_bfloat16 bf16;
typedef unsigned long long u64;
typedef unsigned u32x4 __attribute__((ext_vector_type(4)));
typedef float f32x4 __attribute__((ext_vector_type(4)));
typedef __bf16 bf16x2_t __attribute__((ext_vector_type(2)));

namespace {

constexpr int D = 640, HQ = 32, DH = 192, SK = 32768, WIN = 4096, FF = 1536;
constexpr int NSP = 8, KEYS = WIN / NSP;
constexpr int NWG = HQ * NSP;
constexpr int NT = 512;
constexpr float SCALE = 0.07216878364870322f;
constexpr float LOG2E = 1.4426950408889634f;
constexpr unsigned SPIN_MAX = 1u << 16;
#define VEARLY 4
#define KEARLY 3
constexpr int NFIN = 128, FCOLS = 5;

constexpr size_t G1 = 0;
constexpr size_t G2 = G1 + (size_t)NWG * 288;
constexpr size_t G3 = G2 + (size_t)NWG * 200;
constexpr size_t GEP = G3 + (size_t)NFIN * 2 * NWG * FCOLS;
constexpr size_t GEND = GEP + 16;

__device__ __forceinline__ unsigned fbits(float x) { return __builtin_bit_cast(unsigned, x); }
__device__ __forceinline__ void gst(u64* p, float v, unsigned tag) {
  __hip_atomic_store(p, ((u64)tag << 32) | (u64)fbits(v), __ATOMIC_RELAXED, __HIP_MEMORY_SCOPE_AGENT);
}
__device__ __forceinline__ u64 gld(const u64* p) {
  return __hip_atomic_load(p, __ATOMIC_RELAXED, __HIP_MEMORY_SCOPE_AGENT);
}
#define gok(v) ((unsigned)((v) >> 32) == tag)
__device__ __forceinline__ float gval(u64 v) { return __builtin_bit_cast(float, (unsigned)v); }

__device__ __forceinline__ float bf_lo(unsigned u) { return __builtin_bit_cast(float, u << 16); }
__device__ __forceinline__ float bf_hi(unsigned u) { return __builtin_bit_cast(float, u & 0xffff0000u); }
__device__ __forceinline__ float bfr(float x) { return __bfloat162float(__float2bfloat16(x)); }
__device__ __forceinline__ unsigned short bfbits(float x) {
  return __builtin_bit_cast(unsigned short, __float2bfloat16(x));
}
__device__ __forceinline__ float dot2(unsigned a, unsigned b, float c) {
  return __builtin_amdgcn_fdot2_f32_bf16(__builtin_bit_cast(bf16x2_t, a), __builtin_bit_cast(bf16x2_t, b), c, false);
}
template <int CTRL>
__device__ __forceinline__ float dpp(float x) {
  return __builtin_bit_cast(float, __builtin_amdgcn_mov_dpp(__builtin_bit_cast(int, x), CTRL, 0xf, 0xf, true));
}
__device__ __forceinline__ float grp8_sum(float x) {
  x += dpp<0xB1>(x);
  x += dpp<0x4E>(x);
  x += dpp<0x141>(x);
  return x;
}
__device__ __forceinline__ float xg_sum(float x) {
  x += dpp<0x128>(x);
  auto s = __builtin_amdgcn_permlane16_swap(fbits(x), fbits(x), false, false);
  x = __builtin_bit_cast(float, (unsigned)s[0]) + __builtin_bit_cast(float, (unsigned)s[1]);
  auto t = __builtin_amdgcn_permlane32_swap(fbits(x), fbits(x), false, false);
  return __builtin_bit_cast(float, (unsigned)t[0]) + __builtin_bit_cast(float, (unsigned)t[1]);
}
__device__ __forceinline__ float xg_max(float x) {
  x = fmaxf(x, dpp<0x128>(x));
  auto s = __builtin_amdgcn_permlane16_swap(fbits(x), fbits(x), false, false);
  x = fmaxf(__builtin_bit_cast(float, (unsigned)s[0]), __builtin_bit_cast(float, (unsigned)s[1]));
  auto t = __builtin_amdgcn_permlane32_swap(fbits(x), fbits(x), false, false);
  return fmaxf(__builtin_bit_cast(float, (unsigned)t[0]), __builtin_bit_cast(float, (unsigned)t[1]));
}
__device__ __forceinline__ float wave_sum(float x) { return xg_sum(grp8_sum(x)); }
__device__ __forceinline__ float ex2(float x) { return __builtin_amdgcn_exp2f(x); }
__device__ __forceinline__ float silu(float x) { return x / (1.0f + __expf(-x)); }
__device__ __forceinline__ u32x4 ntld(const bf16* p) {
  return __builtin_nontemporal_load(reinterpret_cast<const u32x4*>(p));
}

#define SCHED_FENCE() __builtin_amdgcn_sched_barrier(0)
#define WAVE_SYNC()                                        \
  do {                                                     \
    __builtin_amdgcn_fence(__ATOMIC_RELEASE, "wavefront"); \
    __builtin_amdgcn_wave_barrier();                       \
    __builtin_amdgcn_fence(__ATOMIC_ACQUIRE, "wavefront"); \
  } while (0)

#define STAMP(i) do { if ((i) == LEAK) { SCHED_FENCE(); tleak = __builtin_amdgcn_s_memrealtime(); SCHED_FENCE(); } } while (0)
constexpr int L_XS = 0;
constexpr int L_PART = L_XS + 12 * D;
constexpr int L_FFP = L_PART + 8 * DH;
constexpr int L_QS = L_FFP + 40 * 96;
constexpr int L_ML = L_QS + 96;
constexpr int L_CV = L_ML + 16;
constexpr int L_FIN = L_CV + 32;
constexpr int L_END = L_FIN + 64;

template <bool CTRL>
__device__ __forceinline__ void body(float* lds, const int h, const int sp, const int w, const bf16* __restrict__ x,
                                     const bf16* __restrict__ kc, const bf16* __restrict__ vc,
                                     const bf16* __restrict__ wq, const bf16* __restrict__ wo,
                                     const bf16* __restrict__ w1, const bf16* __restrict__ w3,
                                     const bf16* __restrict__ w2, u64* __restrict__ gw, bf16* __restrict__ out) {
  const int tid = threadIdx.x, lane = tid & 63;
  const int g = lane >> 3, c = lane & 7;
  const int rec = h * NSP + sp;
  const int R0 = 80 * sp + 10 * w;

  u64 tleak = 0;
  STAMP(0);
  const unsigned epoch = __hip_atomic_load(reinterpret_cast<unsigned*>(gw + GEP), __ATOMIC_RELAXED, __HIP_MEMORY_SCOPE_AGENT);
  const u32x4 xa = *reinterpret_cast<const u32x4*>(x + 8 * lane);
  const u32x4 xb = *reinterpret_cast<const u32x4*>(x + 512 + 8 * (lane & 15));
  u32x4 WQ[2][3];
  {
    const bf16* p0 = wq + (size_t)(R0 + g) * (HQ * DH) + h * DH + 8 * c;
    const bf16* p1 = wq + (size_t)(R0 + 8 + (g & 1)) * (HQ * DH) + h * DH + 8 * c;
#pragma unroll
    for (int j = 0; j < 3; ++j) WQ[0][j] = ntld(p0 + 64 * j);
#pragma unroll
    for (int j = 0; j < 3; ++j) WQ[1][j] = ntld(p1 + 64 * j);
  }
  const int lf = lane < 60 ? lane : 59;
  const int fm = lf / 30, fr5 = (lf - 30 * fm) / 6, fck = lf - 30 * fm - 6 * fr5;
  u32x4 F0, F1;
  {
    const bf16* pf = (fm ? w3 : w1) + (size_t)(R0 + fr5) * FF + 48 * h + 8 * fck;
    F0 = ntld(pf);
    F1 = ntld(pf + 5 * FF);
  }
  const size_t kvbase = ((size_t)h * SK + (SK - WIN) + (size_t)sp * KEYS + 64 * w + g) * DH + 8 * c;
  u32x4 K[8][3], V[8][3];
  SCHED_FENCE();
#pragma unroll
  for (int t = 0; t < KEARLY; ++t)
#pragma unroll
    for (int j = 0; j < 3; ++j) K[t][j] = ntld(kc + kvbase + t * 8 * DH + 64 * j);
  SCHED_FENCE();

  float* xnw = lds + L_XS + w * D;
  {
    float xf[8], xg[8];
#pragma unroll
    for (int i = 0; i < 4; ++i) {
      xf[2 * i] = bf_lo(xa[i]);
      xf[2 * i + 1] = bf_hi(xa[i]);
      xg[2 * i] = bf_lo(xb[i]);
      xg[2 * i + 1] = bf_hi(xb[i]);
    }
    const bool lo16 = lane < 16;
    float sum = 0.f;
#pragma unroll
    for (int i = 0; i < 8; ++i) sum += xf[i] + (lo16 ? xg[i] : 0.f);
    sum = wave_sum(sum);
    const float mean = sum * (1.0f / D);
    float sq = 0.f;
#pragma unroll
    for (int i = 0; i < 8; ++i) {
      const float d1 = xf[i] - mean, d2 = xg[i] - mean;
      sq += d1 * d1 + (lo16 ? d2 * d2 : 0.f);
    }
    sq = wave_sum(sq);
    const float mb = bfr(mean);
    const float vb = bfr(sq * (1.0f / D));
    const float den = bfr(sqrtf(bfr(vb + 1e-5f)));
    f32x4 o0, o1, o2, o3;
#pragma unroll
    for (int i = 0; i < 4; ++i) {
      o0[i] = bfr(bfr(xf[i] - mb) / den);
      o1[i] = bfr(bfr(xf[4 + i] - mb) / den);
      o2[i] = bfr(bfr(xg[i] - mb) / den);
      o3[i] = bfr(bfr(xg[4 + i] - mb) / den);
    }
    *reinterpret_cast<f32x4*>(xnw + 8 * lane) = o0;
    *reinterpret_cast<f32x4*>(xnw + 8 * lane + 4) = o1;
    if (lo16) {
      *reinterpret_cast<f32x4*>(xnw + 512 + 8 * lane) = o2;
      *reinterpret_cast<f32x4*>(xnw + 512 + 8 * lane + 4) = o3;
    }
  }
  WAVE_SYNC();
  {
    float acc[24];
#pragma unroll
    for (int i = 0; i < 24; ++i) acc[i] = 0.f;
    const unsigned x0 = fbits(xnw[R0 + g]);
    const unsigned x1 = g < 2 ? fbits(xnw[R0 + 8 + (g & 1)]) : 0u;
#pragma unroll
    for (int j = 0; j < 3; ++j)
#pragma unroll
      for (int i = 0; i < 4; ++i) {
        acc[8 * j + 2 * i] = dot2(WQ[1][j][i], x1 >> 16, dot2(WQ[0][j][i], x0 >> 16, 0.f));
        acc[8 * j + 2 * i + 1] = dot2(WQ[1][j][i], x1, dot2(WQ[0][j][i], x0, 0.f));
      }
#pragma unroll
    for (int i = 0; i < 24; ++i) acc[i] = xg_sum(acc[i]);
    if (g == 0) {
      float* qp = lds + L_PART + w * DH;
#pragma unroll
      for (int j = 0; j < 3; ++j) {
        *reinterpret_cast<f32x4*>(qp + (c + 8 * j) * 8) = f32x4{acc[8 * j], acc[8 * j + 1], acc[8 * j + 2], acc[8 * j + 3]};
        *reinterpret_cast<f32x4*>(qp + (c + 8 * j) * 8 + 4) =
            f32x4{acc[8 * j + 4], acc[8 * j + 5], acc[8 * j + 6], acc[8 * j + 7]};
      }
    }
    const unsigned y0 = fbits(xnw[R0 + fr5]), y1 = fbits(xnw[R0 + 5 + fr5]);
    float fg[8];
#pragma unroll
    for (int i = 0; i < 4; ++i) {
      fg[2 * i] = dot2(F1[i], y1 >> 16, dot2(F0[i], y0 >> 16, 0.f));
      fg[2 * i + 1] = dot2(F1[i], y1, dot2(F0[i], y0, 0.f));
    }
    if (lane < 60) {
      float* fp = lds + L_FFP + (w * 5 + fr5) * 96 + fm * 48 + 8 * fck;
      *reinterpret_cast<f32x4*>(fp) = f32x4{fg[0], fg[1], fg[2], fg[3]};
      *reinterpret_cast<f32x4*>(fp + 4) = f32x4{fg[4], fg[5], fg[6], fg[7]};
    }
  }
  const int u = tid < 480 ? tid : 479;
  const int rg = u / 80, ch = u - 80 * rg;
  u32x4 WO[5];
  const unsigned tag = __builtin_amdgcn_readfirstlane(epoch) + 1u;
  STAMP(1);
  __syncthreads();
  STAMP(2);
  if constexpr (!CTRL) {
    SCHED_FENCE();
#pragma unroll
    for (int t = KEARLY; t < 8; ++t)
#pragma unroll
      for (int j = 0; j < 3; ++j) K[t][j] = ntld(kc + kvbase + t * 8 * DH + 64 * j);
#pragma unroll
    for (int t = 0; t < VEARLY; ++t)
#pragma unroll
      for (int j = 0; j < 3; ++j) V[t][j] = ntld(vc + kvbase + t * 8 * DH + 64 * j);
    SCHED_FENCE();
  }

  if constexpr (CTRL) {
#pragma unroll
    for (int k = 0; k < 3; ++k) {
      float q = 0.f;
#pragma unroll
      for (int ww = 0; ww < 8; ++ww) q += lds[L_PART + ww * DH + 64 * k + lane];
      gst(gw + G1 + (size_t)rec * 288 + 64 * k + lane, q, tag);
    }
#pragma unroll
    for (int k = 0; k < 2; ++k) {
      const int e = 64 * k + lane;
      if (e < 96) {
        float v = 0.f;
#pragma unroll 8
        for (int r = 0; r < 40; ++r) v += lds[L_FFP + r * 96 + e];
        gst(gw + G1 + (size_t)rec * 288 + 192 + e, v, tag);
      }
    }
    const int l12 = lane < 12 ? lane : 0;
    const int fidx = 192 + (l12 / 6) * 48 + 6 * sp + (l12 % 6);
    const u64* gb = gw + G1 + (size_t)h * NSP * 288;
    u64 v[3][NSP], f[NSP];
    for (unsigned spin = 0;; ++spin) {
      bool ok = true;
#pragma unroll
      for (int s = 0; s < NSP; ++s) {
#pragma unroll
        for (int k = 0; k < 3; ++k) {
          v[k][s] = gld(gb + s * 288 + 64 * k + lane);
          ok = ok && gok(v[k][s]);
        }
        f[s] = gld(gb + s * 288 + fidx);
        ok = ok && gok(f[s]);
      }
      if (__all(ok) || spin >= SPIN_MAX) break;
      __builtin_amdgcn_s_sleep(1);
    }
#pragma unroll
    for (int k = 0; k < 3; ++k) {
      const float q = ((gval(v[k][0]) + gval(v[k][1])) + (gval(v[k][2]) + gval(v[k][3]))) +
                      ((gval(v[k][4]) + gval(v[k][5])) + (gval(v[k][6]) + gval(v[k][7])));
      reinterpret_cast<unsigned short*>(lds + L_QS)[64 * k + lane] = bfbits(q);
    }
    {
      const float a = bfr(((gval(f[0]) + gval(f[1])) + (gval(f[2]) + gval(f[3]))) +
                          ((gval(f[4]) + gval(f[5])) + (gval(f[6]) + gval(f[7]))));
      const float up = __shfl(a, (lane + 6) & 63);
      if (lane < 6) lds[L_CV + 24 + lane] = bfr(bfr(silu(a)) * up);
    }
    SCHED_FENCE();
    STAMP(3);
  }
  __syncthreads();
  STAMP(4);
  SCHED_FENCE();
  if constexpr (CTRL) {
#pragma unroll
    for (int t = KEARLY; t < 8; ++t)
#pragma unroll
      for (int j = 0; j < 3; ++j) K[t][j] = ntld(kc + kvbase + t * 8 * DH + 64 * j);
  }
#pragma unroll
  for (int t = CTRL ? 0 : VEARLY; t < 8; ++t)
#pragma unroll
    for (int j = 0; j < 3; ++j) V[t][j] = ntld(vc + kvbase + t * 8 * DH + 64 * j);
#pragma unroll
  for (int i = 0; i < 4; ++i) WO[i] = ntld(wo + ((size_t)24 * rec + rg + 6 * i) * D + 8 * ch);
  WO[4] = ntld(w2 + ((size_t)6 * rec + rg) * D + 8 * ch);
  SCHED_FENCE();

  u32x4 qf[3];
#pragma unroll
  for (int j = 0; j < 3; ++j) qf[j] = *reinterpret_cast<const u32x4*>(lds + L_QS + (c + 8 * j) * 4);
  float sc[8];
#pragma unroll
  for (int t = 0; t < 8; ++t) {
    float d = 0.f;
#pragma unroll
    for (int j = 0; j < 3; ++j)
#pragma unroll
      for (int i = 0; i < 4; ++i) d = dot2(K[t][j][i], qf[j][i], d);
    d = grp8_sum(d);
    sc[t] = bfr(bfr(d) * SCALE);
  }
  float mw = sc[0];
#pragma unroll
  for (int t = 1; t < 8; ++t) mw = fmaxf(mw, sc[t]);
  mw = xg_max(mw);
  float lw = 0.f;
#pragma unroll
  for (int t = 0; t < 8; ++t) {
    sc[t] = ex2((sc[t] - mw) * LOG2E);
    lw += sc[t];
  }
  lw = xg_sum(lw);
  STAMP(5);
  {
    float acc[24];
#pragma unroll
    for (int i = 0; i < 24; ++i) acc[i] = 0.f;
#pragma unroll
    for (int t = 0; t < 8; ++t) {
      const unsigned plo = bfbits(sc[t]), phi = plo << 16;
#pragma unroll
      for (int j = 0; j < 3; ++j)
#pragma unroll
        for (int i = 0; i < 4; ++i) {
          acc[8 * j + 2 * i] = dot2(V[t][j][i], plo, acc[8 * j + 2 * i]);
          acc[8 * j + 2 * i + 1] = dot2(V[t][j][i], phi, acc[8 * j + 2 * i + 1]);
        }
    }
#pragma unroll
    for (int i = 0; i < 24; ++i) acc[i] = xg_sum(acc[i]);
    if (g == 0) {
      float* op = lds + L_PART + w * DH;
#pragma unroll
      for (int j = 0; j < 3; ++j) {
        *reinterpret_cast<f32x4*>(op + (c + 8 * j) * 8) = f32x4{acc[8 * j], acc[8 * j + 1], acc[8 * j + 2], acc[8 * j + 3]};
        *reinterpret_cast<f32x4*>(op + (c + 8 * j) * 8 + 4) =
            f32x4{acc[8 * j + 4], acc[8 * j + 5], acc[8 * j + 6], acc[8 * j + 7]};
      }
      if (c == 0) {
        lds[L_ML + 2 * w] = mw;
        lds[L_ML + 2 * w + 1] = lw;
      }
    }
  }
  STAMP(6);
  __syncthreads();
  STAMP(7);
  if (tid < DH) {
    float mm[8];
    float M = -INFINITY;
#pragma unroll
    for (int ww = 0; ww < 8; ++ww) {
      mm[ww] = lds[L_ML + 2 * ww];
      M = fmaxf(M, mm[ww]);
    }
    float L = 0.f, a = 0.f;
#pragma unroll
    for (int ww = 0; ww < 8; ++ww) {
      mm[ww] = ex2((mm[ww] - M) * LOG2E);
      L = fmaf(mm[ww], lds[L_ML + 2 * ww + 1], L);
      a = fmaf(mm[ww], lds[L_PART + ww * DH + tid], a);
    }
    u64* pr = gw + G2 + (size_t)rec * 200;
    gst(pr + 8 + tid, a, tag);
    if (tid < 2) gst(pr + tid, tid ? L : M, tag);
  }
  if constexpr (CTRL) {
    const u64* pr = gw + G2 + (size_t)(h * NSP + g) * 200;
    u64 va[3], vml;
    for (unsigned spin = 0;; ++spin) {
      bool ok = true;
#pragma unroll
      for (int i = 0; i < 3; ++i) {
        va[i] = gld(pr + 8 + 24 * sp + c + 8 * i);
        ok = ok && gok(va[i]);
      }
      vml = gld(pr + (c & 1));
      ok = ok && gok(vml);
      if (__all(ok) || spin >= SPIN_MAX) break;
      __builtin_amdgcn_s_sleep(1);
    }
    const float ms = __shfl(gval(vml), lane & 56), ls = __shfl(gval(vml), (lane & 56) + 1);
    const float M = xg_max(ms);
    const float e = ex2((ms - M) * LOG2E);
    const float L = xg_sum(e * ls);
#pragma unroll
    for (int i = 0; i < 3; ++i) {
      const float o = xg_sum(e * gval(va[i]));
      if (g == 0) lds[L_CV + c + 8 * i] = bfr(o / L);
    }
  }
  __syncthreads();
  STAMP(8);
  {
    float y[8], z[8];
#pragma unroll
    for (int i = 0; i < 8; ++i) y[i] = 0.f;
#pragma unroll
    for (int i = 0; i < 4; ++i) {
      const unsigned chi = fbits(lds[L_CV + rg + 6 * i]), clo = chi >> 16;
#pragma unroll
      for (int k = 0; k < 4; ++k) {
        y[2 * k] = dot2(WO[i][k], clo, y[2 * k]);
        y[2 * k + 1] = dot2(WO[i][k], chi, y[2 * k + 1]);
      }
    }
    {
      const unsigned chi = fbits(lds[L_CV + 24 + rg]), clo = chi >> 16;
#pragma unroll
      for (int k = 0; k < 4; ++k) {
        z[2 * k] = dot2(WO[4][k], clo, 0.f);
        z[2 * k + 1] = dot2(WO[4][k], chi, 0.f);
      }
    }
    if (tid < 480) {
      float* yp = lds + L_XS + rg * D + 8 * ch;
      float* zp = lds + L_XS + 6 * D + rg * D + 8 * ch;
      *reinterpret_cast<f32x4*>(yp) = f32x4{y[0], y[1], y[2], y[3]};
      *reinterpret_cast<f32x4*>(yp + 4) = f32x4{y[4], y[5], y[6], y[7]};
      *reinterpret_cast<f32x4*>(zp) = f32x4{z[0], z[1], z[2], z[3]};
      *reinterpret_cast<f32x4*>(zp + 4) = f32x4{z[4], z[5], z[6], z[7]};
    }
  }
  __syncthreads();
#pragma unroll
  for (int k = 0; k < 3; ++k) {
    const int col = tid + NT * k;
    if (col < 2 * D) {
      const int which = col >= D ? 1 : 0;
      const int cc = col - which * D;
      const float* rp = lds + L_XS + which * 6 * D + cc;
      const float v = ((rp[0] + rp[D]) + (rp[2 * D] + rp[3 * D])) + (rp[4 * D] + rp[5 * D]);
      const int cg = cc / FCOLS, ci = cc - FCOLS * cg;
      gst(gw + G3 + (((size_t)cg * 2 + which) * NWG + rec) * FCOLS + ci, v, tag);
    }
  }
  STAMP(9);
  const int b = blockIdx.x;
  if (b < NFIN) {
    const u64* pg = gw + G3 + ((size_t)b * 2 * NWG + tid) * FCOLS;
    u64 v[FCOLS];
    for (unsigned spin = 0;; ++spin) {
      bool ok = true;
#pragma unroll
      for (int k = 0; k < FCOLS; ++k) {
        v[k] = gld(pg + k);
        ok = ok && gok(v[k]);
      }
      if (__all(ok) || spin >= SPIN_MAX) break;
      __builtin_amdgcn_s_sleep(1);
    }
#pragma unroll
    for (int k = 0; k < FCOLS; ++k) {
      const float s = wave_sum(gval(v[k]));
      if (lane == 0) lds[L_FIN + w * 8 + k] = s;
    }
    STAMP(10);
    __syncthreads();
    if (tid < FCOLS) {
      const float* fp = lds + L_FIN + tid;
      const float sb = bfr((fp[0] + fp[8]) + (fp[16] + fp[24]));
      const float fb = bfr((fp[32] + fp[40]) + (fp[48] + fp[56]));
      const float gb2 = bfr(silu(fb));
      const int col = FCOLS * b + tid;
      out[col] = __float2bfloat16(__bfloat162float(x[col]) + bfr(sb * gb2));
      if (b == 0 && tid == 0)
        __hip_atomic_store(reinterpret_cast<unsigned*>(gw + GEP), tag, __ATOMIC_RELAXED, __HIP_MEMORY_SCOPE_AGENT);
    }
  }
  STAMP(11);
  if (LEAKCOND) while (__builtin_amdgcn_s_memrealtime() < tleak + 4000) __builtin_amdgcn_s_sleep(1);
}

__global__ void __launch_bounds__(NT, 2)
fused_decode(const bf16* __restrict__ x, const bf16* __restrict__ kc, const bf16* __restrict__ vc,
             const bf16* __restrict__ wq, const bf16* __restrict__ wo, const bf16* __restrict__ w1,
             const bf16* __restrict__ w3, const bf16* __restrict__ w2, u64* __restrict__ gw, bf16* __restrict__ out) {
  __shared__ __attribute__((aligned(16))) float lds[L_END];
  const int b = blockIdx.x;
  const int xcd = b & 7, jj = b >> 3;
  const int h = xcd * 4 + (jj >> 3), sp = jj & 7;
  const int w = __builtin_amdgcn_readfirstlane(threadIdx.x >> 6);
  if (w == 0)
    body<true>(lds, h, sp, w, x, kc, vc, wq, wo, w1, w3, w2, gw, out);
  else
    body<false>(lds, h, sp, w, x, kc, vc, wq, wo, w1, w3, w2, gw, out);
}

}

extern "C" void kernel_launch(void* const* d_in, const int* in_sizes, int n_in, void* d_out, int out_size, void* d_ws,
                              size_t ws_size, hipStream_t stream) {
  const bf16* x = (const bf16*)d_in[0];
  const bf16* kc = (const bf16*)d_in[1];
  const bf16* vc = (const bf16*)d_in[2];
  const bf16* wq = (const bf16*)d_in[3];
  const bf16* wo = (const bf16*)d_in[4];
  const bf16* w1 = (const bf16*)d_in[6];
  const bf16* w3 = (const bf16*)d_in[7];
  const bf16* w2 = (const bf16*)d_in[8];
  u64* gw = (u64*)d_ws;
  fused_decode<<<NWG, NT, 0, stream>>>(x, kc, vc, wq, wo, w1, w3, w2, gw, (bf16*)d_out);
}
```

```cpp
#include <hip/hip_runtime.h>
#include <hip/hip_bf16.h>
#include <stdint.h>

typedef __hip_bfloat16 bf16;
typedef unsigned long long u64;
typedef unsigned u32x4 __attribute__((ext_vector_type(4)));
typedef float f32x4 __attribute__((ext_vector_type(4)));
typedef __bf16 bf16x2_t __attribute__((ext_vector_type(2)));

namespace {

constexpr int D = 640, HQ = 32, DH = 192, SK = 32768, WIN = 4096, FF = 1536;
constexpr int NSP = 8, KEYS = WIN / NSP;
constexpr int NWG = HQ * NSP;
constexpr int NT = 512;
constexpr float SCALE = 0.07216878364870322f;
constexpr float LOG2E = 1.4426950408889634f;
constexpr unsigned SPIN_MAX = 1u << 16;
#define VEARLY 4
#define KEARLY 3
constexpr int NFIN = 128, FCOLS = 5;

constexpr size_t G1 = 0;
constexpr size_t G2 = G1 + (size_t)NWG * 288;
constexpr size_t G3 = G2 + (size_t)NWG * 200;
constexpr size_t GEP = G3 + (size_t)NFIN * 2 * NWG * FCOLS;
constexpr size_t GEND = GEP + 16;

__device__ __forceinline__ unsigned fbits(float x) { return __builtin_bit_cast(unsigned, x); }
__device__ __forceinline__ void gst(u64* p, float v, unsigned tag) {
  __hip_atomic_store(p, ((u64)tag << 32) | (u64)fbits(v), __ATOMIC_RELAXED, __HIP_MEMORY_SCOPE_AGENT);
}
__device__ __forceinline__ u64 gld(const u64* p) {
  return __hip_atomic_load(p, __ATOMIC_RELAXED, __HIP_MEMORY_SCOPE_AGENT);
}
#define gok(v) ((unsigned)((v) >> 32) == tag)
__device__ __forceinline__ float gval(u64 v) { return __builtin_bit_cast(float, (unsigned)v); }

__device__ __forceinline__ float bf_lo(unsigned u) { return __builtin_bit_cast(float, u << 16); }
__device__ __forceinline__ float bf_hi(unsigned u) { return __builtin_bit_cast(float, u & 0xffff0000u); }
__device__ __forceinline__ float bfr(float x) { return __bfloat162float(__float2bfloat16(x)); }
__device__ __forceinline__ unsigned short bfbits(float x) {
  return __builtin_bit_cast(unsigned short, __float2bfloat16(x));
}
__device__ __forceinline__ float dot2(unsigned a, unsigned b, float c) {
  return __builtin_amdgcn_fdot2_f32_bf16(__builtin_bit_cast(bf16x2_t, a), __builtin_bit_cast(bf16x2_t, b), c, false);
}
template <int CTRL>
__device__ __forceinline__ float dpp(float x) {
  return __builtin_bit_cast(float, __builtin_amdgcn_mov_dpp(__builtin_bit_cast(int, x), CTRL, 0xf, 0xf, true));
}
__device__ __forceinline__ float grp8_sum(float x) {
  x += dpp<0xB1>(x);
  x += dpp<0x4E>(x);
  x += dpp<0x141>(x);
  return x;
}
__device__ __forceinline__ float xg_sum(float x) {
  x += dpp<0x128>(x);
  auto s = __builtin_amdgcn_permlane16_swap(fbits(x), fbits(x), false, false);
  x = __builtin_bit_cast(float, (unsigned)s[0]) + __builtin_bit_cast(float, (unsigned)s[1]);
  auto t = __builtin_amdgcn_permlane32_swap(fbits(x), fbits(x), false, false);
  return __builtin_bit_cast(float, (unsigned)t[0]) + __builtin_bit_cast(float, (unsigned)t[1]);
}
__device__ __forceinline__ float xg_max(float x) {
  x = fmaxf(x, dpp<0x128>(x));
  auto s = __builtin_amdgcn_permlane16_swap(fbits(x), fbits(x), false, false);
  x = fmaxf(__builtin_bit_cast(float, (unsigned)s[0]), __builtin_bit_cast(float, (unsigned)s[1]));
  auto t = __builtin_amdgcn_permlane32_swap(fbits(x), fbits(x), false, false);
  return fmaxf(__builtin_bit_cast(float, (unsigned)t[0]), __builtin_bit_cast(float, (unsigned)t[1]));
}
__device__ __forceinline__ float wave_sum(float x) { return xg_sum(grp8_sum(x)); }
__device__ __forceinline__ float ex2(float x) { return __builtin_amdgcn_exp2f(x); }
__device__ __forceinline__ float silu(float x) { return x / (1.0f + __expf(-x)); }
__device__ __forceinline__ u32x4 ntld(const bf16* p) {
  return __builtin_nontemporal_load(reinterpret_cast<const u32x4*>(p));
}

#define SCHED_FENCE() __builtin_amdgcn_sched_barrier(0)
#define WAVE_SYNC()                                        \
  do {                                                     \
    __builtin_amdgcn_fence(__ATOMIC_RELEASE, "wavefront"); \
    __builtin_amdgcn_wave_barrier();                       \
    __builtin_amdgcn_fence(__ATOMIC_ACQUIRE, "wavefront"); \
  } while (0)

#define STAMP(i)
constexpr int L_XS = 0;
constexpr int L_PART = L_XS + 12 * D;
constexpr int L_FFP = L_PART + 8 * DH;
constexpr int L_QS = L_FFP + 40 * 96;
constexpr int L_ML = L_QS + 96;
constexpr int L_CV = L_ML + 16;
constexpr int L_FIN = L_CV + 32;
constexpr int L_END = L_FIN + 64;

template <bool CTRL>
__device__ __forceinline__ void body(float* lds, const int h, const int sp, const int w, const bf16* __restrict__ x,
                                     const bf16* __restrict__ kc, const bf16* __restrict__ vc,
                                     const bf16* __restrict__ wq, const bf16* __restrict__ wo,
                                     const bf16* __restrict__ w1, const bf16* __restrict__ w3,
                                     const bf16* __restrict__ w2, u64* __restrict__ gw, bf16* __restrict__ out) {
  const int tid = threadIdx.x, lane = tid & 63;
  const int g = lane >> 3, c = lane & 7;
  const int rec = h * NSP + sp;
  const int R0 = 80 * sp + 10 * w;

  STAMP(0);
  const unsigned epoch = __hip_atomic_load(reinterpret_cast<unsigned*>(gw + GEP), __ATOMIC_RELAXED, __HIP_MEMORY_SCOPE_AGENT);
  const u32x4 xa = *reinterpret_cast<const u32x4*>(x + 8 * lane);
  const u32x4 xb = *reinterpret_cast<const u32x4*>(x + 512 + 8 * (lane & 15));
  u32x4 WQ[2][3];
  {
    const bf16* p0 = wq + (size_t)(R0 + g) * (HQ * DH) + h * DH + 8 * c;
    const bf16* p1 = wq + (size_t)(R0 + 8 + (g & 1)) * (HQ * DH) + h * DH + 8 * c;
#pragma unroll
    for (int j = 0; j < 3; ++j) WQ[0][j] = ntld(p0 + 64 * j);
#pragma unroll
    for (int j = 0; j < 3; ++j) WQ[1][j] = ntld(p1 + 64 * j);
  }
  const int lf = lane < 60 ? lane : 59;
  const int fm = lf / 30, fr5 = (lf - 30 * fm) / 6, fck = lf - 30 * fm - 6 * fr5;
  u32x4 F0, F1;
  {
    const bf16* pf = (fm ? w3 : w1) + (size_t)(R0 + fr5) * FF + 48 * h + 8 * fck;
    F0 = ntld(pf);
    F1 = ntld(pf + 5 * FF);
  }
  const size_t kvbase = ((size_t)h * SK + (SK - WIN) + (size_t)sp * KEYS + 64 * w + g) * DH + 8 * c;
  u32x4 K[8][3], V[8][3];
  SCHED_FENCE();
#pragma unroll
  for (int t = 0; t < KEARLY; ++t)
#pragma unroll
    for (int j = 0; j < 3; ++j) K[t][j] = ntld(kc + kvbase + t * 8 * DH + 64 * j);
  SCHED_FENCE();

  float* xnw = lds + L_XS + w * D;
  {
    float xf[8], xg[8];
#pragma unroll
    for (int i = 0; i < 4; ++i) {
      xf[2 * i] = bf_lo(xa[i]);
      xf[2 * i + 1] = bf_hi(xa[i]);
      xg[2 * i] = bf_lo(xb[i]);
      xg[2 * i + 1] = bf_hi(xb[i]);
    }
    const bool lo16 = lane < 16;
    float sum = 0.f;
#pragma unroll
    for (int i = 0; i < 8; ++i) sum += xf[i] + (lo16 ? xg[i] : 0.f);
    sum = wave_sum(sum);
    const float mean = sum * (1.0f / D);
    float sq = 0.f;
#pragma unroll
    for (int i = 0; i < 8; ++i) {
      const float d1 = xf[i] - mean, d2 = xg[i] - mean;
      sq += d1 * d1 + (lo16 ? d2 * d2 : 0.f);
    }
    sq = wave_sum(sq);
    const float mb = bfr(mean);
    const float vb = bfr(sq * (1.0f / D));
    const float den = bfr(sqrtf(bfr(vb + 1e-5f)));
    f32x4 o0, o1, o2, o3;
#pragma unroll
    for (int i = 0; i < 4; ++i) {
      o0[i] = bfr(bfr(xf[i] - mb) / den);
      o1[i] = bfr(bfr(xf[4 + i] - mb) / den);
      o2[i] = bfr(bfr(xg[i] - mb) / den);
      o3[i] = bfr(bfr(xg[4 + i] - mb) / den);
    }
    *reinterpret_cast<f32x4*>(xnw + 8 * lane) = o0;
    *reinterpret_cast<f32x4*>(xnw + 8 * lane + 4) = o1;
    if (lo16) {
      *reinterpret_cast<f32x4*>(xnw + 512 + 8 * lane) = o2;
      *reinterpret_cast<f32x4*>(xnw + 512 + 8 * lane + 4) = o3;
    }
  }
  WAVE_SYNC();
  {
    float acc[24];
#pragma unroll
    for (int i = 0; i < 24; ++i) acc[i] = 0.f;
    const unsigned x0 = fbits(xnw[R0 + g]);
    const unsigned x1 = g < 2 ? fbits(xnw[R0 + 8 + (g & 1)]) : 0u;
#pragma unroll
    for (int j = 0; j < 3; ++j)
#pragma unroll
      for (int i = 0; i < 4; ++i) {
        acc[8 * j + 2 * i] = dot2(WQ[1][j][i], x1 >> 16, dot2(WQ[0][j][i], x0 >> 16, 0.f));
        acc[8 * j + 2 * i + 1] = dot2(WQ[1][j][i], x1, dot2(WQ[0][j][i], x0, 0.f));
      }
#pragma unroll
    for (int i = 0; i < 24; ++i) acc[i] = xg_sum(acc[i]);
    if (g == 0) {
      float* qp = lds + L_PART + w * DH;
#pragma unroll
      for (int j = 0; j < 3; ++j) {
        *reinterpret_cast<f32x4*>(qp + (c + 8 * j) * 8) = f32x4{acc[8 * j], acc[8 * j + 1], acc[8 * j + 2], acc[8 * j + 3]};
        *reinterpret_cast<f32x4*>(qp + (c + 8 * j) * 8 + 4) =
            f32x4{acc[8 * j + 4], acc[8 * j + 5], acc[8 * j + 6], acc[8 * j + 7]};
      }
    }
    const unsigned y0 = fbits(xnw[R0 + fr5]), y1 = fbits(xnw[R0 + 5 + fr5]);
    float fg[8];
#pragma unroll
    for (int i = 0; i < 4; ++i) {
      fg[2 * i] = dot2(F1[i], y1 >> 16, dot2(F0[i], y0 >> 16, 0.f));
      fg[2 * i + 1] = dot2(F1[i], y1, dot2(F0[i], y0, 0.f));
    }
    if (lane < 60) {
      float* fp = lds + L_FFP + (w * 5 + fr5) * 96 + fm * 48 + 8 * fck;
      *reinterpret_cast<f32x4*>(fp) = f32x4{fg[0], fg[1], fg[2], fg[3]};
      *reinterpret_cast<f32x4*>(fp + 4) = f32x4{fg[4], fg[5], fg[6], fg[7]};
    }
  }
  const int u = tid < 480 ? tid : 479;
  const int rg = u / 80, ch = u - 80 * rg;
  u32x4 WO[5];
  const unsigned tag = __builtin_amdgcn_readfirstlane(epoch) + 1u;
  STAMP(1);
  __syncthreads();
  STAMP(2);
  if constexpr (!CTRL) {
    SCHED_FENCE();
#pragma unroll
    for (int t = KEARLY; t < 8; ++t)
#pragma unroll
      for (int j = 0; j < 3; ++j) K[t][j] = ntld(kc + kvbase + t * 8 * DH + 64 * j);
#pragma unroll
    for (int t = 0; t < VEARLY; ++t)
#pragma unroll
      for (int j = 0; j < 3; ++j) V[t][j] = ntld(vc + kvbase + t * 8 * DH + 64 * j);
    SCHED_FENCE();
  }

  if constexpr (CTRL) {
#pragma unroll
    for (int k = 0; k < 3; ++k) {
      float q = 0.f;
#pragma unroll
      for (int ww = 0; ww < 8; ++ww) q += lds[L_PART + ww * DH + 64 * k + lane];
      gst(gw + G1 + (size_t)rec * 288 + 64 * k + lane, q, tag);
    }
#pragma unroll
    for (int k = 0; k < 2; ++k) {
      const int e = 64 * k + lane;
      if (e < 96) {
        float v = 0.f;
#pragma unroll 8
        for (int r = 0; r < 40; ++r) v += lds[L_FFP + r * 96 + e];
        gst(gw + G1 + (size_t)rec * 288 + 192 + e, v, tag);
      }
    }
    const int l12 = lane < 12 ? lane : 0;
    const int fidx = 192 + (l12 / 6) * 48 + 6 * sp + (l12 % 6);
    const u64* gb = gw + G1 + (size_t)h * NSP * 288;
    u64 v[3][NSP], f[NSP];
    for (unsigned spin = 0;; ++spin) {
      bool ok = true;
#pragma unroll
      for (int s = 0; s < NSP; ++s) {
#pragma unroll
        for (int k = 0; k < 3; ++k) {
          v[k][s] = gld(gb + s * 288 + 64 * k + lane);
          ok = ok && gok(v[k][s]);
        }
        f[s] = gld(gb + s * 288 + fidx);
        ok = ok && gok(f[s]);
      }
      if (__all(ok) || spin >= SPIN_MAX) break;
      __builtin_amdgcn_s_sleep(1);
    }
#pragma unroll
    for (int k = 0; k < 3; ++k) {
      const float q = ((gval(v[k][0]) + gval(v[k][1])) + (gval(v[k][2]) + gval(v[k][3]))) +
                      ((gval(v[k][4]) + gval(v[k][5])) + (gval(v[k][6]) + gval(v[k][7])));
      reinterpret_cast<unsigned short*>(lds + L_QS)[64 * k + lane] = bfbits(q);
    }
    {
      const float a = bfr(((gval(f[0]) + gval(f[1])) + (gval(f[2]) + gval(f[3]))) +
                          ((gval(f[4]) + gval(f[5])) + (gval(f[6]) + gval(f[7]))));
      const float up = __shfl(a, (lane + 6) & 63);
      if (lane < 6) lds[L_CV + 24 + lane] = bfr(bfr(silu(a)) * up);
    }
    SCHED_FENCE();
    STAMP(3);
  }
  __syncthreads();
  STAMP(4);
  SCHED_FENCE();
  if constexpr (CTRL) {
#pragma unroll
    for (int t = KEARLY; t < 8; ++t)
#pragma unroll
      for (int j = 0; j < 3; ++j) K[t][j] = ntld(kc + kvbase + t * 8 * DH + 64 * j);
  }
#pragma unroll
  for (int t = CTRL ? 0 : VEARLY; t < 8; ++t)
#pragma unroll
    for (int j = 0; j < 3; ++j) V[t][j] = ntld(vc + kvbase + t * 8 * DH + 64 * j);
#pragma unroll
  for (int i = 0; i < 4; ++i) WO[i] = ntld(wo + ((size_t)24 * rec + rg + 6 * i) * D + 8 * ch);
  WO[4] = ntld(w2 + ((size_t)6 * rec + rg) * D + 8 * ch);
  SCHED_FENCE();

  u32x4 qf[3];
#pragma unroll
  for (int j = 0; j < 3; ++j) qf[j] = *reinterpret_cast<const u32x4*>(lds + L_QS + (c + 8 * j) * 4);
  float sc[8];
#pragma unroll
  for (int t = 0; t < 8; ++t) {
    float d = 0.f;
#pragma unroll
    for (int j = 0; j < 3; ++j)
#pragma unroll
      for (int i = 0; i < 4; ++i) d = dot2(K[t][j][i], qf[j][i], d);
    d = grp8_sum(d);
    sc[t] = bfr(bfr(d) * SCALE);
  }
  float mw = sc[0];
#pragma unroll
  for (int t = 1; t < 8; ++t) mw = fmaxf(mw, sc[t]);
  mw = xg_max(mw);
  float lw = 0.f;
#pragma unroll
  for (int t = 0; t < 8; ++t) {
    sc[t] = ex2((sc[t] - mw) * LOG2E);
    lw += sc[t];
  }
  lw = xg_sum(lw);
  STAMP(5);
  {
    float acc[24];
#pragma unroll
    for (int i = 0; i < 24; ++i) acc[i] = 0.f;
#pragma unroll
    for (int t = 0; t < 8; ++t) {
      const unsigned plo = bfbits(sc[t]), phi = plo << 16;
#pragma unroll
      for (int j = 0; j < 3; ++j)
#pragma unroll
        for (int i = 0; i < 4; ++i) {
          acc[8 * j + 2 * i] = dot2(V[t][j][i], plo, acc[8 * j + 2 * i]);
          acc[8 * j + 2 * i + 1] = dot2(V[t][j][i], phi, acc[8 * j + 2 * i + 1]);
        }
    }
#pragma unroll
    for (int i = 0; i < 24; ++i) acc[i] = xg_sum(acc[i]);
    if (g == 0) {
      float* op = lds + L_PART + w * DH;
#pragma unroll
      for (int j = 0; j < 3; ++j) {
        *reinterpret_cast<f32x4*>(op + (c + 8 * j) * 8) = f32x4{acc[8 * j], acc[8 * j + 1], acc[8 * j + 2], acc[8 * j + 3]};
        *reinterpret_cast<f32x4*>(op + (c + 8 * j) * 8 + 4) =
            f32x4{acc[8 * j + 4], acc[8 * j + 5], acc[8 * j + 6], acc[8 * j + 7]};
      }
      if (c == 0) {
        lds[L_ML + 2 * w] = mw;
        lds[L_ML + 2 * w + 1] = lw;
      }
    }
  }
  STAMP(6);
  __syncthreads();
  STAMP(7);
  if (tid < DH) {
    float mm[8];
    float M = -INFINITY;
#pragma unroll
    for (int ww = 0; ww < 8; ++ww) {
      mm[ww] = lds[L_ML + 2 * ww];
      M = fmaxf(M, mm[ww]);
    }
    float L = 0.f, a = 0.f;
#pragma unroll
    for (int ww = 0; ww < 8; ++ww) {
      mm[ww] = ex2((mm[ww] - M) * LOG2E);
      L = fmaf(mm[ww], lds[L_ML + 2 * ww + 1], L);
      a = fmaf(mm[ww], lds[L_PART + ww * DH + tid], a);
    }
    u64* pr = gw + G2 + (size_t)rec * 200;
    gst(pr + 8 + tid, a, tag);
    if (tid < 2) gst(pr + tid, tid ? L : M, tag);
  }
  if constexpr (CTRL) {
    const u64* pr = gw + G2 + (size_t)(h * NSP + g) * 200;
    u64 va[3], vml;
    for (unsigned spin = 0;; ++spin) {
      bool ok = true;
#pragma unroll
      for (int i = 0; i < 3; ++i) {
        va[i] = gld(pr + 8 + 24 * sp + c + 8 * i);
        ok = ok && gok(va[i]);
      }
      vml = gld(pr + (c & 1));
      ok = ok && gok(vml);
      if (__all(ok) || spin >= SPIN_MAX) break;
      __builtin_amdgcn_s_sleep(1);
    }
    const float ms = __shfl(gval(vml), lane & 56), ls = __shfl(gval(vml), (lane & 56) + 1);
    const float M = xg_max(ms);
    const float e = ex2((ms - M) * LOG2E);
    const float L = xg_sum(e * ls);
#pragma unroll
    for (int i = 0; i < 3; ++i) {
      const float o = xg_sum(e * gval(va[i]));
      if (g == 0) lds[L_CV + c + 8 * i] = bfr(o / L);
    }
  }
  __syncthreads();
  STAMP(8);
  {
    float y[8], z[8];
#pragma unroll
    for (int i = 0; i < 8; ++i) y[i] = 0.f;
#pragma unroll
    for (int i = 0; i < 4; ++i) {
      const unsigned chi = fbits(lds[L_CV + rg + 6 * i]), clo = chi >> 16;
#pragma unroll
      for (int k = 0; k < 4; ++k) {
        y[2 * k] = dot2(WO[i][k], clo, y[2 * k]);
        y[2 * k + 1] = dot2(WO[i][k], chi, y[2 * k + 1]);
      }
    }
    {
      const unsigned chi = fbits(lds[L_CV + 24 + rg]), clo = chi >> 16;
#pragma unroll
      for (int k = 0; k < 4; ++k) {
        z[2 * k] = dot2(WO[4][k], clo, 0.f);
        z[2 * k + 1] = dot2(WO[4][k], chi, 0.f);
      }
    }
    if (tid < 480) {
      float* yp = lds + L_XS + rg * D + 8 * ch;
      float* zp = lds + L_XS + 6 * D + rg * D + 8 * ch;
      *reinterpret_cast<f32x4*>(yp) = f32x4{y[0], y[1], y[2], y[3]};
      *reinterpret_cast<f32x4*>(yp + 4) = f32x4{y[4], y[5], y[6], y[7]};
      *reinterpret_cast<f32x4*>(zp) = f32x4{z[0], z[1], z[2], z[3]};
      *reinterpret_cast<f32x4*>(zp + 4) = f32x4{z[4], z[5], z[6], z[7]};
    }
  }
  __syncthreads();
#pragma unroll
  for (int k = 0; k < 3; ++k) {
    const int col = tid + NT * k;
    if (col < 2 * D) {
      const int which = col >= D ? 1 : 0;
      const int cc = col - which * D;
      const float* rp = lds + L_XS + which * 6 * D + cc;
      const float v = ((rp[0] + rp[D]) + (rp[2 * D] + rp[3 * D])) + (rp[4 * D] + rp[5 * D]);
      const int cg = cc / FCOLS, ci = cc - FCOLS * cg;
      gst(gw + G3 + (((size_t)cg * 2 + which) * NWG + rec) * FCOLS + ci, v, tag);
    }
  }
  STAMP(9);
  const int b = blockIdx.x;
  if (b < NFIN) {
    const u64* pg = gw + G3 + ((size_t)b * 2 * NWG + tid) * FCOLS;
    u64 v[FCOLS];
    for (unsigned spin = 0;; ++spin) {
      bool ok = true;
#pragma unroll
      for (int k = 0; k < FCOLS; ++k) {
        v[k] = gld(pg + k);
        ok = ok && gok(v[k]);
      }
      if (__all(ok) || spin >= SPIN_MAX) break;
      __builtin_amdgcn_s_sleep(1);
    }
#pragma unroll
    for (int k = 0; k < FCOLS; ++k) {
      const float s = wave_sum(gval(v[k]));
      if (lane == 0) lds[L_FIN + w * 8 + k] = s;
    }
    STAMP(10);
    __syncthreads();
    if (tid < FCOLS) {
      const float* fp = lds + L_FIN + tid;
      const float sb = bfr((fp[0] + fp[8]) + (fp[16] + fp[24]));
      const float fb = bfr((fp[32] + fp[40]) + (fp[48] + fp[56]));
      const float gb2 = bfr(silu(fb));
      const int col = FCOLS * b + tid;
      out[col] = __float2bfloat16(__bfloat162float(x[col]) + bfr(sb * gb2));
      if (b == 0 && tid == 0)
        __hip_atomic_store(reinterpret_cast<unsigned*>(gw + GEP), tag, __ATOMIC_RELAXED, __HIP_MEMORY_SCOPE_AGENT);
    }
  }
}

__global__ void __launch_bounds__(NT, 2)
fused_decode(const bf16* __restrict__ x, const bf16* __restrict__ kc, const bf16* __restrict__ vc,
             const bf16* __restrict__ wq, const bf16* __restrict__ wo, const bf16* __restrict__ w1,
             const bf16* __restrict__ w3, const bf16* __restrict__ w2, u64* __restrict__ gw, bf16* __restrict__ out) {
  __shared__ __attribute__((aligned(16))) float lds[L_END];
  const int b = blockIdx.x;
  const int xcd = b & 7, jj = b >> 3;
  const int h = xcd * 4 + (jj >> 3), sp = jj & 7;
  const int w = __builtin_amdgcn_readfirstlane(threadIdx.x >> 6);
  if (w == 0)
    body<true>(lds, h, sp, w, x, kc, vc, wq, wo, w1, w3, w2, gw, out);
  else
    body<false>(lds, h, sp, w, x, kc, vc, wq, wo, w1, w3, w2, gw, out);
}

__global__ void prek(u64* gw) { u64 t = __builtin_amdgcn_s_memrealtime(); if (threadIdx.x == 0 && blockIdx.x == 0) gw[1u << 20] = t; }
__global__ void __launch_bounds__(NT, 2) prek2(u64* gw) { __shared__ float lds[L_END]; u64 t = __builtin_amdgcn_s_memrealtime(); lds[threadIdx.x] = (float)t; __syncthreads(); if (threadIdx.x == 0 && blockIdx.x == 0) gw[1u << 20] = t + (lds[5] == 1.2345f); }
}

extern "C" void kernel_launch(void* const* d_in, const int* in_sizes, int n_in, void* d_out, int out_size, void* d_ws,
                              size_t ws_size, hipStream_t stream) {
  const bf16* x = (const bf16*)d_in[0];
  const bf16* kc = (const bf16*)d_in[1];
  const bf16* vc = (const bf16*)d_in[2];
  const bf16* wq = (const bf16*)d_in[3];
  const bf16* wo = (const bf16*)d_in[4];
  const bf16* w1 = (const bf16*)d_in[6];
  const bf16* w3 = (const bf16*)d_in[7];
  const bf16* w2 = (const bf16*)d_in[8];
  u64* gw = (u64*)d_ws;
  prek<<<1, 64, 0, stream>>>(gw);
  fused_decode<<<NWG, NT, 0, stream>>>(x, kc, vc, wq, wo, w1, w3, w2, gw, (bf16*)d_out);
}
```

```cpp
#include <hip/hip_runtime.h>
#include <hip/hip_bf16.h>
#include <stdint.h>

typedef __hip_bfloat16 bf16;
typedef unsigned long long u64;
typedef unsigned u32x4 __attribute__((ext_vector_type(4)));
typedef float f32x4 __attribute__((ext_vector_type(4)));
typedef __bf16 bf16x2_t __attribute__((ext_vector_type(2)));

namespace {

constexpr int D = 640, HQ = 32, DH = 192, SK = 32768, WIN = 4096, FF = 1536;
constexpr int NSP = 8, KEYS = WIN / NSP;
constexpr int NWG = HQ * NSP;
constexpr int NT = 512;
constexpr float SCALE = 0.07216878364870322f;
constexpr float LOG2E = 1.4426950408889634f;
constexpr unsigned SPIN_MAX = 1u << 16;
#define CKE 8
#define WND 8
#define PQ 4
#define KEARLY 3

constexpr size_t G1 = 0;
constexpr size_t G2 = G1 + (size_t)NWG * 288;
constexpr size_t G3 = G2 + (size_t)NWG * 208;
constexpr size_t GEP = G3 + (size_t)D * 64;

__device__ __forceinline__ unsigned fbits(float x) { return __builtin_bit_cast(unsigned, x); }
__device__ __forceinline__ void gst(u64* p, float v, unsigned tag) {
  __hip_atomic_store(p, ((u64)tag << 32) | (u64)fbits(v), __ATOMIC_RELAXED, __HIP_MEMORY_SCOPE_AGENT);
}
__device__ __forceinline__ u64 gld(const u64* p) {
  return __hip_atomic_load(p, __ATOMIC_RELAXED, __HIP_MEMORY_SCOPE_AGENT);
}
#define gok(v) ((unsigned)((v) >> 32) == tag)
__device__ __forceinline__ float gval(u64 v) { return __builtin_bit_cast(float, (unsigned)v); }

__device__ __forceinline__ float bf_lo(unsigned u) { return __builtin_bit_cast(float, u << 16); }
__device__ __forceinline__ float bf_hi(unsigned u) { return __builtin_bit_cast(float, u & 0xffff0000u); }
__device__ __forceinline__ float bfr(float x) { return __bfloat162float(__float2bfloat16(x)); }
__device__ __forceinline__ unsigned short bfbits(float x) {
  return __builtin_bit_cast(unsigned short, __float2bfloat16(x));
}
__device__ __forceinline__ float dot2(unsigned a, unsigned b, float c) {
  return __builtin_amdgcn_fdot2_f32_bf16(__builtin_bit_cast(bf16x2_t, a), __builtin_bit_cast(bf16x2_t, b), c, false);
}
template <int CTRL>
__device__ __forceinline__ float dpp(float x) {
  return __builtin_bit_cast(float, __builtin_amdgcn_mov_dpp(__builtin_bit_cast(int, x), CTRL, 0xf, 0xf, true));
}
__device__ __forceinline__ float grp8_sum(float x) {
  x += dpp<0xB1>(x);
  x += dpp<0x4E>(x);
  x += dpp<0x141>(x);
  return x;
}
__device__ __forceinline__ float swap16_sum(float x) {
  auto s = __builtin_amdgcn_permlane16_swap(fbits(x), fbits(x), false, false);
  return __builtin_bit_cast(float, (unsigned)s[0]) + __builtin_bit_cast(float, (unsigned)s[1]);
}
__device__ __forceinline__ float swap32_sum(float x) {
  auto t = __builtin_amdgcn_permlane32_swap(fbits(x), fbits(x), false, false);
  return __builtin_bit_cast(float, (unsigned)t[0]) + __builtin_bit_cast(float, (unsigned)t[1]);
}
__device__ __forceinline__ float xg_sum(float x) {
  x += dpp<0x128>(x);
  return swap32_sum(swap16_sum(x));
}
__device__ __forceinline__ float xg_max(float x) {
  x = fmaxf(x, dpp<0x128>(x));
  auto s = __builtin_amdgcn_permlane16_swap(fbits(x), fbits(x), false, false);
  x = fmaxf(__builtin_bit_cast(float, (unsigned)s[0]), __builtin_bit_cast(float, (unsigned)s[1]));
  auto t = __builtin_amdgcn_permlane32_swap(fbits(x), fbits(x), false, false);
  return fmaxf(__builtin_bit_cast(float, (unsigned)t[0]), __builtin_bit_cast(float, (unsigned)t[1]));
}
__device__ __forceinline__ float wave_sum(float x) { return xg_sum(grp8_sum(x)); }
__device__ __forceinline__ float ex2(float x) { return __builtin_amdgcn_exp2f(x); }
__device__ __forceinline__ float silu(float x) { return x / (1.0f + __expf(-x)); }
__device__ __forceinline__ u32x4 ntld(const bf16* p) {
  return __builtin_nontemporal_load(reinterpret_cast<const u32x4*>(p));
}
__device__ __forceinline__ float rdlane(float x, int l) {
  return __builtin_bit_cast(float, __builtin_amdgcn_readlane(__builtin_bit_cast(int, x), l));
}

#define SCHED_FENCE() __builtin_amdgcn_sched_barrier(0)
#define WLD(dst, ptr)                                              \
  do {                                                             \
    asm volatile("s_waitcnt vmcnt(%0)" ::"n"(WND - 1) : "memory"); \
    dst = ntld(ptr);                                               \
    SCHED_FENCE();                                                 \
  } while (0)
#define WAVE_SYNC()                                        \
  do {                                                     \
    __builtin_amdgcn_fence(__ATOMIC_RELEASE, "wavefront"); \
    __builtin_amdgcn_wave_barrier();                       \
    __builtin_amdgcn_fence(__ATOMIC_ACQUIRE, "wavefront"); \
  } while (0)

#define STAMP(i)
constexpr int L_XS = 0;
constexpr int L_PART = L_XS + 2 * 48 * 80;
constexpr int L_FFP = L_PART + 8 * DH;
constexpr int L_QS = L_FFP + 40 * 96;
constexpr int L_ML = L_QS + 96;
constexpr int L_CV = L_ML + 16;
constexpr int L_GU = L_CV + 240;
constexpr int L_FLG = L_GU + 96;
constexpr int L_END = L_FLG + 4;

template <bool CTRL>
__device__ __forceinline__ void body(float* lds, const int h, const int sp, const int w, const bf16* __restrict__ x,
                                     const bf16* __restrict__ kc, const bf16* __restrict__ vc,
                                     const bf16* __restrict__ wq, const bf16* __restrict__ wo,
                                     const bf16* __restrict__ w1, const bf16* __restrict__ w3,
                                     const bf16* __restrict__ w2, u64* __restrict__ gw, bf16* __restrict__ out) {
  const int tid = threadIdx.x, lane = tid & 63;
  const int g = lane >> 3, c = lane & 7;
  const int rec = h * NSP + sp;
  const int R0 = 80 * sp + 10 * w;
  unsigned* flg = reinterpret_cast<unsigned*>(lds + L_FLG);

  STAMP(0);
  const unsigned epoch = __hip_atomic_load(reinterpret_cast<unsigned*>(gw + GEP), __ATOMIC_RELAXED, __HIP_MEMORY_SCOPE_AGENT);
  const u32x4 xa = *reinterpret_cast<const u32x4*>(x + 8 * lane);
  const u32x4 xb = *reinterpret_cast<const u32x4*>(x + 512 + 8 * (lane & 15));
  u32x4 WQ[2][3];
  {
    const bf16* p0 = wq + (size_t)(R0 + g) * (HQ * DH) + h * DH + 8 * c;
    const bf16* p1 = wq + (size_t)(R0 + 8 + (g & 1)) * (HQ * DH) + h * DH + 8 * c;
#pragma unroll
    for (int j = 0; j < 3; ++j) WQ[0][j] = ntld(p0 + 64 * j);
#pragma unroll
    for (int j = 0; j < 3; ++j) WQ[1][j] = ntld(p1 + 64 * j);
  }
  const int lf = lane < 60 ? lane : 59;
  const int fm = lf / 30, fr5 = (lf - 30 * fm) / 6, fck = lf - 30 * fm - 6 * fr5;
  u32x4 F0, F1;
  {
    const bf16* pf = (fm ? w3 : w1) + (size_t)(R0 + fr5) * FF + 48 * h + 8 * fck;
    F0 = ntld(pf);
    F1 = ntld(pf + 5 * FF);
  }
  const size_t kvbase = ((size_t)h * SK + (SK - WIN) + (size_t)sp * KEYS + 64 * ((w + (h >> 3)) & 7) + g) * DH + 8 * c;
#define KVOFF(t) ((((t) + h) & 7) * 8 * DH)
  u32x4 K[8][3], V[8][3];
  if (CTRL && lane < 2) flg[lane] = 0u;
  SCHED_FENCE();
  __syncthreads();
  SCHED_FENCE();
#pragma unroll
  for (int t = 0; t < (CTRL ? CKE : KEARLY); ++t)
#pragma unroll
    for (int j = 0; j < 3; ++j) K[t][j] = ntld(kc + kvbase + KVOFF(t) + 64 * j);
  SCHED_FENCE();
  STAMP(1);

  float* xnw = lds + L_XS + w * D;
  {
    float xf[8], xg[8];
#pragma unroll
    for (int i = 0; i < 4; ++i) {
      xf[2 * i] = bf_lo(xa[i]);
      xf[2 * i + 1] = bf_hi(xa[i]);
      xg[2 * i] = bf_lo(xb[i]);
      xg[2 * i + 1] = bf_hi(xb[i]);
    }
    const bool lo16 = lane < 16;
    float sum = 0.f;
#pragma unroll
    for (int i = 0; i < 8; ++i) sum += xf[i] + (lo16 ? xg[i] : 0.f);
    sum = wave_sum(sum);
    const float mean = sum * (1.0f / D);
    float sq = 0.f;
#pragma unroll
    for (int i = 0; i < 8; ++i) {
      const float d1 = xf[i] - mean, d2 = xg[i] - mean;
      sq += d1 * d1 + (lo16 ? d2 * d2 : 0.f);
    }
    sq = wave_sum(sq);
    const float mb = bfr(mean);
    const float vb = bfr(sq * (1.0f / D));
    const float den = bfr(sqrtf(bfr(vb + 1e-5f)));
    f32x4 o0, o1, o2, o3;
#pragma unroll
    for (int i = 0; i < 4; ++i) {
      o0[i] = bfr(bfr(xf[i] - mb) / den);
      o1[i] = bfr(bfr(xf[4 + i] - mb) / den);
      o2[i] = bfr(bfr(xg[i] - mb) / den);
      o3[i] = bfr(bfr(xg[4 + i] - mb) / den);
    }
    *reinterpret_cast<f32x4*>(xnw + 8 * lane) = o0;
    *reinterpret_cast<f32x4*>(xnw + 8 * lane + 4) = o1;
    if (lo16) {
      *reinterpret_cast<f32x4*>(xnw + 512 + 8 * lane) = o2;
      *reinterpret_cast<f32x4*>(xnw + 512 + 8 * lane + 4) = o3;
    }
  }
  WAVE_SYNC();
  {
    float acc[24];
    const unsigned x0 = fbits(xnw[R0 + g]);
    const unsigned x1 = g < 2 ? fbits(xnw[R0 + 8 + (g & 1)]) : 0u;
#pragma unroll
    for (int j = 0; j < 3; ++j)
#pragma unroll
      for (int i = 0; i < 4; ++i) {
        acc[8 * j + 2 * i] = dot2(WQ[1][j][i], x1 >> 16, dot2(WQ[0][j][i], x0 >> 16, 0.f));
        acc[8 * j + 2 * i + 1] = dot2(WQ[1][j][i], x1, dot2(WQ[0][j][i], x0, 0.f));
      }
#pragma unroll
    for (int i = 0; i < 24; ++i) acc[i] = xg_sum(acc[i]);
    if (g == 0) {
      float* qp = lds + L_PART + w * DH;
#pragma unroll
      for (int j = 0; j < 3; ++j) {
        *reinterpret_cast<f32x4*>(qp + (c + 8 * j) * 8) = f32x4{acc[8 * j], acc[8 * j + 1], acc[8 * j + 2], acc[8 * j + 3]};
        *reinterpret_cast<f32x4*>(qp + (c + 8 * j) * 8 + 4) =
            f32x4{acc[8 * j + 4], acc[8 * j + 5], acc[8 * j + 6], acc[8 * j + 7]};
      }
    }
    const unsigned y0 = fbits(xnw[R0 + fr5]), y1 = fbits(xnw[R0 + 5 + fr5]);
    float fg[8];
#pragma unroll
    for (int i = 0; i < 4; ++i) {
      fg[2 * i] = dot2(F1[i], y1 >> 16, dot2(F0[i], y0 >> 16, 0.f));
      fg[2 * i + 1] = dot2(F1[i], y1, dot2(F0[i], y0, 0.f));
    }
    if (lane < 60) {
      float* fp = lds + L_FFP + (w * 5 + fr5) * 96 + fm * 48 + 8 * fck;
      *reinterpret_cast<f32x4*>(fp) = f32x4{fg[0], fg[1], fg[2], fg[3]};
      *reinterpret_cast<f32x4*>(fp + 4) = f32x4{fg[4], fg[5], fg[6], fg[7]};
    }
  }
  const int u = tid < 480 ? tid : 479;
  const int rg = u / 10, ch = u - 10 * rg;
  u32x4 WO[5];
  const unsigned tag = __builtin_amdgcn_readfirstlane(epoch) + 1u;
  STAMP(2);
  if constexpr (!CTRL) {
    asm volatile("s_waitcnt lgkmcnt(0)" ::: "memory");
    if (lane == 0) __hip_atomic_fetch_add(flg, 1u, __ATOMIC_RELAXED, __HIP_MEMORY_SCOPE_WORKGROUP);
    SCHED_FENCE();
#pragma unroll
    for (int t = KEARLY; t < 8; ++t)
#pragma unroll
      for (int j = 0; j < 3; ++j) WLD(K[t][j], kc + kvbase + KVOFF(t) + 64 * j);
#pragma unroll
    for (int t = 0; t < PQ; ++t)
#pragma unroll
      for (int j = 0; j < 3; ++j) WLD(V[t][j], vc + kvbase + KVOFF(t) + 64 * j);
  } else {
    for (unsigned spin = 0; spin < SPIN_MAX; ++spin) {
      if (__hip_atomic_load(flg, __ATOMIC_RELAXED, __HIP_MEMORY_SCOPE_WORKGROUP) == 7u) break;
      __builtin_amdgcn_s_sleep(1);
    }
    asm volatile("s_waitcnt lgkmcnt(0)" ::: "memory");
#pragma unroll
    for (int k = 0; k < 3; ++k) {
      float q = 0.f;
#pragma unroll
      for (int ww = 0; ww < 8; ++ww) q += lds[L_PART + ww * DH + 64 * k + lane];
      gst(gw + G1 + (size_t)rec * 288 + 64 * k + lane, q, tag);
    }
#pragma unroll
    for (int k = 0; k < 2; ++k) {
      const int e = 64 * k + lane;
      if (e < 96) {
        float v = 0.f;
#pragma unroll 8
        for (int r = 0; r < 40; ++r) v += lds[L_FFP + r * 96 + e];
        gst(gw + G1 + (size_t)rec * 288 + 192 + e, v, tag);
      }
    }
    const u64* gb = gw + G1 + (size_t)h * NSP * 288;
    const int f1 = 256 + (lane & 31);
    u64 v[5][NSP];
    for (unsigned spin = 0;; ++spin) {
      bool ok = true;
#pragma unroll
      for (int s = 0; s < NSP; ++s) {
#pragma unroll
        for (int k = 0; k < 4; ++k) {
          v[k][s] = gld(gb + s * 288 + 64 * k + lane);
          ok = ok && gok(v[k][s]);
        }
        v[4][s] = gld(gb + s * 288 + f1);
        ok = ok && gok(v[4][s]);
      }
      if (__all(ok) || spin >= SPIN_MAX) break;
      __builtin_amdgcn_s_sleep(1);
    }
    float sm[5];
#pragma unroll
    for (int k = 0; k < 5; ++k)
      sm[k] = ((gval(v[k][0]) + gval(v[k][1])) + (gval(v[k][2]) + gval(v[k][3]))) +
              ((gval(v[k][4]) + gval(v[k][5])) + (gval(v[k][6]) + gval(v[k][7])));
#pragma unroll
    for (int k = 0; k < 3; ++k) reinterpret_cast<unsigned short*>(lds + L_QS)[64 * k + lane] = bfbits(sm[k]);
    asm volatile("s_waitcnt lgkmcnt(0)" ::: "memory");
    if (lane == 0) __hip_atomic_store(flg + 1, 1u, __ATOMIC_RELAXED, __HIP_MEMORY_SCOPE_WORKGROUP);
    SCHED_FENCE();
    STAMP(3);
#pragma unroll
    for (int t = CKE; t < 8; ++t)
#pragma unroll
      for (int j = 0; j < 3; ++j) K[t][j] = ntld(kc + kvbase + KVOFF(t) + 64 * j);
#pragma unroll
    for (int t = 0; t < PQ; ++t)
#pragma unroll
      for (int j = 0; j < 3; ++j) V[t][j] = ntld(vc + kvbase + KVOFF(t) + 64 * j);
    SCHED_FENCE();
    lds[L_GU + lane] = bfr(sm[3]);
    if (lane < 32) lds[L_GU + 64 + lane] = bfr(sm[4]);
    WAVE_SYNC();
    if (lane < 48) lds[L_CV + 192 + lane] = bfr(bfr(silu(lds[L_GU + lane])) * lds[L_GU + 48 + lane]);
    SCHED_FENCE();
  }
  if constexpr (!CTRL) {
    for (unsigned spin = 0; spin < (SPIN_MAX << 4); ++spin) {
      if (__hip_atomic_load(flg + 1, __ATOMIC_RELAXED, __HIP_MEMORY_SCOPE_WORKGROUP) != 0u) break;
      __builtin_amdgcn_s_sleep(2);
    }
    asm volatile("s_waitcnt lgkmcnt(0)" ::: "memory");
  }
  STAMP(4);
  SCHED_FENCE();

  u32x4 qf[3];
#pragma unroll
  for (int j = 0; j < 3; ++j) qf[j] = *reinterpret_cast<const u32x4*>(lds + L_QS + (c + 8 * j) * 4);
  float sc[8];
#pragma unroll
  for (int t = 0; t < 8; ++t) {
    float d = 0.f;
#pragma unroll
    for (int j = 0; j < 3; ++j)
#pragma unroll
      for (int i = 0; i < 4; ++i) d = dot2(K[t][j][i], qf[j][i], d);
    d = grp8_sum(d);
    sc[t] = bfr(bfr(d) * SCALE);
  }
  float mw = sc[0];
#pragma unroll
  for (int t = 1; t < 8; ++t) mw = fmaxf(mw, sc[t]);
  mw = xg_max(mw);
  float lw = 0.f;
#pragma unroll
  for (int t = 0; t < 8; ++t) {
    sc[t] = ex2((sc[t] - mw) * LOG2E);
    lw += sc[t];
  }
  lw = xg_sum(lw);
  STAMP(5);
  {
    float acc[24];
#pragma unroll
    for (int i = 0; i < 24; ++i) acc[i] = 0.f;
#pragma unroll
    for (int t = 0; t < 8; ++t) {
      if (t + PQ < 8) {
#pragma unroll
        for (int j = 0; j < 3; ++j) WLD(V[t + PQ][j], vc + kvbase + KVOFF(t + PQ) + 64 * j);
      }
      if (t + PQ == 8) {
#pragma unroll
        for (int i = 0; i < 4; ++i) WLD(WO[i], wo + ((size_t)DH * h + rg + 48 * i) * D + 80 * sp + 8 * ch);
        WLD(WO[4], w2 + ((size_t)48 * h + rg) * D + 80 * sp + 8 * ch);
      }
      const unsigned plo = bfbits(sc[t]), phi = plo << 16;
#pragma unroll
      for (int j = 0; j < 3; ++j)
#pragma unroll
        for (int i = 0; i < 4; ++i) {
          acc[8 * j + 2 * i] = dot2(V[t][j][i], plo, acc[8 * j + 2 * i]);
          acc[8 * j + 2 * i + 1] = dot2(V[t][j][i], phi, acc[8 * j + 2 * i + 1]);
        }
      SCHED_FENCE();
    }
#pragma unroll
    for (int i = 0; i < 24; ++i) acc[i] = xg_sum(acc[i]);
    if (g == 0) {
      float* op = lds + L_PART + w * DH;
#pragma unroll
      for (int j = 0; j < 3; ++j) {
        *reinterpret_cast<f32x4*>(op + (c + 8 * j) * 8) = f32x4{acc[8 * j], acc[8 * j + 1], acc[8 * j + 2], acc[8 * j + 3]};
        *reinterpret_cast<f32x4*>(op + (c + 8 * j) * 8 + 4) =
            f32x4{acc[8 * j + 4], acc[8 * j + 5], acc[8 * j + 6], acc[8 * j + 7]};
      }
      if (c == 0) {
        lds[L_ML + 2 * w] = mw;
        lds[L_ML + 2 * w + 1] = lw;
      }
    }
  }
  STAMP(6);
  __syncthreads();
  STAMP(7);
  if (tid < DH) {
    float mm[8];
    float M = -INFINITY;
#pragma unroll
    for (int ww = 0; ww < 8; ++ww) {
      mm[ww] = lds[L_ML + 2 * ww];
      M = fmaxf(M, mm[ww]);
    }
    float L = 0.f, a = 0.f;
#pragma unroll
    for (int ww = 0; ww < 8; ++ww) {
      mm[ww] = ex2((mm[ww] - M) * LOG2E);
      L = fmaf(mm[ww], lds[L_ML + 2 * ww + 1], L);
      a = fmaf(mm[ww], lds[L_PART + ww * DH + tid], a);
    }
    u64* pr = gw + G2 + (size_t)rec * 208;
    gst(pr + tid, a, tag);
    if (tid < 2) gst(pr + DH + tid, tid ? L : M, tag);
    const u64* pb = gw + G2 + (size_t)h * NSP * 208;
    u64 va[NSP], vml;
    for (unsigned spin = 0;; ++spin) {
      bool ok = true;
#pragma unroll
      for (int s = 0; s < NSP; ++s) {
        va[s] = gld(pb + s * 208 + tid);
        ok = ok && gok(va[s]);
      }
      vml = gld(pb + c * 208 + DH + (g & 1));
      ok = ok && gok(vml);
      if (__all(ok) || spin >= SPIN_MAX) break;
      __builtin_amdgcn_s_sleep(1);
    }
    float ms[NSP];
    float M2 = -INFINITY;
#pragma unroll
    for (int s = 0; s < NSP; ++s) {
      ms[s] = rdlane(gval(vml), s);
      M2 = fmaxf(M2, ms[s]);
    }
    float L2 = 0.f, o = 0.f;
#pragma unroll
    for (int s = 0; s < NSP; ++s) {
      const float e = ex2((ms[s] - M2) * LOG2E);
      L2 = fmaf(e, rdlane(gval(vml), 8 + s), L2);
      o = fmaf(e, gval(va[s]), o);
    }
    lds[L_CV + tid] = bfr(o / L2);
  }
  __syncthreads();
  STAMP(8);
  {
    float y[8], z[8];
#pragma unroll
    for (int i = 0; i < 8; ++i) y[i] = 0.f;
#pragma unroll
    for (int i = 0; i < 4; ++i) {
      const unsigned chi = fbits(lds[L_CV + rg + 48 * i]), clo = chi >> 16;
#pragma unroll
      for (int k = 0; k < 4; ++k) {
        y[2 * k] = dot2(WO[i][k], clo, y[2 * k]);
        y[2 * k + 1] = dot2(WO[i][k], chi, y[2 * k + 1]);
      }
    }
    {
      const unsigned chi = fbits(lds[L_CV + 192 + rg]), clo = chi >> 16;
#pragma unroll
      for (int k = 0; k < 4; ++k) {
        z[2 * k] = dot2(WO[4][k], clo, 0.f);
        z[2 * k + 1] = dot2(WO[4][k], chi, 0.f);
      }
    }
    if (tid < 480) {
      float* yp = lds + L_XS + rg * 80 + 8 * ch;
      float* zp = yp + 48 * 80;
      *reinterpret_cast<f32x4*>(yp) = f32x4{y[0], y[1], y[2], y[3]};
      *reinterpret_cast<f32x4*>(yp + 4) = f32x4{y[4], y[5], y[6], y[7]};
      *reinterpret_cast<f32x4*>(zp) = f32x4{z[0], z[1], z[2], z[3]};
      *reinterpret_cast<f32x4*>(zp + 4) = f32x4{z[4], z[5], z[6], z[7]};
    }
  }
  __syncthreads();
  if (tid < 160) {
    const int which = tid >= 80 ? 1 : 0;
    const int cc = tid - 80 * which;
    const float* rp = lds + L_XS + which * 48 * 80 + cc;
    float a0 = 0.f, a1 = 0.f, a2 = 0.f, a3 = 0.f;
#pragma unroll
    for (int r = 0; r < 48; r += 4) {
      a0 += rp[r * 80];
      a1 += rp[(r + 1) * 80];
      a2 += rp[(r + 2) * 80];
      a3 += rp[(r + 3) * 80];
    }
    gst(gw + G3 + ((size_t)(80 * sp + cc) * 2 + which) * 32 + h, (a0 + a1) + (a2 + a3), tag);
  }
  STAMP(9);
  if ((h & 1) == 0 && w < 5) {
    const int col = 80 * sp + 5 * (h >> 1) + w;
    const float xv = __bfloat162float(x[col]);
    const u64* pg = gw + G3 + (size_t)col * 64 + lane;
    u64 v;
    for (unsigned spin = 0;; ++spin) {
      v = gld(pg);
      if (__all(gok(v)) || spin >= SPIN_MAX) break;
      __builtin_amdgcn_s_sleep(1);
    }
    float s = grp8_sum(gval(v));
    s += dpp<0x128>(s);
    s = swap16_sum(s);
    STAMP(10);
    const float sb = bfr(rdlane(s, 0));
    const float fb = bfr(rdlane(s, 32));
    const float gb2 = bfr(silu(fb));
    if (lane == 0) {
      out[col] = __float2bfloat16(xv + bfr(sb * gb2));
      if (col == 0)
        __hip_atomic_store(reinterpret_cast<unsigned*>(gw + GEP), tag, __ATOMIC_RELAXED, __HIP_MEMORY_SCOPE_AGENT);
    }
  }
  STAMP(11);
}

__global__ void __launch_bounds__(NT, 2)
fused_decode(const bf16* __restrict__ x, const bf16* __restrict__ kc, const bf16* __restrict__ vc,
             const bf16* __restrict__ wq, const bf16* __restrict__ wo, const bf16* __restrict__ w1,
             const bf16* __restrict__ w3, const bf16* __restrict__ w2, u64* __restrict__ gw, bf16* __restrict__ out) {
  __shared__ __attribute__((aligned(16))) float lds[L_END];
  const int b = blockIdx.x;
  const int xcd = b & 7, jj = b >> 3;
  const int h = xcd * 4 + (jj >> 3), sp = jj & 7;
  const int w = __builtin_amdgcn_readfirstlane(threadIdx.x >> 6);
  if (w == 0)
    body<true>(lds, h, sp, w, x, kc, vc, wq, wo, w1, w3, w2, gw, out);
  else
    body<false>(lds, h, sp, w, x, kc, vc, wq, wo, w1, w3, w2, gw, out);
}

}

extern "C" void kernel_launch(void* const* d_in, const int* in_sizes, int n_in, void* d_out, int out_size, void* d_ws,
                              size_t ws_size, hipStream_t stream) {
  const bf16* x = (const bf16*)d_in[0];
  const bf16* kc = (const bf16*)d_in[1];
  const bf16* vc = (const bf16*)d_in[2];
  const bf16* wq = (const bf16*)d_in[3];
  const bf16* wo = (const bf16*)d_in[4];
  const bf16* w1 = (const bf16*)d_in[6];
  const bf16* w3 = (const bf16*)d_in[7];
  const bf16* w2 = (const bf16*)d_in[8];
  u64* gw = (u64*)d_ws;
  fused_decode<<<NWG, NT, 0, stream>>>(x, kc, vc, wq, wo, w1, w3, w2, gw, (bf16*)d_out);
}
```

```cpp
#include <hip/hip_runtime.h>
#include <hip/hip_bf16.h>
#include <stdint.h>

typedef __hip_bfloat16 bf16;
typedef unsigned long long u64;
typedef unsigned u32x4 __attribute__((ext_vector_type(4)));
typedef float f32x4 __attribute__((ext_vector_type(4)));
typedef __bf16 bf16x2_t __attribute__((ext_vector_type(2)));

namespace {

constexpr int D = 640, HQ = 32, DH = 192, SK = 32768, WIN = 4096, FF = 1536;
constexpr int NSP = 8, KEYS = WIN / NSP;
constexpr int NWG = HQ * NSP;
constexpr int NT = 512;
constexpr float SCALE = 0.07216878364870322f;
constexpr float LOG2E = 1.4426950408889634f;
constexpr unsigned SPIN_MAX = 1u << 16;
#define WND 8
#define PQ 3
#define FKE 0

constexpr size_t G1 = 0;
constexpr size_t G2 = G1 + (size_t)NWG * 288;
constexpr size_t G3 = G2 + (size_t)NWG * 208;
constexpr size_t GEP = G3 + (size_t)D * 64;

__device__ __forceinline__ unsigned fbits(float x) { return __builtin_bit_cast(unsigned, x); }
__device__ __forceinline__ float bitsf(unsigned x) { return __builtin_bit_cast(float, x); }
__device__ __forceinline__ void gst(u64* p, float v, unsigned tag) {
  __hip_atomic_store(p, ((u64)tag << 32) | (u64)fbits(v), __ATOMIC_RELAXED, __HIP_MEMORY_SCOPE_AGENT);
}
__device__ __forceinline__ u64 gld(const u64* p) {
  return __hip_atomic_load(p, __ATOMIC_RELAXED, __HIP_MEMORY_SCOPE_AGENT);
}
#define gok(v) ((unsigned)((v) >> 32) == tag)
__device__ __forceinline__ float gval(u64 v) { return __builtin_bit_cast(float, (unsigned)v); }

__device__ __forceinline__ float bf_lo(unsigned u) { return __builtin_bit_cast(float, u << 16); }
__device__ __forceinline__ float bf_hi(unsigned u) { return __builtin_bit_cast(float, u & 0xffff0000u); }
__device__ __forceinline__ float bfr(float x) { return __bfloat162float(__float2bfloat16(x)); }
__device__ __forceinline__ unsigned short bfbits(float x) {
  return __builtin_bit_cast(unsigned short, __float2bfloat16(x));
}
__device__ __forceinline__ float dot2(unsigned a, unsigned b, float c) {
  return __builtin_amdgcn_fdot2_f32_bf16(__builtin_bit_cast(bf16x2_t, a), __builtin_bit_cast(bf16x2_t, b), c, false);
}
template <int CTRL>
__device__ __forceinline__ float dpp(float x) {
  return __builtin_bit_cast(float, __builtin_amdgcn_mov_dpp(__builtin_bit_cast(int, x), CTRL, 0xf, 0xf, true));
}
__device__ __forceinline__ float grp8_sum(float x) {
  x += dpp<0xB1>(x);
  x += dpp<0x4E>(x);
  x += dpp<0x141>(x);
  return x;
}
__device__ __forceinline__ float swap16_add(float a, float b) {
  auto s = __builtin_amdgcn_permlane16_swap(fbits(a), fbits(b), false, false);
  return bitsf((unsigned)s[0]) + bitsf((unsigned)s[1]);
}
__device__ __forceinline__ float swap32_add(float a, float b) {
  auto t = __builtin_amdgcn_permlane32_swap(fbits(a), fbits(b), false, false);
  return bitsf((unsigned)t[0]) + bitsf((unsigned)t[1]);
}
__device__ __forceinline__ float xg_sum(float x) {
  x += dpp<0x128>(x);
  x = swap16_add(x, x);
  return swap32_add(x, x);
}
__device__ __forceinline__ float xg_max(float x) {
  x = fmaxf(x, dpp<0x128>(x));
  auto s = __builtin_amdgcn_permlane16_swap(fbits(x), fbits(x), false, false);
  x = fmaxf(bitsf((unsigned)s[0]), bitsf((unsigned)s[1]));
  auto t = __builtin_amdgcn_permlane32_swap(fbits(x), fbits(x), false, false);
  return fmaxf(bitsf((unsigned)t[0]), bitsf((unsigned)t[1]));
}
__device__ __forceinline__ void xg_scatter(const float (&a)[24], float (&r)[3], const bool b3) {
#pragma unroll
  for (int j = 0; j < 3; ++j) {
    float p[4];
#pragma unroll
    for (int e = 0; e < 4; ++e) p[e] = swap32_add(a[8 * j + e], a[8 * j + e + 4]);
    const float q0 = swap16_add(p[0], p[2]), q1 = swap16_add(p[1], p[3]);
    r[j] = (b3 ? q1 : q0) + dpp<0x128>(b3 ? q0 : q1);
  }
}
__device__ __forceinline__ float wave_sum(float x) { return xg_sum(grp8_sum(x)); }
__device__ __forceinline__ float ex2(float x) { return __builtin_amdgcn_exp2f(x); }
__device__ __forceinline__ float silu(float x) { return x / (1.0f + __expf(-x)); }
__device__ __forceinline__ u32x4 ntld(const bf16* p) {
  return __builtin_nontemporal_load(reinterpret_cast<const u32x4*>(p));
}
__device__ __forceinline__ float rdlane(float x, int l) {
  return __builtin_bit_cast(float, __builtin_amdgcn_readlane(__builtin_bit_cast(int, x), l));
}

#define SCHED_FENCE() __builtin_amdgcn_sched_barrier(0)
#define TS()
#define WLD(dst, ptr)                                              \
  do {                                                             \
    asm volatile("s_waitcnt vmcnt(%0)" ::"n"(WND - 1) : "memory"); \
    TS();                                                          \
    dst = ntld(ptr);                                               \
    SCHED_FENCE();                                                 \
  } while (0)
#define WAVE_SYNC()                                        \
  do {                                                     \
    __builtin_amdgcn_fence(__ATOMIC_RELEASE, "wavefront"); \
    __builtin_amdgcn_wave_barrier();                       \
    __builtin_amdgcn_fence(__ATOMIC_ACQUIRE, "wavefront"); \
  } while (0)

#define STAMP(i)
constexpr int L_XS = 0;
constexpr int L_PART = L_XS + 2 * 48 * 80;
constexpr int L_FFP = L_PART + 8 * DH;
constexpr int L_QS = L_FFP + 20 * 96;
constexpr int L_ML = L_QS + 96;
constexpr int L_CV = L_ML + 16;
constexpr int L_GU = L_CV + 240;
constexpr int L_FLG = L_GU + 96;
constexpr int L_END = L_FLG + 4;

__global__ void __launch_bounds__(NT, 2)
fused_decode(const bf16* __restrict__ x, const bf16* __restrict__ kc, const bf16* __restrict__ vc,
             const bf16* __restrict__ wq, const bf16* __restrict__ wo, const bf16* __restrict__ w1,
             const bf16* __restrict__ w3, const bf16* __restrict__ w2, u64* __restrict__ gw, bf16* __restrict__ out) {
  __shared__ __attribute__((aligned(16))) float lds[L_END];
  const int b = blockIdx.x;
  const int xcd = b & 7, jj = b >> 3;
  const int h = xcd * 4 + (jj >> 3), sp = jj & 7;
  const int w = __builtin_amdgcn_readfirstlane(threadIdx.x >> 6);
  const int tid = threadIdx.x, lane = tid & 63;
  const int g = lane >> 3, c = lane & 7;
  const int rec = h * NSP + sp;
  unsigned* flg = reinterpret_cast<unsigned*>(lds + L_FLG);

  STAMP(0);
  const unsigned epoch = *reinterpret_cast<const unsigned*>(gw + GEP);
  const size_t kvbase = ((size_t)h * SK + (SK - WIN) + (size_t)sp * KEYS + 64 * ((w + (h >> 3)) & 7) + g) * DH + 8 * c;
#define KVOFF(t) ((((t) + h) & 7) * 8 * DH)
  u32x4 K[8][3], V[8][3];

  const unsigned tag = epoch + 1u;
  if (w < 4) {
    const int R0 = 80 * sp + 20 * w;
    const u32x4 xa = *reinterpret_cast<const u32x4*>(x + 8 * lane);
    const u32x4 xb = *reinterpret_cast<const u32x4*>(x + 512 + 8 * (lane & 15));
    u32x4 WQ[3][3], F[4];
    {
      const bf16* p0 = wq + (size_t)(R0 + g) * (HQ * DH) + h * DH + 8 * c;
      const bf16* p2 = wq + (size_t)(R0 + 16 + (g & 3)) * (HQ * DH) + h * DH + 8 * c;
#pragma unroll
      for (int i = 0; i < 2; ++i)
#pragma unroll
        for (int j = 0; j < 3; ++j) WQ[i][j] = ntld(p0 + (size_t)i * 8 * (HQ * DH) + 64 * j);
#pragma unroll
      for (int j = 0; j < 3; ++j) WQ[2][j] = ntld(p2 + 64 * j);
    }
    const int lf = lane < 60 ? lane : 59;
    const int fr5 = lf / 12, frem = lf - 12 * fr5, fm = frem >= 6 ? 1 : 0, fck = frem - 6 * fm;
    {
      const bf16* pf = (fm ? w3 : w1) + (size_t)(R0 + fr5) * FF + 48 * h + 8 * fck;
#pragma unroll
      for (int i = 0; i < 4; ++i) F[i] = ntld(pf + (size_t)5 * i * FF);
    }
    if (tid < 2) flg[tid] = 0u;
    SCHED_FENCE();
    __syncthreads();
    SCHED_FENCE();
    STAMP(1);
    unsigned short* xs = reinterpret_cast<unsigned short*>(lds + L_XS + w * 320);
    const bool lo16 = lane < 16;
    *reinterpret_cast<u32x4*>(xs + 8 * lane) = xa;
    if (lo16) *reinterpret_cast<u32x4*>(xs + 512 + 8 * lane) = xb;
    float xf[8], xg[8];
#pragma unroll
    for (int i = 0; i < 4; ++i) {
      xf[2 * i] = bf_lo(xa[i]);
      xf[2 * i + 1] = bf_hi(xa[i]);
      xg[2 * i] = bf_lo(xb[i]);
      xg[2 * i + 1] = bf_hi(xb[i]);
    }
    float sum = 0.f;
#pragma unroll
    for (int i = 0; i < 8; ++i) sum += xf[i] + (lo16 ? xg[i] : 0.f);
    sum = wave_sum(sum);
    const float mean = sum * (1.0f / D);
    float sq = 0.f;
#pragma unroll
    for (int i = 0; i < 8; ++i) {
      const float d1 = xf[i] - mean, d2 = xg[i] - mean;
      sq += d1 * d1 + (lo16 ? d2 * d2 : 0.f);
    }
    sq = wave_sum(sq);
    const float mb = bfr(mean);
    const float vb = bfr(sq * (1.0f / D));
    const float den = bfr(sqrtf(bfr(vb + 1e-5f)));
    WAVE_SYNC();
    auto xnorm = [&](int r) -> unsigned {
      return fbits(bfr(bfr(bf_lo(xs[r]) - mb) / den));
    };
    {
      float acc[24], r3[3];
      const unsigned x0 = xnorm(R0 + g), x1 = xnorm(R0 + 8 + g);
      const unsigned x2 = g < 4 ? xnorm(R0 + 16 + (g & 3)) : 0u;
#pragma unroll
      for (int j = 0; j < 3; ++j)
#pragma unroll
        for (int i = 0; i < 4; ++i) {
          acc[8 * j + 2 * i] = dot2(WQ[2][j][i], x2 >> 16, dot2(WQ[1][j][i], x1 >> 16, dot2(WQ[0][j][i], x0 >> 16, 0.f)));
          acc[8 * j + 2 * i + 1] = dot2(WQ[2][j][i], x2, dot2(WQ[1][j][i], x1, dot2(WQ[0][j][i], x0, 0.f)));
        }
      xg_scatter(acc, r3, g & 1);
#pragma unroll
      for (int j = 0; j < 3; ++j) lds[L_PART + w * DH + 64 * j + 8 * c + g] = r3[j];
      float fg[8];
#pragma unroll
      for (int i = 0; i < 8; ++i) fg[i] = 0.f;
#pragma unroll
      for (int r = 0; r < 4; ++r) {
        const unsigned y = xnorm(R0 + fr5 + 5 * r);
#pragma unroll
        for (int i = 0; i < 4; ++i) {
          fg[2 * i] = dot2(F[r][i], y >> 16, fg[2 * i]);
          fg[2 * i + 1] = dot2(F[r][i], y, fg[2 * i + 1]);
        }
      }
      if (lane < 60) {
        float* fp = lds + L_FFP + (w * 5 + fr5) * 96 + fm * 48 + 8 * fck;
        *reinterpret_cast<f32x4*>(fp) = f32x4{fg[0], fg[1], fg[2], fg[3]};
        *reinterpret_cast<f32x4*>(fp + 4) = f32x4{fg[4], fg[5], fg[6], fg[7]};
      }
    }
    asm volatile("s_waitcnt lgkmcnt(0)" ::: "memory");
    if (lane == 0) __hip_atomic_fetch_add(flg, 1u, __ATOMIC_RELAXED, __HIP_MEMORY_SCOPE_WORKGROUP);
    SCHED_FENCE();
    STAMP(2);
  } else {
    SCHED_FENCE();
    __syncthreads();
    SCHED_FENCE();
  }
  if (w == 0) {
    for (unsigned spin = 0; spin < (SPIN_MAX << 4); ++spin) {
      if (__hip_atomic_load(flg, __ATOMIC_RELAXED, __HIP_MEMORY_SCOPE_WORKGROUP) == 4u) break;
      __builtin_amdgcn_s_sleep(1);
    }
    asm volatile("s_waitcnt lgkmcnt(0)" ::: "memory");
#pragma unroll
    for (int k = 0; k < 3; ++k) {
      float q = 0.f;
#pragma unroll
      for (int ww = 0; ww < 4; ++ww) q += lds[L_PART + ww * DH + 64 * k + lane];
      gst(gw + G1 + (size_t)rec * 288 + 64 * k + lane, q, tag);
    }
#pragma unroll
    for (int k = 0; k < 2; ++k) {
      const int e = 64 * k + lane;
      if (e < 96) {
        float v = 0.f;
#pragma unroll
        for (int r = 0; r < 20; ++r) v += lds[L_FFP + r * 96 + e];
        gst(gw + G1 + (size_t)rec * 288 + 192 + e, v, tag);
      }
    }
    SCHED_FENCE();
#pragma unroll
    for (int t = 0; t < 8; ++t)
#pragma unroll
      for (int j = 0; j < 3; ++j) K[t][j] = ntld(kc + kvbase + KVOFF(t) + 64 * j);
    SCHED_FENCE();
    const u64* gb = gw + G1 + (size_t)h * NSP * 288;
    const int f1 = 256 + (lane & 31);
    u64 v[5][NSP];
    for (unsigned spin = 0;; ++spin) {
      bool ok = true;
#pragma unroll
      for (int s = 0; s < NSP; ++s) {
#pragma unroll
        for (int k = 0; k < 4; ++k) {
          v[k][s] = gld(gb + s * 288 + 64 * k + lane);
          ok = ok && gok(v[k][s]);
        }
        v[4][s] = gld(gb + s * 288 + f1);
        ok = ok && gok(v[4][s]);
      }
      if (__all(ok) || spin >= SPIN_MAX) break;
      __builtin_amdgcn_s_sleep(1);
    }
    float sm[5];
#pragma unroll
    for (int k = 0; k < 5; ++k)
      sm[k] = ((gval(v[k][0]) + gval(v[k][1])) + (gval(v[k][2]) + gval(v[k][3]))) +
              ((gval(v[k][4]) + gval(v[k][5])) + (gval(v[k][6]) + gval(v[k][7])));
#pragma unroll
    for (int k = 0; k < 3; ++k) reinterpret_cast<unsigned short*>(lds + L_QS)[64 * k + lane] = bfbits(sm[k]);
    asm volatile("s_waitcnt lgkmcnt(0)" ::: "memory");
    if (lane == 0) __hip_atomic_store(flg + 1, 1u, __ATOMIC_RELAXED, __HIP_MEMORY_SCOPE_WORKGROUP);
    SCHED_FENCE();
    STAMP(3);
    lds[L_GU + lane] = bfr(sm[3]);
    if (lane < 32) lds[L_GU + 64 + lane] = bfr(sm[4]);
    WAVE_SYNC();
    if (lane < 48) lds[L_CV + 192 + lane] = bfr(bfr(silu(lds[L_GU + lane])) * lds[L_GU + 48 + lane]);
    SCHED_FENCE();
  } else {
#pragma unroll
    for (int t = 0; t < 8; ++t)
#pragma unroll
      for (int j = 0; j < 3; ++j) WLD(K[t][j], kc + kvbase + KVOFF(t) + 64 * j);
  }
  const int u = (tid & 255) < 240 ? (tid & 255) : 239;
  const int rg = u / 10, ch = u - 10 * rg;
  u32x4 WO[10];
#define VLD(t)                                                                       \
  do {                                                                               \
    _Pragma("unroll") for (int j = 0; j < 3; ++j) WLD(V[t][j], vc + kvbase + KVOFF(t) + 64 * j); \
  } while (0)
#pragma unroll
  for (int t = 0; t < PQ; ++t) VLD(t);
  for (unsigned spin = 0; spin < (SPIN_MAX << 4); ++spin) {
    if (__hip_atomic_load(flg + 1, __ATOMIC_RELAXED, __HIP_MEMORY_SCOPE_WORKGROUP) != 0u) break;
    __builtin_amdgcn_s_sleep(1);
  }
  asm volatile("s_waitcnt lgkmcnt(0)" ::: "memory");
  STAMP(4);
  SCHED_FENCE();

  u32x4 qf[3];
#pragma unroll
  for (int j = 0; j < 3; ++j) qf[j] = *reinterpret_cast<const u32x4*>(lds + L_QS + (c + 8 * j) * 4);
  float sc[8];
#pragma unroll
  for (int t = 0; t < 8; ++t) {
    float d = 0.f;
#pragma unroll
    for (int j = 0; j < 3; ++j)
#pragma unroll
      for (int i = 0; i < 4; ++i) d = dot2(K[t][j][i], qf[j][i], d);
    d = grp8_sum(d);
    sc[t] = bfr(bfr(d) * SCALE);
    if (t == 3) {
      SCHED_FENCE();
      VLD(PQ);
    }
  }
  float mw = sc[0];
#pragma unroll
  for (int t = 1; t < 8; ++t) mw = fmaxf(mw, sc[t]);
  mw = xg_max(mw);
  float lw = 0.f;
#pragma unroll
  for (int t = 0; t < 8; ++t) {
    sc[t] = ex2((sc[t] - mw) * LOG2E);
    lw += sc[t];
  }
  lw = xg_sum(lw);
  STAMP(5);
  SCHED_FENCE();
  {
    constexpr int LEAD = 3;
#pragma unroll
    for (int t = PQ + 1; t < LEAD; ++t) VLD(t);
    float acc[24], r3[3];
#pragma unroll
    for (int i = 0; i < 24; ++i) acc[i] = 0.f;
#pragma unroll
    for (int t = 0; t < 8; ++t) {
      constexpr int L2 = (PQ + 1 > LEAD) ? PQ + 1 : LEAD;
      if (t + L2 < 8) VLD(t + L2);
      const unsigned plo = bfbits(sc[t]), phi = plo << 16;
#pragma unroll
      for (int j = 0; j < 3; ++j)
#pragma unroll
        for (int i = 0; i < 4; ++i) {
          acc[8 * j + 2 * i] = dot2(V[t][j][i], plo, acc[8 * j + 2 * i]);
          acc[8 * j + 2 * i + 1] = dot2(V[t][j][i], phi, acc[8 * j + 2 * i + 1]);
        }
#pragma unroll
      for (int i = 0; i < 24; i += 8)
        asm volatile("" : "+v"(acc[i]), "+v"(acc[i + 1]), "+v"(acc[i + 2]), "+v"(acc[i + 3]), "+v"(acc[i + 4]), "+v"(acc[i + 5]),
                     "+v"(acc[i + 6]), "+v"(acc[i + 7]));
      SCHED_FENCE();
      if (t + L2 >= 8) TS();
    }
    if (w >= 4) {
#pragma unroll
      for (int i = 0; i < 8; ++i) WO[i] = ntld(wo + ((size_t)DH * h + rg + 24 * i) * D + 80 * sp + 8 * ch);
#pragma unroll
      for (int i = 0; i < 2; ++i) WO[8 + i] = ntld(w2 + ((size_t)48 * h + rg + 24 * i) * D + 80 * sp + 8 * ch);
    }
    SCHED_FENCE();
    xg_scatter(acc, r3, g & 1);
#pragma unroll
    for (int j = 0; j < 3; ++j) lds[L_PART + w * DH + 64 * j + 8 * c + g] = r3[j];
    if (lane == 0) {
      lds[L_ML + 2 * w] = mw;
      lds[L_ML + 2 * w + 1] = lw;
    }
  }
  STAMP(6);
  __syncthreads();
  STAMP(7);
  if (tid < DH) {
    float mm[8];
    float M = -INFINITY;
#pragma unroll
    for (int ww = 0; ww < 8; ++ww) {
      mm[ww] = lds[L_ML + 2 * ww];
      M = fmaxf(M, mm[ww]);
    }
    float L = 0.f, a = 0.f;
#pragma unroll
    for (int ww = 0; ww < 8; ++ww) {
      mm[ww] = ex2((mm[ww] - M) * LOG2E);
      L = fmaf(mm[ww], lds[L_ML + 2 * ww + 1], L);
      a = fmaf(mm[ww], lds[L_PART + ww * DH + tid], a);
    }
    u64* pr = gw + G2 + (size_t)rec * 208;
    gst(pr + tid, a, tag);
    if (tid < 2) gst(pr + DH + tid, tid ? L : M, tag);
    const u64* pb = gw + G2 + (size_t)h * NSP * 208;
    u64 va[NSP], vml;
    for (unsigned spin = 0;; ++spin) {
      bool ok = true;
#pragma unroll
      for (int s = 0; s < NSP; ++s) {
        va[s] = gld(pb + s * 208 + tid);
        ok = ok && gok(va[s]);
      }
      vml = gld(pb + c * 208 + DH + (g & 1));
      ok = ok && gok(vml);
      if (__all(ok) || spin >= SPIN_MAX) break;
      __builtin_amdgcn_s_sleep(1);
    }
    float ms[NSP];
    float M2 = -INFINITY;
#pragma unroll
    for (int s = 0; s < NSP; ++s) {
      ms[s] = rdlane(gval(vml), s);
      M2 = fmaxf(M2, ms[s]);
    }
    float L2 = 0.f, o = 0.f;
#pragma unroll
    for (int s = 0; s < NSP; ++s) {
      const float e = ex2((ms[s] - M2) * LOG2E);
      L2 = fmaf(e, rdlane(gval(vml), 8 + s), L2);
      o = fmaf(e, gval(va[s]), o);
    }
    lds[L_CV + tid] = bfr(o / L2);
  }
  __syncthreads();
  STAMP(8);
  if (w >= 4) {
    float y[8], z[8];
#pragma unroll
    for (int i = 0; i < 8; ++i) y[i] = z[i] = 0.f;
#pragma unroll
    for (int i = 0; i < 8; ++i) {
      const unsigned chi = fbits(lds[L_CV + rg + 24 * i]), clo = chi >> 16;
#pragma unroll
      for (int k = 0; k < 4; ++k) {
        y[2 * k] = dot2(WO[i][k], clo, y[2 * k]);
        y[2 * k + 1] = dot2(WO[i][k], chi, y[2 * k + 1]);
      }
    }
#pragma unroll
    for (int i = 0; i < 2; ++i) {
      const unsigned chi = fbits(lds[L_CV + 192 + rg + 24 * i]), clo = chi >> 16;
#pragma unroll
      for (int k = 0; k < 4; ++k) {
        z[2 * k] = dot2(WO[8 + i][k], clo, z[2 * k]);
        z[2 * k + 1] = dot2(WO[8 + i][k], chi, z[2 * k + 1]);
      }
    }
    if (tid < 256 + 240) {
      float* yp = lds + L_XS + rg * 80 + 8 * ch;
      float* zp = yp + 24 * 80;
      *reinterpret_cast<f32x4*>(yp) = f32x4{y[0], y[1], y[2], y[3]};
      *reinterpret_cast<f32x4*>(yp + 4) = f32x4{y[4], y[5], y[6], y[7]};
      *reinterpret_cast<f32x4*>(zp) = f32x4{z[0], z[1], z[2], z[3]};
      *reinterpret_cast<f32x4*>(zp + 4) = f32x4{z[4], z[5], z[6], z[7]};
    }
  }
  __syncthreads();
  if (tid < 160) {
    const int which = tid >= 80 ? 1 : 0;
    const int cc = tid - 80 * which;
    const float* rp = lds + L_XS + which * 24 * 80 + cc;
    float a0 = 0.f, a1 = 0.f, a2 = 0.f, a3 = 0.f;
#pragma unroll
    for (int r = 0; r < 24; r += 4) {
      a0 += rp[r * 80];
      a1 += rp[(r + 1) * 80];
      a2 += rp[(r + 2) * 80];
      a3 += rp[(r + 3) * 80];
    }
    gst(gw + G3 + ((size_t)(80 * sp + cc) * 2 + which) * 32 + h, (a0 + a1) + (a2 + a3), tag);
  }
  STAMP(9);
  if ((h & 1) == 0 && w < 5) {
    const int col = 80 * sp + 5 * (h >> 1) + w;
    const float xv = __bfloat162float(x[col]);
    const u64* pg = gw + G3 + (size_t)col * 64 + lane;
    u64 v = 0;
    for (unsigned spin = 0;; ++spin) {
      v = gld(pg);
      if (__all(gok(v)) || spin >= SPIN_MAX) break;
      __builtin_amdgcn_s_sleep(1);
    }
    float s = grp8_sum(gval(v));
    s += dpp<0x128>(s);
    s = swap16_add(s, s);
    STAMP(10);
    const float sb = bfr(rdlane(s, 0));
    const float fb = bfr(rdlane(s, 32));
    const float gb2 = bfr(silu(fb));
    if (lane == 0) {
      out[col] = __float2bfloat16(xv + bfr(sb * gb2));
      if (col == 0)
        __hip_atomic_store(reinterpret_cast<unsigned*>(gw + GEP), tag, __ATOMIC_RELAXED, __HIP_MEMORY_SCOPE_AGENT);
    }
  }
  STAMP(11);
}

}

extern "C" void kernel_launch(void* const* d_in, const int* in_sizes, int n_in, void* d_out, int out_size, void* d_ws,
                              size_t ws_size, hipStream_t stream) {
  const bf16* x = (const bf16*)d_in[0];
  const bf16* kc = (const bf16*)d_in[1];
  const bf16* vc = (const bf16*)d_in[2];
  const bf16* wq = (const bf16*)d_in[3];
  const bf16* wo = (const bf16*)d_in[4];
  const bf16* w1 = (const bf16*)d_in[6];
  const bf16* w3 = (const bf16*)d_in[7];
  const bf16* w2 = (const bf16*)d_in[8];
  u64* gw = (u64*)d_ws;
  fused_decode<<<NWG, NT, 0, stream>>>(x, kc, vc, wq, wo, w1, w3, w2, gw, (bf16*)d_out);
}
```

```cpp
#define POFF 0
#define PACE 0
#define WOB 8
#define WOS 465
#define PD1 20
#include <hip/hip_runtime.h>
#include <hip/hip_bf16.h>
#include <stdint.h>

typedef __hip_bfloat16 bf16;
typedef unsigned long long u64;
typedef unsigned u32x4 __attribute__((ext_vector_type(4)));
typedef float f32x4 __attribute__((ext_vector_type(4)));
typedef __bf16 bf16x2_t __attribute__((ext_vector_type(2)));

namespace {

constexpr int D = 640, HQ = 32, DH = 192, SK = 32768, WIN = 4096, FF = 1536;
constexpr int NSP = 8, KEYS = WIN / NSP;
constexpr int NWG = HQ * NSP;
constexpr int NT = 512;
constexpr float SCALE = 0.07216878364870322f;
constexpr float LOG2E = 1.4426950408889634f;
constexpr unsigned SPIN_MAX = 1u << 16;
#define WND 8
#define PQ 3
#define M0 9
#define STAG 1

constexpr int G1 = 0;
constexpr int G2 = G1 + NWG * 144;
constexpr int G3Y = G2 + NWG * 104;
constexpr int G3Z = G3Y + 320 * 32;
constexpr int GEP = G3Z + 320 * 32;
constexpr int G4 = GEP + 8;
constexpr int GEND = G4 + 256;

__device__ __forceinline__ unsigned fbits(float x) { return __builtin_bit_cast(unsigned, x); }
__device__ __forceinline__ float bitsf(unsigned x) { return __builtin_bit_cast(float, x); }
__device__ __forceinline__ void gst(u64* p, float v, unsigned tag) {
  __hip_atomic_store(p, ((u64)tag << 32) | (u64)fbits(v), __ATOMIC_RELAXED, __HIP_MEMORY_SCOPE_AGENT);
}
__device__ __forceinline__ u64 gld(const u64* p) {
  return __hip_atomic_load(p, __ATOMIC_RELAXED, __HIP_MEMORY_SCOPE_AGENT);
}
#define gok(v) ((unsigned)((v) >> 32) == tag)
#define gok2(v) ((v)[1] == tag && (v)[3] == tag)
#define gst2(unit, a, b) __builtin_amdgcn_raw_buffer_store_b128(u32x4{fbits(a), tag, fbits(b), tag}, rws, (unit) * 16, 0, 16)
#define gld2(unit, cunit) __builtin_amdgcn_raw_buffer_load_b128(rws, (unit) * 16, (cunit) * 16, 16)
#define MEMBAR() asm volatile("" ::: "memory")
typedef float f32x2 __attribute__((ext_vector_type(2)));
__device__ __forceinline__ float gval(u64 v) { return __builtin_bit_cast(float, (unsigned)v); }

__device__ __forceinline__ float bf_lo(unsigned u) { return __builtin_bit_cast(float, u << 16); }
__device__ __forceinline__ float bf_hi(unsigned u) { return __builtin_bit_cast(float, u & 0xffff0000u); }
__device__ __forceinline__ float bfr(float x) { return __bfloat162float(__float2bfloat16(x)); }
__device__ __forceinline__ unsigned short bfbits(float x) {
  return __builtin_bit_cast(unsigned short, __float2bfloat16(x));
}
__device__ __forceinline__ float dot2(unsigned a, unsigned b, float c) {
  return __builtin_amdgcn_fdot2_f32_bf16(__builtin_bit_cast(bf16x2_t, a), __builtin_bit_cast(bf16x2_t, b), c, false);
}
template <int CTRL>
__device__ __forceinline__ float dpp(float x) {
  return __builtin_bit_cast(float, __builtin_amdgcn_mov_dpp(__builtin_bit_cast(int, x), CTRL, 0xf, 0xf, true));
}
__device__ __forceinline__ float grp8_sum(float x) {
  x += dpp<0xB1>(x);
  x += dpp<0x4E>(x);
  x += dpp<0x141>(x);
  return x;
}
__device__ __forceinline__ float swap16_add(float a, float b) {
  auto s = __builtin_amdgcn_permlane16_swap(fbits(a), fbits(b), false, false);
  return bitsf((unsigned)s[0]) + bitsf((unsigned)s[1]);
}
__device__ __forceinline__ float swap32_add(float a, float b) {
  auto t = __builtin_amdgcn_permlane32_swap(fbits(a), fbits(b), false, false);
  return bitsf((unsigned)t[0]) + bitsf((unsigned)t[1]);
}
__device__ __forceinline__ float xg_sum(float x) {
  x += dpp<0x128>(x);
  x = swap16_add(x, x);
  return swap32_add(x, x);
}
__device__ __forceinline__ float xg_max(float x) {
  x = fmaxf(x, dpp<0x128>(x));
  auto s = __builtin_amdgcn_permlane16_swap(fbits(x), fbits(x), false, false);
  x = fmaxf(bitsf((unsigned)s[0]), bitsf((unsigned)s[1]));
  auto t = __builtin_amdgcn_permlane32_swap(fbits(x), fbits(x), false, false);
  return fmaxf(bitsf((unsigned)t[0]), bitsf((unsigned)t[1]));
}
__device__ __forceinline__ void xg_scatter(const float (&a)[24], float (&r)[3], const bool b3) {
#pragma unroll
  for (int j = 0; j < 3; ++j) {
    float p[4];
#pragma unroll
    for (int e = 0; e < 4; ++e) p[e] = swap32_add(a[8 * j + e], a[8 * j + e + 4]);
    const float q0 = swap16_add(p[0], p[2]), q1 = swap16_add(p[1], p[3]);
    r[j] = (b3 ? q1 : q0) + dpp<0x128>(b3 ? q0 : q1);
  }
}
__device__ __forceinline__ float wave_sum(float x) { return xg_sum(grp8_sum(x)); }
__device__ __forceinline__ float ex2(float x) { return __builtin_amdgcn_exp2f(x); }
__device__ __forceinline__ float silu(float x) { return x / (1.0f + __expf(-x)); }
__device__ __forceinline__ u32x4 ntld(const bf16* p) {
  return __builtin_nontemporal_load(reinterpret_cast<const u32x4*>(p));
}
__device__ __forceinline__ float rdlane(float x, int l) {
  return __builtin_bit_cast(float, __builtin_amdgcn_readlane(__builtin_bit_cast(int, x), l));
}

#define SCHED_FENCE() __builtin_amdgcn_sched_barrier(0)
#define TS()
#define PACEW(m10)                                                                                  \
  do {                                                                                              \
    while ((int)((unsigned)__builtin_amdgcn_s_memrealtime() - (tb + (unsigned)((m10) * PACE / 100))) < 0) \
      __builtin_amdgcn_s_sleep(1);                                                                  \
  } while (0)
#define WLD(dst, ptr, m)                                           \
  do {                                                             \
    asm volatile("s_waitcnt vmcnt(%0)" ::"n"(WND - 1) : "memory"); \
    if ((m) >= M0) PACEW(10 * (m));                                \
    TS();                                                          \
    dst = ntld(ptr);                                               \
    SCHED_FENCE();                                                 \
  } while (0)
#define WAVE_SYNC()                                        \
  do {                                                     \
    __builtin_amdgcn_fence(__ATOMIC_RELEASE, "wavefront"); \
    __builtin_amdgcn_wave_barrier();                       \
    __builtin_amdgcn_fence(__ATOMIC_ACQUIRE, "wavefront"); \
  } while (0)

#define STAMP(i)
constexpr int L_WO = 0;
constexpr int L_XS = L_WO + DH * 40;
constexpr int L_PART = L_XS + 4 * 320;
constexpr int L_FFP = L_PART + 8 * DH;
constexpr int L_ZP = L_FFP + 20 * 96;
constexpr int L_QS = L_ZP + 6 * 80;
constexpr int L_ML = L_QS + 96;
constexpr int L_O = L_ML + 16;
constexpr int L_H = L_O + 128;
constexpr int L_GU = L_H + 48;
constexpr int L_FLG = L_GU + 96;
constexpr int L_END = L_FLG + 4;

__global__ void __launch_bounds__(NT, 2)
fused_decode(const bf16* __restrict__ x, const bf16* __restrict__ kc, const bf16* __restrict__ vc,
             const bf16* __restrict__ wq, const bf16* __restrict__ wo, const bf16* __restrict__ w1,
             const bf16* __restrict__ w3, const bf16* __restrict__ w2, u64* __restrict__ gw, bf16* __restrict__ out) {
  __shared__ __attribute__((aligned(16))) float lds[L_END];
  const int b = blockIdx.x;
  const int xcd = b & 7, jj = b >> 3;
  const int h = xcd * 4 + (jj >> 3), sp = jj & 7;
  const int w = __builtin_amdgcn_readfirstlane(threadIdx.x >> 6);
  const int tid = threadIdx.x, lane = tid & 63;
  const int g = lane >> 3, c = lane & 7;
  const int rec = h * NSP + sp;
  unsigned* flg = reinterpret_cast<unsigned*>(lds + L_FLG);

  const unsigned epoch = *reinterpret_cast<const unsigned*>(gw + 2 * GEP);
  const auto rws = __builtin_amdgcn_make_buffer_rsrc((void*)gw, (short)0, GEND * 16, 0x00020000);
  STAMP(0);
  const unsigned tb = (unsigned)__builtin_amdgcn_s_memrealtime() + POFF + STAG * w * PACE / 80;
  const size_t kvbase = ((size_t)h * SK + (SK - WIN) + (size_t)sp * KEYS + 64 * ((w + (h >> 3)) & 7) + g) * DH + 8 * c;
#define KVOFF(t) ((((t) + h) & 7) * 8 * DH)
  u32x4 K[8][3], V[8][3];

  const unsigned tag = epoch + 1u;
  if (w < 4) {
    const int R0 = 80 * sp + 20 * w;
    const u32x4 xa = *reinterpret_cast<const u32x4*>(x + 8 * lane);
    const u32x4 xb = *reinterpret_cast<const u32x4*>(x + 512 + 8 * (lane & 15));
    u32x4 WQ[3][3], F[4];
    {
      const bf16* p0 = wq + (size_t)(R0 + g) * (HQ * DH) + h * DH + 8 * c;
      const bf16* p2 = wq + (size_t)(R0 + 16 + (g & 3)) * (HQ * DH) + h * DH + 8 * c;
#pragma unroll
      for (int i = 0; i < 2; ++i)
#pragma unroll
        for (int j = 0; j < 3; ++j) WQ[i][j] = ntld(p0 + (size_t)i * 8 * (HQ * DH) + 64 * j);
#pragma unroll
      for (int j = 0; j < 3; ++j) WQ[2][j] = ntld(p2 + 64 * j);
    }
    const int lf = lane < 60 ? lane : 59;
    const int fr5 = lf / 12, frem = lf - 12 * fr5, fm = frem >= 6 ? 1 : 0, fck = frem - 6 * fm;
    {
      const bf16* pf = (fm ? w3 : w1) + (size_t)(R0 + fr5) * FF + 48 * h + 8 * fck;
#pragma unroll
      for (int i = 0; i < 4; ++i) F[i] = ntld(pf + (size_t)5 * i * FF);
    }
    if (tid < 3) flg[tid] = 0u;
    SCHED_FENCE();
    __syncthreads();
    SCHED_FENCE();
    STAMP(1);
    unsigned short* xs = reinterpret_cast<unsigned short*>(lds + L_XS + w * 320);
    const bool lo16 = lane < 16;
    *reinterpret_cast<u32x4*>(xs + 8 * lane) = xa;
    if (lo16) *reinterpret_cast<u32x4*>(xs + 512 + 8 * lane) = xb;
    float xf[8], xg[8];
#pragma unroll
    for (int i = 0; i < 4; ++i) {
      xf[2 * i] = bf_lo(xa[i]);
      xf[2 * i + 1] = bf_hi(xa[i]);
      xg[2 * i] = bf_lo(xb[i]);
      xg[2 * i + 1] = bf_hi(xb[i]);
    }
    float sum = 0.f;
#pragma unroll
    for (int i = 0; i < 8; ++i) sum += xf[i] + (lo16 ? xg[i] : 0.f);
    sum = wave_sum(sum);
    const float mean = sum * (1.0f / D);
    float sq = 0.f;
#pragma unroll
    for (int i = 0; i < 8; ++i) {
      const float d1 = xf[i] - mean, d2 = xg[i] - mean;
      sq += d1 * d1 + (lo16 ? d2 * d2 : 0.f);
    }
    sq = wave_sum(sq);
    const float mb = bfr(mean);
    const float vb = bfr(sq * (1.0f / D));
    const float den = bfr(sqrtf(bfr(vb + 1e-5f)));
    WAVE_SYNC();
    auto xnorm = [&](int r) -> unsigned {
      return fbits(bfr(bfr(bf_lo(xs[r]) - mb) / den));
    };
    {
      float acc[24], r3[3];
      const unsigned x0 = xnorm(R0 + g), x1 = xnorm(R0 + 8 + g);
      const unsigned x2 = g < 4 ? xnorm(R0 + 16 + (g & 3)) : 0u;
#pragma unroll
      for (int j = 0; j < 3; ++j)
#pragma unroll
        for (int i = 0; i < 4; ++i) {
          acc[8 * j + 2 * i] = dot2(WQ[2][j][i], x2 >> 16, dot2(WQ[1][j][i], x1 >> 16, dot2(WQ[0][j][i], x0 >> 16, 0.f)));
          acc[8 * j + 2 * i + 1] = dot2(WQ[2][j][i], x2, dot2(WQ[1][j][i], x1, dot2(WQ[0][j][i], x0, 0.f)));
        }
      xg_scatter(acc, r3, g & 1);
#pragma unroll
      for (int j = 0; j < 3; ++j) lds[L_PART + w * DH + 64 * j + 8 * c + g] = r3[j];
      float fg[8];
#pragma unroll
      for (int i = 0; i < 8; ++i) fg[i] = 0.f;
#pragma unroll
      for (int r = 0; r < 4; ++r) {
        const unsigned y = xnorm(R0 + fr5 + 5 * r);
#pragma unroll
        for (int i = 0; i < 4; ++i) {
          fg[2 * i] = dot2(F[r][i], y >> 16, fg[2 * i]);
          fg[2 * i + 1] = dot2(F[r][i], y, fg[2 * i + 1]);
        }
      }
      if (lane < 60) {
        float* fp = lds + L_FFP + (w * 5 + fr5) * 96 + fm * 48 + 8 * fck;
        *reinterpret_cast<f32x4*>(fp) = f32x4{fg[0], fg[1], fg[2], fg[3]};
        *reinterpret_cast<f32x4*>(fp + 4) = f32x4{fg[4], fg[5], fg[6], fg[7]};
      }
    }
    asm volatile("s_waitcnt lgkmcnt(0)" ::: "memory");
    if (lane == 0) __hip_atomic_fetch_add(flg, 1u, __ATOMIC_RELAXED, __HIP_MEMORY_SCOPE_WORKGROUP);
    SCHED_FENCE();
    STAMP(2);
  } else {
    SCHED_FENCE();
    __syncthreads();
    SCHED_FENCE();
  }
  if (w == 0) {
    for (unsigned spin = 0; spin < (SPIN_MAX << 4); ++spin) {
      if (__hip_atomic_load(flg, __ATOMIC_RELAXED, __HIP_MEMORY_SCOPE_WORKGROUP) == 4u) break;
      __builtin_amdgcn_s_sleep(1);
    }
    asm volatile("s_waitcnt lgkmcnt(0)" ::: "memory");
    {
      const f32x2* pp = reinterpret_cast<const f32x2*>(lds + L_PART);
      const f32x2 qa = (pp[lane] + pp[96 + lane]) + (pp[192 + lane] + pp[288 + lane]);
      gst2(G1 + rec * 144 + lane, qa[0], qa[1]);
      if (lane < 32) {
        const f32x2 qb = (pp[64 + lane] + pp[160 + lane]) + (pp[256 + lane] + pp[352 + lane]);
        gst2(G1 + rec * 144 + 64 + lane, qb[0], qb[1]);
      }
      if (lane < 48) {
        const f32x2* fp = reinterpret_cast<const f32x2*>(lds + L_FFP) + lane;
        f32x2 va = fp[0], vb = fp[48];
#pragma unroll
        for (int r = 2; r < 20; r += 2) {
          va += fp[r * 48];
          vb += fp[(r + 1) * 48];
        }
        va += vb;
        gst2(G1 + rec * 144 + 96 + lane, va[0], va[1]);
      }
    }
    SCHED_FENCE();
    const int lz = lane < 60 ? lane : 59;
    const int zr = lz / 10, zc = lz - 10 * zr;
    u32x4 W2[8];
#pragma unroll
    for (int i = 0; i < 8; ++i) W2[i] = ntld(w2 + ((size_t)48 * h + zr + 6 * i) * D + 80 * sp + 8 * zc);
#pragma unroll
    for (int t = 0; t < 8; ++t)
#pragma unroll
      for (int j = 0; j < 3; ++j) K[t][j] = ntld(kc + kvbase + KVOFF(t) + 64 * j);
    SCHED_FENCE();
    const int gb = G1 + h * NSP * 144 + lane;
    const int gc = G1 + (h * NSP + (lane >> 4)) * 144 + 128 + (lane & 15);
    u32x4 va[NSP], vb[NSP], vc2[2];
    for (unsigned spin = 0;; ++spin) {
      MEMBAR();
      bool ok = true;
#pragma unroll
      for (int s = 0; s < NSP; ++s) {
        va[s] = gld2(gb, s * 144);
        vb[s] = gld2(gb, s * 144 + 64);
      }
      vc2[0] = gld2(gc, 0);
      vc2[1] = gld2(gc, 4 * 144);
#pragma unroll
      for (int s = 0; s < NSP; ++s) ok = ok && gok2(va[s]) && gok2(vb[s]);
      ok = ok && gok2(vc2[0]) && gok2(vc2[1]);
      if (__all(ok) || spin >= SPIN_MAX) break;
      __builtin_amdgcn_s_sleep(1);
    }
#define SUM8(v, e) (((bitsf(v[0][e]) + bitsf(v[1][e])) + (bitsf(v[2][e]) + bitsf(v[3][e]))) + ((bitsf(v[4][e]) + bitsf(v[5][e])) + (bitsf(v[6][e]) + bitsf(v[7][e]))))
    const float a0 = SUM8(va, 0), a1 = SUM8(va, 2), b0 = SUM8(vb, 0), b1 = SUM8(vb, 2);
    reinterpret_cast<unsigned*>(lds + L_QS)[lane] = (unsigned)bfbits(a0) | ((unsigned)bfbits(a1) << 16);
    if (lane < 32) reinterpret_cast<unsigned*>(lds + L_QS)[64 + lane] = (unsigned)bfbits(b0) | ((unsigned)bfbits(b1) << 16);
    asm volatile("s_waitcnt lgkmcnt(0)" ::: "memory");
    if (lane == 0) __hip_atomic_store(flg + 1, 1u, __ATOMIC_RELAXED, __HIP_MEMORY_SCOPE_WORKGROUP);
    SCHED_FENCE();
    STAMP(3);
    float c0 = bitsf(vc2[0][0]) + bitsf(vc2[1][0]), c1 = bitsf(vc2[0][2]) + bitsf(vc2[1][2]);
    c0 = swap16_add(c0, c0);
    c1 = swap16_add(c1, c1);
    c0 = swap32_add(c0, c0);
    c1 = swap32_add(c1, c1);
    if (lane >= 32) *reinterpret_cast<f32x2*>(lds + L_GU + 2 * (lane - 32)) = f32x2{bfr(b0), bfr(b1)};
    if (lane < 16) *reinterpret_cast<f32x2*>(lds + L_GU + 64 + 2 * lane) = f32x2{bfr(c0), bfr(c1)};
    WAVE_SYNC();
    if (lane < 48) lds[L_H + lane] = bfr(bfr(silu(lds[L_GU + lane])) * lds[L_GU + 48 + lane]);
    WAVE_SYNC();
    {
      float z[8];
#pragma unroll
      for (int i = 0; i < 8; ++i) z[i] = 0.f;
#pragma unroll
      for (int i = 0; i < 8; ++i) {
        const unsigned chi = fbits(lds[L_H + zr + 6 * i]), clo = chi >> 16;
#pragma unroll
        for (int k = 0; k < 4; ++k) {
          z[2 * k] = dot2(W2[i][k], clo, z[2 * k]);
          z[2 * k + 1] = dot2(W2[i][k], chi, z[2 * k + 1]);
        }
      }
      if (lane < 60) {
        float* zp = lds + L_ZP + zr * 80 + 8 * zc;
        *reinterpret_cast<f32x4*>(zp) = f32x4{z[0], z[1], z[2], z[3]};
        *reinterpret_cast<f32x4*>(zp + 4) = f32x4{z[4], z[5], z[6], z[7]};
      }
      WAVE_SYNC();
      if (lane < 40) {
        const f32x2* zp = reinterpret_cast<const f32x2*>(lds + L_ZP) + lane;
        const f32x2 zs = ((zp[0] + zp[40]) + (zp[80] + zp[120])) + (zp[160] + zp[200]);
        gst2(G3Z + (40 * sp + lane) * 32 + h, zs[0], zs[1]);
      }
    }
    SCHED_FENCE();
  } else {
#pragma unroll
    for (int t = 0; t < 8; ++t)
#pragma unroll
      for (int j = 0; j < 3; ++j) WLD(K[t][j], kc + kvbase + KVOFF(t) + 64 * j, 3 * t + j);
  }
  const int u = (tid & 255) < 240 ? (tid & 255) : 239;
  const int rg = u / 10, ch = u - 10 * rg;
  u32x4 WO[8];
  const auto rwo = __builtin_amdgcn_make_buffer_rsrc((void*)(wo + (size_t)DH * h * D + 80 * sp), (short)0, DH * D * 2, 0x00020000);
  const int wofs = (rg * D + 8 * ch) * 2;
#define WOLD(i)                                                                      \
  do {                                                                               \
    PACEW(WOS + 5 * (i));                                                            \
    WO[i] = __builtin_amdgcn_raw_buffer_load_b128(rwo, wofs, (i) * 24 * D * 2, 2);   \
    SCHED_FENCE();                                                                   \
  } while (0)
#define VLD(t)                                                                       \
  do {                                                                               \
    _Pragma("unroll") for (int j = 0; j < 3; ++j) WLD(V[t][j], vc + kvbase + KVOFF(t) + 64 * j, 24 + 3 * (t) + j); \
  } while (0)
#pragma unroll
  for (int t = 0; t < PQ; ++t) VLD(t);
  for (unsigned spin = 0; spin < (SPIN_MAX << 4); ++spin) {
    if (__hip_atomic_load(flg + 1, __ATOMIC_RELAXED, __HIP_MEMORY_SCOPE_WORKGROUP) != 0u) break;
    __builtin_amdgcn_s_sleep(1);
  }
  asm volatile("s_waitcnt lgkmcnt(0)" ::: "memory");
  STAMP(4);
  SCHED_FENCE();

  u32x4 qf[3];
#pragma unroll
  for (int j = 0; j < 3; ++j) qf[j] = *reinterpret_cast<const u32x4*>(lds + L_QS + (c + 8 * j) * 4);
  float sc[8];
#pragma unroll
  for (int t = 0; t < 8; ++t) {
    float d = 0.f;
#pragma unroll
    for (int j = 0; j < 3; ++j)
#pragma unroll
      for (int i = 0; i < 4; ++i) d = dot2(K[t][j][i], qf[j][i], d);
    d = grp8_sum(d);
    sc[t] = bfr(bfr(d) * SCALE);
    if (t == 3) {
      SCHED_FENCE();
      VLD(PQ);
    }
  }
  float mw = sc[0];
#pragma unroll
  for (int t = 1; t < 8; ++t) mw = fmaxf(mw, sc[t]);
  mw = xg_max(mw);
  float lw = 0.f;
#pragma unroll
  for (int t = 0; t < 8; ++t) {
    sc[t] = ex2((sc[t] - mw) * LOG2E);
    lw += sc[t];
  }
  lw = xg_sum(lw);
  STAMP(5);
  SCHED_FENCE();
  {
    constexpr int LEAD = 3;
#pragma unroll
    for (int t = PQ + 1; t < LEAD; ++t) VLD(t);
    float acc[24], r3[3];
#pragma unroll
    for (int i = 0; i < 24; ++i) acc[i] = 0.f;
    constexpr int L2 = (PQ + 1 > LEAD) ? PQ + 1 : LEAD;
#define PVT(t)                                                                                                          \
  do {                                                                                                                  \
    const unsigned plo = bfbits(sc[t]), phi = plo << 16;                                                                \
    _Pragma("unroll") for (int j = 0; j < 3; ++j) _Pragma("unroll") for (int i = 0; i < 4; ++i) {                       \
      acc[8 * j + 2 * i] = dot2(V[t][j][i], plo, acc[8 * j + 2 * i]);                                                   \
      acc[8 * j + 2 * i + 1] = dot2(V[t][j][i], phi, acc[8 * j + 2 * i + 1]);                                           \
    }                                                                                                                   \
    _Pragma("unroll") for (int i = 0; i < 24; i += 8)                                                                   \
      asm volatile("" : "+v"(acc[i]), "+v"(acc[i + 1]), "+v"(acc[i + 2]), "+v"(acc[i + 3]), "+v"(acc[i + 4]),           \
                   "+v"(acc[i + 5]), "+v"(acc[i + 6]), "+v"(acc[i + 7]));            \
    SCHED_FENCE();                                                                                                      \
  } while (0)
#pragma unroll
    for (int t = 0; t + L2 < 8; ++t) {
      VLD(t + L2);
      PVT(t);
    }
    if (w >= 4) {
#pragma unroll
      for (int t = 8 - L2; t < 8; ++t) {
#pragma unroll
        for (int i = 0; i < WOB; ++i)
          if ((t == 7) ? (i >= t + L2 - 8) : (i == t + L2 - 8)) WOLD(i);
        PVT(t);
      }
    } else {
#pragma unroll
      for (int t = 8 - L2; t < 8; ++t) PVT(t);
    }
    SCHED_FENCE();
    xg_scatter(acc, r3, g & 1);
#pragma unroll
    for (int j = 0; j < 3; ++j) lds[L_PART + w * DH + 64 * j + 8 * c + g] = r3[j];
    if (lane == 0) {
      lds[L_ML + 2 * w] = mw;
      lds[L_ML + 2 * w + 1] = lw;
    }
  }
  STAMP(6);
  __syncthreads();
  STAMP(7);
  unsigned wp[24];
  float gate0 = 0.f, gate1 = 0.f;
  const bool fin = (w == 2) && (h < 20);
  const int fu = (40 * sp + 2 * h) * 32 + lane;
  if (w < 2) {
    if (tid < 97) {
      const int t2 = tid < 96 ? tid : 95;
      float mm[8], ll[8];
#pragma unroll
      for (int ww = 0; ww < 8; ww += 2) {
        const f32x4 q4 = *reinterpret_cast<const f32x4*>(lds + L_ML + 2 * ww);
        mm[ww] = q4[0];
        ll[ww] = q4[1];
        mm[ww + 1] = q4[2];
        ll[ww + 1] = q4[3];
      }
      const float M = fmaxf(fmaxf(fmaxf(mm[0], mm[1]), fmaxf(mm[2], mm[3])), fmaxf(fmaxf(mm[4], mm[5]), fmaxf(mm[6], mm[7])));
      float L = 0.f, a0 = 0.f, a1 = 0.f;
#pragma unroll
      for (int ww = 0; ww < 8; ++ww) {
        const float e = ex2((mm[ww] - M) * LOG2E);
        const f32x2 p2 = *reinterpret_cast<const f32x2*>(lds + L_PART + ww * DH + 2 * t2);
        L = fmaf(e, ll[ww], L);
        a0 = fmaf(e, p2[0], a0);
        a1 = fmaf(e, p2[1], a1);
      }
      gst2(G2 + rec * 104 + tid, tid < 96 ? a0 : M, tid < 96 ? a1 : L);
    }
    STAMP(12);
    const int t2 = tid < 96 ? tid : 95;
    const int pb = G2 + h * NSP * 104 + t2, pm = G2 + (h * NSP + c) * 104 + 96;
    u32x4 va[NSP], vml;
    if (PD1) __builtin_amdgcn_s_sleep(PD1);
    for (unsigned spin = 0;; ++spin) {
      MEMBAR();
#pragma unroll
      for (int s = 0; s < NSP; ++s) va[s] = gld2(pb, s * 104);
      vml = gld2(pm, 0);
      bool ok = gok2(vml);
#pragma unroll
      for (int s = 0; s < NSP; ++s) ok = ok && gok2(va[s]);
      if (__all(ok) || spin >= SPIN_MAX) break;
      __builtin_amdgcn_s_sleep(1);
    }
    STAMP(13);
    const float ms = bitsf(vml[0]);
    float M2 = fmaxf(ms, dpp<0xB1>(ms));
    M2 = fmaxf(M2, dpp<0x4E>(M2));
    M2 = fmaxf(M2, dpp<0x141>(M2));
    const float es = ex2((ms - M2) * LOG2E);
    const float L2 = grp8_sum(es * bitsf(vml[2]));
    float o0 = 0.f, o1 = 0.f;
#pragma unroll
    for (int s = 0; s < NSP; ++s) {
      const float e = rdlane(es, s);
      o0 = fmaf(e, bitsf(va[s][0]), o0);
      o1 = fmaf(e, bitsf(va[s][2]), o1);
    }
    if (tid < 96) {
      unsigned short* po = reinterpret_cast<unsigned short*>(lds + L_O) + ((2 * tid & 31) * 4 + (tid >> 5)) * 2 + (tid >> 4 & 1);
      po[0] = bfbits(o0 / L2);
      po[8] = bfbits(o1 / L2);
    }
  } else if (w == 2) {
    if (fin) {
      u32x4 v;
      for (unsigned spin = 0;; ++spin) {
        MEMBAR();
        v = gld2(G3Z + fu, 0);
        if (__all(gok2(v)) || spin >= SPIN_MAX) break;
        __builtin_amdgcn_s_sleep(1);
      }
      float s0 = grp8_sum(bitsf(v[0])), s1 = grp8_sum(bitsf(v[2]));
      s0 += dpp<0x128>(s0);
      s1 += dpp<0x128>(s1);
      s0 = swap16_add(s0, s0);
      s1 = swap16_add(s1, s1);
      gate0 = bfr(silu(bfr(s0)));
      gate1 = bfr(silu(bfr(s1)));
    }
  } else {
    if (w >= 4) {
#pragma unroll
      for (int i = WOB; i < 8; ++i) WOLD(i);
      if (tid < 256 + 240) {
#pragma unroll
        for (int i = 0; i < 8; ++i) *reinterpret_cast<u32x4*>(lds + L_WO + u * 4 + i * 960) = WO[i];
      }
      asm volatile("s_waitcnt lgkmcnt(0)" ::: "memory");
      if (lane == 0) __hip_atomic_fetch_add(flg + 2, 1u, __ATOMIC_RELAXED, __HIP_MEMORY_SCOPE_WORKGROUP);
      STAMP(12);
    }
    for (unsigned spin = 0; spin < (SPIN_MAX << 4); ++spin) {
      if (__hip_atomic_load(flg + 2, __ATOMIC_RELAXED, __HIP_MEMORY_SCOPE_WORKGROUP) == 4u) break;
      __builtin_amdgcn_s_sleep(1);
    }
    asm volatile("s_waitcnt lgkmcnt(0)" ::: "memory");
    const float* wl = lds + L_WO + (lane >> 1) * 40 + (2 * (w - 3) + (lane & 1)) * 4;
#pragma unroll
    for (int k = 0; k < 3; ++k) {
      const u32x4 A = *reinterpret_cast<const u32x4*>(wl + 64 * k * 40), B = *reinterpret_cast<const u32x4*>(wl + (64 * k + 32) * 40);
#pragma unroll
      for (int q = 0; q < 4; ++q) {
        wp[8 * k + 2 * q] = __builtin_amdgcn_perm(B[q], A[q], 0x05040100u);
        wp[8 * k + 2 * q + 1] = __builtin_amdgcn_perm(B[q], A[q], 0x07060302u);
      }
    }
#pragma unroll
    for (int i = 0; i < 24; i += 8)
      asm volatile("" : "+v"(wp[i]), "+v"(wp[i + 1]), "+v"(wp[i + 2]), "+v"(wp[i + 3]), "+v"(wp[i + 4]), "+v"(wp[i + 5]), "+v"(wp[i + 6]), "+v"(wp[i + 7]));
  }
  STAMP(14);
  __syncthreads();
  STAMP(8);
  if (w >= 3) {
    const u32x4 op = *reinterpret_cast<const u32x4*>(lds + L_O + (lane >> 1) * 4);
    float y[8];
#pragma unroll
    for (int e = 0; e < 8; ++e) y[e] = dot2(wp[16 + e], op[2], dot2(wp[8 + e], op[1], dot2(wp[e], op[0], 0.f)));
    float p[4];
#pragma unroll
    for (int e = 0; e < 4; ++e) p[e] = swap32_add(y[e], y[e + 4]);
    float q0 = swap16_add(p[0], p[2]), q1 = swap16_add(p[1], p[3]);
    q0 += dpp<0x128>(q0);
    q1 += dpp<0x128>(q1);
    q0 += dpp<0x124>(q0);
    q1 += dpp<0x124>(q1);
    q0 += dpp<0x4E>(q0);
    q1 += dpp<0x4E>(q1);
    if ((lane & 14) == 0) gst2(G3Y + (40 * sp + 4 * (2 * (w - 3) + (lane & 1)) + (lane >> 4)) * 32 + h, q0, q1);
  }
  STAMP(9);
  if (fin) {
    const int col = 80 * sp + 4 * h + (lane >> 5) * 2;
    const unsigned xv = *reinterpret_cast<const unsigned*>(x + col);
    u32x4 v;
    for (unsigned spin = 0;; ++spin) {
      MEMBAR();
      v = gld2(G3Y + fu, 0);
      if (__all(gok2(v)) || spin >= SPIN_MAX) break;
      __builtin_amdgcn_s_sleep(1);
    }
    float s0 = grp8_sum(bitsf(v[0])), s1 = grp8_sum(bitsf(v[2]));
    s0 += dpp<0x128>(s0);
    s1 += dpp<0x128>(s1);
    s0 = swap16_add(s0, s0);
    s1 = swap16_add(s1, s1);
    STAMP(10);
    if ((lane & 31) == 0) {
      const unsigned r0 = bfbits(bf_lo(xv) + bfr(bfr(s0) * gate0)), r1 = bfbits(bf_hi(xv) + bfr(bfr(s1) * gate1));
      *reinterpret_cast<unsigned*>(out + col) = r0 | (r1 << 16);
      if (col == 0)
        __hip_atomic_store(reinterpret_cast<unsigned*>(gw + 2 * GEP), tag, __ATOMIC_RELAXED, __HIP_MEMORY_SCOPE_AGENT);
    }
  }
  STAMP(11);
}

}

extern "C" void kernel_launch(void* const* d_in, const int* in_sizes, int n_in, void* d_out, int out_size, void* d_ws,
                              size_t ws_size, hipStream_t stream) {
  const bf16* x = (const bf16*)d_in[0];
  const bf16* kc = (const bf16*)d_in[1];
  const bf16* vc = (const bf16*)d_in[2];
  const bf16* wq = (const bf16*)d_in[3];
  const bf16* wo = (const bf16*)d_in[4];
  const bf16* w1 = (const bf16*)d_in[6];
  const bf16* w3 = (const bf16*)d_in[7];
  const bf16* w2 = (const bf16*)d_in[8];
  u64* gw = (u64*)d_ws;
  fused_decode<<<NWG, NT, 0, stream>>>(x, kc, vc, wq, wo, w1, w3, w2, gw, (bf16*)d_out);
}
```

```cpp
#include <hip/hip_runtime.h>
#include <hip/hip_bf16.h>
#include <stdint.h>

typedef __hip_bfloat16 bf16;
typedef unsigned long long u64;
typedef unsigned u32x4 __attribute__((ext_vector_type(4)));
typedef float f32x4 __attribute__((ext_vector_type(4)));
typedef __bf16 bf16x2_t __attribute__((ext_vector_type(2)));

namespace {

constexpr int D = 640, HQ = 32, DH = 192, SK = 32768, WIN = 4096, FF = 1536;
constexpr int NSP = 8, KEYS = WIN / NSP;
constexpr int NWG = HQ * NSP;
constexpr int NT = 512;
constexpr float SCALE = 0.07216878364870322f;
constexpr float LOG2E = 1.4426950408889634f;
constexpr unsigned SPIN_MAX = 1u << 16;
#define WND 8
#define PQ 5
#define POFF 0
#define PACE 0
#define M0 9
#define STAG 1
#define WOS 465
#define PD1 14
#define LIK 1
#define OST 0
#define KE 8
#define WOB 8

constexpr int G1 = 0;
constexpr int G2 = G1 + NWG * 144;
constexpr int G3Y = G2 + NWG * 104;
constexpr int G3Z = G3Y + 320 * 32;
constexpr int GEP = G3Z + 320 * 32;
constexpr int G4 = GEP + 8;
constexpr int GEND = G4 + 256;

__device__ __forceinline__ unsigned fbits(float x) { return __builtin_bit_cast(unsigned, x); }
__device__ __forceinline__ float bitsf(unsigned x) { return __builtin_bit_cast(float, x); }
__device__ __forceinline__ void gst(u64* p, float v, unsigned tag) {
  __hip_atomic_store(p, ((u64)tag << 32) | (u64)fbits(v), __ATOMIC_RELAXED, __HIP_MEMORY_SCOPE_AGENT);
}
__device__ __forceinline__ u64 gld(const u64* p) {
  return __hip_atomic_load(p, __ATOMIC_RELAXED, __HIP_MEMORY_SCOPE_AGENT);
}
#define gok(v) ((unsigned)((v) >> 32) == tag)
#define gok2(v) ((v)[1] == tag && (v)[3] == tag)
#define gst2(unit, a, b) __builtin_amdgcn_raw_buffer_store_b128(u32x4{fbits(a), tag, fbits(b), tag}, rws, (unit) * 16, 0, 16)
#define gld2(unit, cunit) __builtin_amdgcn_raw_buffer_load_b128(rws, (unit) * 16, (cunit) * 16, 16)
#define MEMBAR() asm volatile("" ::: "memory")
typedef float f32x2 __attribute__((ext_vector_type(2)));
__device__ __forceinline__ float gval(u64 v) { return __builtin_bit_cast(float, (unsigned)v); }

__device__ __forceinline__ float bf_lo(unsigned u) { return __builtin_bit_cast(float, u << 16); }
__device__ __forceinline__ float bf_hi(unsigned u) { return __builtin_bit_cast(float, u & 0xffff0000u); }
__device__ __forceinline__ float bfr(float x) { return __bfloat162float(__float2bfloat16(x)); }
__device__ __forceinline__ unsigned short bfbits(float x) {
  return __builtin_bit_cast(unsigned short, __float2bfloat16(x));
}
__device__ __forceinline__ float dot2(unsigned a, unsigned b, float c) {
  return __builtin_amdgcn_fdot2_f32_bf16(__builtin_bit_cast(bf16x2_t, a), __builtin_bit_cast(bf16x2_t, b), c, false);
}
template <int CTRL>
__device__ __forceinline__ float dpp(float x) {
  return __builtin_bit_cast(float, __builtin_amdgcn_mov_dpp(__builtin_bit_cast(int, x), CTRL, 0xf, 0xf, true));
}
__device__ __forceinline__ float grp8_sum(float x) {
  x += dpp<0xB1>(x);
  x += dpp<0x4E>(x);
  x += dpp<0x141>(x);
  return x;
}
__device__ __forceinline__ float swap16_add(float a, float b) {
  auto s = __builtin_amdgcn_permlane16_swap(fbits(a), fbits(b), false, false);
  return bitsf((unsigned)s[0]) + bitsf((unsigned)s[1]);
}
__device__ __forceinline__ float swap32_add(float a, float b) {
  auto t = __builtin_amdgcn_permlane32_swap(fbits(a), fbits(b), false, false);
  return bitsf((unsigned)t[0]) + bitsf((unsigned)t[1]);
}
__device__ __forceinline__ float xg_sum(float x) {
  x += dpp<0x128>(x);
  x = swap16_add(x, x);
  return swap32_add(x, x);
}
__device__ __forceinline__ float xg_max(float x) {
  x = fmaxf(x, dpp<0x128>(x));
  auto s = __builtin_amdgcn_permlane16_swap(fbits(x), fbits(x), false, false);
  x = fmaxf(bitsf((unsigned)s[0]), bitsf((unsigned)s[1]));
  auto t = __builtin_amdgcn_permlane32_swap(fbits(x), fbits(x), false, false);
  return fmaxf(bitsf((unsigned)t[0]), bitsf((unsigned)t[1]));
}
__device__ __forceinline__ void xg_scatter(const float (&a)[24], float (&r)[3], const bool b3) {
#pragma unroll
  for (int j = 0; j < 3; ++j) {
    float p[4];
#pragma unroll
    for (int e = 0; e < 4; ++e) p[e] = swap32_add(a[8 * j + e], a[8 * j + e + 4]);
    const float q0 = swap16_add(p[0], p[2]), q1 = swap16_add(p[1], p[3]);
    r[j] = (b3 ? q1 : q0) + dpp<0x128>(b3 ? q0 : q1);
  }
}
__device__ __forceinline__ float wave_sum(float x) { return xg_sum(grp8_sum(x)); }
__device__ __forceinline__ float ex2(float x) { return __builtin_amdgcn_exp2f(x); }
__device__ __forceinline__ float silu(float x) { return x / (1.0f + __expf(-x)); }
__device__ __forceinline__ u32x4 ntld(const bf16* p) {
  return __builtin_nontemporal_load(reinterpret_cast<const u32x4*>(p));
}
__device__ __forceinline__ float rdlane(float x, int l) {
  return __builtin_bit_cast(float, __builtin_amdgcn_readlane(__builtin_bit_cast(int, x), l));
}

#define SCHED_FENCE() __builtin_amdgcn_sched_barrier(0)
#define TS()
#define PACEW(m10)
#define WLD(dst, ptr, m)                                           \
  do {                                                             \
    asm volatile("s_waitcnt vmcnt(%0)" ::"n"(WND - 1) : "memory"); \
    if ((m) >= M0) PACEW(10 * (m));                                \
    TS();                                                          \
    dst = ntld(ptr);                                               \
    SCHED_FENCE();                                                 \
  } while (0)
#define WAVE_SYNC()                                        \
  do {                                                     \
    __builtin_amdgcn_fence(__ATOMIC_RELEASE, "wavefront"); \
    __builtin_amdgcn_wave_barrier();                       \
    __builtin_amdgcn_fence(__ATOMIC_ACQUIRE, "wavefront"); \
  } while (0)

#define STAMP(i)
constexpr int L_WO = 0;
constexpr int L_XS = L_WO + DH * 40;
constexpr int PS = DH + 4;
constexpr int L_PART = L_XS + 4 * 320;
constexpr int L_FFP = L_PART + 8 * PS;
constexpr int L_ZP = L_FFP + 20 * 96;
constexpr int L_QS = L_ZP + 6 * 80;
constexpr int L_ML = L_QS + 96;
constexpr int L_O = L_ML + 16;
constexpr int L_H = L_O + 128;
constexpr int L_GU = L_H + 48;
constexpr int L_FLG = L_GU + 96;
constexpr int L_END = L_FLG + 4;

__global__ void __launch_bounds__(NT, 2)
fused_decode(const bf16* __restrict__ x, const bf16* __restrict__ kc, const bf16* __restrict__ vc,
             const bf16* __restrict__ wq, const bf16* __restrict__ wo, const bf16* __restrict__ w1,
             const bf16* __restrict__ w3, const bf16* __restrict__ w2, u64* __restrict__ gw, bf16* __restrict__ out) {
  __shared__ __attribute__((aligned(16))) float lds[L_END];
  const int b = blockIdx.x;
  const int xcd = b & 7, jj = b >> 3;
  const int h = xcd * 4 + (jj >> 3), sp = jj & 7;
  const int w = __builtin_amdgcn_readfirstlane(threadIdx.x >> 6);
  const int tid = threadIdx.x, lane = tid & 63;
  const int g = lane >> 3, c = lane & 7;
  const int rec = h * NSP + sp;
  unsigned* flg = reinterpret_cast<unsigned*>(lds + L_FLG);

  asm volatile("" ::"s"(x), "s"(kc), "s"(vc), "s"(wq), "s"(wo), "s"(w1), "s"(w3), "s"(w2), "s"(gw), "s"(out));
  const auto rws = __builtin_amdgcn_make_buffer_rsrc((void*)gw, (short)0, GEND * 16, 0x00020000);
  STAMP(0);
  const size_t kvbase = ((size_t)h * SK + (SK - WIN) + (size_t)sp * KEYS + 64 * ((w + (h >> 3)) & 7) + g) * DH + 8 * c;
#define KVOFF(t) ((((t) + h) & 7) * 8 * DH)
  u32x4 K[8][3], V[8][3];

  unsigned epoch, tag;
  if (w < 4) {
    const int R0 = 80 * sp + 20 * w;
    const u32x4 xa = *reinterpret_cast<const u32x4*>(x + 8 * lane);
    const u32x4 xb = *reinterpret_cast<const u32x4*>(x + 512 + 8 * (lane & 15));
    u32x4 WQ[3][3], F[4];
    {
      const bf16* p0 = wq + (size_t)(R0 + g) * (HQ * DH) + h * DH + 8 * c;
      const bf16* p2 = wq + (size_t)(R0 + 16 + (g & 3)) * (HQ * DH) + h * DH + 8 * c;
#pragma unroll
      for (int i = 0; i < 2; ++i)
#pragma unroll
        for (int j = 0; j < 3; ++j) WQ[i][j] = ntld(p0 + (size_t)i * 8 * (HQ * DH) + 64 * j);
#pragma unroll
      for (int j = 0; j < 3; ++j) WQ[2][j] = ntld(p2 + 64 * j);
    }
    const int lf = lane < 60 ? lane : 59;
    const int fr5 = lf / 12, frem = lf - 12 * fr5, fm = frem >= 6 ? 1 : 0, fck = frem - 6 * fm;
    {
      const bf16* pf = (fm ? w3 : w1) + (size_t)(R0 + fr5) * FF + 48 * h + 8 * fck;
#pragma unroll
      for (int i = 0; i < 4; ++i) F[i] = ntld(pf + (size_t)5 * i * FF);
    }
    SCHED_FENCE();
    epoch = __builtin_amdgcn_raw_buffer_load_b32(rws, 0, GEP * 16, 0);
    if (tid < 4) flg[tid] = 0u;
    SCHED_FENCE();
    __syncthreads();
    SCHED_FENCE();
    STAMP(1);
    unsigned short* xs = reinterpret_cast<unsigned short*>(lds + L_XS + w * 320);
    const bool lo16 = lane < 16;
    *reinterpret_cast<u32x4*>(xs + 8 * lane) = xa;
    if (lo16) *reinterpret_cast<u32x4*>(xs + 512 + 8 * lane) = xb;
    float xf[8], xg[8];
#pragma unroll
    for (int i = 0; i < 4; ++i) {
      xf[2 * i] = bf_lo(xa[i]);
      xf[2 * i + 1] = bf_hi(xa[i]);
      xg[2 * i] = bf_lo(xb[i]);
      xg[2 * i + 1] = bf_hi(xb[i]);
    }
    float sum = 0.f;
#pragma unroll
    for (int i = 0; i < 8; ++i) sum += xf[i] + (lo16 ? xg[i] : 0.f);
    sum = wave_sum(sum);
    const float mean = sum * (1.0f / D);
    float sq = 0.f;
#pragma unroll
    for (int i = 0; i < 8; ++i) {
      const float d1 = xf[i] - mean, d2 = xg[i] - mean;
      sq += d1 * d1 + (lo16 ? d2 * d2 : 0.f);
    }
    sq = wave_sum(sq);
    const float mb = bfr(mean);
    const float vb = bfr(sq * (1.0f / D));
    const float den = bfr(sqrtf(bfr(vb + 1e-5f)));
    WAVE_SYNC();
    auto xnorm = [&](int r) -> unsigned {
      return fbits(bfr(bfr(bf_lo(xs[r]) - mb) / den));
    };
    {
      float acc[24], r3[3];
      const unsigned x0 = xnorm(R0 + g), x1 = xnorm(R0 + 8 + g);
      const unsigned x2 = g < 4 ? xnorm(R0 + 16 + (g & 3)) : 0u;
#pragma unroll
      for (int j = 0; j < 3; ++j)
#pragma unroll
        for (int i = 0; i < 4; ++i) {
          acc[8 * j + 2 * i] = dot2(WQ[2][j][i], x2 >> 16, dot2(WQ[1][j][i], x1 >> 16, dot2(WQ[0][j][i], x0 >> 16, 0.f)));
          acc[8 * j + 2 * i + 1] = dot2(WQ[2][j][i], x2, dot2(WQ[1][j][i], x1, dot2(WQ[0][j][i], x0, 0.f)));
        }
      xg_scatter(acc, r3, g & 1);
#pragma unroll
      for (int j = 0; j < 3; ++j) lds[L_PART + w * DH + 64 * j + 8 * c + g] = r3[j];
      float fg[8];
#pragma unroll
      for (int i = 0; i < 8; ++i) fg[i] = 0.f;
#pragma unroll
      for (int r = 0; r < 4; ++r) {
        const unsigned y = xnorm(R0 + fr5 + 5 * r);
#pragma unroll
        for (int i = 0; i < 4; ++i) {
          fg[2 * i] = dot2(F[r][i], y >> 16, fg[2 * i]);
          fg[2 * i + 1] = dot2(F[r][i], y, fg[2 * i + 1]);
        }
      }
      if (lane < 60) {
        float* fp = lds + L_FFP + (w * 5 + fr5) * 96 + fm * 48 + 8 * fck;
        *reinterpret_cast<f32x4*>(fp) = f32x4{fg[0], fg[1], fg[2], fg[3]};
        *reinterpret_cast<f32x4*>(fp + 4) = f32x4{fg[4], fg[5], fg[6], fg[7]};
      }
    }
    asm volatile("s_waitcnt lgkmcnt(0)" ::: "memory");
    if (lane == 0) __hip_atomic_fetch_add(flg, 1u, __ATOMIC_RELAXED, __HIP_MEMORY_SCOPE_WORKGROUP);
    SCHED_FENCE();
    STAMP(2);
  } else {
#pragma unroll
    for (int m = 0; m < KE; ++m) K[m / 3][m % 3] = ntld(kc + kvbase + KVOFF(m / 3) + 64 * (m % 3));
    SCHED_FENCE();
    epoch = __builtin_amdgcn_raw_buffer_load_b32(rws, 0, GEP * 16, 0);
    SCHED_FENCE();
    __syncthreads();
    SCHED_FENCE();
  }
  if (w == 0) {
    for (unsigned spin = 0; spin < (SPIN_MAX << 4); ++spin) {
      if (__hip_atomic_load(flg, __ATOMIC_RELAXED, __HIP_MEMORY_SCOPE_WORKGROUP) == 4u) break;
      __builtin_amdgcn_s_sleep(1);
    }
    asm volatile("s_waitcnt lgkmcnt(0)" ::: "memory");
    tag = __builtin_amdgcn_readfirstlane(epoch) + 1u;
    {
      const f32x2* pp = reinterpret_cast<const f32x2*>(lds + L_PART);
      const f32x2 qa = (pp[lane] + pp[96 + lane]) + (pp[192 + lane] + pp[288 + lane]);
      gst2(G1 + rec * 144 + lane, qa[0], qa[1]);
      if (lane < 32) {
        const f32x2 qb = (pp[64 + lane] + pp[160 + lane]) + (pp[256 + lane] + pp[352 + lane]);
        gst2(G1 + rec * 144 + 64 + lane, qb[0], qb[1]);
      }
      if (lane < 48) {
        const f32x2* fp = reinterpret_cast<const f32x2*>(lds + L_FFP) + lane;
        f32x2 va = fp[0], vb = fp[48];
#pragma unroll
        for (int r = 2; r < 20; r += 2) {
          va += fp[r * 48];
          vb += fp[(r + 1) * 48];
        }
        va += vb;
        gst2(G1 + rec * 144 + 96 + lane, va[0], va[1]);
      }
    }
    SCHED_FENCE();
    const int lz = lane < 60 ? lane : 59;
    const int zr = lz / 10, zc = lz - 10 * zr;
    u32x4 W2[8];
#pragma unroll
    for (int i = 0; i < 8; ++i) W2[i] = ntld(w2 + ((size_t)48 * h + zr + 6 * i) * D + 80 * sp + 8 * zc);
#pragma unroll
    for (int t = 0; t < 8; ++t)
#pragma unroll
      for (int j = 0; j < 3; ++j) K[t][j] = ntld(kc + kvbase + KVOFF(t) + 64 * j);
    SCHED_FENCE();
    const int gb = G1 + h * NSP * 144 + lane;
    const int gc = G1 + (h * NSP + (lane >> 4)) * 144 + 128 + (lane & 15);
    u32x4 va[NSP], vb[NSP], vc2[2];
    for (unsigned spin = 0;; ++spin) {
      MEMBAR();
      bool ok = true;
#pragma unroll
      for (int s = 0; s < NSP; ++s) {
        va[s] = gld2(gb, s * 144);
        vb[s] = gld2(gb, s * 144 + 64);
      }
      vc2[0] = gld2(gc, 0);
      vc2[1] = gld2(gc, 4 * 144);
#pragma unroll
      for (int s = 0; s < NSP; ++s) ok = ok && gok2(va[s]) && gok2(vb[s]);
      ok = ok && gok2(vc2[0]) && gok2(vc2[1]);
      if (__all(ok) || spin >= SPIN_MAX) break;
      __builtin_amdgcn_s_sleep(1);
    }
#define SUM8(v, e) (((bitsf(v[0][e]) + bitsf(v[1][e])) + (bitsf(v[2][e]) + bitsf(v[3][e]))) + ((bitsf(v[4][e]) + bitsf(v[5][e])) + (bitsf(v[6][e]) + bitsf(v[7][e]))))
    const float a0 = SUM8(va, 0), a1 = SUM8(va, 2), b0 = SUM8(vb, 0), b1 = SUM8(vb, 2);
    reinterpret_cast<unsigned*>(lds + L_QS)[lane] = (unsigned)bfbits(a0) | ((unsigned)bfbits(a1) << 16);
    if (lane < 32) reinterpret_cast<unsigned*>(lds + L_QS)[64 + lane] = (unsigned)bfbits(b0) | ((unsigned)bfbits(b1) << 16);
    asm volatile("s_waitcnt lgkmcnt(0)" ::: "memory");
    if (lane == 0) __hip_atomic_store(flg + 1, 1u, __ATOMIC_RELAXED, __HIP_MEMORY_SCOPE_WORKGROUP);
    SCHED_FENCE();
    STAMP(3);
    float c0 = bitsf(vc2[0][0]) + bitsf(vc2[1][0]), c1 = bitsf(vc2[0][2]) + bitsf(vc2[1][2]);
    c0 = swap16_add(c0, c0);
    c1 = swap16_add(c1, c1);
    c0 = swap32_add(c0, c0);
    c1 = swap32_add(c1, c1);
    if (lane >= 32) *reinterpret_cast<f32x2*>(lds + L_GU + 2 * (lane - 32)) = f32x2{bfr(b0), bfr(b1)};
    if (lane < 16) *reinterpret_cast<f32x2*>(lds + L_GU + 64 + 2 * lane) = f32x2{bfr(c0), bfr(c1)};
    WAVE_SYNC();
    if (lane < 48) lds[L_H + lane] = bfr(bfr(silu(lds[L_GU + lane])) * lds[L_GU + 48 + lane]);
    WAVE_SYNC();
    {
      float z[8];
#pragma unroll
      for (int i = 0; i < 8; ++i) z[i] = 0.f;
#pragma unroll
      for (int i = 0; i < 8; ++i) {
        const unsigned chi = fbits(lds[L_H + zr + 6 * i]), clo = chi >> 16;
#pragma unroll
        for (int k = 0; k < 4; ++k) {
          z[2 * k] = dot2(W2[i][k], clo, z[2 * k]);
          z[2 * k + 1] = dot2(W2[i][k], chi, z[2 * k + 1]);
        }
      }
      if (lane < 60) {
        float* zp = lds + L_ZP + zr * 80 + 8 * zc;
        *reinterpret_cast<f32x4*>(zp) = f32x4{z[0], z[1], z[2], z[3]};
        *reinterpret_cast<f32x4*>(zp + 4) = f32x4{z[4], z[5], z[6], z[7]};
      }
      WAVE_SYNC();
      if (lane < 40) {
        const f32x2* zp = reinterpret_cast<const f32x2*>(lds + L_ZP) + lane;
        const f32x2 zs = ((zp[0] + zp[40]) + (zp[80] + zp[120])) + (zp[160] + zp[200]);
        gst2(G3Z + (40 * sp + lane) * 32 + h, zs[0], zs[1]);
      }
    }
    SCHED_FENCE();
  } else if (w < 4) {
#pragma unroll
    for (int m = 0; m < 24; ++m) WLD(K[m / 3][m % 3], kc + kvbase + KVOFF(m / 3) + 64 * (m % 3), m);
    tag = __builtin_amdgcn_readfirstlane(epoch) + 1u;
    SCHED_FENCE();
  } else {
#pragma unroll
    for (int m = KE; m < 24; ++m) WLD(K[m / 3][m % 3], kc + kvbase + KVOFF(m / 3) + 64 * (m % 3), m);
    tag = __builtin_amdgcn_readfirstlane(epoch) + 1u;
    SCHED_FENCE();
  }
  const int u = (tid & 255) < 240 ? (tid & 255) : 239;
  const int rg = u / 10, ch = u - 10 * rg;
  u32x4 WO[8];
  const auto rwo = __builtin_amdgcn_make_buffer_rsrc((void*)(wo + (size_t)DH * h * D + 80 * sp), (short)0, DH * D * 2, 0x00020000);
  const int wofs = (rg * D + 8 * ch) * 2;
#define WOLD(i)                                                                      \
  do {                                                                               \
    PACEW(WOS + 5 * (i));                                                            \
    WO[i] = __builtin_amdgcn_raw_buffer_load_b128(rwo, wofs, (i) * 24 * D * 2, 2);   \
    SCHED_FENCE();                                                                   \
  } while (0)
#define VLD(t)                                                                       \
  do {                                                                               \
    _Pragma("unroll") for (int j = 0; j < 3; ++j) WLD(V[t][j], vc + kvbase + KVOFF(t) + 64 * j, 24 + 3 * (t) + j); \
  } while (0)
#pragma unroll
  for (int t = 0; t < PQ; ++t) VLD(t);
  for (unsigned spin = 0; spin < (SPIN_MAX << 4); ++spin) {
    if (__hip_atomic_load(flg + 1, __ATOMIC_RELAXED, __HIP_MEMORY_SCOPE_WORKGROUP) != 0u) break;
    __builtin_amdgcn_s_sleep(1);
  }
  asm volatile("s_waitcnt lgkmcnt(0)" ::: "memory");
  STAMP(4);
  SCHED_FENCE();

  u32x4 qf[3];
#pragma unroll
  for (int j = 0; j < 3; ++j) qf[j] = *reinterpret_cast<const u32x4*>(lds + L_QS + (c + 8 * j) * 4);
  float sc[8];
#pragma unroll
  for (int t = 0; t < 8; ++t) {
    float d = 0.f;
#pragma unroll
    for (int j = 0; j < 3; ++j)
#pragma unroll
      for (int i = 0; i < 4; ++i) d = dot2(K[t][j][i], qf[j][i], d);
    d = grp8_sum(d);
    sc[t] = bfr(bfr(d) * SCALE);
    if (t == 3) {
      SCHED_FENCE();
      VLD(PQ);
    }
  }
  float mw = sc[0];
#pragma unroll
  for (int t = 1; t < 8; ++t) mw = fmaxf(mw, sc[t]);
  mw = xg_max(mw);
  if (lane == 0) {
    lds[L_ML + w] = mw;
    __hip_atomic_fetch_add(flg + 3, 1u, __ATOMIC_RELAXED, __HIP_MEMORY_SCOPE_WORKGROUP);
  }
  float lw = 0.f;
#pragma unroll
  for (int t = 0; t < 8; ++t) {
    sc[t] = ex2((sc[t] - mw) * LOG2E);
    lw += sc[t];
  }
  lw = xg_sum(lw);
  STAMP(5);
  SCHED_FENCE();
  float Mg;
  {
    constexpr int LEAD = 3;
#pragma unroll
    for (int t = PQ + 1; t < LEAD; ++t) VLD(t);
    float acc[24], r3[3];
#pragma unroll
    for (int i = 0; i < 24; ++i) acc[i] = 0.f;
    constexpr int L2 = (PQ + 1 > LEAD) ? PQ + 1 : LEAD;
#define PVT(t)                                                                                                          \
  do {                                                                                                                  \
    const unsigned plo = bfbits(sc[t]), phi = plo << 16;                                                                \
    _Pragma("unroll") for (int j = 0; j < 3; ++j) _Pragma("unroll") for (int i = 0; i < 4; ++i) {                       \
      acc[8 * j + 2 * i] = dot2(V[t][j][i], plo, acc[8 * j + 2 * i]);                                                   \
      acc[8 * j + 2 * i + 1] = dot2(V[t][j][i], phi, acc[8 * j + 2 * i + 1]);                                           \
    }                                                                                                                   \
    _Pragma("unroll") for (int i = 0; i < 24; i += 8)                                                                   \
      asm volatile("" : "+v"(acc[i]), "+v"(acc[i + 1]), "+v"(acc[i + 2]), "+v"(acc[i + 3]), "+v"(acc[i + 4]),           \
                   "+v"(acc[i + 5]), "+v"(acc[i + 6]), "+v"(acc[i + 7]));            \
    SCHED_FENCE();                                                                                                      \
  } while (0)
#define WGMAX()                                                                                                         \
  do {                                                                                                                  \
    for (unsigned spin = 0; spin < (SPIN_MAX << 4); ++spin) {                                                           \
      if (__hip_atomic_load(flg + 3, __ATOMIC_RELAXED, __HIP_MEMORY_SCOPE_WORKGROUP) == 8u) break;                      \
      __builtin_amdgcn_s_sleep(1);                                                                                      \
    }                                                                                                                   \
    MEMBAR();                                                                                                           \
    const f32x4 ma = *reinterpret_cast<const f32x4*>(lds + L_ML), mb2 = *reinterpret_cast<const f32x4*>(lds + L_ML + 4); \
    Mg = fmaxf(fmaxf(fmaxf(ma[0], ma[1]), fmaxf(ma[2], ma[3])), fmaxf(fmaxf(mb2[0], mb2[1]), fmaxf(mb2[2], mb2[3])));   \
    ew = ex2((mw - Mg) * LOG2E);                                                                                        \
    SCHED_FENCE();                                                                                                      \
  } while (0)
    float ew;
#pragma unroll
    for (int t = 0; t + L2 < 8; ++t) {
      VLD(t + L2);
      PVT(t);
    }
    if (w >= 4) {
#pragma unroll
      for (int t = 8 - L2; t < 8; ++t) {
#pragma unroll
        for (int i = 0; i < WOB; ++i)
          if ((t == 7) ? (i >= t + L2 - 8) : (i == t + L2 - 8)) WOLD(i);
        if (t == 6) WGMAX();
        PVT(t);
      }
    } else {
#pragma unroll
      for (int t = 8 - L2; t < 8; ++t) {
        if (t == 6) WGMAX();
        PVT(t);
      }
    }
    SCHED_FENCE();
    xg_scatter(acc, r3, g & 1);
#pragma unroll
    for (int j = 0; j < 3; ++j) lds[L_PART + w * PS + 64 * j + 8 * c + g] = r3[j] * ew;
    if (lane == 0) lds[L_PART + w * PS + DH] = lw * ew;
  }
  STAMP(6);
  __syncthreads();
  STAMP(7);
  float gate0 = 0.f, gate1 = 0.f;
  unsigned xv = 0u;
  const int col = 80 * sp + 4 * h + (lane >> 5) * 2;
  const bool fin = (w == 2) && (h < 20);
  const int fu = (40 * sp + 2 * h) * 32 + lane;
  if (__builtin_expect(w < 2, LIK)) {
    if (tid < 97) {
      f32x2 p2[8];
#pragma unroll
      for (int ww = 0; ww < 8; ++ww) p2[ww] = *reinterpret_cast<const f32x2*>(lds + L_PART + ww * PS + 2 * tid);
      const f32x2 us = ((p2[0] + p2[1]) + (p2[2] + p2[3])) + ((p2[4] + p2[5]) + (p2[6] + p2[7]));
      gst2(G2 + rec * 104 + tid, tid < 96 ? us[0] : Mg, tid < 96 ? us[1] : us[0]);
    }
    STAMP(12);
    const int t2 = tid < 96 ? tid : 95;
    const int pb = G2 + h * NSP * 104 + t2, pm = G2 + (h * NSP + c) * 104 + 96;
    u32x4 va[NSP], vml;
    if (PD1) __builtin_amdgcn_s_sleep(PD1);
    for (unsigned spin = 0;; ++spin) {
      MEMBAR();
#pragma unroll
      for (int s = 0; s < NSP; ++s) va[s] = gld2(pb, s * 104);
      vml = gld2(pm, 0);
      bool ok = gok2(vml);
#pragma unroll
      for (int s = 0; s < NSP; ++s) ok = ok && gok2(va[s]);
      if (__all(ok) || spin >= SPIN_MAX) break;
      __builtin_amdgcn_s_sleep(1);
    }
    STAMP(13);
    const float ms = bitsf(vml[0]);
    float M2 = fmaxf(ms, dpp<0xB1>(ms));
    M2 = fmaxf(M2, dpp<0x4E>(M2));
    M2 = fmaxf(M2, dpp<0x141>(M2));
    const float es = ex2((ms - M2) * LOG2E);
    const float L2 = grp8_sum(es * bitsf(vml[2]));
    float o0 = 0.f, o1 = 0.f;
#pragma unroll
    for (int s = 0; s < NSP; ++s) {
      const float e = rdlane(es, s);
      o0 = fmaf(e, bitsf(va[s][0]), o0);
      o1 = fmaf(e, bitsf(va[s][2]), o1);
    }
    if (tid < 96) {
      unsigned short* po = reinterpret_cast<unsigned short*>(lds + L_O) + ((2 * tid & 31) * 4 + (tid >> 5)) * 2 + (tid >> 4 & 1);
      const float rl = __builtin_amdgcn_rcpf(L2);
      po[0] = bfbits(o0 * rl);
      po[8] = bfbits(o1 * rl);
    }
    STAMP(14);
    __syncthreads();
    STAMP(8);
  } else if (w == 2) {
    if (fin) {
      xv = *reinterpret_cast<const unsigned*>(x + col);
      u32x4 v;
      for (unsigned spin = 0;; ++spin) {
        MEMBAR();
        v = gld2(G3Z + fu, 0);
        if (__all(gok2(v)) || spin >= SPIN_MAX) break;
        __builtin_amdgcn_s_sleep(1);
      }
      float s0 = grp8_sum(bitsf(v[0])), s1 = grp8_sum(bitsf(v[2]));
      s0 += dpp<0x128>(s0);
      s1 += dpp<0x128>(s1);
      s0 = swap16_add(s0, s0);
      s1 = swap16_add(s1, s1);
      gate0 = bfr(silu(bfr(s0)));
      gate1 = bfr(silu(bfr(s1)));
      asm volatile("" : "+v"(xv));
    }
    STAMP(14);
    __syncthreads();
    STAMP(8);
  } else {
    unsigned wp[24];
    if (w >= 4) {
#pragma unroll
      for (int i = WOB; i < 8; ++i) WOLD(i);
      if (tid < 256 + 240) {
#pragma unroll
        for (int i = 0; i < 8; ++i) *reinterpret_cast<u32x4*>(lds + L_WO + u * 4 + i * 960) = WO[i];
      }
      asm volatile("s_waitcnt lgkmcnt(0)" ::: "memory");
      if (lane == 0) __hip_atomic_fetch_add(flg + 2, 1u, __ATOMIC_RELAXED, __HIP_MEMORY_SCOPE_WORKGROUP);
      STAMP(12);
    }
    for (unsigned spin = 0; spin < (SPIN_MAX << 4); ++spin) {
      if (__hip_atomic_load(flg + 2, __ATOMIC_RELAXED, __HIP_MEMORY_SCOPE_WORKGROUP) == 4u) break;
      __builtin_amdgcn_s_sleep(1);
    }
    asm volatile("s_waitcnt lgkmcnt(0)" ::: "memory");
    const float* wl = lds + L_WO + (lane >> 1) * 40 + (2 * (w - 3) + (lane & 1)) * 4;
#pragma unroll
    for (int k = 0; k < 3; ++k) {
      const u32x4 A = *reinterpret_cast<const u32x4*>(wl + 64 * k * 40), B = *reinterpret_cast<const u32x4*>(wl + (64 * k + 32) * 40);
#pragma unroll
      for (int q = 0; q < 4; ++q) {
        wp[8 * k + 2 * q] = __builtin_amdgcn_perm(B[q], A[q], 0x05040100u);
        wp[8 * k + 2 * q + 1] = __builtin_amdgcn_perm(B[q], A[q], 0x07060302u);
      }
    }
#pragma unroll
    for (int i = 0; i < 24; i += 8)
      asm volatile("" : "+v"(wp[i]), "+v"(wp[i + 1]), "+v"(wp[i + 2]), "+v"(wp[i + 3]), "+v"(wp[i + 4]), "+v"(wp[i + 5]), "+v"(wp[i + 6]), "+v"(wp[i + 7]));
    STAMP(14);
    __syncthreads();
    STAMP(8);
    const u32x4 op = *reinterpret_cast<const u32x4*>(lds + L_O + (lane >> 1) * 4);
    float y[8];
#pragma unroll
    for (int e = 0; e < 8; ++e) y[e] = dot2(wp[16 + e], op[2], dot2(wp[8 + e], op[1], dot2(wp[e], op[0], 0.f)));
    float p[4];
#pragma unroll
    for (int e = 0; e < 4; ++e) p[e] = swap32_add(y[e], y[e + 4]);
    float q0 = swap16_add(p[0], p[2]), q1 = swap16_add(p[1], p[3]);
    q0 += dpp<0x128>(q0);
    q1 += dpp<0x128>(q1);
    q0 += dpp<0x124>(q0);
    q1 += dpp<0x124>(q1);
    q0 += dpp<0x4E>(q0);
    q1 += dpp<0x4E>(q1);
    if ((lane & 14) == 0) gst2(G3Y + (40 * sp + 4 * (2 * (w - 3) + (lane & 1)) + (lane >> 4)) * 32 + h, q0, q1);
  }
  STAMP(9);
  if (fin) {
    u32x4 v;
    for (unsigned spin = 0;; ++spin) {
      MEMBAR();
      v = gld2(G3Y + fu, 0);
      if (__all(gok2(v)) || spin >= SPIN_MAX) break;
      __builtin_amdgcn_s_sleep(1);
    }
    float s0 = grp8_sum(bitsf(v[0])), s1 = grp8_sum(bitsf(v[2]));
    s0 += dpp<0x128>(s0);
    s1 += dpp<0x128>(s1);
    s0 = swap16_add(s0, s0);
    s1 = swap16_add(s1, s1);
    STAMP(10);
    if ((lane & 31) == 0) {
      const unsigned r0 = bfbits(bf_lo(xv) + bfr(bfr(s0) * gate0)), r1 = bfbits(bf_hi(xv) + bfr(bfr(s1) * gate1));
      *reinterpret_cast<unsigned*>(out + col) = r0 | (r1 << 16);
      if (col == 0)
        __hip_atomic_store(reinterpret_cast<unsigned*>(gw + 2 * GEP), tag, __ATOMIC_RELAXED, __HIP_MEMORY_SCOPE_AGENT);
    }
  }
  STAMP(11);
}

}

extern "C" void kernel_launch(void* const* d_in, const int* in_sizes, int n_in, void* d_out, int out_size, void* d_ws,
                              size_t ws_size, hipStream_t stream) {
  const bf16* x = (const bf16*)d_in[0];
  const bf16* kc = (const bf16*)d_in[1];
  const bf16* vc = (const bf16*)d_in[2];
  const bf16* wq = (const bf16*)d_in[3];
  const bf16* wo = (const bf16*)d_in[4];
  const bf16* w1 = (const bf16*)d_in[6];
  const bf16* w3 = (const bf16*)d_in[7];
  const bf16* w2 = (const bf16*)d_in[8];
  u64* gw = (u64*)d_ws;
  fused_decode<<<NWG, NT, 0, stream>>>(x, kc, vc, wq, wo, w1, w3, w2, gw, (bf16*)d_out);
}
```

```cpp
#include <hip/hip_runtime.h>
#include <hip/hip_bf16.h>
#include <stdint.h>

typedef __hip_bfloat16 bf16;
typedef unsigned long long u64;
typedef unsigned u32x4 __attribute__((ext_vector_type(4)));
typedef float f32x4 __attribute__((ext_vector_type(4)));
typedef __bf16 bf16x2_t __attribute__((ext_vector_type(2)));

namespace {

constexpr int D = 640, HQ = 32, DH = 192, SK = 32768, WIN = 4096, FF = 1536;
constexpr int NSP = 8, KEYS = WIN / NSP;
constexpr int NWG = HQ * NSP;
constexpr int NT = 512;
constexpr float SCALE = 0.07216878364870322f;
constexpr float LOG2E = 1.4426950408889634f;
constexpr unsigned SPIN_MAX = 1u << 16;
constexpr int WND = 8;
constexpr int PQ = 5;
constexpr int KE = 8;
constexpr int PD1 = 14;
constexpr int WOB = 8;

constexpr int G1 = 0;
constexpr int G2 = G1 + NWG * 144;
constexpr int G3Y = G2 + NWG * 104;
constexpr int G3Z = G3Y + 320 * 32;
constexpr int GEP = G3Z + 320 * 32;
constexpr int GEND = GEP + 8 + 256;

__device__ __forceinline__ unsigned fbits(float x) { return __builtin_bit_cast(unsigned, x); }
__device__ __forceinline__ float bitsf(unsigned x) { return __builtin_bit_cast(float, x); }
#define gok2(v) ((v)[1] == tag && (v)[3] == tag)
#define gst2(unit, a, b) __builtin_amdgcn_raw_buffer_store_b128(u32x4{fbits(a), tag, fbits(b), tag}, rws, (unit) * 16, 0, 16)
#define gld2(unit, cunit) __builtin_amdgcn_raw_buffer_load_b128(rws, (unit) * 16, (cunit) * 16, 16)
#define MEMBAR() asm volatile("" ::: "memory")
typedef float f32x2 __attribute__((ext_vector_type(2)));

__device__ __forceinline__ float bf_lo(unsigned u) { return __builtin_bit_cast(float, u << 16); }
__device__ __forceinline__ float bf_hi(unsigned u) { return __builtin_bit_cast(float, u & 0xffff0000u); }
__device__ __forceinline__ float bfr(float x) { return __bfloat162float(__float2bfloat16(x)); }
__device__ __forceinline__ unsigned short bfbits(float x) {
  return __builtin_bit_cast(unsigned short, __float2bfloat16(x));
}
__device__ __forceinline__ float dot2(unsigned a, unsigned b, float c) {
  return __builtin_amdgcn_fdot2_f32_bf16(__builtin_bit_cast(bf16x2_t, a), __builtin_bit_cast(bf16x2_t, b), c, false);
}
template <int CTRL>
__device__ __forceinline__ float dpp(float x) {
  return __builtin_bit_cast(float, __builtin_amdgcn_mov_dpp(__builtin_bit_cast(int, x), CTRL, 0xf, 0xf, true));
}
__device__ __forceinline__ float grp8_sum(float x) {
  x += dpp<0xB1>(x);
  x += dpp<0x4E>(x);
  x += dpp<0x141>(x);
  return x;
}
__device__ __forceinline__ float swap16_add(float a, float b) {
  auto s = __builtin_amdgcn_permlane16_swap(fbits(a), fbits(b), false, false);
  return bitsf((unsigned)s[0]) + bitsf((unsigned)s[1]);
}
__device__ __forceinline__ float swap32_add(float a, float b) {
  auto t = __builtin_amdgcn_permlane32_swap(fbits(a), fbits(b), false, false);
  return bitsf((unsigned)t[0]) + bitsf((unsigned)t[1]);
}
__device__ __forceinline__ float xg_sum(float x) {
  x += dpp<0x128>(x);
  x = swap16_add(x, x);
  return swap32_add(x, x);
}
__device__ __forceinline__ float xg_max(float x) {
  x = fmaxf(x, dpp<0x128>(x));
  auto s = __builtin_amdgcn_permlane16_swap(fbits(x), fbits(x), false, false);
  x = fmaxf(bitsf((unsigned)s[0]), bitsf((unsigned)s[1]));
  auto t = __builtin_amdgcn_permlane32_swap(fbits(x), fbits(x), false, false);
  return fmaxf(bitsf((unsigned)t[0]), bitsf((unsigned)t[1]));
}
__device__ __forceinline__ void xg_scatter(const float (&a)[24], float (&r)[3], const bool b3) {
#pragma unroll
  for (int j = 0; j < 3; ++j) {
    float p[4];
#pragma unroll
    for (int e = 0; e < 4; ++e) p[e] = swap32_add(a[8 * j + e], a[8 * j + e + 4]);
    const float q0 = swap16_add(p[0], p[2]), q1 = swap16_add(p[1], p[3]);
    r[j] = (b3 ? q1 : q0) + dpp<0x128>(b3 ? q0 : q1);
  }
}
__device__ __forceinline__ float wave_sum(float x) { return xg_sum(grp8_sum(x)); }
__device__ __forceinline__ float ex2(float x) { return __builtin_amdgcn_exp2f(x); }
__device__ __forceinline__ float silu(float x) { return x / (1.0f + __expf(-x)); }
__device__ __forceinline__ u32x4 ntld(const bf16* p) {
  return __builtin_nontemporal_load(reinterpret_cast<const u32x4*>(p));
}
__device__ __forceinline__ float rdlane(float x, int l) {
  return __builtin_bit_cast(float, __builtin_amdgcn_readlane(__builtin_bit_cast(int, x), l));
}

#define SCHED_FENCE() __builtin_amdgcn_sched_barrier(0)
#define WLD(dst, ptr, m)                                           \
  do {                                                             \
    asm volatile("s_waitcnt vmcnt(%0)" ::"n"(WND - 1) : "memory"); \
    dst = ntld(ptr);                                               \
    SCHED_FENCE();                                                 \
  } while (0)
#define WAVE_SYNC()                                        \
  do {                                                     \
    __builtin_amdgcn_fence(__ATOMIC_RELEASE, "wavefront"); \
    __builtin_amdgcn_wave_barrier();                       \
    __builtin_amdgcn_fence(__ATOMIC_ACQUIRE, "wavefront"); \
  } while (0)

constexpr int L_WO = 0;
constexpr int L_XS = L_WO + DH * 40;
constexpr int PS = DH + 4;
constexpr int L_PART = L_XS + 4 * 320;
constexpr int L_FFP = L_PART + 8 * PS;
constexpr int L_ZP = L_FFP + 20 * 96;
constexpr int L_QS = L_ZP + 6 * 80;
constexpr int L_ML = L_QS + 96;
constexpr int L_O = L_ML + 16;
constexpr int L_H = L_O + 128;
constexpr int L_GU = L_H + 48;
constexpr int L_FLG = L_GU + 96;
constexpr int L_END = L_FLG + 4;

__global__ void __launch_bounds__(NT, 2)
fused_decode(const bf16* __restrict__ x, const bf16* __restrict__ kc, const bf16* __restrict__ vc,
             const bf16* __restrict__ wq, const bf16* __restrict__ wo, const bf16* __restrict__ w1,
             const bf16* __restrict__ w3, const bf16* __restrict__ w2, u64* __restrict__ gw, bf16* __restrict__ out) {
  __shared__ __attribute__((aligned(16))) float lds[L_END];
  const int b = blockIdx.x;
  const int xcd = b & 7, jj = b >> 3;
  const int h = xcd * 4 + (jj >> 3), sp = jj & 7;
  const int w = __builtin_amdgcn_readfirstlane(threadIdx.x >> 6);
  const int tid = threadIdx.x, lane = tid & 63;
  const int g = lane >> 3, c = lane & 7;
  const int rec = h * NSP + sp;
  unsigned* flg = reinterpret_cast<unsigned*>(lds + L_FLG);

  asm volatile("" ::"s"(x), "s"(kc), "s"(vc), "s"(wq), "s"(wo), "s"(w1), "s"(w3), "s"(w2), "s"(gw), "s"(out));
  const auto rws = __builtin_amdgcn_make_buffer_rsrc((void*)gw, (short)0, GEND * 16, 0x00020000);
  const size_t kvbase = ((size_t)h * SK + (SK - WIN) + (size_t)sp * KEYS + 64 * ((w + (h >> 3)) & 7) + g) * DH + 8 * c;
#define KVOFF(t) ((((t) + h) & 7) * 8 * DH)
  u32x4 K[8][3], V[8][3];

  unsigned epoch, tag;
  if (w < 4) {
    const int R0 = 80 * sp + 20 * w;
    const u32x4 xa = *reinterpret_cast<const u32x4*>(x + 8 * lane);
    const u32x4 xb = *reinterpret_cast<const u32x4*>(x + 512 + 8 * (lane & 15));
    u32x4 WQ[3][3], F[4];
    {
      const bf16* p0 = wq + (size_t)(R0 + g) * (HQ * DH) + h * DH + 8 * c;
      const bf16* p2 = wq + (size_t)(R0 + 16 + (g & 3)) * (HQ * DH) + h * DH + 8 * c;
#pragma unroll
      for (int i = 0; i < 2; ++i)
#pragma unroll
        for (int j = 0; j < 3; ++j) WQ[i][j] = ntld(p0 + (size_t)i * 8 * (HQ * DH) + 64 * j);
#pragma unroll
      for (int j = 0; j < 3; ++j) WQ[2][j] = ntld(p2 + 64 * j);
    }
    const int lf = lane < 60 ? lane : 59;
    const int fr5 = lf / 12, frem = lf - 12 * fr5, fm = frem >= 6 ? 1 : 0, fck = frem - 6 * fm;
    {
      const bf16* pf = (fm ? w3 : w1) + (size_t)(R0 + fr5) * FF + 48 * h + 8 * fck;
#pragma unroll
      for (int i = 0; i < 4; ++i) F[i] = ntld(pf + (size_t)5 * i * FF);
    }
    SCHED_FENCE();
    epoch = __builtin_amdgcn_raw_buffer_load_b32(rws, 0, GEP * 16, 0);
    if (tid < 4) flg[tid] = 0u;
    SCHED_FENCE();
    __syncthreads();
    SCHED_FENCE();
    unsigned short* xs = reinterpret_cast<unsigned short*>(lds + L_XS + w * 320);
    const bool lo16 = lane < 16;
    *reinterpret_cast<u32x4*>(xs + 8 * lane) = xa;
    if (lo16) *reinterpret_cast<u32x4*>(xs + 512 + 8 * lane) = xb;
    float xf[8], xg[8];
#pragma unroll
    for (int i = 0; i < 4; ++i) {
      xf[2 * i] = bf_lo(xa[i]);
      xf[2 * i + 1] = bf_hi(xa[i]);
      xg[2 * i] = bf_lo(xb[i]);
      xg[2 * i + 1] = bf_hi(xb[i]);
    }
    float sum = 0.f;
#pragma unroll
    for (int i = 0; i < 8; ++i) sum += xf[i] + (lo16 ? xg[i] : 0.f);
    sum = wave_sum(sum);
    const float mean = sum * (1.0f / D);
    float sq = 0.f;
#pragma unroll
    for (int i = 0; i < 8; ++i) {
      const float d1 = xf[i] - mean, d2 = xg[i] - mean;
      sq += d1 * d1 + (lo16 ? d2 * d2 : 0.f);
    }
    sq = wave_sum(sq);
    const float mb = bfr(mean);
    const float vb = bfr(sq * (1.0f / D));
    const float den = bfr(sqrtf(bfr(vb + 1e-5f)));
    WAVE_SYNC();
    auto xnorm = [&](int r) -> unsigned {
      return fbits(bfr(bfr(bf_lo(xs[r]) - mb) / den));
    };
    {
      float acc[24], r3[3];
      const unsigned x0 = xnorm(R0 + g), x1 = xnorm(R0 + 8 + g);
      const unsigned x2 = g < 4 ? xnorm(R0 + 16 + (g & 3)) : 0u;
#pragma unroll
      for (int j = 0; j < 3; ++j)
#pragma unroll
        for (int i = 0; i < 4; ++i) {
          acc[8 * j + 2 * i] = dot2(WQ[2][j][i], x2 >> 16, dot2(WQ[1][j][i], x1 >> 16, dot2(WQ[0][j][i], x0 >> 16, 0.f)));
          acc[8 * j + 2 * i + 1] = dot2(WQ[2][j][i], x2, dot2(WQ[1][j][i], x1, dot2(WQ[0][j][i], x0, 0.f)));
        }
      xg_scatter(acc, r3, g & 1);
#pragma unroll
      for (int j = 0; j < 3; ++j) lds[L_PART + w * DH + 64 * j + 8 * c + g] = r3[j];
      float fg[8];
#pragma unroll
      for (int i = 0; i < 8; ++i) fg[i] = 0.f;
#pragma unroll
      for (int r = 0; r < 4; ++r) {
        const unsigned y = xnorm(R0 + fr5 + 5 * r);
#pragma unroll
        for (int i = 0; i < 4; ++i) {
          fg[2 * i] = dot2(F[r][i], y >> 16, fg[2 * i]);
          fg[2 * i + 1] = dot2(F[r][i], y, fg[2 * i + 1]);
        }
      }
      if (lane < 60) {
        float* fp = lds + L_FFP + (w * 5 + fr5) * 96 + fm * 48 + 8 * fck;
        *reinterpret_cast<f32x4*>(fp) = f32x4{fg[0], fg[1], fg[2], fg[3]};
        *reinterpret_cast<f32x4*>(fp + 4) = f32x4{fg[4], fg[5], fg[6], fg[7]};
      }
    }
    asm volatile("s_waitcnt lgkmcnt(0)" ::: "memory");
    if (lane == 0) __hip_atomic_fetch_add(flg, 1u, __ATOMIC_RELAXED, __HIP_MEMORY_SCOPE_WORKGROUP);
    SCHED_FENCE();
  } else {
#pragma unroll
    for (int m = 0; m < KE; ++m) K[m / 3][m % 3] = ntld(kc + kvbase + KVOFF(m / 3) + 64 * (m % 3));
    SCHED_FENCE();
    epoch = __builtin_amdgcn_raw_buffer_load_b32(rws, 0, GEP * 16, 0);
    SCHED_FENCE();
    __syncthreads();
    SCHED_FENCE();
  }
  if (w == 0) {
    for (unsigned spin = 0; spin < (SPIN_MAX << 4); ++spin) {
      if (__hip_atomic_load(flg, __ATOMIC_RELAXED, __HIP_MEMORY_SCOPE_WORKGROUP) == 4u) break;
      __builtin_amdgcn_s_sleep(1);
    }
    asm volatile("s_waitcnt lgkmcnt(0)" ::: "memory");
    tag = __builtin_amdgcn_readfirstlane(epoch) + 1u;
    {
      const f32x2* pp = reinterpret_cast<const f32x2*>(lds + L_PART);
      const f32x2 qa = (pp[lane] + pp[96 + lane]) + (pp[192 + lane] + pp[288 + lane]);
      gst2(G1 + rec * 144 + lane, qa[0], qa[1]);
      if (lane < 32) {
        const f32x2 qb = (pp[64 + lane] + pp[160 + lane]) + (pp[256 + lane] + pp[352 + lane]);
        gst2(G1 + rec * 144 + 64 + lane, qb[0], qb[1]);
      }
      if (lane < 48) {
        const f32x2* fp = reinterpret_cast<const f32x2*>(lds + L_FFP) + lane;
        f32x2 va = fp[0], vb = fp[48];
#pragma unroll
        for (int r = 2; r < 20; r += 2) {
          va += fp[r * 48];
          vb += fp[(r + 1) * 48];
        }
        va += vb;
        gst2(G1 + rec * 144 + 96 + lane, va[0], va[1]);
      }
    }
    SCHED_FENCE();
    const int lz = lane < 60 ? lane : 59;
    const int zr = lz / 10, zc = lz - 10 * zr;
    u32x4 W2[8];
#pragma unroll
    for (int i = 0; i < 8; ++i) W2[i] = ntld(w2 + ((size_t)48 * h + zr + 6 * i) * D + 80 * sp + 8 * zc);
#pragma unroll
    for (int t = 0; t < 8; ++t)
#pragma unroll
      for (int j = 0; j < 3; ++j) K[t][j] = ntld(kc + kvbase + KVOFF(t) + 64 * j);
    SCHED_FENCE();
    const int gb = G1 + h * NSP * 144 + lane;
    const int gc = G1 + (h * NSP + (lane >> 4)) * 144 + 128 + (lane & 15);
    u32x4 va[NSP], vb[NSP], vc2[2];
    for (unsigned spin = 0;; ++spin) {
      MEMBAR();
      bool ok = true;
#pragma unroll
      for (int s = 0; s < NSP; ++s) {
        va[s] = gld2(gb, s * 144);
        vb[s] = gld2(gb, s * 144 + 64);
      }
      vc2[0] = gld2(gc, 0);
      vc2[1] = gld2(gc, 4 * 144);
#pragma unroll
      for (int s = 0; s < NSP; ++s) ok = ok && gok2(va[s]) && gok2(vb[s]);
      ok = ok && gok2(vc2[0]) && gok2(vc2[1]);
      if (__all(ok) || spin >= SPIN_MAX) break;
      __builtin_amdgcn_s_sleep(1);
    }
#define SUM8(v, e) (((bitsf(v[0][e]) + bitsf(v[1][e])) + (bitsf(v[2][e]) + bitsf(v[3][e]))) + ((bitsf(v[4][e]) + bitsf(v[5][e])) + (bitsf(v[6][e]) + bitsf(v[7][e]))))
    const float a0 = SUM8(va, 0), a1 = SUM8(va, 2), b0 = SUM8(vb, 0), b1 = SUM8(vb, 2);
    reinterpret_cast<unsigned*>(lds + L_QS)[lane] = (unsigned)bfbits(a0) | ((unsigned)bfbits(a1) << 16);
    if (lane < 32) reinterpret_cast<unsigned*>(lds + L_QS)[64 + lane] = (unsigned)bfbits(b0) | ((unsigned)bfbits(b1) << 16);
    asm volatile("s_waitcnt lgkmcnt(0)" ::: "memory");
    if (lane == 0) __hip_atomic_store(flg + 1, 1u, __ATOMIC_RELAXED, __HIP_MEMORY_SCOPE_WORKGROUP);
    SCHED_FENCE();
    float c0 = bitsf(vc2[0][0]) + bitsf(vc2[1][0]), c1 = bitsf(vc2[0][2]) + bitsf(vc2[1][2]);
    c0 = swap16_add(c0, c0);
    c1 = swap16_add(c1, c1);
    c0 = swap32_add(c0, c0);
    c1 = swap32_add(c1, c1);
    if (lane >= 32) *reinterpret_cast<f32x2*>(lds + L_GU + 2 * (lane - 32)) = f32x2{bfr(b0), bfr(b1)};
    if (lane < 16) *reinterpret_cast<f32x2*>(lds + L_GU + 64 + 2 * lane) = f32x2{bfr(c0), bfr(c1)};
    WAVE_SYNC();
    if (lane < 48) lds[L_H + lane] = bfr(bfr(silu(lds[L_GU + lane])) * lds[L_GU + 48 + lane]);
    WAVE_SYNC();
    {
      float z[8];
#pragma unroll
      for (int i = 0; i < 8; ++i) z[i] = 0.f;
#pragma unroll
      for (int i = 0; i < 8; ++i) {
        const unsigned chi = fbits(lds[L_H + zr + 6 * i]), clo = chi >> 16;
#pragma unroll
        for (int k = 0; k < 4; ++k) {
          z[2 * k] = dot2(W2[i][k], clo, z[2 * k]);
          z[2 * k + 1] = dot2(W2[i][k], chi, z[2 * k + 1]);
        }
      }
      if (lane < 60) {
        float* zp = lds + L_ZP + zr * 80 + 8 * zc;
        *reinterpret_cast<f32x4*>(zp) = f32x4{z[0], z[1], z[2], z[3]};
        *reinterpret_cast<f32x4*>(zp + 4) = f32x4{z[4], z[5], z[6], z[7]};
      }
      WAVE_SYNC();
      if (lane < 40) {
        const f32x2* zp = reinterpret_cast<const f32x2*>(lds + L_ZP) + lane;
        const f32x2 zs = ((zp[0] + zp[40]) + (zp[80] + zp[120])) + (zp[160] + zp[200]);
        gst2(G3Z + (40 * sp + lane) * 32 + h, zs[0], zs[1]);
      }
    }
    SCHED_FENCE();
  } else if (w < 4) {
#pragma unroll
    for (int m = 0; m < 24; ++m) WLD(K[m / 3][m % 3], kc + kvbase + KVOFF(m / 3) + 64 * (m % 3), m);
    tag = __builtin_amdgcn_readfirstlane(epoch) + 1u;
    SCHED_FENCE();
  } else {
#pragma unroll
    for (int m = KE; m < 24; ++m) WLD(K[m / 3][m % 3], kc + kvbase + KVOFF(m / 3) + 64 * (m % 3), m);
    tag = __builtin_amdgcn_readfirstlane(epoch) + 1u;
    SCHED_FENCE();
  }
  const int u = (tid & 255) < 240 ? (tid & 255) : 239;
  const int rg = u / 10, ch = u - 10 * rg;
  u32x4 WO[8];
  const auto rwo = __builtin_amdgcn_make_buffer_rsrc((void*)(wo + (size_t)DH * h * D + 80 * sp), (short)0, DH * D * 2, 0x00020000);
  const int wofs = (rg * D + 8 * ch) * 2;
#define WOLD(i)                                                                      \
  do {                                                                               \
    WO[i] = __builtin_amdgcn_raw_buffer_load_b128(rwo, wofs, (i) * 24 * D * 2, 2);   \
    SCHED_FENCE();                                                                   \
  } while (0)
#define VLD(t)                                                                       \
  do {                                                                               \
    _Pragma("unroll") for (int j = 0; j < 3; ++j) WLD(V[t][j], vc + kvbase + KVOFF(t) + 64 * j, 24 + 3 * (t) + j); \
  } while (0)
#pragma unroll
  for (int t = 0; t < PQ; ++t) VLD(t);
  for (unsigned spin = 0; spin < (SPIN_MAX << 4); ++spin) {
    if (__hip_atomic_load(flg + 1, __ATOMIC_RELAXED, __HIP_MEMORY_SCOPE_WORKGROUP) != 0u) break;
    __builtin_amdgcn_s_sleep(1);
  }
  asm volatile("s_waitcnt lgkmcnt(0)" ::: "memory");
  SCHED_FENCE();

  u32x4 qf[3];
#pragma unroll
  for (int j = 0; j < 3; ++j) qf[j] = *reinterpret_cast<const u32x4*>(lds + L_QS + (c + 8 * j) * 4);
  float sc[8];
#pragma unroll
  for (int t = 0; t < 8; ++t) {
    float d = 0.f;
#pragma unroll
    for (int j = 0; j < 3; ++j)
#pragma unroll
      for (int i = 0; i < 4; ++i) d = dot2(K[t][j][i], qf[j][i], d);
    d = grp8_sum(d);
    sc[t] = bfr(bfr(d) * SCALE);
    if (t == 3) {
      SCHED_FENCE();
      VLD(PQ);
    }
  }
  float mw = sc[0];
#pragma unroll
  for (int t = 1; t < 8; ++t) mw = fmaxf(mw, sc[t]);
  mw = xg_max(mw);
  if (lane == 0) {
    lds[L_ML + w] = mw;
    __hip_atomic_fetch_add(flg + 3, 1u, __ATOMIC_RELAXED, __HIP_MEMORY_SCOPE_WORKGROUP);
  }
  float lw = 0.f;
#pragma unroll
  for (int t = 0; t < 8; ++t) {
    sc[t] = ex2((sc[t] - mw) * LOG2E);
    lw += sc[t];
  }
  lw = xg_sum(lw);
  SCHED_FENCE();
  float Mg;
  {
    constexpr int LEAD = 3;
#pragma unroll
    for (int t = PQ + 1; t < LEAD; ++t) VLD(t);
    float acc[24], r3[3];
#pragma unroll
    for (int i = 0; i < 24; ++i) acc[i] = 0.f;
    constexpr int L2 = (PQ + 1 > LEAD) ? PQ + 1 : LEAD;
#define PVT(t)                                                                                                          \
  do {                                                                                                                  \
    const unsigned plo = bfbits(sc[t]), phi = plo << 16;                                                                \
    _Pragma("unroll") for (int j = 0; j < 3; ++j) _Pragma("unroll") for (int i = 0; i < 4; ++i) {                       \
      acc[8 * j + 2 * i] = dot2(V[t][j][i], plo, acc[8 * j + 2 * i]);                                                   \
      acc[8 * j + 2 * i + 1] = dot2(V[t][j][i], phi, acc[8 * j + 2 * i + 1]);                                           \
    }                                                                                                                   \
    _Pragma("unroll") for (int i = 0; i < 24; i += 8)                                                                   \
      asm volatile("" : "+v"(acc[i]), "+v"(acc[i + 1]), "+v"(acc[i + 2]), "+v"(acc[i + 3]), "+v"(acc[i + 4]),           \
                   "+v"(acc[i + 5]), "+v"(acc[i + 6]), "+v"(acc[i + 7]));            \
    SCHED_FENCE();                                                                                                      \
  } while (0)
#define WGMAX()                                                                                                         \
  do {                                                                                                                  \
    for (unsigned spin = 0; spin < (SPIN_MAX << 4); ++spin) {                                                           \
      if (__hip_atomic_load(flg + 3, __ATOMIC_RELAXED, __HIP_MEMORY_SCOPE_WORKGROUP) == 8u) break;                      \
      __builtin_amdgcn_s_sleep(1);                                                                                      \
    }                                                                                                                   \
    MEMBAR();                                                                                                           \
    const f32x4 ma = *reinterpret_cast<const f32x4*>(lds + L_ML), mb2 = *reinterpret_cast<const f32x4*>(lds + L_ML + 4); \
    Mg = fmaxf(fmaxf(fmaxf(ma[0], ma[1]), fmaxf(ma[2], ma[3])), fmaxf(fmaxf(mb2[0], mb2[1]), fmaxf(mb2[2], mb2[3])));   \
    ew = ex2((mw - Mg) * LOG2E);                                                                                        \
    SCHED_FENCE();                                                                                                      \
  } while (0)
    float ew;
#pragma unroll
    for (int t = 0; t + L2 < 8; ++t) {
      VLD(t + L2);
      PVT(t);
    }
    if (w >= 4) {
#pragma unroll
      for (int t = 8 - L2; t < 8; ++t) {
#pragma unroll
        for (int i = 0; i < WOB; ++i)
          if ((t == 7) ? (i >= t + L2 - 8) : (i == t + L2 - 8)) WOLD(i);
        if (t == 6) WGMAX();
        PVT(t);
      }
    } else {
#pragma unroll
      for (int t = 8 - L2; t < 8; ++t) {
        if (t == 6) WGMAX();
        PVT(t);
      }
    }
    SCHED_FENCE();
    xg_scatter(acc, r3, g & 1);
#pragma unroll
    for (int j = 0; j < 3; ++j) lds[L_PART + w * PS + 64 * j + 8 * c + g] = r3[j] * ew;
    if (lane == 0) lds[L_PART + w * PS + DH] = lw * ew;
  }
  __syncthreads();
  float gate0 = 0.f, gate1 = 0.f;
  unsigned xv = 0u;
  const int col = 80 * sp + 4 * h + (lane >> 5) * 2;
  const bool fin = (w == 2) && (h < 20);
  const int fu = (40 * sp + 2 * h) * 32 + lane;
  if (__builtin_expect(w < 2, 1)) {
    if (tid < 97) {
      f32x2 p2[8];
#pragma unroll
      for (int ww = 0; ww < 8; ++ww) p2[ww] = *reinterpret_cast<const f32x2*>(lds + L_PART + ww * PS + 2 * tid);
      const f32x2 us = ((p2[0] + p2[1]) + (p2[2] + p2[3])) + ((p2[4] + p2[5]) + (p2[6] + p2[7]));
      gst2(G2 + rec * 104 + tid, tid < 96 ? us[0] : Mg, tid < 96 ? us[1] : us[0]);
    }
    const int t2 = tid < 96 ? tid : 95;
    const int pb = G2 + h * NSP * 104 + t2, pm = G2 + (h * NSP + c) * 104 + 96;
    u32x4 va[NSP], vml;
    if (PD1) __builtin_amdgcn_s_sleep(PD1);
    for (unsigned spin = 0;; ++spin) {
      MEMBAR();
#pragma unroll
      for (int s = 0; s < NSP; ++s) va[s] = gld2(pb, s * 104);
      vml = gld2(pm, 0);
      bool ok = gok2(vml);
#pragma unroll
      for (int s = 0; s < NSP; ++s) ok = ok && gok2(va[s]);
      if (__all(ok) || spin >= SPIN_MAX) break;
      __builtin_amdgcn_s_sleep(1);
    }
    const float ms = bitsf(vml[0]);
    float M2 = fmaxf(ms, dpp<0xB1>(ms));
    M2 = fmaxf(M2, dpp<0x4E>(M2));
    M2 = fmaxf(M2, dpp<0x141>(M2));
    const float es = ex2((ms - M2) * LOG2E);
    const float L2 = grp8_sum(es * bitsf(vml[2]));
    float o0 = 0.f, o1 = 0.f;
#pragma unroll
    for (int s = 0; s < NSP; ++s) {
      const float e = rdlane(es, s);
      o0 = fmaf(e, bitsf(va[s][0]), o0);
      o1 = fmaf(e, bitsf(va[s][2]), o1);
    }
    if (tid < 96) {
      unsigned short* po = reinterpret_cast<unsigned short*>(lds + L_O) + ((2 * tid & 31) * 4 + (tid >> 5)) * 2 + (tid >> 4 & 1);
      const float rl = __builtin_amdgcn_rcpf(L2);
      po[0] = bfbits(o0 * rl);
      po[8] = bfbits(o1 * rl);
    }
    __syncthreads();
  } else if (w == 2) {
    if (fin) {
      xv = *reinterpret_cast<const unsigned*>(x + col);
      u32x4 v;
      for (unsigned spin = 0;; ++spin) {
        MEMBAR();
        v = gld2(G3Z + fu, 0);
        if (__all(gok2(v)) || spin >= SPIN_MAX) break;
        __builtin_amdgcn_s_sleep(1);
      }
      float s0 = grp8_sum(bitsf(v[0])), s1 = grp8_sum(bitsf(v[2]));
      s0 += dpp<0x128>(s0);
      s1 += dpp<0x128>(s1);
      s0 = swap16_add(s0, s0);
      s1 = swap16_add(s1, s1);
      gate0 = bfr(silu(bfr(s0)));
      gate1 = bfr(silu(bfr(s1)));
      asm volatile("" : "+v"(xv));
    }
    __syncthreads();
  } else {
    unsigned wp[24];
    if (w >= 4) {
#pragma unroll
      for (int i = WOB; i < 8; ++i) WOLD(i);
      if (tid < 256 + 240) {
#pragma unroll
        for (int i = 0; i < 8; ++i) *reinterpret_cast<u32x4*>(lds + L_WO + u * 4 + i * 960) = WO[i];
      }
      asm volatile("s_waitcnt lgkmcnt(0)" ::: "memory");
      if (lane == 0) __hip_atomic_fetch_add(flg + 2, 1u, __ATOMIC_RELAXED, __HIP_MEMORY_SCOPE_WORKGROUP);
    }
    for (unsigned spin = 0; spin < (SPIN_MAX << 4); ++spin) {
      if (__hip_atomic_load(flg + 2, __ATOMIC_RELAXED, __HIP_MEMORY_SCOPE_WORKGROUP) == 4u) break;
      __builtin_amdgcn_s_sleep(1);
    }
    asm volatile("s_waitcnt lgkmcnt(0)" ::: "memory");
    const float* wl = lds + L_WO + (lane >> 1) * 40 + (2 * (w - 3) + (lane & 1)) * 4;
#pragma unroll
    for (int k = 0; k < 3; ++k) {
      const u32x4 A = *reinterpret_cast<const u32x4*>(wl + 64 * k * 40), B = *reinterpret_cast<const u32x4*>(wl + (64 * k + 32) * 40);
#pragma unroll
      for (int q = 0; q < 4; ++q) {
        wp[8 * k + 2 * q] = __builtin_amdgcn_perm(B[q], A[q], 0x05040100u);
        wp[8 * k + 2 * q + 1] = __builtin_amdgcn_perm(B[q], A[q], 0x07060302u);
      }
    }
#pragma unroll
    for (int i = 0; i < 24; i += 8)
      asm volatile("" : "+v"(wp[i]), "+v"(wp[i + 1]), "+v"(wp[i + 2]), "+v"(wp[i + 3]), "+v"(wp[i + 4]), "+v"(wp[i + 5]), "+v"(wp[i + 6]), "+v"(wp[i + 7]));
    __syncthreads();
    const u32x4 op = *reinterpret_cast<const u32x4*>(lds + L_O + (lane >> 1) * 4);
    float y[8];
#pragma unroll
    for (int e = 0; e < 8; ++e) y[e] = dot2(wp[16 + e], op[2], dot2(wp[8 + e], op[1], dot2(wp[e], op[0], 0.f)));
    float p[4];
#pragma unroll
    for (int e = 0; e < 4; ++e) p[e] = swap32_add(y[e], y[e + 4]);
    float q0 = swap16_add(p[0], p[2]), q1 = swap16_add(p[1], p[3]);
    q0 += dpp<0x128>(q0);
    q1 += dpp<0x128>(q1);
    q0 += dpp<0x124>(q0);
    q1 += dpp<0x124>(q1);
    q0 += dpp<0x4E>(q0);
    q1 += dpp<0x4E>(q1);
    if ((lane & 14) == 0) gst2(G3Y + (40 * sp + 4 * (2 * (w - 3) + (lane & 1)) + (lane >> 4)) * 32 + h, q0, q1);
  }
  if (fin) {
    u32x4 v;
    for (unsigned spin = 0;; ++spin) {
      MEMBAR();
      v = gld2(G3Y + fu, 0);
      if (__all(gok2(v)) || spin >= SPIN_MAX) break;
      __builtin_amdgcn_s_sleep(1);
    }
    float s0 = grp8_sum(bitsf(v[0])), s1 = grp8_sum(bitsf(v[2]));
    s0 += dpp<0x128>(s0);
    s1 += dpp<0x128>(s1);
    s0 = swap16_add(s0, s0);
    s1 = swap16_add(s1, s1);
    if ((lane & 31) == 0) {
      const unsigned r0 = bfbits(bf_lo(xv) + bfr(bfr(s0) * gate0)), r1 = bfbits(bf_hi(xv) + bfr(bfr(s1) * gate1));
      *reinterpret_cast<unsigned*>(out + col) = r0 | (r1 << 16);
      if (col == 0)
        __hip_atomic_store(reinterpret_cast<unsigned*>(gw + 2 * GEP), tag, __ATOMIC_RELAXED, __HIP_MEMORY_SCOPE_AGENT);
    }
  }
}

}

extern "C" void kernel_launch(void* const* d_in, const int* in_sizes, int n_in, void* d_out, int out_size, void* d_ws,
                              size_t ws_size, hipStream_t stream) {
  const bf16* x = (const bf16*)d_in[0];
  const bf16* kc = (const bf16*)d_in[1];
  const bf16* vc = (const bf16*)d_in[2];
  const bf16* wq = (const bf16*)d_in[3];
  const bf16* wo = (const bf16*)d_in[4];
  const bf16* w1 = (const bf16*)d_in[6];
  const bf16* w3 = (const bf16*)d_in[7];
  const bf16* w2 = (const bf16*)d_in[8];
  u64* gw = (u64*)d_ws;
  fused_decode<<<NWG, NT, 0, stream>>>(x, kc, vc, wq, wo, w1, w3, w2, gw, (bf16*)d_out);
}
```

```cpp
#include <hip/hip_runtime.h>
#include <hip/hip_bf16.h>
#include <stdint.h>

typedef __hip_bfloat16 bf16;
typedef unsigned long long u64;
typedef unsigned u32x4 __attribute__((ext_vector_type(4)));
typedef float f32x4 __attribute__((ext_vector_type(4)));
typedef __bf16 bf16x2_t __attribute__((ext_vector_type(2)));

namespace {

constexpr int D = 640, HQ = 32, DH = 192, SK = 32768, WIN = 4096, FF = 1536;
constexpr int NSP = 8, KEYS = WIN / NSP;
constexpr int NWG = HQ * NSP;
constexpr int NT = 512;
constexpr float SCALE = 0.07216878364870322f;
constexpr float LOG2E = 1.4426950408889634f;
constexpr unsigned SPIN_MAX = 1u << 16;
constexpr int WND = 8;
constexpr int PQ = 5;
constexpr int KE = 8;
constexpr int PD1 = 14;
constexpr int WOB = 8;

constexpr int G1 = 0;
constexpr int G2 = G1 + NWG * 144;
constexpr int G3Y = G2 + NWG * 104;
constexpr int G3Z = G3Y + 320 * 32;
constexpr int GEP = G3Z + 320 * 32;
constexpr int GEND = GEP + 8 + 256;

__device__ __forceinline__ unsigned fbits(float x) { return __builtin_bit_cast(unsigned, x); }
__device__ __forceinline__ float bitsf(unsigned x) { return __builtin_bit_cast(float, x); }
#define gok2(v) ((v)[1] == tag && (v)[3] == tag)
#define gst2(unit, a, b) __builtin_amdgcn_raw_buffer_store_b128(u32x4{fbits(a), tag, fbits(b), tag}, rws, (unit) * 16, 0, 16)
#define gld2(unit, cunit) __builtin_amdgcn_raw_buffer_load_b128(rws, (unit) * 16, (cunit) * 16, 16)
#define MEMBAR() asm volatile("" ::: "memory")
typedef float f32x2 __attribute__((ext_vector_type(2)));

__device__ __forceinline__ float bf_lo(unsigned u) { return __builtin_bit_cast(float, u << 16); }
__device__ __forceinline__ float bf_hi(unsigned u) { return __builtin_bit_cast(float, u & 0xffff0000u); }
__device__ __forceinline__ float bfr(float x) { return __bfloat162float(__float2bfloat16(x)); }
__device__ __forceinline__ unsigned short bfbits(float x) {
  return __builtin_bit_cast(unsigned short, __float2bfloat16(x));
}
__device__ __forceinline__ float dot2(unsigned a, unsigned b, float c) {
  return __builtin_amdgcn_fdot2_f32_bf16(__builtin_bit_cast(bf16x2_t, a), __builtin_bit_cast(bf16x2_t, b), c, false);
}
template <int CTRL>
__device__ __forceinline__ float dpp(float x) {
  return __builtin_bit_cast(float, __builtin_amdgcn_mov_dpp(__builtin_bit_cast(int, x), CTRL, 0xf, 0xf, true));
}
__device__ __forceinline__ float grp8_sum(float x) {
  x += dpp<0xB1>(x);
  x += dpp<0x4E>(x);
  x += dpp<0x141>(x);
  return x;
}
__device__ __forceinline__ float swap16_add(float a, float b) {
  auto s = __builtin_amdgcn_permlane16_swap(fbits(a), fbits(b), false, false);
  return bitsf((unsigned)s[0]) + bitsf((unsigned)s[1]);
}
__device__ __forceinline__ float swap32_add(float a, float b) {
  auto t = __builtin_amdgcn_permlane32_swap(fbits(a), fbits(b), false, false);
  return bitsf((unsigned)t[0]) + bitsf((unsigned)t[1]);
}
__device__ __forceinline__ float xg_sum(float x) {
  x += dpp<0x128>(x);
  x = swap16_add(x, x);
  return swap32_add(x, x);
}
__device__ __forceinline__ float xg_max(float x) {
  x = fmaxf(x, dpp<0x128>(x));
  auto s = __builtin_amdgcn_permlane16_swap(fbits(x), fbits(x), false, false);
  x = fmaxf(bitsf((unsigned)s[0]), bitsf((unsigned)s[1]));
  auto t = __builtin_amdgcn_permlane32_swap(fbits(x), fbits(x), false, false);
  return fmaxf(bitsf((unsigned)t[0]), bitsf((unsigned)t[1]));
}
__device__ __forceinline__ void xg_scatter(const float (&a)[24], float (&r)[3], const bool b3) {
#pragma unroll
  for (int j = 0; j < 3; ++j) {
    float p[4];
#pragma unroll
    for (int e = 0; e < 4; ++e) p[e] = swap32_add(a[8 * j + e], a[8 * j + e + 4]);
    const float q0 = swap16_add(p[0], p[2]), q1 = swap16_add(p[1], p[3]);
    r[j] = (b3 ? q1 : q0) + dpp<0x128>(b3 ? q0 : q1);
  }
}
__device__ __forceinline__ float wave_sum(float x) { return xg_sum(grp8_sum(x)); }
__device__ __forceinline__ float ex2(float x) { return __builtin_amdgcn_exp2f(x); }
__device__ __forceinline__ float silu(float x) { return x / (1.0f + __expf(-x)); }
__device__ __forceinline__ u32x4 ntld(const bf16* p) {
  return __builtin_nontemporal_load(reinterpret_cast<const u32x4*>(p));
}
__device__ __forceinline__ float rdlane(float x, int l) {
  return __builtin_bit_cast(float, __builtin_amdgcn_readlane(__builtin_bit_cast(int, x), l));
}

#define SCHED_FENCE() __builtin_amdgcn_sched_barrier(0)
#define WLD(dst, ptr, m)                                           \
  do {                                                             \
    asm volatile("s_waitcnt vmcnt(%0)" ::"n"(WND - 1) : "memory"); \
    dst = ntld(ptr);                                               \
    SCHED_FENCE();                                                 \
  } while (0)
#define WAVE_SYNC()                                        \
  do {                                                     \
    __builtin_amdgcn_fence(__ATOMIC_RELEASE, "wavefront"); \
    __builtin_amdgcn_wave_barrier();                       \
    __builtin_amdgcn_fence(__ATOMIC_ACQUIRE, "wavefront"); \
  } while (0)

constexpr int L_WO = 0;
constexpr int L_XS = L_WO + DH * 40;
constexpr int PS = DH + 4;
constexpr int L_PART = L_XS + 4 * 320;
constexpr int L_FFP = L_PART + 8 * PS;
constexpr int L_ZP = L_FFP + 20 * 96;
constexpr int L_QS = L_ZP + 6 * 80;
constexpr int L_ML = L_QS + 96;
constexpr int L_O = L_ML + 16;
constexpr int L_H = L_O + 128;
constexpr int L_GU = L_H + 48;
constexpr int L_FLG = L_GU + 96;
constexpr int L_END = L_FLG + 4;

__global__ void __launch_bounds__(NT, 2)
fused_decode(const bf16* __restrict__ x, const bf16* __restrict__ kc, const bf16* __restrict__ vc,
             const bf16* __restrict__ wq, const bf16* __restrict__ wo, const bf16* __restrict__ w1,
             const bf16* __restrict__ w3, const bf16* __restrict__ w2, u64* __restrict__ gw, bf16* __restrict__ out) {
  __shared__ __attribute__((aligned(16))) float lds[L_END];
  const int b = blockIdx.x;
  const int xcd = b & 7, jj = b >> 3;
  const int h = xcd * 4 + (jj >> 3), sp = jj & 7;
  const int w = __builtin_amdgcn_readfirstlane(threadIdx.x >> 6);
  const int tid = threadIdx.x, lane = tid & 63;
  const int g = lane >> 3, c = lane & 7;
  const int rec = h * NSP + sp;
  unsigned* flg = reinterpret_cast<unsigned*>(lds + L_FLG);

  asm volatile("" ::"s"(x), "s"(kc), "s"(vc), "s"(wq), "s"(wo), "s"(w1), "s"(w3), "s"(w2), "s"(gw), "s"(out));
  const auto rws = __builtin_amdgcn_make_buffer_rsrc((void*)gw, (short)0, GEND * 16, 0x00020000);
  const size_t kvbase = ((size_t)h * SK + (SK - WIN) + (size_t)sp * KEYS + 64 * ((w + (h >> 3)) & 7) + g) * DH + 8 * c;
#define KVOFF(t) ((((t) + h) & 7) * 8 * DH)
  u32x4 K[8][3], V[8][3];

  unsigned epoch, tag;
  if (w < 4) {
    const int R0 = 80 * sp + 20 * w;
    const u32x4 xa = *reinterpret_cast<const u32x4*>(x + 8 * lane);
    const u32x4 xb = *reinterpret_cast<const u32x4*>(x + 512 + 8 * (lane & 15));
    u32x4 WQ[3][3], F[4];
    {
      const bf16* p0 = wq + (size_t)(R0 + g) * (HQ * DH) + h * DH + 8 * c;
      const bf16* p2 = wq + (size_t)(R0 + 16 + (g & 3)) * (HQ * DH) + h * DH + 8 * c;
#pragma unroll
      for (int i = 0; i < 2; ++i)
#pragma unroll
        for (int j = 0; j < 3; ++j) WQ[i][j] = ntld(p0 + (size_t)i * 8 * (HQ * DH) + 64 * j);
#pragma unroll
      for (int j = 0; j < 3; ++j) WQ[2][j] = ntld(p2 + 64 * j);
    }
    const int lf = lane < 60 ? lane : 59;
    const int fr5 = lf / 12, frem = lf - 12 * fr5, fm = frem >= 6 ? 1 : 0, fck = frem - 6 * fm;
    {
      const bf16* pf = (fm ? w3 : w1) + (size_t)(R0 + fr5) * FF + 48 * h + 8 * fck;
#pragma unroll
      for (int i = 0; i < 4; ++i) F[i] = ntld(pf + (size_t)5 * i * FF);
    }
    SCHED_FENCE();
    epoch = __builtin_amdgcn_raw_buffer_load_b32(rws, 0, GEP * 16, 0);
    if (tid < 4) flg[tid] = 0u;
    SCHED_FENCE();
    __syncthreads();
    SCHED_FENCE();
    unsigned short* xs = reinterpret_cast<unsigned short*>(lds + L_XS + w * 320);
    const bool lo16 = lane < 16;
    *reinterpret_cast<u32x4*>(xs + 8 * lane) = xa;
    if (lo16) *reinterpret_cast<u32x4*>(xs + 512 + 8 * lane) = xb;
    float xf[8], xg[8];
#pragma unroll
    for (int i = 0; i < 4; ++i) {
      xf[2 * i] = bf_lo(xa[i]);
      xf[2 * i + 1] = bf_hi(xa[i]);
      xg[2 * i] = bf_lo(xb[i]);
      xg[2 * i + 1] = bf_hi(xb[i]);
    }
    float sum = 0.f;
#pragma unroll
    for (int i = 0; i < 8; ++i) sum += xf[i] + (lo16 ? xg[i] : 0.f);
    sum = wave_sum(sum);
    const float mean = sum * (1.0f / D);
    float sq = 0.f;
#pragma unroll
    for (int i = 0; i < 8; ++i) {
      const float d1 = xf[i] - mean, d2 = xg[i] - mean;
      sq += d1 * d1 + (lo16 ? d2 * d2 : 0.f);
    }
    sq = wave_sum(sq);
    const float mb = bfr(mean);
    const float vb = bfr(sq * (1.0f / D));
    const float den = bfr(sqrtf(bfr(vb + 1e-5f)));
    WAVE_SYNC();
    auto xnorm = [&](int r) -> unsigned {
      return fbits(bfr(bfr(bf_lo(xs[r]) - mb) / den));
    };
    {
      float acc[24], r3[3];
      const unsigned x0 = xnorm(R0 + g), x1 = xnorm(R0 + 8 + g);
      const unsigned x2 = g < 4 ? xnorm(R0 + 16 + (g & 3)) : 0u;
#pragma unroll
      for (int j = 0; j < 3; ++j)
#pragma unroll
        for (int i = 0; i < 4; ++i) {
          acc[8 * j + 2 * i] = dot2(WQ[2][j][i], x2 >> 16, dot2(WQ[1][j][i], x1 >> 16, dot2(WQ[0][j][i], x0 >> 16, 0.f)));
          acc[8 * j + 2 * i + 1] = dot2(WQ[2][j][i], x2, dot2(WQ[1][j][i], x1, dot2(WQ[0][j][i], x0, 0.f)));
        }
      xg_scatter(acc, r3, g & 1);
#pragma unroll
      for (int j = 0; j < 3; ++j) lds[L_PART + w * DH + 64 * j + 8 * c + g] = r3[j];
      float fg[8];
#pragma unroll
      for (int i = 0; i < 8; ++i) fg[i] = 0.f;
#pragma unroll
      for (int r = 0; r < 4; ++r) {
        const unsigned y = xnorm(R0 + fr5 + 5 * r);
#pragma unroll
        for (int i = 0; i < 4; ++i) {
          fg[2 * i] = dot2(F[r][i], y >> 16, fg[2 * i]);
          fg[2 * i + 1] = dot2(F[r][i], y, fg[2 * i + 1]);
        }
      }
      if (lane < 60) {
        float* fp = lds + L_FFP + (w * 5 + fr5) * 96 + fm * 48 + 8 * fck;
        *reinterpret_cast<f32x4*>(fp) = f32x4{fg[0], fg[1], fg[2], fg[3]};
        *reinterpret_cast<f32x4*>(fp + 4) = f32x4{fg[4], fg[5], fg[6], fg[7]};
      }
    }
    asm volatile("s_waitcnt lgkmcnt(0)" ::: "memory");
    if (lane == 0) __hip_atomic_fetch_add(flg, 1u, __ATOMIC_RELAXED, __HIP_MEMORY_SCOPE_WORKGROUP);
    SCHED_FENCE();
  } else {
#pragma unroll
    for (int m = 0; m < KE; ++m) K[m / 3][m % 3] = ntld(kc + kvbase + KVOFF(m / 3) + 64 * (m % 3));
    SCHED_FENCE();
    epoch = __builtin_amdgcn_raw_buffer_load_b32(rws, 0, GEP * 16, 0);
    SCHED_FENCE();
    __syncthreads();
    SCHED_FENCE();
  }
  if (w == 0) {
    for (unsigned spin = 0; spin < (SPIN_MAX << 4); ++spin) {
      if (__hip_atomic_load(flg, __ATOMIC_RELAXED, __HIP_MEMORY_SCOPE_WORKGROUP) == 4u) break;
      __builtin_amdgcn_s_sleep(1);
    }
    asm volatile("s_waitcnt lgkmcnt(0)" ::: "memory");
    tag = __builtin_amdgcn_readfirstlane(epoch) + 1u;
    {
      const f32x2* pp = reinterpret_cast<const f32x2*>(lds + L_PART);
      const f32x2 qa = (pp[lane] + pp[96 + lane]) + (pp[192 + lane] + pp[288 + lane]);
      gst2(G1 + rec * 144 + lane, qa[0], qa[1]);
      if (lane < 32) {
        const f32x2 qb = (pp[64 + lane] + pp[160 + lane]) + (pp[256 + lane] + pp[352 + lane]);
        gst2(G1 + rec * 144 + 64 + lane, qb[0], qb[1]);
      }
      if (lane < 48) {
        const f32x2* fp = reinterpret_cast<const f32x2*>(lds + L_FFP) + lane;
        f32x2 va = fp[0], vb = fp[48];
#pragma unroll
        for (int r = 2; r < 20; r += 2) {
          va += fp[r * 48];
          vb += fp[(r + 1) * 48];
        }
        va += vb;
        gst2(G1 + rec * 144 + 96 + lane, va[0], va[1]);
      }
    }
    SCHED_FENCE();
    const int lz = lane < 60 ? lane : 59;
    const int zr = lz / 10, zc = lz - 10 * zr;
    u32x4 W2[8];
#pragma unroll
    for (int i = 0; i < 8; ++i) W2[i] = ntld(w2 + ((size_t)48 * h + zr + 6 * i) * D + 80 * sp + 8 * zc);
#pragma unroll
    for (int t = 0; t < 8; ++t)
#pragma unroll
      for (int j = 0; j < 3; ++j) K[t][j] = ntld(kc + kvbase + KVOFF(t) + 64 * j);
    SCHED_FENCE();
    const int gb = G1 + h * NSP * 144 + lane;
    const int gc = G1 + (h * NSP + (lane >> 4)) * 144 + 128 + (lane & 15);
    u32x4 va[NSP], vb[NSP], vc2[2];
    for (unsigned spin = 0;; ++spin) {
      MEMBAR();
      bool ok = true;
#pragma unroll
      for (int s = 0; s < NSP; ++s) {
        va[s] = gld2(gb, s * 144);
        vb[s] = gld2(gb, s * 144 + 64);
      }
      vc2[0] = gld2(gc, 0);
      vc2[1] = gld2(gc, 4 * 144);
#pragma unroll
      for (int s = 0; s < NSP; ++s) ok = ok && gok2(va[s]) && gok2(vb[s]);
      ok = ok && gok2(vc2[0]) && gok2(vc2[1]);
      if (__all(ok) || spin >= SPIN_MAX) break;
      __builtin_amdgcn_s_sleep(1);
    }
#define SUM8(v, e) (((bitsf(v[0][e]) + bitsf(v[1][e])) + (bitsf(v[2][e]) + bitsf(v[3][e]))) + ((bitsf(v[4][e]) + bitsf(v[5][e])) + (bitsf(v[6][e]) + bitsf(v[7][e]))))
    const float a0 = SUM8(va, 0), a1 = SUM8(va, 2), b0 = SUM8(vb, 0), b1 = SUM8(vb, 2);
    reinterpret_cast<unsigned*>(lds + L_QS)[lane] = (unsigned)bfbits(a0) | ((unsigned)bfbits(a1) << 16);
    if (lane < 32) reinterpret_cast<unsigned*>(lds + L_QS)[64 + lane] = (unsigned)bfbits(b0) | ((unsigned)bfbits(b1) << 16);
    asm volatile("s_waitcnt lgkmcnt(0)" ::: "memory");
    if (lane == 0) __hip_atomic_store(flg + 1, 1u, __ATOMIC_RELAXED, __HIP_MEMORY_SCOPE_WORKGROUP);
    SCHED_FENCE();
    float c0 = bitsf(vc2[0][0]) + bitsf(vc2[1][0]), c1 = bitsf(vc2[0][2]) + bitsf(vc2[1][2]);
    c0 = swap16_add(c0, c0);
    c1 = swap16_add(c1, c1);
    c0 = swap32_add(c0, c0);
    c1 = swap32_add(c1, c1);
    if (lane >= 32) *reinterpret_cast<f32x2*>(lds + L_GU + 2 * (lane - 32)) = f32x2{bfr(b0), bfr(b1)};
    if (lane < 16) *reinterpret_cast<f32x2*>(lds + L_GU + 64 + 2 * lane) = f32x2{bfr(c0), bfr(c1)};
    WAVE_SYNC();
    if (lane < 48) lds[L_H + lane] = bfr(bfr(silu(lds[L_GU + lane])) * lds[L_GU + 48 + lane]);
    WAVE_SYNC();
    {
      float z[8];
#pragma unroll
      for (int i = 0; i < 8; ++i) z[i] = 0.f;
#pragma unroll
      for (int i = 0; i < 8; ++i) {
        const unsigned chi = fbits(lds[L_H + zr + 6 * i]), clo = chi >> 16;
#pragma unroll
        for (int k = 0; k < 4; ++k) {
          z[2 * k] = dot2(W2[i][k], clo, z[2 * k]);
          z[2 * k + 1] = dot2(W2[i][k], chi, z[2 * k + 1]);
        }
      }
      if (lane < 60) {
        float* zp = lds + L_ZP + zr * 80 + 8 * zc;
        *reinterpret_cast<f32x4*>(zp) = f32x4{z[0], z[1], z[2], z[3]};
        *reinterpret_cast<f32x4*>(zp + 4) = f32x4{z[4], z[5], z[6], z[7]};
      }
    }
    SCHED_FENCE();
  } else if (w < 4) {
#pragma unroll
    for (int m = 0; m < 24; ++m) WLD(K[m / 3][m % 3], kc + kvbase + KVOFF(m / 3) + 64 * (m % 3), m);
    tag = __builtin_amdgcn_readfirstlane(epoch) + 1u;
    SCHED_FENCE();
  } else {
#pragma unroll
    for (int m = KE; m < 24; ++m) WLD(K[m / 3][m % 3], kc + kvbase + KVOFF(m / 3) + 64 * (m % 3), m);
    tag = __builtin_amdgcn_readfirstlane(epoch) + 1u;
    SCHED_FENCE();
  }
  const int u = (tid & 255) < 240 ? (tid & 255) : 239;
  const int rg = u / 10, ch = u - 10 * rg;
  u32x4 WO[8];
  const auto rwo = __builtin_amdgcn_make_buffer_rsrc((void*)(wo + (size_t)DH * h * D + 80 * sp), (short)0, DH * D * 2, 0x00020000);
  const int wofs = (rg * D + 8 * ch) * 2;
#define WOLD(i)                                                                      \
  do {                                                                               \
    WO[i] = __builtin_amdgcn_raw_buffer_load_b128(rwo, wofs, (i) * 24 * D * 2, 2);   \
    SCHED_FENCE();                                                                   \
  } while (0)
#define VLD(t)                                                                       \
  do {                                                                               \
    _Pragma("unroll") for (int j = 0; j < 3; ++j) WLD(V[t][j], vc + kvbase + KVOFF(t) + 64 * j, 24 + 3 * (t) + j); \
  } while (0)
#pragma unroll
  for (int t = 0; t < PQ; ++t) VLD(t);
  for (unsigned spin = 0; spin < (SPIN_MAX << 4); ++spin) {
    if (__hip_atomic_load(flg + 1, __ATOMIC_RELAXED, __HIP_MEMORY_SCOPE_WORKGROUP) != 0u) break;
    __builtin_amdgcn_s_sleep(1);
  }
  asm volatile("s_waitcnt lgkmcnt(0)" ::: "memory");
  SCHED_FENCE();

  u32x4 qf[3];
#pragma unroll
  for (int j = 0; j < 3; ++j) qf[j] = *reinterpret_cast<const u32x4*>(lds + L_QS + (c + 8 * j) * 4);
  float sc[8];
#pragma unroll
  for (int t = 0; t < 8; ++t) {
    float d = 0.f;
#pragma unroll
    for (int j = 0; j < 3; ++j)
#pragma unroll
      for (int i = 0; i < 4; ++i) d = dot2(K[t][j][i], qf[j][i], d);
    d = grp8_sum(d);
    sc[t] = bfr(bfr(d) * SCALE);
    if (t == 3) {
      SCHED_FENCE();
      VLD(PQ);
    }
  }
  float mw = sc[0];
#pragma unroll
  for (int t = 1; t < 8; ++t) mw = fmaxf(mw, sc[t]);
  mw = xg_max(mw);
  if (lane == 0) {
    lds[L_ML + w] = mw;
    __hip_atomic_fetch_add(flg + 3, 1u, __ATOMIC_RELAXED, __HIP_MEMORY_SCOPE_WORKGROUP);
  }
  float lw = 0.f;
#pragma unroll
  for (int t = 0; t < 8; ++t) {
    sc[t] = ex2((sc[t] - mw) * LOG2E);
    lw += sc[t];
  }
  lw = xg_sum(lw);
  SCHED_FENCE();
  float Mg;
  {
    constexpr int LEAD = 3;
#pragma unroll
    for (int t = PQ + 1; t < LEAD; ++t) VLD(t);
    float acc[24], r3[3];
#pragma unroll
    for (int i = 0; i < 24; ++i) acc[i] = 0.f;
    constexpr int L2 = (PQ + 1 > LEAD) ? PQ + 1 : LEAD;
#define PVT(t)                                                                                                          \
  do {                                                                                                                  \
    const unsigned plo = bfbits(sc[t]), phi = plo << 16;                                                                \
    _Pragma("unroll") for (int j = 0; j < 3; ++j) _Pragma("unroll") for (int i = 0; i < 4; ++i) {                       \
      acc[8 * j + 2 * i] = dot2(V[t][j][i], plo, acc[8 * j + 2 * i]);                                                   \
      acc[8 * j + 2 * i + 1] = dot2(V[t][j][i], phi, acc[8 * j + 2 * i + 1]);                                           \
    }                                                                                                                   \
    _Pragma("unroll") for (int i = 0; i < 24; i += 8)                                                                   \
      asm volatile("" : "+v"(acc[i]), "+v"(acc[i + 1]), "+v"(acc[i + 2]), "+v"(acc[i + 3]), "+v"(acc[i + 4]),           \
                   "+v"(acc[i + 5]), "+v"(acc[i + 6]), "+v"(acc[i + 7]));            \
    SCHED_FENCE();                                                                                                      \
  } while (0)
#define WGMAX()                                                                                                         \
  do {                                                                                                                  \
    for (unsigned spin = 0; spin < (SPIN_MAX << 4); ++spin) {                                                           \
      if (__hip_atomic_load(flg + 3, __ATOMIC_RELAXED, __HIP_MEMORY_SCOPE_WORKGROUP) == 8u) break;                      \
      __builtin_amdgcn_s_sleep(1);                                                                                      \
    }                                                                                                                   \
    MEMBAR();                                                                                                           \
    const f32x4 ma = *reinterpret_cast<const f32x4*>(lds + L_ML), mb2 = *reinterpret_cast<const f32x4*>(lds + L_ML + 4); \
    Mg = fmaxf(fmaxf(fmaxf(ma[0], ma[1]), fmaxf(ma[2], ma[3])), fmaxf(fmaxf(mb2[0], mb2[1]), fmaxf(mb2[2], mb2[3])));   \
    ew = ex2((mw - Mg) * LOG2E);                                                                                        \
    SCHED_FENCE();                                                                                                      \
  } while (0)
    float ew;
#pragma unroll
    for (int t = 0; t + L2 < 8; ++t) {
      VLD(t + L2);
      PVT(t);
    }
    if (w >= 4) {
#pragma unroll
      for (int t = 8 - L2; t < 8; ++t) {
#pragma unroll
        for (int i = 0; i < WOB; ++i)
          if ((t == 7) ? (i >= t + L2 - 8) : (i == t + L2 - 8)) WOLD(i);
        if (t == 6) WGMAX();
        PVT(t);
      }
    } else {
#pragma unroll
      for (int t = 8 - L2; t < 8; ++t) {
        if (t == 6) WGMAX();
        PVT(t);
      }
    }
    SCHED_FENCE();
    xg_scatter(acc, r3, g & 1);
#pragma unroll
    for (int j = 0; j < 3; ++j) lds[L_PART + w * PS + 64 * j + 8 * c + g] = r3[j] * ew;
    if (lane == 0) lds[L_PART + w * PS + DH] = lw * ew;
  }
  __syncthreads();
  float gate0 = 0.f, gate1 = 0.f;
  unsigned xv = 0u;
  const int col = 80 * sp + 4 * h + (lane >> 5) * 2;
  const bool fin = (w == 2) && (h < 20);
  const int fu = (40 * sp + 2 * h) * 32 + lane;
  if (__builtin_expect(w < 2, 1)) {
    if (tid < 97) {
      f32x2 p2[8];
#pragma unroll
      for (int ww = 0; ww < 8; ++ww) p2[ww] = *reinterpret_cast<const f32x2*>(lds + L_PART + ww * PS + 2 * tid);
      const f32x2 us = ((p2[0] + p2[1]) + (p2[2] + p2[3])) + ((p2[4] + p2[5]) + (p2[6] + p2[7]));
      gst2(G2 + rec * 104 + tid, tid < 96 ? us[0] : Mg, tid < 96 ? us[1] : us[0]);
    }
    const int t2 = tid < 96 ? tid : 95;
    const int pb = G2 + h * NSP * 104 + t2, pm = G2 + (h * NSP + c) * 104 + 96;
    u32x4 va[NSP], vml;
    if (PD1) __builtin_amdgcn_s_sleep(PD1);
    for (unsigned spin = 0;; ++spin) {
      MEMBAR();
#pragma unroll
      for (int s = 0; s < NSP; ++s) va[s] = gld2(pb, s * 104);
      vml = gld2(pm, 0);
      bool ok = gok2(vml);
#pragma unroll
      for (int s = 0; s < NSP; ++s) ok = ok && gok2(va[s]);
      if (__all(ok) || spin >= SPIN_MAX) break;
      __builtin_amdgcn_s_sleep(1);
    }
    const float ms = bitsf(vml[0]);
    float M2 = fmaxf(ms, dpp<0xB1>(ms));
    M2 = fmaxf(M2, dpp<0x4E>(M2));
    M2 = fmaxf(M2, dpp<0x141>(M2));
    const float es = ex2((ms - M2) * LOG2E);
    const float L2 = grp8_sum(es * bitsf(vml[2]));
    float o0 = 0.f, o1 = 0.f;
#pragma unroll
    for (int s = 0; s < NSP; ++s) {
      const float e = rdlane(es, s);
      o0 = fmaf(e, bitsf(va[s][0]), o0);
      o1 = fmaf(e, bitsf(va[s][2]), o1);
    }
    if (tid < 96) {
      unsigned short* po = reinterpret_cast<unsigned short*>(lds + L_O) + ((2 * tid & 31) * 4 + (tid >> 5)) * 2 + (tid >> 4 & 1);
      const float rl = __builtin_amdgcn_rcpf(L2);
      po[0] = bfbits(o0 * rl);
      po[8] = bfbits(o1 * rl);
    }
    __syncthreads();
  } else if (w == 2) {
    if (lane < 40) {
      const f32x2* zp = reinterpret_cast<const f32x2*>(lds + L_ZP) + lane;
      const f32x2 zs = ((zp[0] + zp[40]) + (zp[80] + zp[120])) + (zp[160] + zp[200]);
      gst2(G3Z + h * 320 + 40 * sp + lane, zs[0], zs[1]);
    }
    SCHED_FENCE();
    __syncthreads();
    if (fin) {
      xv = *reinterpret_cast<const unsigned*>(x + col);
      u32x4 v;
      for (unsigned spin = 0;; ++spin) {
        MEMBAR();
        v = gld2(G3Z + (lane & 31) * 320 + 40 * sp + 2 * h + (lane >> 5), 0);
        if (__all(gok2(v)) || spin >= SPIN_MAX) break;
        __builtin_amdgcn_s_sleep(1);
      }
      float s0 = grp8_sum(bitsf(v[0])), s1 = grp8_sum(bitsf(v[2]));
      s0 += dpp<0x128>(s0);
      s1 += dpp<0x128>(s1);
      s0 = swap16_add(s0, s0);
      s1 = swap16_add(s1, s1);
      gate0 = bfr(silu(bfr(s0)));
      gate1 = bfr(silu(bfr(s1)));
      asm volatile("" : "+v"(xv));
    }
  } else {
    unsigned wp[24];
    if (w >= 4) {
#pragma unroll
      for (int i = WOB; i < 8; ++i) WOLD(i);
      if (tid < 256 + 240) {
#pragma unroll
        for (int i = 0; i < 8; ++i) *reinterpret_cast<u32x4*>(lds + L_WO + u * 4 + i * 960) = WO[i];
      }
      asm volatile("s_waitcnt lgkmcnt(0)" ::: "memory");
      if (lane == 0) __hip_atomic_fetch_add(flg + 2, 1u, __ATOMIC_RELAXED, __HIP_MEMORY_SCOPE_WORKGROUP);
    }
    for (unsigned spin = 0; spin < (SPIN_MAX << 4); ++spin) {
      if (__hip_atomic_load(flg + 2, __ATOMIC_RELAXED, __HIP_MEMORY_SCOPE_WORKGROUP) == 4u) break;
      __builtin_amdgcn_s_sleep(1);
    }
    asm volatile("s_waitcnt lgkmcnt(0)" ::: "memory");
    const float* wl = lds + L_WO + (lane >> 1) * 40 + (2 * (w - 3) + (lane & 1)) * 4;
#pragma unroll
    for (int k = 0; k < 3; ++k) {
      const u32x4 A = *reinterpret_cast<const u32x4*>(wl + 64 * k * 40), B = *reinterpret_cast<const u32x4*>(wl + (64 * k + 32) * 40);
#pragma unroll
      for (int q = 0; q < 4; ++q) {
        wp[8 * k + 2 * q] = __builtin_amdgcn_perm(B[q], A[q], 0x05040100u);
        wp[8 * k + 2 * q + 1] = __builtin_amdgcn_perm(B[q], A[q], 0x07060302u);
      }
    }
#pragma unroll
    for (int i = 0; i < 24; i += 8)
      asm volatile("" : "+v"(wp[i]), "+v"(wp[i + 1]), "+v"(wp[i + 2]), "+v"(wp[i + 3]), "+v"(wp[i + 4]), "+v"(wp[i + 5]), "+v"(wp[i + 6]), "+v"(wp[i + 7]));
    __syncthreads();
    const u32x4 op = *reinterpret_cast<const u32x4*>(lds + L_O + (lane >> 1) * 4);
    float y[8];
#pragma unroll
    for (int e = 0; e < 8; ++e) y[e] = dot2(wp[16 + e], op[2], dot2(wp[8 + e], op[1], dot2(wp[e], op[0], 0.f)));
    float p[4];
#pragma unroll
    for (int e = 0; e < 4; ++e) p[e] = swap32_add(y[e], y[e + 4]);
    float q0 = swap16_add(p[0], p[2]), q1 = swap16_add(p[1], p[3]);
    q0 += dpp<0x128>(q0);
    q1 += dpp<0x128>(q1);
    q0 += dpp<0x124>(q0);
    q1 += dpp<0x124>(q1);
    q0 += dpp<0x4E>(q0);
    q1 += dpp<0x4E>(q1);
    if ((lane & 14) == 0) gst2(G3Y + (40 * sp + 4 * (2 * (w - 3) + (lane & 1)) + (lane >> 4)) * 32 + h, q0, q1);
  }
  if (fin) {
    u32x4 v;
    for (unsigned spin = 0;; ++spin) {
      MEMBAR();
      v = gld2(G3Y + fu, 0);
      if (__all(gok2(v)) || spin >= SPIN_MAX) break;
      __builtin_amdgcn_s_sleep(1);
    }
    float s0 = grp8_sum(bitsf(v[0])), s1 = grp8_sum(bitsf(v[2]));
    s0 += dpp<0x128>(s0);
    s1 += dpp<0x128>(s1);
    s0 = swap16_add(s0, s0);
    s1 = swap16_add(s1, s1);
    if ((lane & 31) == 0) {
      const unsigned r0 = bfbits(bf_lo(xv) + bfr(bfr(s0) * gate0)), r1 = bfbits(bf_hi(xv) + bfr(bfr(s1) * gate1));
      *reinterpret_cast<unsigned*>(out + col) = r0 | (r1 << 16);
      if (col == 0)
        __hip_atomic_store(reinterpret_cast<unsigned*>(gw + 2 * GEP), tag, __ATOMIC_RELAXED, __HIP_MEMORY_SCOPE_AGENT);
    }
  }
}

}

extern "C" void kernel_launch(void* const* d_in, const int* in_sizes, int n_in, void* d_out, int out_size, void* d_ws,
                              size_t ws_size, hipStream_t stream) {
  const bf16* x = (const bf16*)d_in[0];
  const bf16* kc = (const bf16*)d_in[1];
  const bf16* vc = (const bf16*)d_in[2];
  const bf16* wq = (const bf16*)d_in[3];
  const bf16* wo = (const bf16*)d_in[4];
  const bf16* w1 = (const bf16*)d_in[6];
  const bf16* w3 = (const bf16*)d_in[7];
  const bf16* w2 = (const bf16*)d_in[8];
  u64* gw = (u64*)d_ws;
  fused_decode<<<NWG, NT, 0, stream>>>(x, kc, vc, wq, wo, w1, w3, w2, gw, (bf16*)d_out);
}
```

```cpp
#include <hip/hip_runtime.h>
#include <hip/hip_bf16.h>
#include <stdint.h>

typedef __hip_bfloat16 bf16;
typedef unsigned long long u64;
typedef unsigned u32x4 __attribute__((ext_vector_type(4)));
typedef float f32x4 __attribute__((ext_vector_type(4)));
typedef __bf16 bf16x2_t __attribute__((ext_vector_type(2)));

namespace {

constexpr int D = 640, HQ = 32, DH = 192, SK = 32768, WIN = 4096, FF = 1536;
constexpr int NSP = 8, KEYS = WIN / NSP;
constexpr int NWG = HQ * NSP;
constexpr int NT = 512;
constexpr float SCALE = 0.07216878364870322f;
constexpr float LOG2E = 1.4426950408889634f;
constexpr unsigned SPIN_MAX = 1u << 16;
constexpr int WND = 8;
constexpr int WOL = 7;
constexpr int PQ = 5;
constexpr int KE = 8;
constexpr int PD1 = 14;
constexpr int WOB = 8;

constexpr int G1 = 0;
constexpr int G2 = G1 + HQ * 480;
constexpr int G3Y = G2 + NWG * 104;
constexpr int G3Z = G3Y + 320 * 32;
constexpr int GEP = G3Z + 320 * 32;
constexpr int GEND = GEP + 8 + 256;

__device__ __forceinline__ unsigned fbits(float x) { return __builtin_bit_cast(unsigned, x); }
__device__ __forceinline__ float bitsf(unsigned x) { return __builtin_bit_cast(float, x); }
#define gok2(v) ((v)[1] == tag && (v)[3] == tag)
#define gst2(unit, a, b) __builtin_amdgcn_raw_buffer_store_b128(u32x4{fbits(a), tag, fbits(b), tag}, rws, (unit) * 16, 0, 16)
#define gld2(unit, cunit) __builtin_amdgcn_raw_buffer_load_b128(rws, (unit) * 16, (cunit) * 16, 16)
#define MEMBAR() asm volatile("" ::: "memory")
typedef float f32x2 __attribute__((ext_vector_type(2)));

__device__ __forceinline__ float bf_lo(unsigned u) { return __builtin_bit_cast(float, u << 16); }
__device__ __forceinline__ float bf_hi(unsigned u) { return __builtin_bit_cast(float, u & 0xffff0000u); }
__device__ __forceinline__ float bfr(float x) { return __bfloat162float(__float2bfloat16(x)); }
__device__ __forceinline__ unsigned short bfbits(float x) {
  return __builtin_bit_cast(unsigned short, __float2bfloat16(x));
}
__device__ __forceinline__ float dot2(unsigned a, unsigned b, float c) {
  return __builtin_amdgcn_fdot2_f32_bf16(__builtin_bit_cast(bf16x2_t, a), __builtin_bit_cast(bf16x2_t, b), c, false);
}
template <int CTRL>
__device__ __forceinline__ float dpp(float x) {
  return __builtin_bit_cast(float, __builtin_amdgcn_mov_dpp(__builtin_bit_cast(int, x), CTRL, 0xf, 0xf, true));
}
__device__ __forceinline__ float grp8_sum(float x) {
  x += dpp<0xB1>(x);
  x += dpp<0x4E>(x);
  x += dpp<0x141>(x);
  return x;
}
__device__ __forceinline__ float swap16_add(float a, float b) {
  auto s = __builtin_amdgcn_permlane16_swap(fbits(a), fbits(b), false, false);
  return bitsf((unsigned)s[0]) + bitsf((unsigned)s[1]);
}
__device__ __forceinline__ float swap32_add(float a, float b) {
  auto t = __builtin_amdgcn_permlane32_swap(fbits(a), fbits(b), false, false);
  return bitsf((unsigned)t[0]) + bitsf((unsigned)t[1]);
}
__device__ __forceinline__ float xg_sum(float x) {
  x += dpp<0x128>(x);
  x = swap16_add(x, x);
  return swap32_add(x, x);
}
__device__ __forceinline__ float xg_max(float x) {
  x = fmaxf(x, dpp<0x128>(x));
  auto s = __builtin_amdgcn_permlane16_swap(fbits(x), fbits(x), false, false);
  x = fmaxf(bitsf((unsigned)s[0]), bitsf((unsigned)s[1]));
  auto t = __builtin_amdgcn_permlane32_swap(fbits(x), fbits(x), false, false);
  return fmaxf(bitsf((unsigned)t[0]), bitsf((unsigned)t[1]));
}
template <int NJ>
__device__ __forceinline__ void xg_scatter(const float (&a)[8 * NJ], float (&r)[NJ], const bool b3) {
#pragma unroll
  for (int j = 0; j < NJ; ++j) {
    float p[4];
#pragma unroll
    for (int e = 0; e < 4; ++e) p[e] = swap32_add(a[8 * j + e], a[8 * j + e + 4]);
    const float q0 = swap16_add(p[0], p[2]), q1 = swap16_add(p[1], p[3]);
    r[j] = (b3 ? q1 : q0) + dpp<0x128>(b3 ? q0 : q1);
  }
}
__device__ __forceinline__ float wave_sum(float x) { return xg_sum(grp8_sum(x)); }
__device__ __forceinline__ float ex2(float x) { return __builtin_amdgcn_exp2f(x); }
__device__ __forceinline__ float silu(float x) { return x / (1.0f + __expf(-x)); }
__device__ __forceinline__ float rdlane(float x, int l) {
  return __builtin_bit_cast(float, __builtin_amdgcn_readlane(__builtin_bit_cast(int, x), l));
}

#define SCHED_FENCE() __builtin_amdgcn_sched_barrier(0)
#define WLD(dst, ld)                                                          \
  do {                                                                        \
    asm volatile("s_waitcnt vmcnt(%0)" ::"n"(WND - 1) : "memory");            \
    if (w < 4) asm volatile("s_waitcnt vmcnt(%0)" ::"n"(WOL - 1) : "memory"); \
    dst = ld;                                                                 \
    SCHED_FENCE();                                                            \
  } while (0)
#define WAVE_SYNC()                                        \
  do {                                                     \
    __builtin_amdgcn_fence(__ATOMIC_RELEASE, "wavefront"); \
    __builtin_amdgcn_wave_barrier();                       \
    __builtin_amdgcn_fence(__ATOMIC_ACQUIRE, "wavefront"); \
  } while (0)

constexpr int L_WO = 0;
constexpr int L_XS = L_WO + DH * 40;
constexpr int PS = DH + 4;
constexpr int L_PART = L_XS + 4 * 320;
constexpr int L_XN = L_PART + 8 * PS;
constexpr int L_ZP = L_XN + 4 * 64;
constexpr int L_QS = L_ZP + 6 * 80;
constexpr int L_ML = L_QS + 96;
constexpr int L_O = L_ML + 16;
constexpr int L_H = L_O + 128;
constexpr int L_GU = L_H + 48;
constexpr int L_FLG = L_GU + 96;
constexpr int L_END = L_FLG + 4;

__global__ void __launch_bounds__(NT, 2)
fused_decode(const bf16* __restrict__ x, const bf16* __restrict__ kc, const bf16* __restrict__ vc,
             const bf16* __restrict__ wq, const bf16* __restrict__ wo, const bf16* __restrict__ w1,
             const bf16* __restrict__ w3, const bf16* __restrict__ w2, u64* __restrict__ gw, bf16* __restrict__ out) {
  __shared__ __attribute__((aligned(16))) float lds[L_END];
  const int b = blockIdx.x;
  const int xcd = b & 7, jj = b >> 3;
  const int h = xcd * 4 + (jj >> 3), sp = jj & 7;
  const int w = __builtin_amdgcn_readfirstlane(threadIdx.x >> 6);
  const int tid = threadIdx.x, lane = tid & 63;
  const int g = lane >> 3, c = lane & 7;
  const int rec = h * NSP + sp;
  unsigned* flg = reinterpret_cast<unsigned*>(lds + L_FLG);

  asm volatile("" ::"s"(x), "s"(kc), "s"(vc), "s"(wq), "s"(wo), "s"(w1), "s"(w3), "s"(w2), "s"(gw), "s"(out));
  const auto rws = __builtin_amdgcn_make_buffer_rsrc((void*)gw, (short)0, GEND * 16, 0x00020000);
  const size_t kvw = ((size_t)h * SK + (SK - WIN) + (size_t)sp * KEYS + 64 * ((w + (h >> 3)) & 7)) * DH;
  const auto rk = __builtin_amdgcn_make_buffer_rsrc((void*)(kc + kvw), (short)0, 64 * DH * 2, 0x00020000);
  const auto rv = __builtin_amdgcn_make_buffer_rsrc((void*)(vc + kvw), (short)0, 64 * DH * 2, 0x00020000);
  const int kvo = (g * DH + 8 * c) * 2;
#define KVOFF(t) ((((t) + h) & 7) * 8 * DH * 2)
#define KLD(t, j) __builtin_amdgcn_raw_buffer_load_b128(rk, kvo + 128 * (j), KVOFF(t), 2)
#define VLD1(t, j) __builtin_amdgcn_raw_buffer_load_b128(rv, kvo + 128 * (j), KVOFF(t), 2)
  u32x4 K[8][3], V[8][3];

  unsigned epoch, tag;
  if (w < 4) {
    const int fw = 4 * sp + w, ct = fw >= 20 ? 2 : fw >= 10 ? 1 : 0;
    const int r0 = 64 * (fw - 10 * ct);
    const int R = 160 * (sp & 3) + 40 * w;
    const u32x4 xa = *reinterpret_cast<const u32x4*>(x + 8 * lane);
    const u32x4 xb = *reinterpret_cast<const u32x4*>(x + 512 + 8 * (lane & 15));
    u32x4 WQ[8], F[5];
    {
      const auto rq = __builtin_amdgcn_make_buffer_rsrc((void*)wq, (short)0, D * HQ * DH * 2, 0x00020000);
      const int qo = ((r0 + 8 * g) * (HQ * DH) + h * DH + 64 * ct + 8 * c) * 2;
#pragma unroll
      for (int i = 0; i < 8; ++i) WQ[i] = __builtin_amdgcn_raw_buffer_load_b128(rq, qo, i * HQ * DH * 2, 2);
      const auto rf = __builtin_amdgcn_make_buffer_rsrc((void*)(sp >= 4 ? w3 : w1), (short)0, D * FF * 2, 0x00020000);
      const int fo = c < 6 ? ((R + 5 * g) * FF + 48 * h + 8 * c) * 2 : 0x40000000;
#pragma unroll
      for (int i = 0; i < 5; ++i) F[i] = __builtin_amdgcn_raw_buffer_load_b128(rf, fo, i * FF * 2, 2);
    }
    SCHED_FENCE();
    epoch = __builtin_amdgcn_raw_buffer_load_b32(rws, 0, GEP * 16, 0);
    if (tid < 4) flg[tid] = 0u;
    SCHED_FENCE();
    __syncthreads();
    SCHED_FENCE();
    unsigned short* xs = reinterpret_cast<unsigned short*>(lds + L_XS + w * 320);
    const bool lo16 = lane < 16;
    *reinterpret_cast<u32x4*>(xs + 8 * lane) = xa;
    if (lo16) *reinterpret_cast<u32x4*>(xs + 512 + 8 * lane) = xb;
    float xf[8], xg[8];
#pragma unroll
    for (int i = 0; i < 4; ++i) {
      xf[2 * i] = bf_lo(xa[i]);
      xf[2 * i + 1] = bf_hi(xa[i]);
      xg[2 * i] = bf_lo(xb[i]);
      xg[2 * i + 1] = bf_hi(xb[i]);
    }
    float sum = 0.f;
#pragma unroll
    for (int i = 0; i < 8; ++i) sum += xf[i] + (lo16 ? xg[i] : 0.f);
    sum = wave_sum(sum);
    const float mean = sum * (1.0f / D);
    float sq = 0.f;
#pragma unroll
    for (int i = 0; i < 8; ++i) {
      const float d1 = xf[i] - mean, d2 = xg[i] - mean;
      sq += d1 * d1 + (lo16 ? d2 * d2 : 0.f);
    }
    sq = wave_sum(sq);
    const float mb = bfr(mean);
    const float vb = bfr(sq * (1.0f / D));
    const float den = bfr(sqrtf(bfr(vb + 1e-5f)));
    WAVE_SYNC();
    auto xnorm = [&](int r) -> unsigned short { return bfbits(bfr(bf_lo(xs[r]) - mb) / den); };
    {
      unsigned short* xn = reinterpret_cast<unsigned short*>(lds + L_XN + w * 64);
      const int l5 = lane < 40 ? lane : 39;
      xn[lane] = xnorm((r0 < D ? r0 : 0) + lane);
      xn[64 + l5 + 3 * (l5 / 5)] = xnorm(R + l5);
      WAVE_SYNC();
      const u32x4 XQ = *reinterpret_cast<const u32x4*>(xn + 8 * g), XF = *reinterpret_cast<const u32x4*>(xn + 64 + 8 * g);
      float acc[16], r2[2];
#pragma unroll
      for (int i = 0; i < 16; ++i) acc[i] = 0.f;
#pragma unroll
      for (int i = 0; i < 8; ++i) {
        const unsigned d = XQ[i >> 1], lo = (i & 1) ? d >> 16 : d & 0xffffu, hi = (i & 1) ? d & 0xffff0000u : d << 16;
#pragma unroll
        for (int k = 0; k < 4; ++k) {
          acc[2 * k] = dot2(WQ[i][k], lo, acc[2 * k]);
          acc[2 * k + 1] = dot2(WQ[i][k], hi, acc[2 * k + 1]);
        }
      }
#pragma unroll
      for (int i = 0; i < 5; ++i) {
        const unsigned d = XF[i >> 1], lo = (i & 1) ? d >> 16 : d & 0xffffu, hi = (i & 1) ? d & 0xffff0000u : d << 16;
#pragma unroll
        for (int k = 0; k < 4; ++k) {
          acc[8 + 2 * k] = dot2(F[i][k], lo, acc[8 + 2 * k]);
          acc[8 + 2 * k + 1] = dot2(F[i][k], hi, acc[8 + 2 * k + 1]);
        }
      }
      xg_scatter<2>(acc, r2, g & 1);
#pragma unroll
      for (int j = 0; j < 2; ++j) lds[L_PART + w * 128 + 64 * j + 8 * c + g] = r2[j];
    }
    asm volatile("s_waitcnt lgkmcnt(0)" ::: "memory");
    if (lane == 0) __hip_atomic_fetch_add(flg, 1u, __ATOMIC_RELAXED, __HIP_MEMORY_SCOPE_WORKGROUP);
    SCHED_FENCE();
  } else {
#pragma unroll
    for (int m = 0; m < KE; ++m) K[m / 3][m % 3] = KLD(m / 3, m % 3);
    SCHED_FENCE();
    epoch = __builtin_amdgcn_raw_buffer_load_b32(rws, 0, GEP * 16, 0);
    SCHED_FENCE();
    __syncthreads();
    SCHED_FENCE();
  }
  const int lz = lane < 60 ? lane : 59;
  const int zr = lz / 10, zc = lz - 10 * zr;
  int w2o = (zr * D + 8 * zc) * 2;
  asm volatile("" : "+v"(w2o));
  if (w == 0) {
    for (unsigned spin = 0; spin < (SPIN_MAX << 4); ++spin) {
      if (__hip_atomic_load(flg, __ATOMIC_RELAXED, __HIP_MEMORY_SCOPE_WORKGROUP) == 4u) break;
      __builtin_amdgcn_s_sleep(1);
    }
    asm volatile("s_waitcnt lgkmcnt(0)" ::: "memory");
    tag = __builtin_amdgcn_readfirstlane(epoch) + 1u;
    const f32x2* pp = reinterpret_cast<const f32x2*>(lds + L_PART) + lane;
    const f32x2 a01 = pp[0] + pp[64], a23 = pp[128] + pp[192];
    const bool two = sp == 2 && lane < 32;
    const f32x2 sm = two ? a01 : a01 + a23;
    const int ct0 = sp >= 5 ? 2 : sp >= 3 ? 1 : 0, slot = sp - (sp >= 5 ? 5 : sp >= 3 ? 2 : 0);
    const int un = lane < 32 ? slot * 96 + ct0 * 32 + lane : 288 + (sp & 3) * 48 + (sp >> 2) * 24 + lane - 32;
    if (lane < 56) gst2(G1 + h * 480 + un, sm[0], sm[1]);
    if (two) gst2(G1 + h * 480 + 32 + lane, a23[0], a23[1]);
    SCHED_FENCE();
  }
  int wop = w;
  asm volatile("" : "+s"(wop));
  if (wop != 0) {
    if (w < 4) {
#pragma unroll
      for (int m = 0; m < 24; ++m) WLD(K[m / 3][m % 3], KLD(m / 3, m % 3));
      tag = __builtin_amdgcn_readfirstlane(epoch) + 1u;
      SCHED_FENCE();
    } else {
#pragma unroll
      for (int m = KE; m < 24; ++m) WLD(K[m / 3][m % 3], KLD(m / 3, m % 3));
      tag = __builtin_amdgcn_readfirstlane(epoch) + 1u;
      SCHED_FENCE();
    }
  } else {
    u32x4 W2[8];
    const auto rw2 = __builtin_amdgcn_make_buffer_rsrc((void*)(w2 + (size_t)48 * h * D + 80 * sp), (short)0, 48 * D * 2, 0x00020000);
#pragma unroll
    for (int i = 0; i < 8; ++i) W2[i] = __builtin_amdgcn_raw_buffer_load_b128(rw2, w2o, i * 6 * D * 2, 2);
#pragma unroll
    for (int t = 0; t < 8; ++t)
#pragma unroll
      for (int j = 0; j < 3; ++j) K[t][j] = KLD(t, j);
    SCHED_FENCE();
    const int row = lane >> 4, l16 = lane & 15;
    const int gb = G1 + h * 480 + lane;
    const int gc = G1 + h * 480 + (row < 3 ? row : 2) * 96 + 64 + l16;
    const int gf = G1 + h * 480 + 288 + row * 48 + l16;
    u32x4 va[3], vb[2], vf[3];
    for (unsigned spin = 0;; ++spin) {
      MEMBAR();
#pragma unroll
      for (int s = 0; s < 3; ++s) va[s] = gld2(gb, s * 96);
#pragma unroll
      for (int s = 0; s < 2; ++s) vb[s] = gld2(gc, s * 16);
#pragma unroll
      for (int s = 0; s < 3; ++s) vf[s] = gld2(gf, s * 16);
      bool ok = gok2(vb[0]) && gok2(vb[1]);
#pragma unroll
      for (int s = 0; s < 3; ++s) ok = ok && gok2(va[s]) && gok2(vf[s]);
      if (__all(ok) || spin >= SPIN_MAX) break;
      __builtin_amdgcn_s_sleep(1);
    }
    {
      const float a0 = (bitsf(va[0][0]) + bitsf(va[1][0])) + bitsf(va[2][0]), a1 = (bitsf(va[0][2]) + bitsf(va[1][2])) + bitsf(va[2][2]);
      reinterpret_cast<unsigned*>(lds + L_QS)[lane] = (unsigned)bfbits(a0) | ((unsigned)bfbits(a1) << 16);
      const float p0 = swap16_add(row < 3 ? bitsf(vb[0][0]) : 0.f, row < 3 ? bitsf(vb[0][2]) : 0.f);
      const float p1 = swap16_add(row < 3 ? bitsf(vb[1][0]) : 0.f, row < 3 ? bitsf(vb[1][2]) : 0.f);
      const float bq = swap32_add(p0, p1);
      reinterpret_cast<unsigned short*>(lds + L_QS)[2 * (64 + 16 * (row >> 1) + l16) + (row & 1)] = bfbits(bq);
    }
    asm volatile("s_waitcnt lgkmcnt(0)" ::: "memory");
    if (lane == 0) __hip_atomic_store(flg + 1, 1u, __ATOMIC_RELAXED, __HIP_MEMORY_SCOPE_WORKGROUP);
    SCHED_FENCE();
    {
      const float p0 = swap16_add(bitsf(vf[0][0]), bitsf(vf[0][2])), p1 = swap16_add(bitsf(vf[1][0]), bitsf(vf[1][2]));
      const float p2 = swap16_add(bitsf(vf[2][0]), bitsf(vf[2][2]));
      const float f01 = swap32_add(p0, p1), f2 = swap32_add(p2, p2);
      lds[L_GU + 2 * (16 * (row >> 1) + l16) + (row & 1)] = bfr(f01);
      if (lane < 32) lds[L_GU + 64 + 2 * l16 + row] = bfr(f2);
    }
    WAVE_SYNC();
    if (lane < 48) lds[L_H + lane] = bfr(bfr(silu(lds[L_GU + lane])) * lds[L_GU + 48 + lane]);
    WAVE_SYNC();
    {
      float z[8];
#pragma unroll
      for (int i = 0; i < 8; ++i) z[i] = 0.f;
#pragma unroll
      for (int i = 0; i < 8; ++i) {
        const unsigned chi = fbits(lds[L_H + zr + 6 * i]), clo = chi >> 16;
#pragma unroll
        for (int k = 0; k < 4; ++k) {
          z[2 * k] = dot2(W2[i][k], clo, z[2 * k]);
          z[2 * k + 1] = dot2(W2[i][k], chi, z[2 * k + 1]);
        }
      }
      if (lane < 60) {
        float* zp = lds + L_ZP + zr * 80 + 8 * zc;
        *reinterpret_cast<f32x4*>(zp) = f32x4{z[0], z[1], z[2], z[3]};
        *reinterpret_cast<f32x4*>(zp + 4) = f32x4{z[4], z[5], z[6], z[7]};
      }
    }
    SCHED_FENCE();
  }
  const int u = (tid & 255) < 240 ? (tid & 255) : 239;
  const int rg = u / 10, ch = u - 10 * rg;
  u32x4 WO[8];
  const auto rwo = __builtin_amdgcn_make_buffer_rsrc((void*)(wo + (size_t)DH * h * D + 80 * sp), (short)0, DH * D * 2, 0x00020000);
  const int wofs = (rg * D + 8 * ch) * 2;
#define WOLD(i)                                                                      \
  do {                                                                               \
    WO[i] = __builtin_amdgcn_raw_buffer_load_b128(rwo, wofs, (i) * 24 * D * 2, 2);   \
    SCHED_FENCE();                                                                   \
  } while (0)
#define VLD(t)                                                                       \
  do {                                                                               \
    _Pragma("unroll") for (int j = 0; j < 3; ++j) WLD(V[t][j], VLD1(t, j));          \
  } while (0)
#pragma unroll
  for (int t = 0; t < PQ; ++t) VLD(t);
  for (unsigned spin = 0; spin < (SPIN_MAX << 4); ++spin) {
    if (__hip_atomic_load(flg + 1, __ATOMIC_RELAXED, __HIP_MEMORY_SCOPE_WORKGROUP) != 0u) break;
    __builtin_amdgcn_s_sleep(1);
  }
  asm volatile("s_waitcnt lgkmcnt(0)" ::: "memory");
  SCHED_FENCE();

  u32x4 qf[3];
#pragma unroll
  for (int j = 0; j < 3; ++j) qf[j] = *reinterpret_cast<const u32x4*>(lds + L_QS + (c + 8 * j) * 4);
  float sc[8];
#pragma unroll
  for (int t = 0; t < 8; ++t) {
    float d = 0.f;
#pragma unroll
    for (int j = 0; j < 3; ++j)
#pragma unroll
      for (int i = 0; i < 4; ++i) d = dot2(K[t][j][i], qf[j][i], d);
    d = grp8_sum(d);
    sc[t] = bfr(bfr(d) * SCALE);
    if (t == 3) {
      SCHED_FENCE();
      VLD(PQ);
    }
  }
  float mw = sc[0];
#pragma unroll
  for (int t = 1; t < 8; ++t) mw = fmaxf(mw, sc[t]);
  mw = xg_max(mw);
  if (lane == 0) {
    lds[L_ML + w] = mw;
    __hip_atomic_fetch_add(flg + 3, 1u, __ATOMIC_RELAXED, __HIP_MEMORY_SCOPE_WORKGROUP);
  }
  float lw = 0.f;
#pragma unroll
  for (int t = 0; t < 8; ++t) {
    sc[t] = ex2((sc[t] - mw) * LOG2E);
    lw += sc[t];
  }
  lw = xg_sum(lw);
  SCHED_FENCE();
  float Mg;
  {
    constexpr int LEAD = 3;
#pragma unroll
    for (int t = PQ + 1; t < LEAD; ++t) VLD(t);
    float acc[24], r3[3];
#pragma unroll
    for (int i = 0; i < 24; ++i) acc[i] = 0.f;
    constexpr int L2 = (PQ + 1 > LEAD) ? PQ + 1 : LEAD;
#define PVT(t)                                                                                                          \
  do {                                                                                                                  \
    const unsigned plo = bfbits(sc[t]), phi = plo << 16;                                                                \
    _Pragma("unroll") for (int j = 0; j < 3; ++j) _Pragma("unroll") for (int i = 0; i < 4; ++i) {                       \
      acc[8 * j + 2 * i] = dot2(V[t][j][i], plo, acc[8 * j + 2 * i]);                                                   \
      acc[8 * j + 2 * i + 1] = dot2(V[t][j][i], phi, acc[8 * j + 2 * i + 1]);                                           \
    }                                                                                                                   \
    _Pragma("unroll") for (int i = 0; i < 24; i += 8)                                                                   \
      asm volatile("" : "+v"(acc[i]), "+v"(acc[i + 1]), "+v"(acc[i + 2]), "+v"(acc[i + 3]), "+v"(acc[i + 4]),           \
                   "+v"(acc[i + 5]), "+v"(acc[i + 6]), "+v"(acc[i + 7]));            \
    SCHED_FENCE();                                                                                                      \
  } while (0)
#define WGMAX()                                                                                                         \
  do {                                                                                                                  \
    for (unsigned spin = 0; spin < (SPIN_MAX << 4); ++spin) {                                                           \
      if (__hip_atomic_load(flg + 3, __ATOMIC_RELAXED, __HIP_MEMORY_SCOPE_WORKGROUP) == 8u) break;                      \
      __builtin_amdgcn_s_sleep(1);                                                                                      \
    }                                                                                                                   \
    MEMBAR();                                                                                                           \
    const f32x4 ma = *reinterpret_cast<const f32x4*>(lds + L_ML), mb2 = *reinterpret_cast<const f32x4*>(lds + L_ML + 4); \
    Mg = fmaxf(fmaxf(fmaxf(ma[0], ma[1]), fmaxf(ma[2], ma[3])), fmaxf(fmaxf(mb2[0], mb2[1]), fmaxf(mb2[2], mb2[3])));   \
    ew = ex2((mw - Mg) * LOG2E);                                                                                        \
    SCHED_FENCE();                                                                                                      \
  } while (0)
    float ew;
#pragma unroll
    for (int t = 0; t + L2 < 8; ++t) {
      VLD(t + L2);
      PVT(t);
    }
    if (w >= 4) {
#pragma unroll
      for (int t = 8 - L2; t < 8; ++t) {
#pragma unroll
        for (int i = 0; i < WOB; ++i)
          if ((t == 7) ? (i >= t + L2 - 8) : (i == t + L2 - 8)) WOLD(i);
        if (t == 6) WGMAX();
        PVT(t);
      }
    } else {
#pragma unroll
      for (int t = 8 - L2; t < 8; ++t) {
        if (t == 6) WGMAX();
        PVT(t);
      }
    }
    SCHED_FENCE();
    xg_scatter<3>(acc, r3, g & 1);
#pragma unroll
    for (int j = 0; j < 3; ++j) lds[L_PART + w * PS + 64 * j + 8 * c + g] = r3[j] * ew;
    if (lane == 0) lds[L_PART + w * PS + DH] = lw * ew;
  }
  __syncthreads();
  float gate0 = 0.f, gate1 = 0.f;
  unsigned xv = 0u;
  const int col = 80 * sp + 4 * h + (lane >> 5) * 2;
  const bool fin = (w == 2) && (h < 20);
  const int fu = (40 * sp + 2 * h) * 32 + lane;
  if (__builtin_expect(w < 2, 1)) {
    if (tid < 97) {
      f32x2 p2[8];
#pragma unroll
      for (int ww = 0; ww < 8; ++ww) p2[ww] = *reinterpret_cast<const f32x2*>(lds + L_PART + ww * PS + 2 * tid);
      const f32x2 us = ((p2[0] + p2[1]) + (p2[2] + p2[3])) + ((p2[4] + p2[5]) + (p2[6] + p2[7]));
      gst2(G2 + rec * 104 + tid, tid < 96 ? us[0] : Mg, tid < 96 ? us[1] : us[0]);
    }
    const int t2 = tid < 96 ? tid : 95;
    const int pb = G2 + h * NSP * 104 + t2, pm = G2 + (h * NSP + c) * 104 + 96;
    u32x4 va[NSP], vml;
    if (PD1) __builtin_amdgcn_s_sleep(PD1);
    for (unsigned spin = 0;; ++spin) {
      MEMBAR();
#pragma unroll
      for (int s = 0; s < NSP; ++s) va[s] = gld2(pb, s * 104);
      vml = gld2(pm, 0);
      bool ok = gok2(vml);
#pragma unroll
      for (int s = 0; s < NSP; ++s) ok = ok && gok2(va[s]);
      if (__all(ok) || spin >= SPIN_MAX) break;
      __builtin_amdgcn_s_sleep(1);
    }
    const float ms = bitsf(vml[0]);
    float M2 = fmaxf(ms, dpp<0xB1>(ms));
    M2 = fmaxf(M2, dpp<0x4E>(M2));
    M2 = fmaxf(M2, dpp<0x141>(M2));
    const float es = ex2((ms - M2) * LOG2E);
    const float L2 = grp8_sum(es * bitsf(vml[2]));
    float o0 = 0.f, o1 = 0.f;
#pragma unroll
    for (int s = 0; s < NSP; ++s) {
      const float e = rdlane(es, s);
      o0 = fmaf(e, bitsf(va[s][0]), o0);
      o1 = fmaf(e, bitsf(va[s][2]), o1);
    }
    if (tid < 96) {
      unsigned short* po = reinterpret_cast<unsigned short*>(lds + L_O) + ((2 * tid & 31) * 4 + (tid >> 5)) * 2 + (tid >> 4 & 1);
      const float rl = __builtin_amdgcn_rcpf(L2);
      po[0] = bfbits(o0 * rl);
      po[8] = bfbits(o1 * rl);
    }
    __syncthreads();
  } else if (w == 2) {
    if (lane < 40) {
      const f32x2* zp = reinterpret_cast<const f32x2*>(lds + L_ZP) + lane;
      const f32x2 zs = ((zp[0] + zp[40]) + (zp[80] + zp[120])) + (zp[160] + zp[200]);
      gst2(G3Z + h * 320 + 40 * sp + lane, zs[0], zs[1]);
    }
    SCHED_FENCE();
    __syncthreads();
    if (fin) {
      xv = *reinterpret_cast<const unsigned*>(x + col);
      u32x4 v;
      for (unsigned spin = 0;; ++spin) {
        MEMBAR();
        v = gld2(G3Z + (lane & 31) * 320 + 40 * sp + 2 * h + (lane >> 5), 0);
        if (__all(gok2(v)) || spin >= SPIN_MAX) break;
        __builtin_amdgcn_s_sleep(1);
      }
      float s0 = grp8_sum(bitsf(v[0])), s1 = grp8_sum(bitsf(v[2]));
      s0 += dpp<0x128>(s0);
      s1 += dpp<0x128>(s1);
      s0 = swap16_add(s0, s0);
      s1 = swap16_add(s1, s1);
      gate0 = bfr(silu(bfr(s0)));
      gate1 = bfr(silu(bfr(s1)));
      asm volatile("" : "+v"(xv));
    }
  } else {
    unsigned wp[24];
    if (w >= 4) {
#pragma unroll
      for (int i = WOB; i < 8; ++i) WOLD(i);
      if (tid < 256 + 240) {
#pragma unroll
        for (int i = 0; i < 8; ++i) *reinterpret_cast<u32x4*>(lds + L_WO + u * 4 + i * 960) = WO[i];
      }
      asm volatile("s_waitcnt lgkmcnt(0)" ::: "memory");
      if (lane == 0) __hip_atomic_fetch_add(flg + 2, 1u, __ATOMIC_RELAXED, __HIP_MEMORY_SCOPE_WORKGROUP);
    }
    for (unsigned spin = 0; spin < (SPIN_MAX << 4); ++spin) {
      if (__hip_atomic_load(flg + 2, __ATOMIC_RELAXED, __HIP_MEMORY_SCOPE_WORKGROUP) == 4u) break;
      __builtin_amdgcn_s_sleep(1);
    }
    asm volatile("s_waitcnt lgkmcnt(0)" ::: "memory");
    const float* wl = lds + L_WO + (lane >> 1) * 40 + (2 * (w - 3) + (lane & 1)) * 4;
#pragma unroll
    for (int k = 0; k < 3; ++k) {
      const u32x4 A = *reinterpret_cast<const u32x4*>(wl + 64 * k * 40), B = *reinterpret_cast<const u32x4*>(wl + (64 * k + 32) * 40);
#pragma unroll
      for (int q = 0; q < 4; ++q) {
        wp[8 * k + 2 * q] = __builtin_amdgcn_perm(B[q], A[q], 0x05040100u);
        wp[8 * k + 2 * q + 1] = __builtin_amdgcn_perm(B[q], A[q], 0x07060302u);
      }
    }
#pragma unroll
    for (int i = 0; i < 24; i += 8)
      asm volatile("" : "+v"(wp[i]), "+v"(wp[i + 1]), "+v"(wp[i + 2]), "+v"(wp[i + 3]), "+v"(wp[i + 4]), "+v"(wp[i + 5]), "+v"(wp[i + 6]), "+v"(wp[i + 7]));
    __syncthreads();
    const u32x4 op = *reinterpret_cast<const u32x4*>(lds + L_O + (lane >> 1) * 4);
    float y[8];
#pragma unroll
    for (int e = 0; e < 8; ++e) y[e] = dot2(wp[16 + e], op[2], dot2(wp[8 + e], op[1], dot2(wp[e], op[0], 0.f)));
    float p[4];
#pragma unroll
    for (int e = 0; e < 4; ++e) p[e] = swap32_add(y[e], y[e + 4]);
    float q0 = swap16_add(p[0], p[2]), q1 = swap16_add(p[1], p[3]);
    q0 += dpp<0x128>(q0);
    q1 += dpp<0x128>(q1);
    q0 += dpp<0x124>(q0);
    q1 += dpp<0x124>(q1);
    q0 += dpp<0x4E>(q0);
    q1 += dpp<0x4E>(q1);
    if ((lane & 14) == 0) gst2(G3Y + (40 * sp + 4 * (2 * (w - 3) + (lane & 1)) + (lane >> 4)) * 32 + h, q0, q1);
  }
  if (fin) {
    u32x4 v;
    for (unsigned spin = 0;; ++spin) {
      MEMBAR();
      v = gld2(G3Y + fu, 0);
      if (__all(gok2(v)) || spin >= SPIN_MAX) break;
      __builtin_amdgcn_s_sleep(1);
    }
    float s0 = grp8_sum(bitsf(v[0])), s1 = grp8_sum(bitsf(v[2]));
    s0 += dpp<0x128>(s0);
    s1 += dpp<0x128>(s1);
    s0 = swap16_add(s0, s0);
    s1 = swap16_add(s1, s1);
    if ((lane & 31) == 0) {
      const unsigned r0 = bfbits(bf_lo(xv) + bfr(bfr(s0) * gate0)), r1 = bfbits(bf_hi(xv) + bfr(bfr(s1) * gate1));
      *reinterpret_cast<unsigned*>(out + col) = r0 | (r1 << 16);
      if (col == 0)
        __hip_atomic_store(reinterpret_cast<unsigned*>(gw + 2 * GEP), tag, __ATOMIC_RELAXED, __HIP_MEMORY_SCOPE_AGENT);
    }
  }
}

}

extern "C" void kernel_launch(void* const* d_in, const int* in_sizes, int n_in, void* d_out, int out_size, void* d_ws,
                              size_t ws_size, hipStream_t stream) {
  const bf16* x = (const bf16*)d_in[0];
  const bf16* kc = (const bf16*)d_in[1];
  const bf16* vc = (const bf16*)d_in[2];
  const bf16* wq = (const bf16*)d_in[3];
  const bf16* wo = (const bf16*)d_in[4];
  const bf16* w1 = (const bf16*)d_in[6];
  const bf16* w3 = (const bf16*)d_in[7];
  const bf16* w2 = (const bf16*)d_in[8];
  u64* gw = (u64*)d_ws;
  fused_decode<<<NWG, NT, 0, stream>>>(x, kc, vc, wq, wo, w1, w3, w2, gw, (bf16*)d_out);
}
```

```cpp
#include <hip/hip_runtime.h>
#include <hip/hip_bf16.h>
#include <stdint.h>

typedef __hip_bfloat16 bf16;
typedef unsigned long long u64;
typedef unsigned u32x4 __attribute__((ext_vector_type(4)));
typedef float f32x4 __attribute__((ext_vector_type(4)));
typedef __bf16 bf16x2_t __attribute__((ext_vector_type(2)));

namespace {

constexpr int D = 640, HQ = 32, DH = 192, SK = 32768, WIN = 4096, FF = 1536;
constexpr int NSP = 8, KEYS = WIN / NSP;
constexpr int NWG = HQ * NSP;
constexpr int NT = 512;
constexpr float SCALE = 0.07216878364870322f;
constexpr float LOG2E = 1.4426950408889634f;
constexpr unsigned SPIN_MAX = 1u << 16;
constexpr int WND = 8;
constexpr int WOL = 7;
constexpr int PQ = 5;
constexpr int KE = 8;
constexpr int PD1 = 14;
constexpr int WOB = 8;

constexpr int G1 = 0;
constexpr int G2 = G1 + HQ * 480;
constexpr int G3Y = G2 + NWG * 104;
constexpr int G3Z = G3Y + 320 * 32;
constexpr int GEP = G3Z + 320 * 32;
constexpr int GEND = GEP + 8 + 256;

__device__ __forceinline__ unsigned fbits(float x) { return __builtin_bit_cast(unsigned, x); }
__device__ __forceinline__ float bitsf(unsigned x) { return __builtin_bit_cast(float, x); }
#define gok2(v) ((v)[1] == tag && (v)[3] == tag)
#define gst2(unit, a, b) __builtin_amdgcn_raw_buffer_store_b128(u32x4{fbits(a), tag, fbits(b), tag}, rws, (unit) * 16, 0, 16)
#define gld2(unit, cunit) __builtin_amdgcn_raw_buffer_load_b128(rws, (unit) * 16, (cunit) * 16, 16)
#define MEMBAR() asm volatile("" ::: "memory")
typedef float f32x2 __attribute__((ext_vector_type(2)));

__device__ __forceinline__ float bf_lo(unsigned u) { return __builtin_bit_cast(float, u << 16); }
__device__ __forceinline__ float bf_hi(unsigned u) { return __builtin_bit_cast(float, u & 0xffff0000u); }
__device__ __forceinline__ float bfr(float x) { return __bfloat162float(__float2bfloat16(x)); }
__device__ __forceinline__ unsigned short bfbits(float x) {
  return __builtin_bit_cast(unsigned short, __float2bfloat16(x));
}
__device__ __forceinline__ float dot2(unsigned a, unsigned b, float c) {
  return __builtin_amdgcn_fdot2_f32_bf16(__builtin_bit_cast(bf16x2_t, a), __builtin_bit_cast(bf16x2_t, b), c, false);
}
template <int CTRL>
__device__ __forceinline__ float dpp(float x) {
  return __builtin_bit_cast(float, __builtin_amdgcn_mov_dpp(__builtin_bit_cast(int, x), CTRL, 0xf, 0xf, true));
}
__device__ __forceinline__ float grp8_sum(float x) {
  x += dpp<0xB1>(x);
  x += dpp<0x4E>(x);
  x += dpp<0x141>(x);
  return x;
}
__device__ __forceinline__ float swap16_add(float a, float b) {
  auto s = __builtin_amdgcn_permlane16_swap(fbits(a), fbits(b), false, false);
  return bitsf((unsigned)s[0]) + bitsf((unsigned)s[1]);
}
__device__ __forceinline__ float swap32_add(float a, float b) {
  auto t = __builtin_amdgcn_permlane32_swap(fbits(a), fbits(b), false, false);
  return bitsf((unsigned)t[0]) + bitsf((unsigned)t[1]);
}
__device__ __forceinline__ float xg_sum(float x) {
  x += dpp<0x128>(x);
  x = swap16_add(x, x);
  return swap32_add(x, x);
}
__device__ __forceinline__ float xg_max(float x) {
  x = fmaxf(x, dpp<0x128>(x));
  auto s = __builtin_amdgcn_permlane16_swap(fbits(x), fbits(x), false, false);
  x = fmaxf(bitsf((unsigned)s[0]), bitsf((unsigned)s[1]));
  auto t = __builtin_amdgcn_permlane32_swap(fbits(x), fbits(x), false, false);
  return fmaxf(bitsf((unsigned)t[0]), bitsf((unsigned)t[1]));
}
template <int NJ>
__device__ __forceinline__ void xg_scatter(const float (&a)[8 * NJ], float (&r)[NJ], const bool b3) {
#pragma unroll
  for (int j = 0; j < NJ; ++j) {
    float p[4];
#pragma unroll
    for (int e = 0; e < 4; ++e) p[e] = swap32_add(a[8 * j + e], a[8 * j + e + 4]);
    const float q0 = swap16_add(p[0], p[2]), q1 = swap16_add(p[1], p[3]);
    r[j] = (b3 ? q1 : q0) + dpp<0x128>(b3 ? q0 : q1);
  }
}
__device__ __forceinline__ float wave_sum(float x) { return xg_sum(grp8_sum(x)); }
__device__ __forceinline__ float ex2(float x) { return __builtin_amdgcn_exp2f(x); }
__device__ __forceinline__ float silu(float x) { return x / (1.0f + __expf(-x)); }
__device__ __forceinline__ float rdlane(float x, int l) {
  return __builtin_bit_cast(float, __builtin_amdgcn_readlane(__builtin_bit_cast(int, x), l));
}

#define SCHED_FENCE() __builtin_amdgcn_sched_barrier(0)
#define WLD(dst, ld)                                                          \
  do {                                                                        \
    asm volatile("s_waitcnt vmcnt(%0)" ::"n"(WND - 1) : "memory");            \
    if (w < 4) asm volatile("s_waitcnt vmcnt(%0)" ::"n"(WOL - 1) : "memory"); \
    dst = ld;                                                                 \
    SCHED_FENCE();                                                            \
  } while (0)
#define WAVE_SYNC()                                        \
  do {                                                     \
    __builtin_amdgcn_fence(__ATOMIC_RELEASE, "wavefront"); \
    __builtin_amdgcn_wave_barrier();                       \
    __builtin_amdgcn_fence(__ATOMIC_ACQUIRE, "wavefront"); \
  } while (0)

constexpr int L_WO = 0;
constexpr int L_XS = L_WO + DH * 40;
constexpr int PS = DH + 4;
constexpr int L_PART = L_XS + 4 * 320;
constexpr int L_XN = L_PART + 8 * PS;
constexpr int L_ZP = L_XN + 4 * 64;
constexpr int L_QS = L_ZP + 6 * 80;
constexpr int L_ML = L_QS + 96;
constexpr int L_O = L_ML + 16;
constexpr int L_H = L_O + 128;
constexpr int L_GU = L_H + 48;
constexpr int L_FLG = L_GU + 96;
constexpr int L_END = L_FLG + 4;

__global__ void __launch_bounds__(NT, 2)
fused_decode(const bf16* __restrict__ x, const bf16* __restrict__ kc, const bf16* __restrict__ vc,
             const bf16* __restrict__ wq, const bf16* __restrict__ wo, const bf16* __restrict__ w1,
             const bf16* __restrict__ w3, const bf16* __restrict__ w2, u64* __restrict__ gw, bf16* __restrict__ out) {
  __shared__ __attribute__((aligned(16))) float lds[L_END];
  const int b = blockIdx.x;
  const int xcd = b & 7, jj = b >> 3;
  const int h = xcd * 4 + (jj >> 3), sp = jj & 7;
  const int ha = jj, spa = xcd;
  const int w = __builtin_amdgcn_readfirstlane(threadIdx.x >> 6);
  const int tid = threadIdx.x, lane = tid & 63;
  const int g = lane >> 3, c = lane & 7;
  const int rec = ha * NSP + spa;
  unsigned* flg = reinterpret_cast<unsigned*>(lds + L_FLG);

  asm volatile("" ::"s"(x), "s"(kc), "s"(vc), "s"(wq), "s"(wo), "s"(w1), "s"(w3), "s"(w2), "s"(gw), "s"(out));
  const auto rws = __builtin_amdgcn_make_buffer_rsrc((void*)gw, (short)0, GEND * 16, 0x00020000);
  const size_t kvw = ((size_t)ha * SK + (SK - WIN) + (size_t)spa * KEYS + 64 * ((w + (ha >> 3)) & 7)) * DH;
  const auto rk = __builtin_amdgcn_make_buffer_rsrc((void*)(kc + kvw), (short)0, 64 * DH * 2, 0x00020000);
  const auto rv = __builtin_amdgcn_make_buffer_rsrc((void*)(vc + kvw), (short)0, 64 * DH * 2, 0x00020000);
  const int kvo = (g * DH + 8 * c) * 2;
#define KVOFF(t) ((((t) + ha) & 7) * 8 * DH * 2)
#define KLD(t, j) __builtin_amdgcn_raw_buffer_load_b128(rk, kvo + 128 * (j), KVOFF(t), 2)
#define VLD1(t, j) __builtin_amdgcn_raw_buffer_load_b128(rv, kvo + 128 * (j), KVOFF(t), 2)
  u32x4 K[8][3], V[8][3];

  unsigned epoch, tag;
  if (w < 4) {
    const int fw = 4 * sp + w, ct = fw >= 20 ? 2 : fw >= 10 ? 1 : 0;
    const int r0 = 64 * (fw - 10 * ct);
    const int R = 160 * (sp & 3) + 40 * w;
    const u32x4 xa = *reinterpret_cast<const u32x4*>(x + 8 * lane);
    const u32x4 xb = *reinterpret_cast<const u32x4*>(x + 512 + 8 * (lane & 15));
    u32x4 WQ[8], F[5];
    {
      const auto rq = __builtin_amdgcn_make_buffer_rsrc((void*)wq, (short)0, D * HQ * DH * 2, 0x00020000);
      const int qo = ((r0 + 8 * g) * (HQ * DH) + h * DH + 64 * ct + 8 * c) * 2;
#pragma unroll
      for (int i = 0; i < 8; ++i) WQ[i] = __builtin_amdgcn_raw_buffer_load_b128(rq, qo, i * HQ * DH * 2, 2);
      const auto rf = __builtin_amdgcn_make_buffer_rsrc((void*)(sp >= 4 ? w3 : w1), (short)0, D * FF * 2, 0x00020000);
      const int fo = c < 6 ? ((R + 5 * g) * FF + 48 * h + 8 * c) * 2 : 0x40000000;
#pragma unroll
      for (int i = 0; i < 5; ++i) F[i] = __builtin_amdgcn_raw_buffer_load_b128(rf, fo, i * FF * 2, 2);
    }
    SCHED_FENCE();
    epoch = __builtin_amdgcn_raw_buffer_load_b32(rws, 0, GEP * 16, 0);
    if (tid < 4) flg[tid] = 0u;
    SCHED_FENCE();
    __syncthreads();
    SCHED_FENCE();
    unsigned short* xs = reinterpret_cast<unsigned short*>(lds + L_XS + w * 320);
    const bool lo16 = lane < 16;
    *reinterpret_cast<u32x4*>(xs + 8 * lane) = xa;
    if (lo16) *reinterpret_cast<u32x4*>(xs + 512 + 8 * lane) = xb;
    float xf[8], xg[8];
#pragma unroll
    for (int i = 0; i < 4; ++i) {
      xf[2 * i] = bf_lo(xa[i]);
      xf[2 * i + 1] = bf_hi(xa[i]);
      xg[2 * i] = bf_lo(xb[i]);
      xg[2 * i + 1] = bf_hi(xb[i]);
    }
    float sum = 0.f;
#pragma unroll
    for (int i = 0; i < 8; ++i) sum += xf[i] + (lo16 ? xg[i] : 0.f);
    sum = wave_sum(sum);
    const float mean = sum * (1.0f / D);
    float sq = 0.f;
#pragma unroll
    for (int i = 0; i < 8; ++i) {
      const float d1 = xf[i] - mean, d2 = xg[i] - mean;
      sq += d1 * d1 + (lo16 ? d2 * d2 : 0.f);
    }
    sq = wave_sum(sq);
    const float mb = bfr(mean);
    const float vb = bfr(sq * (1.0f / D));
    const float den = bfr(sqrtf(bfr(vb + 1e-5f)));
    WAVE_SYNC();
    auto xnorm = [&](int r) -> unsigned short { return bfbits(bfr(bf_lo(xs[r]) - mb) / den); };
    {
      unsigned short* xn = reinterpret_cast<unsigned short*>(lds + L_XN + w * 64);
      const int l5 = lane < 40 ? lane : 39;
      xn[lane] = xnorm((r0 < D ? r0 : 0) + lane);
      xn[64 + l5 + 3 * (l5 / 5)] = xnorm(R + l5);
      WAVE_SYNC();
      const u32x4 XQ = *reinterpret_cast<const u32x4*>(xn + 8 * g), XF = *reinterpret_cast<const u32x4*>(xn + 64 + 8 * g);
      float acc[16], r2[2];
#pragma unroll
      for (int i = 0; i < 16; ++i) acc[i] = 0.f;
#pragma unroll
      for (int i = 0; i < 8; ++i) {
        const unsigned d = XQ[i >> 1], lo = (i & 1) ? d >> 16 : d & 0xffffu, hi = (i & 1) ? d & 0xffff0000u : d << 16;
#pragma unroll
        for (int k = 0; k < 4; ++k) {
          acc[2 * k] = dot2(WQ[i][k], lo, acc[2 * k]);
          acc[2 * k + 1] = dot2(WQ[i][k], hi, acc[2 * k + 1]);
        }
      }
#pragma unroll
      for (int i = 0; i < 5; ++i) {
        const unsigned d = XF[i >> 1], lo = (i & 1) ? d >> 16 : d & 0xffffu, hi = (i & 1) ? d & 0xffff0000u : d << 16;
#pragma unroll
        for (int k = 0; k < 4; ++k) {
          acc[8 + 2 * k] = dot2(F[i][k], lo, acc[8 + 2 * k]);
          acc[8 + 2 * k + 1] = dot2(F[i][k], hi, acc[8 + 2 * k + 1]);
        }
      }
      xg_scatter<2>(acc, r2, g & 1);
#pragma unroll
      for (int j = 0; j < 2; ++j) lds[L_PART + w * 128 + 64 * j + 8 * c + g] = r2[j];
    }
    asm volatile("s_waitcnt lgkmcnt(0)" ::: "memory");
    if (lane == 0) __hip_atomic_fetch_add(flg, 1u, __ATOMIC_RELAXED, __HIP_MEMORY_SCOPE_WORKGROUP);
    SCHED_FENCE();
  } else {
#pragma unroll
    for (int m = 0; m < KE; ++m) K[m / 3][m % 3] = KLD(m / 3, m % 3);
    SCHED_FENCE();
    epoch = __builtin_amdgcn_raw_buffer_load_b32(rws, 0, GEP * 16, 0);
    SCHED_FENCE();
    __syncthreads();
    SCHED_FENCE();
  }
  const int lz = lane < 60 ? lane : 59;
  const int zr = lz / 10, zc = lz - 10 * zr;
  int w2o = (zr * D + 8 * zc) * 2;
  asm volatile("" : "+v"(w2o));
  if (w == 0) {
    for (unsigned spin = 0; spin < (SPIN_MAX << 4); ++spin) {
      if (__hip_atomic_load(flg, __ATOMIC_RELAXED, __HIP_MEMORY_SCOPE_WORKGROUP) == 4u) break;
      __builtin_amdgcn_s_sleep(1);
    }
    asm volatile("s_waitcnt lgkmcnt(0)" ::: "memory");
    tag = __builtin_amdgcn_readfirstlane(epoch) + 1u;
    const f32x2* pp = reinterpret_cast<const f32x2*>(lds + L_PART) + lane;
    const f32x2 a01 = pp[0] + pp[64], a23 = pp[128] + pp[192];
    const bool two = sp == 2 && lane < 32;
    const f32x2 sm = two ? a01 : a01 + a23;
    const int ct0 = sp >= 5 ? 2 : sp >= 3 ? 1 : 0, slot = sp - (sp >= 5 ? 5 : sp >= 3 ? 2 : 0);
    const int un = lane < 32 ? slot * 96 + ct0 * 32 + lane : 288 + (sp & 3) * 48 + (sp >> 2) * 24 + lane - 32;
    if (lane < 56) gst2(G1 + h * 480 + un, sm[0], sm[1]);
    if (two) gst2(G1 + h * 480 + 32 + lane, a23[0], a23[1]);
    SCHED_FENCE();
  }
  int wop = w;
  asm volatile("" : "+s"(wop));
  if (wop != 0) {
    if (w < 4) {
#pragma unroll
      for (int m = 0; m < 24; ++m) WLD(K[m / 3][m % 3], KLD(m / 3, m % 3));
      tag = __builtin_amdgcn_readfirstlane(epoch) + 1u;
      SCHED_FENCE();
    } else {
#pragma unroll
      for (int m = KE; m < 24; ++m) WLD(K[m / 3][m % 3], KLD(m / 3, m % 3));
      tag = __builtin_amdgcn_readfirstlane(epoch) + 1u;
      SCHED_FENCE();
    }
  } else {
    u32x4 W2[8];
    const auto rw2 = __builtin_amdgcn_make_buffer_rsrc((void*)(w2 + (size_t)48 * h * D + 80 * sp), (short)0, 48 * D * 2, 0x00020000);
#pragma unroll
    for (int i = 0; i < 8; ++i) W2[i] = __builtin_amdgcn_raw_buffer_load_b128(rw2, w2o, i * 6 * D * 2, 2);
#pragma unroll
    for (int t = 0; t < 8; ++t)
#pragma unroll
      for (int j = 0; j < 3; ++j) K[t][j] = KLD(t, j);
    SCHED_FENCE();
    const int row = lane >> 4, l16 = lane & 15;
    const int gb = G1 + ha * 480 + lane;
    const int gc = G1 + ha * 480 + (row < 3 ? row : 2) * 96 + 64 + l16;
    const int gf = G1 + h * 480 + 288 + row * 48 + l16;
    u32x4 va[3], vb[2], vf[3];
    for (unsigned spin = 0;; ++spin) {
      MEMBAR();
#pragma unroll
      for (int s = 0; s < 3; ++s) va[s] = gld2(gb, s * 96);
#pragma unroll
      for (int s = 0; s < 2; ++s) vb[s] = gld2(gc, s * 16);
#pragma unroll
      for (int s = 0; s < 3; ++s) vf[s] = gld2(gf, s * 16);
      bool ok = gok2(vb[0]) && gok2(vb[1]);
#pragma unroll
      for (int s = 0; s < 3; ++s) ok = ok && gok2(va[s]) && gok2(vf[s]);
      if (__all(ok) || spin >= SPIN_MAX) break;
      __builtin_amdgcn_s_sleep(1);
    }
    {
      const float a0 = (bitsf(va[0][0]) + bitsf(va[1][0])) + bitsf(va[2][0]), a1 = (bitsf(va[0][2]) + bitsf(va[1][2])) + bitsf(va[2][2]);
      reinterpret_cast<unsigned*>(lds + L_QS)[lane] = (unsigned)bfbits(a0) | ((unsigned)bfbits(a1) << 16);
      const float p0 = swap16_add(row < 3 ? bitsf(vb[0][0]) : 0.f, row < 3 ? bitsf(vb[0][2]) : 0.f);
      const float p1 = swap16_add(row < 3 ? bitsf(vb[1][0]) : 0.f, row < 3 ? bitsf(vb[1][2]) : 0.f);
      const float bq = swap32_add(p0, p1);
      reinterpret_cast<unsigned short*>(lds + L_QS)[2 * (64 + 16 * (row >> 1) + l16) + (row & 1)] = bfbits(bq);
    }
    asm volatile("s_waitcnt lgkmcnt(0)" ::: "memory");
    if (lane == 0) __hip_atomic_store(flg + 1, 1u, __ATOMIC_RELAXED, __HIP_MEMORY_SCOPE_WORKGROUP);
    SCHED_FENCE();
    {
      const float p0 = swap16_add(bitsf(vf[0][0]), bitsf(vf[0][2])), p1 = swap16_add(bitsf(vf[1][0]), bitsf(vf[1][2]));
      const float p2 = swap16_add(bitsf(vf[2][0]), bitsf(vf[2][2]));
      const float f01 = swap32_add(p0, p1), f2 = swap32_add(p2, p2);
      lds[L_GU + 2 * (16 * (row >> 1) + l16) + (row & 1)] = bfr(f01);
      if (lane < 32) lds[L_GU + 64 + 2 * l16 + row] = bfr(f2);
    }
    WAVE_SYNC();
    if (lane < 48) lds[L_H + lane] = bfr(bfr(silu(lds[L_GU + lane])) * lds[L_GU + 48 + lane]);
    WAVE_SYNC();
    {
      float z[8];
#pragma unroll
      for (int i = 0; i < 8; ++i) z[i] = 0.f;
#pragma unroll
      for (int i = 0; i < 8; ++i) {
        const unsigned chi = fbits(lds[L_H + zr + 6 * i]), clo = chi >> 16;
#pragma unroll
        for (int k = 0; k < 4; ++k) {
          z[2 * k] = dot2(W2[i][k], clo, z[2 * k]);
          z[2 * k + 1] = dot2(W2[i][k], chi, z[2 * k + 1]);
        }
      }
      if (lane < 60) {
        float* zp = lds + L_ZP + zr * 80 + 8 * zc;
        *reinterpret_cast<f32x4*>(zp) = f32x4{z[0], z[1], z[2], z[3]};
        *reinterpret_cast<f32x4*>(zp + 4) = f32x4{z[4], z[5], z[6], z[7]};
      }
    }
    SCHED_FENCE();
  }
  const int u = (tid & 255) < 240 ? (tid & 255) : 239;
  const int rg = u / 10, ch = u - 10 * rg;
  u32x4 WO[8];
  const auto rwo = __builtin_amdgcn_make_buffer_rsrc((void*)(wo + (size_t)DH * h * D + 80 * sp), (short)0, DH * D * 2, 0x00020000);
  const int wofs = (rg * D + 8 * ch) * 2;
#define WOLD(i)                                                                      \
  do {                                                                               \
    WO[i] = __builtin_amdgcn_raw_buffer_load_b128(rwo, wofs, (i) * 24 * D * 2, 2);   \
    SCHED_FENCE();                                                                   \
  } while (0)
#define VLD(t)                                                                       \
  do {                                                                               \
    _Pragma("unroll") for (int j = 0; j < 3; ++j) WLD(V[t][j], VLD1(t, j));          \
  } while (0)
#pragma unroll
  for (int t = 0; t < PQ; ++t) VLD(t);
  for (unsigned spin = 0; spin < (SPIN_MAX << 4); ++spin) {
    if (__hip_atomic_load(flg + 1, __ATOMIC_RELAXED, __HIP_MEMORY_SCOPE_WORKGROUP) != 0u) break;
    __builtin_amdgcn_s_sleep(1);
  }
  asm volatile("s_waitcnt lgkmcnt(0)" ::: "memory");
  SCHED_FENCE();

  u32x4 qf[3];
#pragma unroll
  for (int j = 0; j < 3; ++j) qf[j] = *reinterpret_cast<const u32x4*>(lds + L_QS + (c + 8 * j) * 4);
  float sc[8];
#pragma unroll
  for (int t = 0; t < 8; ++t) {
    float d = 0.f;
#pragma unroll
    for (int j = 0; j < 3; ++j)
#pragma unroll
      for (int i = 0; i < 4; ++i) d = dot2(K[t][j][i], qf[j][i], d);
    d = grp8_sum(d);
    sc[t] = bfr(bfr(d) * SCALE);
    if (t == 3) {
      SCHED_FENCE();
      VLD(PQ);
    }
  }
  float mw = sc[0];
#pragma unroll
  for (int t = 1; t < 8; ++t) mw = fmaxf(mw, sc[t]);
  mw = xg_max(mw);
  if (lane == 0) {
    lds[L_ML + w] = mw;
    __hip_atomic_fetch_add(flg + 3, 1u, __ATOMIC_RELAXED, __HIP_MEMORY_SCOPE_WORKGROUP);
  }
  float lw = 0.f;
#pragma unroll
  for (int t = 0; t < 8; ++t) {
    sc[t] = ex2((sc[t] - mw) * LOG2E);
    lw += sc[t];
  }
  lw = xg_sum(lw);
  SCHED_FENCE();
  float Mg;
  {
    constexpr int LEAD = 3;
#pragma unroll
    for (int t = PQ + 1; t < LEAD; ++t) VLD(t);
    float acc[24], r3[3];
#pragma unroll
    for (int i = 0; i < 24; ++i) acc[i] = 0.f;
    constexpr int L2 = (PQ + 1 > LEAD) ? PQ + 1 : LEAD;
#define PVT(t)                                                                                                          \
  do {                                                                                                                  \
    const unsigned plo = bfbits(sc[t]), phi = plo << 16;                                                                \
    _Pragma("unroll") for (int j = 0; j < 3; ++j) _Pragma("unroll") for (int i = 0; i < 4; ++i) {                       \
      acc[8 * j + 2 * i] = dot2(V[t][j][i], plo, acc[8 * j + 2 * i]);                                                   \
      acc[8 * j + 2 * i + 1] = dot2(V[t][j][i], phi, acc[8 * j + 2 * i + 1]);                                           \
    }                                                                                                                   \
    _Pragma("unroll") for (int i = 0; i < 24; i += 8)                                                                   \
      asm volatile("" : "+v"(acc[i]), "+v"(acc[i + 1]), "+v"(acc[i + 2]), "+v"(acc[i + 3]), "+v"(acc[i + 4]),           \
                   "+v"(acc[i + 5]), "+v"(acc[i + 6]), "+v"(acc[i + 7]));            \
    SCHED_FENCE();                                                                                                      \
  } while (0)
#define WGMAX()                                                                                                         \
  do {                                                                                                                  \
    for (unsigned spin = 0; spin < (SPIN_MAX << 4); ++spin) {                                                           \
      if (__hip_atomic_load(flg + 3, __ATOMIC_RELAXED, __HIP_MEMORY_SCOPE_WORKGROUP) == 8u) break;                      \
      __builtin_amdgcn_s_sleep(1);                                                                                      \
    }                                                                                                                   \
    MEMBAR();                                                                                                           \
    const f32x4 ma = *reinterpret_cast<const f32x4*>(lds + L_ML), mb2 = *reinterpret_cast<const f32x4*>(lds + L_ML + 4); \
    Mg = fmaxf(fmaxf(fmaxf(ma[0], ma[1]), fmaxf(ma[2], ma[3])), fmaxf(fmaxf(mb2[0], mb2[1]), fmaxf(mb2[2], mb2[3])));   \
    ew = ex2((mw - Mg) * LOG2E);                                                                                        \
    SCHED_FENCE();                                                                                                      \
  } while (0)
    float ew;
#pragma unroll
    for (int t = 0; t + L2 < 8; ++t) {
      VLD(t + L2);
      PVT(t);
    }
    if (w >= 4) {
#pragma unroll
      for (int t = 8 - L2; t < 8; ++t) {
#pragma unroll
        for (int i = 0; i < WOB; ++i)
          if ((t == 7) ? (i >= t + L2 - 8) : (i == t + L2 - 8)) WOLD(i);
        if (t == 6) WGMAX();
        PVT(t);
      }
    } else {
#pragma unroll
      for (int t = 8 - L2; t < 8; ++t) {
        if (t == 6) WGMAX();
        PVT(t);
      }
    }
    SCHED_FENCE();
    xg_scatter<3>(acc, r3, g & 1);
#pragma unroll
    for (int j = 0; j < 3; ++j) lds[L_PART + w * PS + 64 * j + 8 * c + g] = r3[j] * ew;
    if (lane == 0) lds[L_PART + w * PS + DH] = lw * ew;
  }
  __syncthreads();
  float gate0 = 0.f, gate1 = 0.f;
  unsigned xv = 0u;
  const int col = 80 * sp + 4 * h + (lane >> 5) * 2;
  const bool fin = (w == 2) && (h < 20);
  const int fu = (40 * sp + 2 * h) * 32 + lane;
  if (__builtin_expect(w < 2, 1)) {
    if (tid < 97) {
      f32x2 p2[8];
#pragma unroll
      for (int ww = 0; ww < 8; ++ww) p2[ww] = *reinterpret_cast<const f32x2*>(lds + L_PART + ww * PS + 2 * tid);
      const f32x2 us = ((p2[0] + p2[1]) + (p2[2] + p2[3])) + ((p2[4] + p2[5]) + (p2[6] + p2[7]));
      gst2(G2 + rec * 104 + tid, tid < 96 ? us[0] : Mg, tid < 96 ? us[1] : us[0]);
    }
    const int t2 = tid < 96 ? tid : 95;
    const int pb = G2 + h * NSP * 104 + t2, pm = G2 + (h * NSP + c) * 104 + 96;
    u32x4 va[NSP], vml;
    if (PD1) __builtin_amdgcn_s_sleep(PD1);
    for (unsigned spin = 0;; ++spin) {
      MEMBAR();
#pragma unroll
      for (int s = 0; s < NSP; ++s) va[s] = gld2(pb, s * 104);
      vml = gld2(pm, 0);
      bool ok = gok2(vml);
#pragma unroll
      for (int s = 0; s < NSP; ++s) ok = ok && gok2(va[s]);
      if (__all(ok) || spin >= SPIN_MAX) break;
      __builtin_amdgcn_s_sleep(1);
    }
    const float ms = bitsf(vml[0]);
    float M2 = fmaxf(ms, dpp<0xB1>(ms));
    M2 = fmaxf(M2, dpp<0x4E>(M2));
    M2 = fmaxf(M2, dpp<0x141>(M2));
    const float es = ex2((ms - M2) * LOG2E);
    const float L2 = grp8_sum(es * bitsf(vml[2]));
    float o0 = 0.f, o1 = 0.f;
#pragma unroll
    for (int s = 0; s < NSP; ++s) {
      const float e = rdlane(es, s);
      o0 = fmaf(e, bitsf(va[s][0]), o0);
      o1 = fmaf(e, bitsf(va[s][2]), o1);
    }
    if (tid < 96) {
      unsigned short* po = reinterpret_cast<unsigned short*>(lds + L_O) + ((2 * tid & 31) * 4 + (tid >> 5)) * 2 + (tid >> 4 & 1);
      const float rl = __builtin_amdgcn_rcpf(L2);
      po[0] = bfbits(o0 * rl);
      po[8] = bfbits(o1 * rl);
    }
    __syncthreads();
  } else if (w == 2) {
    if (lane < 40) {
      const f32x2* zp = reinterpret_cast<const f32x2*>(lds + L_ZP) + lane;
      const f32x2 zs = ((zp[0] + zp[40]) + (zp[80] + zp[120])) + (zp[160] + zp[200]);
      gst2(G3Z + h * 320 + 40 * sp + lane, zs[0], zs[1]);
    }
    SCHED_FENCE();
    __syncthreads();
    if (fin) {
      xv = *reinterpret_cast<const unsigned*>(x + col);
      u32x4 v;
      for (unsigned spin = 0;; ++spin) {
        MEMBAR();
        v = gld2(G3Z + (lane & 31) * 320 + 40 * sp + 2 * h + (lane >> 5), 0);
        if (__all(gok2(v)) || spin >= SPIN_MAX) break;
        __builtin_amdgcn_s_sleep(1);
      }
      float s0 = grp8_sum(bitsf(v[0])), s1 = grp8_sum(bitsf(v[2]));
      s0 += dpp<0x128>(s0);
      s1 += dpp<0x128>(s1);
      s0 = swap16_add(s0, s0);
      s1 = swap16_add(s1, s1);
      gate0 = bfr(silu(bfr(s0)));
      gate1 = bfr(silu(bfr(s1)));
      asm volatile("" : "+v"(xv));
    }
  } else {
    unsigned wp[24];
    if (w >= 4) {
#pragma unroll
      for (int i = WOB; i < 8; ++i) WOLD(i);
      if (tid < 256 + 240) {
#pragma unroll
        for (int i = 0; i < 8; ++i) *reinterpret_cast<u32x4*>(lds + L_WO + u * 4 + i * 960) = WO[i];
      }
      asm volatile("s_waitcnt lgkmcnt(0)" ::: "memory");
      if (lane == 0) __hip_atomic_fetch_add(flg + 2, 1u, __ATOMIC_RELAXED, __HIP_MEMORY_SCOPE_WORKGROUP);
    }
    for (unsigned spin = 0; spin < (SPIN_MAX << 4); ++spin) {
      if (__hip_atomic_load(flg + 2, __ATOMIC_RELAXED, __HIP_MEMORY_SCOPE_WORKGROUP) == 4u) break;
      __builtin_amdgcn_s_sleep(1);
    }
    asm volatile("s_waitcnt lgkmcnt(0)" ::: "memory");
    const float* wl = lds + L_WO + (lane >> 1) * 40 + (2 * (w - 3) + (lane & 1)) * 4;
#pragma unroll
    for (int k = 0; k < 3; ++k) {
      const u32x4 A = *reinterpret_cast<const u32x4*>(wl + 64 * k * 40), B = *reinterpret_cast<const u32x4*>(wl + (64 * k + 32) * 40);
#pragma unroll
      for (int q = 0; q < 4; ++q) {
        wp[8 * k + 2 * q] = __builtin_amdgcn_perm(B[q], A[q], 0x05040100u);
        wp[8 * k + 2 * q + 1] = __builtin_amdgcn_perm(B[q], A[q], 0x07060302u);
      }
    }
#pragma unroll
    for (int i = 0; i < 24; i += 8)
      asm volatile("" : "+v"(wp[i]), "+v"(wp[i + 1]), "+v"(wp[i + 2]), "+v"(wp[i + 3]), "+v"(wp[i + 4]), "+v"(wp[i + 5]), "+v"(wp[i + 6]), "+v"(wp[i + 7]));
    __syncthreads();
    const u32x4 op = *reinterpret_cast<const u32x4*>(lds + L_O + (lane >> 1) * 4);
    float y[8];
#pragma unroll
    for (int e = 0; e < 8; ++e) y[e] = dot2(wp[16 + e], op[2], dot2(wp[8 + e], op[1], dot2(wp[e], op[0], 0.f)));
    float p[4];
#pragma unroll
    for (int e = 0; e < 4; ++e) p[e] = swap32_add(y[e], y[e + 4]);
    float q0 = swap16_add(p[0], p[2]), q1 = swap16_add(p[1], p[3]);
    q0 += dpp<0x128>(q0);
    q1 += dpp<0x128>(q1);
    q0 += dpp<0x124>(q0);
    q1 += dpp<0x124>(q1);
    q0 += dpp<0x4E>(q0);
    q1 += dpp<0x4E>(q1);
    if ((lane & 14) == 0) gst2(G3Y + (40 * sp + 4 * (2 * (w - 3) + (lane & 1)) + (lane >> 4)) * 32 + h, q0, q1);
  }
  if (fin) {
    u32x4 v;
    for (unsigned spin = 0;; ++spin) {
      MEMBAR();
      v = gld2(G3Y + fu, 0);
      if (__all(gok2(v)) || spin >= SPIN_MAX) break;
      __builtin_amdgcn_s_sleep(1);
    }
    float s0 = grp8_sum(bitsf(v[0])), s1 = grp8_sum(bitsf(v[2]));
    s0 += dpp<0x128>(s0);
    s1 += dpp<0x128>(s1);
    s0 = swap16_add(s0, s0);
    s1 = swap16_add(s1, s1);
    if ((lane & 31) == 0) {
      const unsigned r0 = bfbits(bf_lo(xv) + bfr(bfr(s0) * gate0)), r1 = bfbits(bf_hi(xv) + bfr(bfr(s1) * gate1));
      *reinterpret_cast<unsigned*>(out + col) = r0 | (r1 << 16);
      if (col == 0)
        __hip_atomic_store(reinterpret_cast<unsigned*>(gw + 2 * GEP), tag, __ATOMIC_RELAXED, __HIP_MEMORY_SCOPE_AGENT);
    }
  }
}

}

extern "C" void kernel_launch(void* const* d_in, const int* in_sizes, int n_in, void* d_out, int out_size, void* d_ws,
                              size_t ws_size, hipStream_t stream) {
  const bf16* x = (const bf16*)d_in[0];
  const bf16* kc = (const bf16*)d_in[1];
  const bf16* vc = (const bf16*)d_in[2];
  const bf16* wq = (const bf16*)d_in[3];
  const bf16* wo = (const bf16*)d_in[4];
  const bf16* w1 = (const bf16*)d_in[6];
  const bf16* w3 = (const bf16*)d_in[7];
  const bf16* w2 = (const bf16*)d_in[8];
  u64* gw = (u64*)d_ws;
  fused_decode<<<NWG, NT, 0, stream>>>(x, kc, vc, wq, wo, w1, w3, w2, gw, (bf16*)d_out);
}
```

```cpp
#include <hip/hip_runtime.h>
#include <hip/hip_bf16.h>
#include <stdint.h>

typedef __hip_bfloat16 bf16;
typedef unsigned long long u64;
typedef unsigned u32x4 __attribute__((ext_vector_type(4)));
typedef unsigned u32x2 __attribute__((ext_vector_type(2)));
typedef float f32x4 __attribute__((ext_vector_type(4)));
typedef __bf16 bf16x2_t __attribute__((ext_vector_type(2)));

namespace {

constexpr int D = 640, HQ = 32, DH = 192, SK = 32768, WIN = 4096, FF = 1536;
constexpr int NSP = 8;
constexpr int NWG = HQ * NSP;
constexpr int NT = 512;
constexpr float SCALE = 0.07216878364870322f;
constexpr float LOG2E = 1.4426950408889634f;
constexpr unsigned SPIN_MAX = 1u << 16;
constexpr int WND = 8;
constexpr int WOL = 7;
constexpr int PQ = 4;
constexpr int NF = 7;
constexpr int DX[8] = {2, -1, 2, -1, 1, -3, 1, -1};
constexpr unsigned dx_pack() {
  unsigned r = 0;
  for (int i = 0; i < 8; ++i) r |= (unsigned)(DX[i] + 4) << (4 * i);
  return r;
}
constexpr unsigned dx_pos() {
  unsigned r = 0, a = 0;
  for (int i = 0; i < 8; ++i) { r |= a << (4 * i); a += DX[i] > 0 ? DX[i] : 0; }
  return r;
}
constexpr unsigned long long dx_don() {
  unsigned long long r = 0;
  int k = 0;
  for (int i = 0; i < 8; ++i)
    for (int q = 0; q < -DX[i]; ++q) r |= (unsigned long long)(i * 4 + q) << (5 * k++);
  return r;
}
constexpr bool dx_ok() {
  int a = 0, p = 0;
  bool in = true;
  for (int i = 0; i < 8; ++i) { a += DX[i]; p += DX[i] > 0 ? DX[i] : 0; in = in && DX[i] >= -4 && DX[i] <= 4; }
  return a == 0 && p <= 8 && in;
}
constexpr int dx_check[dx_ok() ? 1 : -1] = {0};
constexpr int KE = 8;
constexpr int PD1 = 14;
constexpr int WOB = 8;

constexpr int G1 = 0;
constexpr int G1F = G1 + HQ * 288;
constexpr int G2 = G1F + 8 * 960;
constexpr int G3Y = G2 + NWG * 104;
constexpr int G3Z = G3Y + 320 * 32;
constexpr int GEP = G3Z + 320 * 32;
constexpr int GEND = GEP + 8;

__device__ __forceinline__ unsigned fbits(float x) { return __builtin_bit_cast(unsigned, x); }
__device__ __forceinline__ float bitsf(unsigned x) { return __builtin_bit_cast(float, x); }
#define gok2(v) ((v)[1] == tag && (v)[3] == tag)
#define gst2(unit, a, b) __builtin_amdgcn_raw_buffer_store_b128(u32x4{fbits(a), tag, fbits(b), tag}, rws, (unit) * 16, 0, 16)
#define gld2(unit, cunit) __builtin_amdgcn_raw_buffer_load_b128(rws, (unit) * 16, (cunit) * 16, 16)
#define MEMBAR() asm volatile("" ::: "memory")
typedef float f32x2 __attribute__((ext_vector_type(2)));

__device__ __forceinline__ float bf_lo(unsigned u) { return __builtin_bit_cast(float, u << 16); }
__device__ __forceinline__ float bf_hi(unsigned u) { return __builtin_bit_cast(float, u & 0xffff0000u); }
__device__ __forceinline__ float bfr(float x) { return __bfloat162float(__float2bfloat16(x)); }
__device__ __forceinline__ unsigned short bfbits(float x) {
  return __builtin_bit_cast(unsigned short, __float2bfloat16(x));
}
__device__ __forceinline__ float dot2(unsigned a, unsigned b, float c) {
  return __builtin_amdgcn_fdot2_f32_bf16(__builtin_bit_cast(bf16x2_t, a), __builtin_bit_cast(bf16x2_t, b), c, false);
}
template <int CTRL>
__device__ __forceinline__ float dpp(float x) {
  return __builtin_bit_cast(float, __builtin_amdgcn_mov_dpp(__builtin_bit_cast(int, x), CTRL, 0xf, 0xf, true));
}
__device__ __forceinline__ float grp8_sum(float x) {
  x += dpp<0xB1>(x);
  x += dpp<0x4E>(x);
  x += dpp<0x141>(x);
  return x;
}
__device__ __forceinline__ float swap16_add(float a, float b) {
  auto s = __builtin_amdgcn_permlane16_swap(fbits(a), fbits(b), false, false);
  return bitsf((unsigned)s[0]) + bitsf((unsigned)s[1]);
}
__device__ __forceinline__ float swap32_add(float a, float b) {
  auto t = __builtin_amdgcn_permlane32_swap(fbits(a), fbits(b), false, false);
  return bitsf((unsigned)t[0]) + bitsf((unsigned)t[1]);
}
__device__ __forceinline__ float xg_sum(float x) {
  x += dpp<0x128>(x);
  x = swap16_add(x, x);
  return swap32_add(x, x);
}
__device__ __forceinline__ float xg_max(float x) {
  x = fmaxf(x, dpp<0x128>(x));
  auto s = __builtin_amdgcn_permlane16_swap(fbits(x), fbits(x), false, false);
  x = fmaxf(bitsf((unsigned)s[0]), bitsf((unsigned)s[1]));
  auto t = __builtin_amdgcn_permlane32_swap(fbits(x), fbits(x), false, false);
  return fmaxf(bitsf((unsigned)t[0]), bitsf((unsigned)t[1]));
}
template <int NJ>
__device__ __forceinline__ void xg_scatter(const float (&a)[8 * NJ], float (&r)[NJ], const bool b3) {
#pragma unroll
  for (int j = 0; j < NJ; ++j) {
    float p[4];
#pragma unroll
    for (int e = 0; e < 4; ++e) p[e] = swap32_add(a[8 * j + e], a[8 * j + e + 4]);
    const float q0 = swap16_add(p[0], p[2]), q1 = swap16_add(p[1], p[3]);
    r[j] = (b3 ? q1 : q0) + dpp<0x128>(b3 ? q0 : q1);
  }
}
__device__ __forceinline__ float wave_sum(float x) { return xg_sum(grp8_sum(x)); }
__device__ __forceinline__ float ex2(float x) { return __builtin_amdgcn_exp2f(x); }
__device__ __forceinline__ float silu(float x) { return x / (1.0f + __expf(-x)); }
__device__ __forceinline__ float rdlane(float x, int l) {
  return __builtin_bit_cast(float, __builtin_amdgcn_readlane(__builtin_bit_cast(int, x), l));
}

#define SCHED_FENCE() __builtin_amdgcn_sched_barrier(0)
#define ALD(dst, rs, vo, so, imm) \
  asm volatile("buffer_load_dwordx4 %0, %1, %2, %3 offen offset:%4 nt" : "=v"(dst) : "v"(vo), "s"(rs), "s"(so), "n"(imm) : "memory")
#define AWL(dst, rs, vo, so, imm)                                                                                      \
  asm volatile("s_waitcnt vmcnt(8)\n\ts_cmp_le_i32 %5, 8\n\ts_cbranch_scc0 4\n\ts_waitcnt vmcnt(7)\n\t"                 \
               "s_cmp_le_i32 %5, 7\n\ts_cbranch_scc0 1\n\ts_waitcnt vmcnt(6)\n\t"                                        \
               "buffer_load_dwordx4 %0, %1, %2, %3 offen offset:%4 nt"                                                 \
               : "=v"(dst) : "v"(vo), "s"(rs), "s"(so), "n"(imm), "s"(wl) : "memory", "scc")
#define AWAIT(v, n) asm volatile("s_waitcnt vmcnt(%1)" : "+v"(v) : "n"(n) : "memory")
#define TIE(v) asm volatile("" : "+v"(v))
__device__ __forceinline__ u32x4 mkrs(const void* p, unsigned bytes) {
  const u64 a = (u64)p;
  return u32x4{(unsigned)a, (unsigned)(a >> 32) & 0xffffu, bytes, 0x00020000u};
}
#define WAVE_SYNC()                                        \
  do {                                                     \
    __builtin_amdgcn_fence(__ATOMIC_RELEASE, "wavefront"); \
    __builtin_amdgcn_wave_barrier();                       \
    __builtin_amdgcn_fence(__ATOMIC_ACQUIRE, "wavefront"); \
  } while (0)

constexpr int L_WO = 0;
constexpr int L_XS = L_WO + DH * 40;
constexpr int PS = DH + 4;
constexpr int L_PART = L_XS + 4 * 320;
constexpr int L_XN = L_PART + 8 * PS;
constexpr int L_ZP = L_XN + 4 * 64;
constexpr int L_QS = L_ZP + 6 * 80;
constexpr int L_ML = L_QS + 96;
constexpr int L_O = L_ML + 16;
constexpr int L_H = L_O + 128;
constexpr int L_GU = L_H + 48;
constexpr int L_FLG = L_GU + 96;
constexpr int L_END = L_FLG + 4;

__global__ void __launch_bounds__(NT, 2)
fused_decode(const bf16* __restrict__ x, const bf16* __restrict__ kc, const bf16* __restrict__ vc,
             const bf16* __restrict__ wq, const bf16* __restrict__ wo, const bf16* __restrict__ w1,
             const bf16* __restrict__ w3, const bf16* __restrict__ w2, u64* __restrict__ gw, bf16* __restrict__ out) {
  __shared__ __attribute__((aligned(16))) float lds[L_END];
  const int b = blockIdx.x;
  const int xcd = b & 7, jj = b >> 3;
  const int h = xcd * 4 + (jj >> 3), sp = jj & 7;
  const int ha = jj, spa = xcd;
  const int w = __builtin_amdgcn_readfirstlane(threadIdx.x >> 6);
  const int tid = threadIdx.x, lane = tid & 63;
  const int g = lane >> 3, c = lane & 7;
  const int rec = ha * NSP + spa;
  unsigned* flg = reinterpret_cast<unsigned*>(lds + L_FLG);

  asm volatile("" ::"s"(x), "s"(kc), "s"(vc), "s"(wq), "s"(wo), "s"(w1), "s"(w3), "s"(w2), "s"(gw), "s"(out));
  const auto rws = __builtin_amdgcn_make_buffer_rsrc((void*)gw, (short)0, GEND * 16, 0x00020000);
  const size_t kvw = ((size_t)ha * SK + (SK - WIN)) * DH;
  const u32x4 rk = mkrs(kc + kvw, WIN * DH * 2), rv = mkrs(vc + kvw, WIN * DH * 2);
  const u32x4 rwa = mkrs(gw, GEND * 16);
  const int kvo = (g * DH + 8 * c) * 2;
  const int dx = (int)((dx_pack() >> (4 * xcd)) & 15u) - 4;
  const int wr = w < 4 ? (w + 3) & 3 : w;
  const int nt = 8 + ((unsigned)(wr - dx) >> 31) - ((unsigned)(7 - w + dx) >> 31);
  const int don = (int)(dx_don() >> (5 * (((dx_pos() >> (4 * xcd)) + wr) & 7u))) & 31;
  const int kvb = (spa * 8 + ((w + (ha >> 3)) & 7)) * 64 * DH * 2;
  const int ko8 = (((don >> 2) * 8 + ((7 - (don & 3) + (ha >> 3)) & 7)) * 8 + ((7 + ha) & 7)) * 8 * DH * 2;
  const int wl = __builtin_amdgcn_readfirstlane((w < 4 ? WOL : WND) + (nt - 8) + (dx < 0 ? 1 : 0));
#define KVOFF(t) ((t) == 8 ? ko8 : kvb + (((t) + ha) & 7) * 8 * DH * 2)
#define KLD(t, j) ALD(K[t][j], rk, kvo, KVOFF(t), 128 * (j))
#define KWL(t, j) AWL(K[t][j], rk, kvo, KVOFF(t), 128 * (j))
#define VWL(t, j) AWL(V[t][j], rv, kvo, KVOFF(t), 128 * (j))
#define EPLD() asm volatile("buffer_load_dword %0, off, %1, %2" : "=v"(epoch) : "s"(rwa), "s"(GEP * 16) : "memory")
  u32x4 K[NF + 2][3], V[NF + 2][3];

  const int fi = (xcd & 1) ? 19 : 8;
  const bool fidle = (unsigned)(jj - fi) < 2u;
  const int fp = jj > fi ? jj - 2 : jj;
  const int fcol = fp / 5, frp = fp - 5 * fcol;
  unsigned epoch, tag;
  if (w < 4) {
    const int fw = 4 * sp + w, ct = fw >= 20 ? 2 : fw >= 10 ? 1 : 0;
    const int r0 = 64 * (fw - 10 * ct);
    const int R = 128 * frp + 32 * w;
    const u32x4 xa = *reinterpret_cast<const u32x4*>(x + 8 * lane);
    const u32x4 xb = *reinterpret_cast<const u32x4*>(x + 512 + 8 * (lane & 15));
    u32x4 WQ[8], F[4];
    {
      const auto rq = __builtin_amdgcn_make_buffer_rsrc((void*)wq, (short)0, D * HQ * DH * 2, 0x00020000);
      const int qo = ((r0 + 8 * g) * (HQ * DH) + h * DH + 64 * ct + 8 * c) * 2;
#pragma unroll
      for (int i = 0; i < 8; ++i) WQ[i] = __builtin_amdgcn_raw_buffer_load_b128(rq, qo, i * HQ * DH * 2, 2);
      const auto rf = __builtin_amdgcn_make_buffer_rsrc((void*)(fcol >= 3 ? w3 : w1), (short)0, fidle ? 0 : D * FF * 2, 0x00020000);
      const int fo = ((R + 4 * g) * FF + 192 * xcd + 64 * (fcol >= 3 ? fcol - 3 : fcol) + 8 * c) * 2;
#pragma unroll
      for (int i = 0; i < 4; ++i) F[i] = __builtin_amdgcn_raw_buffer_load_b128(rf, fo, i * FF * 2, 2);
    }
    SCHED_FENCE();
    EPLD();
    if (tid < 4) flg[tid] = 0u;
    SCHED_FENCE();
    __syncthreads();
    SCHED_FENCE();
    unsigned short* xs = reinterpret_cast<unsigned short*>(lds + L_XS + w * 320);
    const bool lo16 = lane < 16;
    *reinterpret_cast<u32x4*>(xs + 8 * lane) = xa;
    if (lo16) *reinterpret_cast<u32x4*>(xs + 512 + 8 * lane) = xb;
    float xf[8], xg[8];
#pragma unroll
    for (int i = 0; i < 4; ++i) {
      xf[2 * i] = bf_lo(xa[i]);
      xf[2 * i + 1] = bf_hi(xa[i]);
      xg[2 * i] = bf_lo(xb[i]);
      xg[2 * i + 1] = bf_hi(xb[i]);
    }
    float sum = 0.f;
#pragma unroll
    for (int i = 0; i < 8; ++i) sum += xf[i] + (lo16 ? xg[i] : 0.f);
    sum = wave_sum(sum);
    const float mean = sum * (1.0f / D);
    float sq = 0.f;
#pragma unroll
    for (int i = 0; i < 8; ++i) {
      const float d1 = xf[i] - mean, d2 = xg[i] - mean;
      sq += d1 * d1 + (lo16 ? d2 * d2 : 0.f);
    }
    sq = wave_sum(sq);
    const float mb = bfr(mean);
    const float vb = bfr(sq * (1.0f / D));
    const float den = bfr(sqrtf(bfr(vb + 1e-5f)));
    WAVE_SYNC();
    auto xnorm = [&](int r) -> unsigned short { return bfbits(bfr(bf_lo(xs[r]) - mb) / den); };
    {
      unsigned short* xn = reinterpret_cast<unsigned short*>(lds + L_XN + w * 64);
      xn[lane] = xnorm((r0 < D ? r0 : 0) + lane);
      xn[64 + (lane & 31)] = xnorm(R + (lane & 31));
      WAVE_SYNC();
      const u32x4 XQ = *reinterpret_cast<const u32x4*>(xn + 8 * g);
      const u32x2 XF = *reinterpret_cast<const u32x2*>(xn + 64 + 4 * g);
      float acc[16], r2[2];
#pragma unroll
      for (int i = 0; i < 16; ++i) acc[i] = 0.f;
#pragma unroll
      for (int i = 0; i < 8; ++i) {
        const unsigned d = XQ[i >> 1], lo = (i & 1) ? d >> 16 : d & 0xffffu, hi = (i & 1) ? d & 0xffff0000u : d << 16;
#pragma unroll
        for (int k = 0; k < 4; ++k) {
          acc[2 * k] = dot2(WQ[i][k], lo, acc[2 * k]);
          acc[2 * k + 1] = dot2(WQ[i][k], hi, acc[2 * k + 1]);
        }
      }
#pragma unroll
      for (int i = 0; i < 4; ++i) {
        const unsigned d = XF[i >> 1], lo = (i & 1) ? d >> 16 : d & 0xffffu, hi = (i & 1) ? d & 0xffff0000u : d << 16;
#pragma unroll
        for (int k = 0; k < 4; ++k) {
          acc[8 + 2 * k] = dot2(F[i][k], lo, acc[8 + 2 * k]);
          acc[8 + 2 * k + 1] = dot2(F[i][k], hi, acc[8 + 2 * k + 1]);
        }
      }
      xg_scatter<2>(acc, r2, g & 1);
#pragma unroll
      for (int j = 0; j < 2; ++j) lds[L_PART + w * 128 + 64 * j + 8 * c + g] = r2[j];
    }
    asm volatile("s_waitcnt lgkmcnt(0)" ::: "memory");
    if (lane == 0) __hip_atomic_fetch_add(flg, 1u, __ATOMIC_RELAXED, __HIP_MEMORY_SCOPE_WORKGROUP);
    SCHED_FENCE();
  } else {
#pragma unroll
    for (int m = 0; m < KE; ++m) KLD(m / 3, m % 3);
    SCHED_FENCE();
    EPLD();
    SCHED_FENCE();
    __syncthreads();
    SCHED_FENCE();
  }
  const int lz = lane < 60 ? lane : 59;
  const int zr = lz / 10, zc = lz - 10 * zr;
  int w2o = (zr * D + 8 * zc) * 2;
  asm volatile("" : "+v"(w2o));
  if (w == 0) {
    for (unsigned spin = 0; spin < (SPIN_MAX << 4); ++spin) {
      if (__hip_atomic_load(flg, __ATOMIC_RELAXED, __HIP_MEMORY_SCOPE_WORKGROUP) == 4u) break;
      __builtin_amdgcn_s_sleep(1);
    }
    asm volatile("s_waitcnt lgkmcnt(0)" ::: "memory");
    AWAIT(epoch, 0);
    tag = __builtin_amdgcn_readfirstlane(epoch) + 1u;
    const f32x2* pp = reinterpret_cast<const f32x2*>(lds + L_PART) + lane;
    const f32x2 a01 = pp[0] + pp[64], a23 = pp[128] + pp[192];
    const bool two = sp == 2 && lane < 32;
    const f32x2 sm = two ? a01 : a01 + a23;
    const int ct0 = sp >= 5 ? 2 : sp >= 3 ? 1 : 0, slot = sp - (sp >= 5 ? 5 : sp >= 3 ? 2 : 0);
    const int un = lane < 32 ? G1 + h * 288 + slot * 96 + ct0 * 32 + lane : G1F + xcd * 960 + fp * 32 + lane - 32;
    if (lane < 32 || !fidle) gst2(un, sm[0], sm[1]);
    if (two) gst2(G1 + h * 288 + 32 + lane, a23[0], a23[1]);
    SCHED_FENCE();
  }
  int wop = w;
  asm volatile("" : "+s"(wop));
  if (wop != 0) {
    if (w < 4) {
#pragma unroll
      for (int m = 0; m < KE; ++m) KWL(m / 3, m % 3);
    }
#pragma unroll
    for (int m = KE; m < 3 * NF; ++m) KWL(m / 3, m % 3);
    TIE(epoch);
    tag = __builtin_amdgcn_readfirstlane(epoch) + 1u;
    SCHED_FENCE();
  } else {
    u32x4 W2[8];
    const auto rw2 = __builtin_amdgcn_make_buffer_rsrc((void*)(w2 + (size_t)48 * h * D + 80 * sp), (short)0, 48 * D * 2, 0x00020000);
#pragma unroll
    for (int i = 0; i < 8; ++i) W2[i] = __builtin_amdgcn_raw_buffer_load_b128(rw2, w2o, i * 6 * D * 2, 2);
#pragma unroll
    for (int t = 0; t < NF; ++t)
#pragma unroll
      for (int j = 0; j < 3; ++j) KLD(t, j);
    SCHED_FENCE();
    const int row = lane >> 4, l16 = lane & 15;
    const int gb = G1 + ha * 288 + lane;
    const int gc = G1 + ha * 288 + (row < 3 ? row : 2) * 96 + 64 + l16;
    const int l48 = lane < 48 ? lane : 47, fm = l48 >= 24 ? 1 : 0, fpr = 24 * (h & 3) + l48 - 24 * fm;
    const int gf = G1F + xcd * 960 + (3 * fm + (fpr >> 5)) * 160 + (fpr & 31);
    u32x4 va[3], vb[2], vf[5];
    for (unsigned spin = 0;; ++spin) {
      MEMBAR();
#pragma unroll
      for (int s = 0; s < 3; ++s) va[s] = gld2(gb, s * 96);
#pragma unroll
      for (int s = 0; s < 2; ++s) vb[s] = gld2(gc, s * 16);
#pragma unroll
      for (int s = 0; s < 5; ++s) vf[s] = gld2(gf, s * 32);
      bool ok = gok2(vb[0]) && gok2(vb[1]) && gok2(vf[3]) && gok2(vf[4]);
#pragma unroll
      for (int s = 0; s < 3; ++s) ok = ok && gok2(va[s]) && gok2(vf[s]);
      if (__all(ok) || spin >= SPIN_MAX) break;
      __builtin_amdgcn_s_sleep(1);
    }
    {
      const float a0 = (bitsf(va[0][0]) + bitsf(va[1][0])) + bitsf(va[2][0]), a1 = (bitsf(va[0][2]) + bitsf(va[1][2])) + bitsf(va[2][2]);
      reinterpret_cast<unsigned*>(lds + L_QS)[lane] = (unsigned)bfbits(a0) | ((unsigned)bfbits(a1) << 16);
      const float p0 = swap16_add(row < 3 ? bitsf(vb[0][0]) : 0.f, row < 3 ? bitsf(vb[0][2]) : 0.f);
      const float p1 = swap16_add(row < 3 ? bitsf(vb[1][0]) : 0.f, row < 3 ? bitsf(vb[1][2]) : 0.f);
      const float bq = swap32_add(p0, p1);
      reinterpret_cast<unsigned short*>(lds + L_QS)[2 * (64 + 16 * (row >> 1) + l16) + (row & 1)] = bfbits(bq);
    }
    asm volatile("s_waitcnt lgkmcnt(0)" ::: "memory");
    if (lane == 0) __hip_atomic_store(flg + 1, 1u, __ATOMIC_RELAXED, __HIP_MEMORY_SCOPE_WORKGROUP);
    SCHED_FENCE();
    {
      const float f0 = ((bitsf(vf[0][0]) + bitsf(vf[1][0])) + (bitsf(vf[2][0]) + bitsf(vf[3][0]))) + bitsf(vf[4][0]);
      const float f1 = ((bitsf(vf[0][2]) + bitsf(vf[1][2])) + (bitsf(vf[2][2]) + bitsf(vf[3][2]))) + bitsf(vf[4][2]);
      *reinterpret_cast<f32x2*>(lds + L_GU + 2 * l48) = f32x2{bfr(f0), bfr(f1)};
    }
    WAVE_SYNC();
    if (lane < 48) lds[L_H + lane] = bfr(bfr(silu(lds[L_GU + lane])) * lds[L_GU + 48 + lane]);
    WAVE_SYNC();
    {
      float z[8];
#pragma unroll
      for (int i = 0; i < 8; ++i) z[i] = 0.f;
#pragma unroll
      for (int i = 0; i < 8; ++i) {
        const unsigned chi = fbits(lds[L_H + zr + 6 * i]), clo = chi >> 16;
#pragma unroll
        for (int k = 0; k < 4; ++k) {
          z[2 * k] = dot2(W2[i][k], clo, z[2 * k]);
          z[2 * k + 1] = dot2(W2[i][k], chi, z[2 * k + 1]);
        }
      }
      if (lane < 60) {
        float* zp = lds + L_ZP + zr * 80 + 8 * zc;
        *reinterpret_cast<f32x4*>(zp) = f32x4{z[0], z[1], z[2], z[3]};
        *reinterpret_cast<f32x4*>(zp + 4) = f32x4{z[4], z[5], z[6], z[7]};
      }
    }
    SCHED_FENCE();
  }
  const int u = (tid & 255) < 240 ? (tid & 255) : 239;
  const int rg = u / 10, ch = u - 10 * rg;
  u32x4 WO[8];
  const u32x4 rwo = mkrs(wo + (size_t)DH * h * D + 80 * sp, DH * D * 2);
  const int wofs = (rg * D + 8 * ch) * 2;
#define WOLC(i)                                                                                                            \
  asm volatile("s_cmp_lt_u32 %4, 4\n\ts_cbranch_scc1 2\n\tbuffer_load_dwordx4 %0, %1, %2, %3 offen nt"                     \
               : "=v"(WO[i]) : "v"(wofs), "s"(rwo), "s"((i) * 24 * D * 2), "s"(w) : "memory", "scc")
#define VLD(t)                                                       \
  do {                                                               \
    _Pragma("unroll") for (int j = 0; j < 3; ++j) VWL(t, j);         \
  } while (0)
#pragma unroll
  for (int t = NF; t < NF + 2; ++t)
    if (t < nt) {
#pragma unroll
      for (int j = 0; j < 3; ++j) KWL(t, j);
      VLD(t);
    }
#pragma unroll
  for (int t = 0; t < PQ; ++t) VLD(t);
  for (unsigned spin = 0; spin < (SPIN_MAX << 4); ++spin) {
    if (__hip_atomic_load(flg + 1, __ATOMIC_RELAXED, __HIP_MEMORY_SCOPE_WORKGROUP) != 0u) break;
    __builtin_amdgcn_s_sleep(1);
  }
  asm volatile("s_waitcnt lgkmcnt(0)" ::: "memory");
  SCHED_FENCE();

  u32x4 qf[3];
#pragma unroll
  for (int j = 0; j < 3; ++j) qf[j] = *reinterpret_cast<const u32x4*>(lds + L_QS + (c + 8 * j) * 4);
  float sc[NF + 2];
#pragma unroll
  for (int t = 0; t < NF + 2; ++t) {
    sc[t] = -INFINITY;
    if (t < NF || t < nt) {
      float d = 0.f;
#pragma unroll
      for (int j = 0; j < 3; ++j) {
        TIE(K[t][j]);
#pragma unroll
        for (int i = 0; i < 4; ++i) d = dot2(K[t][j][i], qf[j][i], d);
      }
      d = grp8_sum(d);
      sc[t] = bfr(bfr(d) * SCALE);
    }
    if (t == 3) {
      SCHED_FENCE();
      VLD(PQ);
    }
  }
  float mw = sc[0];
#pragma unroll
  for (int t = 1; t < NF + 2; ++t) mw = fmaxf(mw, sc[t]);
  mw = xg_max(mw);
  if (lane == 0) {
    lds[L_ML + w] = mw;
    __hip_atomic_fetch_add(flg + 3, 1u, __ATOMIC_RELAXED, __HIP_MEMORY_SCOPE_WORKGROUP);
  }
  float lw = 0.f;
#pragma unroll
  for (int t = 0; t < NF + 2; ++t) {
    sc[t] = ex2((sc[t] - mw) * LOG2E);
    lw += sc[t];
  }
  lw = xg_sum(lw);
  SCHED_FENCE();
  float Mg;
  {
    float acc[24], r3[3];
#pragma unroll
    for (int i = 0; i < 24; ++i) acc[i] = 0.f;
#define PVT(t, nw)                                                                                                          \
  do {                                                                                                                  \
    unsigned plo = bfbits(sc[t]), phi = plo << 16;                                                                      \
    _Pragma("unroll") for (int j = 0; j < 3; ++j) {                                                                     \
      if ((t) >= NF || (t) < NF - 3) asm volatile("" : "+v"(plo), "+v"(phi));                                           \
      else asm volatile("s_waitcnt vmcnt(%2)\n\ts_cmp_lt_u32 %4, 4\n\ts_cbranch_scc0 1\n\ts_waitcnt vmcnt(%3)"            \
                        : "+v"(plo), "+v"(phi)                                                                          \
                        : "n"((NF - 1 - (t)) * 3 + 2 - j + (nw)), "n"((NF - 1 - (t)) * 3 + 2 - j), "s"(w) : "memory", "scc"); \
      _Pragma("unroll") for (int i = 0; i < 4; ++i) {                                                                   \
        acc[8 * j + 2 * i] = dot2(V[t][j][i], plo, acc[8 * j + 2 * i]);                                                 \
        acc[8 * j + 2 * i + 1] = dot2(V[t][j][i], phi, acc[8 * j + 2 * i + 1]);                                         \
      }                                                                                                                 \
    }                                                                                                                   \
    _Pragma("unroll") for (int i = 0; i < 24; i += 8)                                                                   \
      asm volatile("" : "+v"(acc[i]), "+v"(acc[i + 1]), "+v"(acc[i + 2]), "+v"(acc[i + 3]), "+v"(acc[i + 4]),           \
                   "+v"(acc[i + 5]), "+v"(acc[i + 6]), "+v"(acc[i + 7]));            \
    SCHED_FENCE();                                                                                                      \
  } while (0)
#define WGMAX()                                                                                                         \
  do {                                                                                                                  \
    for (unsigned spin = 0; spin < (SPIN_MAX << 4); ++spin) {                                                           \
      if (__hip_atomic_load(flg + 3, __ATOMIC_RELAXED, __HIP_MEMORY_SCOPE_WORKGROUP) == 8u) break;                      \
      __builtin_amdgcn_s_sleep(1);                                                                                      \
    }                                                                                                                   \
    MEMBAR();                                                                                                           \
    const f32x4 ma = *reinterpret_cast<const f32x4*>(lds + L_ML), mb2 = *reinterpret_cast<const f32x4*>(lds + L_ML + 4); \
    Mg = fmaxf(fmaxf(fmaxf(ma[0], ma[1]), fmaxf(ma[2], ma[3])), fmaxf(fmaxf(mb2[0], mb2[1]), fmaxf(mb2[2], mb2[3])));   \
    ew = ex2((mw - Mg) * LOG2E);                                                                                        \
    SCHED_FENCE();                                                                                                      \
  } while (0)
    float ew;
    VLD(PQ + 1);
    if (NF < nt) PVT(NF, 0);
    VLD(PQ + 2);
    if (NF + 1 < nt) PVT(NF + 1, 0);
    PVT(0, 0);
#pragma unroll
    for (int t = 1; t < NF; ++t) {
#pragma unroll
      for (int i = 0; i < WOB; ++i)
        if ((t == NF - 1) ? (i >= t - 1) : (i == t - 1)) WOLC(i);
      if (t == NF - 2) WGMAX();
      PVT(t, t == NF - 1 ? WOB : t);
    }
    SCHED_FENCE();
    xg_scatter<3>(acc, r3, g & 1);
#pragma unroll
    for (int j = 0; j < 3; ++j) lds[L_PART + w * PS + 64 * j + 8 * c + g] = r3[j] * ew;
    if (lane == 0) lds[L_PART + w * PS + DH] = lw * ew;
  }
  __syncthreads();
  float gate0 = 0.f, gate1 = 0.f;
  unsigned xv = 0u;
  const int col = 80 * sp + 4 * h + (lane >> 5) * 2;
  const bool fin = (w == 2) && (h < 20);
  const int fu = (40 * sp + 2 * h) * 32 + lane;
  if (__builtin_expect(w < 2, 1)) {
    if (tid < 97) {
      f32x2 p2[8];
#pragma unroll
      for (int ww = 0; ww < 8; ++ww) p2[ww] = *reinterpret_cast<const f32x2*>(lds + L_PART + ww * PS + 2 * tid);
      const f32x2 us = ((p2[0] + p2[1]) + (p2[2] + p2[3])) + ((p2[4] + p2[5]) + (p2[6] + p2[7]));
      gst2(G2 + rec * 104 + tid, tid < 96 ? us[0] : Mg, tid < 96 ? us[1] : us[0]);
    }
    const int t2 = tid < 96 ? tid : 95;
    const int pb = G2 + h * NSP * 104 + t2, pm = G2 + (h * NSP + c) * 104 + 96;
    u32x4 va[NSP], vml;
    if (PD1) __builtin_amdgcn_s_sleep(PD1);
    for (unsigned spin = 0;; ++spin) {
      MEMBAR();
#pragma unroll
      for (int s = 0; s < NSP; ++s) va[s] = gld2(pb, s * 104);
      vml = gld2(pm, 0);
      bool ok = gok2(vml);
#pragma unroll
      for (int s = 0; s < NSP; ++s) ok = ok && gok2(va[s]);
      if (__all(ok) || spin >= SPIN_MAX) break;
      __builtin_amdgcn_s_sleep(1);
    }
    const float ms = bitsf(vml[0]);
    float M2 = fmaxf(ms, dpp<0xB1>(ms));
    M2 = fmaxf(M2, dpp<0x4E>(M2));
    M2 = fmaxf(M2, dpp<0x141>(M2));
    const float es = ex2((ms - M2) * LOG2E);
    const float L2 = grp8_sum(es * bitsf(vml[2]));
    float o0 = 0.f, o1 = 0.f;
#pragma unroll
    for (int s = 0; s < NSP; ++s) {
      const float e = rdlane(es, s);
      o0 = fmaf(e, bitsf(va[s][0]), o0);
      o1 = fmaf(e, bitsf(va[s][2]), o1);
    }
    if (tid < 96) {
      unsigned short* po = reinterpret_cast<unsigned short*>(lds + L_O) + ((2 * tid & 31) * 4 + (tid >> 5)) * 2 + (tid >> 4 & 1);
      const float rl = __builtin_amdgcn_rcpf(L2);
      po[0] = bfbits(o0 * rl);
      po[8] = bfbits(o1 * rl);
    }
    __syncthreads();
  } else if (w == 2) {
    if (lane < 40) {
      const f32x2* zp = reinterpret_cast<const f32x2*>(lds + L_ZP) + lane;
      const f32x2 zs = ((zp[0] + zp[40]) + (zp[80] + zp[120])) + (zp[160] + zp[200]);
      gst2(G3Z + h * 320 + 40 * sp + lane, zs[0], zs[1]);
    }
    SCHED_FENCE();
    __syncthreads();
    if (fin) {
      xv = *reinterpret_cast<const unsigned*>(x + col);
      u32x4 v;
      for (unsigned spin = 0;; ++spin) {
        MEMBAR();
        v = gld2(G3Z + (lane & 31) * 320 + 40 * sp + 2 * h + (lane >> 5), 0);
        if (__all(gok2(v)) || spin >= SPIN_MAX) break;
        __builtin_amdgcn_s_sleep(1);
      }
      float s0 = grp8_sum(bitsf(v[0])), s1 = grp8_sum(bitsf(v[2]));
      s0 += dpp<0x128>(s0);
      s1 += dpp<0x128>(s1);
      s0 = swap16_add(s0, s0);
      s1 = swap16_add(s1, s1);
      gate0 = bfr(silu(bfr(s0)));
      gate1 = bfr(silu(bfr(s1)));
      asm volatile("" : "+v"(xv));
    }
  } else {
    unsigned wp[24];
    if (w >= 4) {
#pragma unroll
      for (int i = 0; i < 8; ++i) {
        AWAIT(WO[i], 7 - i);
        *reinterpret_cast<u32x4*>(lds + L_WO + u * 4 + i * 960) = WO[i];
      }
      asm volatile("s_waitcnt lgkmcnt(0)" ::: "memory");
      if (lane == 0) __hip_atomic_fetch_add(flg + 2, 1u, __ATOMIC_RELAXED, __HIP_MEMORY_SCOPE_WORKGROUP);
    }
    for (unsigned spin = 0; spin < (SPIN_MAX << 4); ++spin) {
      if (__hip_atomic_load(flg + 2, __ATOMIC_RELAXED, __HIP_MEMORY_SCOPE_WORKGROUP) == 4u) break;
      __builtin_amdgcn_s_sleep(1);
    }
    asm volatile("s_waitcnt lgkmcnt(0)" ::: "memory");
    const float* wsl = lds + L_WO + (lane >> 1) * 40 + (2 * (w - 3) + (lane & 1)) * 4;
#pragma unroll
    for (int k = 0; k < 3; ++k) {
      const u32x4 A = *reinterpret_cast<const u32x4*>(wsl + 64 * k * 40), B = *reinterpret_cast<const u32x4*>(wsl + (64 * k + 32) * 40);
#pragma unroll
      for (int q = 0; q < 4; ++q) {
        wp[8 * k + 2 * q] = __builtin_amdgcn_perm(B[q], A[q], 0x05040100u);
        wp[8 * k + 2 * q + 1] = __builtin_amdgcn_perm(B[q], A[q], 0x07060302u);
      }
    }
#pragma unroll
    for (int i = 0; i < 24; i += 8)
      asm volatile("" : "+v"(wp[i]), "+v"(wp[i + 1]), "+v"(wp[i + 2]), "+v"(wp[i + 3]), "+v"(wp[i + 4]), "+v"(wp[i + 5]), "+v"(wp[i + 6]), "+v"(wp[i + 7]));
    __syncthreads();
    const u32x4 op = *reinterpret_cast<const u32x4*>(lds + L_O + (lane >> 1) * 4);
    float y[8];
#pragma unroll
    for (int e = 0; e < 8; ++e) y[e] = dot2(wp[16 + e], op[2], dot2(wp[8 + e], op[1], dot2(wp[e], op[0], 0.f)));
    float p[4];
#pragma unroll
    for (int e = 0; e < 4; ++e) p[e] = swap32_add(y[e], y[e + 4]);
    float q0 = swap16_add(p[0], p[2]), q1 = swap16_add(p[1], p[3]);
    q0 += dpp<0x128>(q0);
    q1 += dpp<0x128>(q1);
    q0 += dpp<0x124>(q0);
    q1 += dpp<0x124>(q1);
    q0 += dpp<0x4E>(q0);
    q1 += dpp<0x4E>(q1);
    if ((lane & 14) == 0) gst2(G3Y + (40 * sp + 4 * (2 * (w - 3) + (lane & 1)) + (lane >> 4)) * 32 + h, q0, q1);
  }
  if (fin) {
    u32x4 v;
    for (unsigned spin = 0;; ++spin) {
      MEMBAR();
      v = gld2(G3Y + fu, 0);
      if (__all(gok2(v)) || spin >= SPIN_MAX) break;
      __builtin_amdgcn_s_sleep(1);
    }
    float s0 = grp8_sum(bitsf(v[0])), s1 = grp8_sum(bitsf(v[2]));
    s0 += dpp<0x128>(s0);
    s1 += dpp<0x128>(s1);
    s0 = swap16_add(s0, s0);
    s1 = swap16_add(s1, s1);
    if ((lane & 31) == 0) {
      const unsigned r0 = bfbits(bf_lo(xv) + bfr(bfr(s0) * gate0)), r1 = bfbits(bf_hi(xv) + bfr(bfr(s1) * gate1));
      *reinterpret_cast<unsigned*>(out + col) = r0 | (r1 << 16);
      if (col == 0)
        __hip_atomic_store(reinterpret_cast<unsigned*>(gw + 2 * GEP), tag, __ATOMIC_RELAXED, __HIP_MEMORY_SCOPE_AGENT);
    }
  }
}

}

extern "C" void kernel_launch(void* const* d_in, const int* in_sizes, int n_in, void* d_out, int out_size, void* d_ws,
                              size_t ws_size, hipStream_t stream) {
  const bf16* x = (const bf16*)d_in[0];
  const bf16* kc = (const bf16*)d_in[1];
  const bf16* vc = (const bf16*)d_in[2];
  const bf16* wq = (const bf16*)d_in[3];
  const bf16* wo = (const bf16*)d_in[4];
  const bf16* w1 = (const bf16*)d_in[6];
  const bf16* w3 = (const bf16*)d_in[7];
  const bf16* w2 = (const bf16*)d_in[8];
  u64* gw = (u64*)d_ws;
  fused_decode<<<NWG, NT, 0, stream>>>(x, kc, vc, wq, wo, w1, w3, w2, gw, (bf16*)d_out);
}
```

```cpp
#include <hip/hip_runtime.h>
#include <hip/hip_bf16.h>
#include <stdint.h>

typedef __hip_bfloat16 bf16;
typedef unsigned long long u64;
typedef __attribute__((address_space(1))) unsigned long long gu64;
typedef unsigned u32x4 __attribute__((ext_vector_type(4)));
typedef unsigned u32x2 __attribute__((ext_vector_type(2)));
typedef float f32x4 __attribute__((ext_vector_type(4)));
typedef __bf16 bf16x2_t __attribute__((ext_vector_type(2)));

namespace {

constexpr int D = 640, HQ = 32, DH = 192, SK = 32768, WIN = 4096, FF = 1536;
constexpr int NSP = 8;
constexpr int NWG = HQ * NSP;
constexpr int NT = 512;
constexpr float SCALE = 0.07216878364870322f;
constexpr float LOG2E = 1.4426950408889634f;
constexpr unsigned SPIN_MAX = 1u << 16;
constexpr int WND = 8;
constexpr int WOL = 7;
constexpr int PQ = 4;
constexpr int NF = 7;
constexpr int DX[8] = {2, -1, 2, -1, 1, -3, 1, -1};
constexpr unsigned dx_pack() {
  unsigned r = 0;
  for (int i = 0; i < 8; ++i) r |= (unsigned)(DX[i] + 4) << (4 * i);
  return r;
}
constexpr unsigned dx_pos() {
  unsigned r = 0, a = 0;
  for (int i = 0; i < 8; ++i) { r |= a << (4 * i); a += DX[i] > 0 ? DX[i] : 0; }
  return r;
}
constexpr unsigned long long dx_don() {
  unsigned long long r = 0;
  int k = 0;
  for (int i = 0; i < 8; ++i)
    for (int q = 0; q < -DX[i]; ++q) r |= (unsigned long long)(i * 4 + q) << (5 * k++);
  return r;
}
constexpr bool dx_ok() {
  int a = 0, p = 0;
  bool in = true;
  for (int i = 0; i < 8; ++i) { a += DX[i]; p += DX[i] > 0 ? DX[i] : 0; in = in && DX[i] >= -4 && DX[i] <= 4; }
  return a == 0 && p <= 8 && in;
}
constexpr int dx_check[dx_ok() ? 1 : -1] = {0};
constexpr int KE = 8;
constexpr int PD1 = 14;
constexpr int WOB = 8;

constexpr int G1 = 0;
constexpr int G1F = G1 + HQ * 288;
constexpr int G2 = G1F + 8 * 960;
constexpr int G3Y = G2 + NWG * 104;
constexpr int G3Z = G3Y + 320 * 32;
constexpr int GG = G3Z + 320 * 32;
constexpr int GZF = GG + 320;
constexpr int GA = GZF + 256;
constexpr int GEP = GA + 640;
constexpr int GEND = GEP + 8;

__device__ __forceinline__ unsigned fbits(float x) { return __builtin_bit_cast(unsigned, x); }
__device__ __forceinline__ float bitsf(unsigned x) { return __builtin_bit_cast(float, x); }
#define gok2(v) ((v)[1] == tag && (v)[3] == tag)
#define gst2(unit, a, b) __builtin_amdgcn_raw_buffer_store_b128(u32x4{fbits(a), tag, fbits(b), tag}, rws, (unit) * 16, 0, 16)
#define gld2(unit, cunit) __builtin_amdgcn_raw_buffer_load_b128(rws, (unit) * 16, (cunit) * 16, 16)
#define MEMBAR() asm volatile("" ::: "memory")
typedef float f32x2 __attribute__((ext_vector_type(2)));

__device__ __forceinline__ float bf_lo(unsigned u) { return __builtin_bit_cast(float, u << 16); }
__device__ __forceinline__ float bf_hi(unsigned u) { return __builtin_bit_cast(float, u & 0xffff0000u); }
__device__ __forceinline__ float bfr(float x) { return __bfloat162float(__float2bfloat16(x)); }
__device__ __forceinline__ unsigned short bfbits(float x) {
  return __builtin_bit_cast(unsigned short, __float2bfloat16(x));
}
__device__ __forceinline__ float dot2(unsigned a, unsigned b, float c) {
  return __builtin_amdgcn_fdot2_f32_bf16(__builtin_bit_cast(bf16x2_t, a), __builtin_bit_cast(bf16x2_t, b), c, false);
}
template <int CTRL>
__device__ __forceinline__ float dpp(float x) {
  return __builtin_bit_cast(float, __builtin_amdgcn_mov_dpp(__builtin_bit_cast(int, x), CTRL, 0xf, 0xf, true));
}
__device__ __forceinline__ float grp8_sum(float x) {
  x += dpp<0xB1>(x);
  x += dpp<0x4E>(x);
  x += dpp<0x141>(x);
  return x;
}
__device__ __forceinline__ float swap16_add(float a, float b) {
  auto s = __builtin_amdgcn_permlane16_swap(fbits(a), fbits(b), false, false);
  return bitsf((unsigned)s[0]) + bitsf((unsigned)s[1]);
}
__device__ __forceinline__ float swap32_add(float a, float b) {
  auto t = __builtin_amdgcn_permlane32_swap(fbits(a), fbits(b), false, false);
  return bitsf((unsigned)t[0]) + bitsf((unsigned)t[1]);
}
__device__ __forceinline__ float xg_sum(float x) {
  x += dpp<0x128>(x);
  x = swap16_add(x, x);
  return swap32_add(x, x);
}
__device__ __forceinline__ float xg_max(float x) {
  x = fmaxf(x, dpp<0x128>(x));
  auto s = __builtin_amdgcn_permlane16_swap(fbits(x), fbits(x), false, false);
  x = fmaxf(bitsf((unsigned)s[0]), bitsf((unsigned)s[1]));
  auto t = __builtin_amdgcn_permlane32_swap(fbits(x), fbits(x), false, false);
  return fmaxf(bitsf((unsigned)t[0]), bitsf((unsigned)t[1]));
}
template <int NJ>
__device__ __forceinline__ void xg_scatter(const float (&a)[8 * NJ], float (&r)[NJ], const bool b3) {
#pragma unroll
  for (int j = 0; j < NJ; ++j) {
    float p[4];
#pragma unroll
    for (int e = 0; e < 4; ++e) p[e] = swap32_add(a[8 * j + e], a[8 * j + e + 4]);
    const float q0 = swap16_add(p[0], p[2]), q1 = swap16_add(p[1], p[3]);
    r[j] = (b3 ? q1 : q0) + dpp<0x128>(b3 ? q0 : q1);
  }
}
__device__ __forceinline__ float wave_sum(float x) { return xg_sum(grp8_sum(x)); }
__device__ __forceinline__ float ex2(float x) { return __builtin_amdgcn_exp2f(x); }
__device__ __forceinline__ float silu(float x) { return x / (1.0f + __expf(-x)); }
__device__ __forceinline__ float rdlane(float x, int l) {
  return __builtin_bit_cast(float, __builtin_amdgcn_readlane(__builtin_bit_cast(int, x), l));
}

__device__ __forceinline__ u64 fx_enc(float v) {
  const bool bad = !(fabsf(v) < 512.f), small = fabsf(v) < 0x1p-9f;
  const u64 d = __builtin_bit_cast(u64, (double)(bad ? 0.f : v) + (262144.0 + 512.0));
  const unsigned hi = (unsigned)(d >> 32) + ((1u << 27) - (0x411u << 20)) + (bad ? 1u << 22 : 0u) + (small ? 1u << 17 : 0u);
  return ((u64)hi << 32) | (unsigned)d;
}
__device__ __forceinline__ float fx_dec(u64 t) {
  const unsigned hi = ((unsigned)(t >> 32) & 0x1ffffu) | (0x411u << 20);
  return (float)(__builtin_bit_cast(double, ((u64)hi << 32) | (unsigned)t) - (262144.0 + 32 * 512.0));
}
__device__ __forceinline__ bool fx_exact(u64 old, u64 add) {
  const unsigned o = (unsigned)(old >> 49) & 0x3ffu, a = (unsigned)(add >> 49) & 0x3ffu;
  return o + a < 32u;
}
#define SCHED_FENCE() __builtin_amdgcn_sched_barrier(0)
#define ALD(dst, rs, vo, so, imm) \
  asm volatile("buffer_load_dwordx4 %0, %1, %2, %3 offen offset:%4 nt" : "=v"(dst) : "v"(vo), "s"(rs), "s"(so), "n"(imm) : "memory")
#define AWL(dst, rs, vo, so, imm)                                                                                      \
  asm volatile("s_waitcnt vmcnt(8)\n\ts_cmp_le_i32 %5, 8\n\ts_cbranch_scc0 4\n\ts_waitcnt vmcnt(7)\n\t"                 \
               "s_cmp_le_i32 %5, 7\n\ts_cbranch_scc0 1\n\ts_waitcnt vmcnt(6)\n\t"                                        \
               "buffer_load_dwordx4 %0, %1, %2, %3 offen offset:%4 nt"                                                 \
               : "=v"(dst) : "v"(vo), "s"(rs), "s"(so), "n"(imm), "s"(wl) : "memory", "scc")
#define AWAIT(v, n) asm volatile("s_waitcnt vmcnt(%1)" : "+v"(v) : "n"(n) : "memory")
#define TIE(v) asm volatile("" : "+v"(v))
__device__ __forceinline__ u32x4 mkrs(const void* p, unsigned bytes) {
  const u64 a = (u64)p;
  return u32x4{(unsigned)a, (unsigned)(a >> 32) & 0xffffu, bytes, 0x00020000u};
}
#define WAVE_SYNC()                                        \
  do {                                                     \
    __builtin_amdgcn_fence(__ATOMIC_RELEASE, "wavefront"); \
    __builtin_amdgcn_wave_barrier();                       \
    __builtin_amdgcn_fence(__ATOMIC_ACQUIRE, "wavefront"); \
  } while (0)

constexpr int L_WO = 0;
constexpr int L_XS = L_WO + DH * 40;
constexpr int PS = DH + 4;
constexpr int L_PART = L_XS + 4 * 320;
constexpr int L_XN = L_PART + 8 * PS;
constexpr int L_ZP = L_XN + 4 * 64;
constexpr int L_QS = L_ZP + 6 * 80;
constexpr int L_ML = L_QS + 96;
constexpr int L_O = L_ML + 16;
constexpr int L_H = L_O + 128;
constexpr int L_GU = L_H + 48;
constexpr int L_FLG = L_GU + 96;
constexpr int L_END = L_FLG + 4;

__global__ void __launch_bounds__(NT, 2)
fused_decode(const bf16* __restrict__ x, const bf16* __restrict__ kc, const bf16* __restrict__ vc,
             const bf16* __restrict__ wq, const bf16* __restrict__ wo, const bf16* __restrict__ w1,
             const bf16* __restrict__ w3, const bf16* __restrict__ w2, u64* __restrict__ gw, bf16* __restrict__ out) {
  __shared__ __attribute__((aligned(16))) float lds[L_END];
  const int b = blockIdx.x;
  const int xcd = b & 7, jj = b >> 3;
  const int h = xcd * 4 + (jj >> 3), sp = jj & 7;
  const int ha = jj, spa = xcd;
  const int w = __builtin_amdgcn_readfirstlane(threadIdx.x >> 6);
  const int tid = threadIdx.x, lane = tid & 63;
  const int g = lane >> 3, c = lane & 7;
  const int rec = ha * NSP + spa;
  unsigned* flg = reinterpret_cast<unsigned*>(lds + L_FLG);

  asm volatile("" ::"s"(x), "s"(kc), "s"(vc), "s"(wq), "s"(wo), "s"(w1), "s"(w3), "s"(w2), "s"(gw), "s"(out));
  const auto rws = __builtin_amdgcn_make_buffer_rsrc((void*)gw, (short)0, GEND * 16, 0x00020000);
  const size_t kvw = ((size_t)ha * SK + (SK - WIN)) * DH;
  const u32x4 rk = mkrs(kc + kvw, WIN * DH * 2), rv = mkrs(vc + kvw, WIN * DH * 2);
  const u32x4 rwa = mkrs(gw, GEND * 16);
  const int kvo = (g * DH + 8 * c) * 2;
  const int dx = (int)((dx_pack() >> (4 * xcd)) & 15u) - 4;
  const int wr = w < 4 ? (w + 3) & 3 : w;
  const int nt = 8 + ((unsigned)(wr - dx) >> 31) - ((unsigned)(7 - w + dx) >> 31);
  const int don = (int)(dx_don() >> (5 * (((dx_pos() >> (4 * xcd)) + wr) & 7u))) & 31;
  const int kvb = (spa * 8 + ((w + (ha >> 3)) & 7)) * 64 * DH * 2;
  const int ko8 = (((don >> 2) * 8 + ((7 - (don & 3) + (ha >> 3)) & 7)) * 8 + ((7 + ha) & 7)) * 8 * DH * 2;
  const int wl = __builtin_amdgcn_readfirstlane((w < 4 ? WOL : WND) + (nt - 8) + (dx < 0 ? 1 : 0));
#define KVOFF(t) ((t) == 8 ? ko8 : kvb + (((t) + ha) & 7) * 8 * DH * 2)
#define KLD(t, j) ALD(K[t][j], rk, kvo, KVOFF(t), 128 * (j))
#define KWL(t, j) AWL(K[t][j], rk, kvo, KVOFF(t), 128 * (j))
#define VWL(t, j) AWL(V[t][j], rv, kvo, KVOFF(t), 128 * (j))
#define EPLD() asm volatile("buffer_load_dword %0, off, %1, %2" : "=v"(epoch) : "s"(rwa), "s"(GEP * 16) : "memory")
  u32x4 K[NF + 2][3], V[NF + 2][3];

  const int fi = (xcd & 1) ? 19 : 8;
  const bool fidle = (unsigned)(jj - fi) < 2u;
  const int fp = jj > fi ? jj - 2 : jj;
  const int fcol = fp / 5, frp = fp - 5 * fcol;
  unsigned epoch, tag;
  if (w < 4) {
    const int fw = 4 * sp + w, ct = fw >= 20 ? 2 : fw >= 10 ? 1 : 0;
    const int r0 = 64 * (fw - 10 * ct);
    const int R = 128 * frp + 32 * w;
    const u32x4 xa = *reinterpret_cast<const u32x4*>(x + 8 * lane);
    const u32x4 xb = *reinterpret_cast<const u32x4*>(x + 512 + 8 * (lane & 15));
    u32x4 WQ[8], F[4];
    {
      const auto rq = __builtin_amdgcn_make_buffer_rsrc((void*)wq, (short)0, D * HQ * DH * 2, 0x00020000);
      const int qo = ((r0 + 8 * g) * (HQ * DH) + h * DH + 64 * ct + 8 * c) * 2;
#pragma unroll
      for (int i = 0; i < 8; ++i) WQ[i] = __builtin_amdgcn_raw_buffer_load_b128(rq, qo, i * HQ * DH * 2, 2);
      const auto rf = __builtin_amdgcn_make_buffer_rsrc((void*)(fcol >= 3 ? w3 : w1), (short)0, fidle ? 0 : D * FF * 2, 0x00020000);
      const int fo = ((R + 4 * g) * FF + 192 * xcd + 64 * (fcol >= 3 ? fcol - 3 : fcol) + 8 * c) * 2;
#pragma unroll
      for (int i = 0; i < 4; ++i) F[i] = __builtin_amdgcn_raw_buffer_load_b128(rf, fo, i * FF * 2, 2);
    }
    if (fidle && tid < 40)
      __builtin_amdgcn_raw_buffer_store_b128(u32x4{0u, 0u, 0u, 0u}, rws, (GA + (jj - fi) * 320 + 40 * xcd + lane) * 16, 0, 16);
    SCHED_FENCE();
    EPLD();
    if (tid < 4) flg[tid] = 0u;
    SCHED_FENCE();
    __syncthreads();
    SCHED_FENCE();
    unsigned short* xs = reinterpret_cast<unsigned short*>(lds + L_XS + w * 320);
    const bool lo16 = lane < 16;
    *reinterpret_cast<u32x4*>(xs + 8 * lane) = xa;
    if (lo16) *reinterpret_cast<u32x4*>(xs + 512 + 8 * lane) = xb;
    float xf[8], xg[8];
#pragma unroll
    for (int i = 0; i < 4; ++i) {
      xf[2 * i] = bf_lo(xa[i]);
      xf[2 * i + 1] = bf_hi(xa[i]);
      xg[2 * i] = bf_lo(xb[i]);
      xg[2 * i + 1] = bf_hi(xb[i]);
    }
    float sum = 0.f;
#pragma unroll
    for (int i = 0; i < 8; ++i) sum += xf[i] + (lo16 ? xg[i] : 0.f);
    sum = wave_sum(sum);
    const float mean = sum * (1.0f / D);
    float sq = 0.f;
#pragma unroll
    for (int i = 0; i < 8; ++i) {
      const float d1 = xf[i] - mean, d2 = xg[i] - mean;
      sq += d1 * d1 + (lo16 ? d2 * d2 : 0.f);
    }
    sq = wave_sum(sq);
    const float mb = bfr(mean);
    const float vb = bfr(sq * (1.0f / D));
    const float den = bfr(sqrtf(bfr(vb + 1e-5f)));
    WAVE_SYNC();
    auto xnorm = [&](int r) -> unsigned short { return bfbits(bfr(bf_lo(xs[r]) - mb) / den); };
    {
      unsigned short* xn = reinterpret_cast<unsigned short*>(lds + L_XN + w * 64);
      xn[lane] = xnorm((r0 < D ? r0 : 0) + lane);
      xn[64 + (lane & 31)] = xnorm(R + (lane & 31));
      WAVE_SYNC();
      const u32x4 XQ = *reinterpret_cast<const u32x4*>(xn + 8 * g);
      const u32x2 XF = *reinterpret_cast<const u32x2*>(xn + 64 + 4 * g);
      float acc[16], r2[2];
#pragma unroll
      for (int i = 0; i < 16; ++i) acc[i] = 0.f;
#pragma unroll
      for (int i = 0; i < 8; ++i) {
        const unsigned d = XQ[i >> 1], lo = (i & 1) ? d >> 16 : d & 0xffffu, hi = (i & 1) ? d & 0xffff0000u : d << 16;
#pragma unroll
        for (int k = 0; k < 4; ++k) {
          acc[2 * k] = dot2(WQ[i][k], lo, acc[2 * k]);
          acc[2 * k + 1] = dot2(WQ[i][k], hi, acc[2 * k + 1]);
        }
      }
#pragma unroll
      for (int i = 0; i < 4; ++i) {
        const unsigned d = XF[i >> 1], lo = (i & 1) ? d >> 16 : d & 0xffffu, hi = (i & 1) ? d & 0xffff0000u : d << 16;
#pragma unroll
        for (int k = 0; k < 4; ++k) {
          acc[8 + 2 * k] = dot2(F[i][k], lo, acc[8 + 2 * k]);
          acc[8 + 2 * k + 1] = dot2(F[i][k], hi, acc[8 + 2 * k + 1]);
        }
      }
      xg_scatter<2>(acc, r2, g & 1);
#pragma unroll
      for (int j = 0; j < 2; ++j) lds[L_PART + w * 128 + 64 * j + 8 * c + g] = r2[j];
    }
    asm volatile("s_waitcnt lgkmcnt(0)" ::: "memory");
    if (lane == 0) __hip_atomic_fetch_add(flg, 1u, __ATOMIC_RELAXED, __HIP_MEMORY_SCOPE_WORKGROUP);
    SCHED_FENCE();
  } else {
#pragma unroll
    for (int m = 0; m < KE; ++m) KLD(m / 3, m % 3);
    SCHED_FENCE();
    EPLD();
    SCHED_FENCE();
    __syncthreads();
    SCHED_FENCE();
  }
  const int lz = lane < 60 ? lane : 59;
  const int zr = lz / 10, zc = lz - 10 * zr;
  int w2o = (zr * D + 8 * zc) * 2;
  asm volatile("" : "+v"(w2o));
  if (w == 0) {
    for (unsigned spin = 0; spin < (SPIN_MAX << 4); ++spin) {
      if (__hip_atomic_load(flg, __ATOMIC_RELAXED, __HIP_MEMORY_SCOPE_WORKGROUP) == 4u) break;
      __builtin_amdgcn_s_sleep(1);
    }
    asm volatile("s_waitcnt lgkmcnt(0)" ::: "memory");
    AWAIT(epoch, 0);
    tag = __builtin_amdgcn_readfirstlane(epoch) + 1u;
    const f32x2* pp = reinterpret_cast<const f32x2*>(lds + L_PART) + lane;
    const f32x2 a01 = pp[0] + pp[64], a23 = pp[128] + pp[192];
    const bool two = sp == 2 && lane < 32;
    const f32x2 sm = two ? a01 : a01 + a23;
    const int ct0 = sp >= 5 ? 2 : sp >= 3 ? 1 : 0, slot = sp - (sp >= 5 ? 5 : sp >= 3 ? 2 : 0);
    const int uf = fidle ? GZF + 32 * xcd + 16 * (jj - fi) : G1F + xcd * 960 + fp * 32;
    const int un = lane < 32 ? G1 + h * 288 + slot * 96 + ct0 * 32 + lane : uf + lane - 32;
    if (lane < 33 || !fidle) gst2(un, sm[0], sm[1]);
    if (two) gst2(G1 + h * 288 + 32 + lane, a23[0], a23[1]);
    SCHED_FENCE();
  }
  int wop = w;
  asm volatile("" : "+s"(wop));
  if (wop != 0) {
    if (w < 4) {
#pragma unroll
      for (int m = 0; m < KE; ++m) KWL(m / 3, m % 3);
    }
#pragma unroll
    for (int m = KE; m < 3 * NF; ++m) KWL(m / 3, m % 3);
    TIE(epoch);
    tag = __builtin_amdgcn_readfirstlane(epoch) + 1u;
    SCHED_FENCE();
  } else {
    u32x4 W2[8];
    const auto rw2 = __builtin_amdgcn_make_buffer_rsrc((void*)(w2 + (size_t)48 * h * D + 80 * sp), (short)0, 48 * D * 2, 0x00020000);
#pragma unroll
    for (int i = 0; i < 8; ++i) W2[i] = __builtin_amdgcn_raw_buffer_load_b128(rw2, w2o, i * 6 * D * 2, 2);
#pragma unroll
    for (int t = 0; t < NF; ++t)
#pragma unroll
      for (int j = 0; j < 3; ++j) KLD(t, j);
    SCHED_FENCE();
    const int row = lane >> 4, l16 = lane & 15;
    const int gb = G1 + ha * 288 + lane;
    const int gc = row < 3 ? G1 + ha * 288 + row * 96 + 64 + l16 : GZF + 32 * sp;
    const int l48 = lane < 48 ? lane : 47, fm = l48 >= 24 ? 1 : 0, fpr = 24 * (h & 3) + l48 - 24 * fm;
    const int gf = G1F + xcd * 960 + (3 * fm + (fpr >> 5)) * 160 + (fpr & 31);
    u32x4 va[3], vb[2], vf[5];
    for (unsigned spin = 0;; ++spin) {
      MEMBAR();
#pragma unroll
      for (int s = 0; s < 3; ++s) va[s] = gld2(gb, s * 96);
#pragma unroll
      for (int s = 0; s < 2; ++s) vb[s] = gld2(gc, s * 16);
#pragma unroll
      for (int s = 0; s < 5; ++s) vf[s] = gld2(gf, s * 32);
      bool ok = gok2(vb[0]) && gok2(vb[1]) && gok2(vf[3]) && gok2(vf[4]);
#pragma unroll
      for (int s = 0; s < 3; ++s) ok = ok && gok2(va[s]) && gok2(vf[s]);
      if (__all(ok) || spin >= SPIN_MAX) break;
      __builtin_amdgcn_s_sleep(1);
    }
    {
      const float a0 = (bitsf(va[0][0]) + bitsf(va[1][0])) + bitsf(va[2][0]), a1 = (bitsf(va[0][2]) + bitsf(va[1][2])) + bitsf(va[2][2]);
      reinterpret_cast<unsigned*>(lds + L_QS)[lane] = (unsigned)bfbits(a0) | ((unsigned)bfbits(a1) << 16);
      const float p0 = swap16_add(row < 3 ? bitsf(vb[0][0]) : 0.f, row < 3 ? bitsf(vb[0][2]) : 0.f);
      const float p1 = swap16_add(row < 3 ? bitsf(vb[1][0]) : 0.f, row < 3 ? bitsf(vb[1][2]) : 0.f);
      const float bq = swap32_add(p0, p1);
      reinterpret_cast<unsigned short*>(lds + L_QS)[2 * (64 + 16 * (row >> 1) + l16) + (row & 1)] = bfbits(bq);
    }
    asm volatile("s_waitcnt lgkmcnt(0)" ::: "memory");
    if (lane == 0) __hip_atomic_store(flg + 1, 1u, __ATOMIC_RELAXED, __HIP_MEMORY_SCOPE_WORKGROUP);
    SCHED_FENCE();
    {
      const float f0 = ((bitsf(vf[0][0]) + bitsf(vf[1][0])) + (bitsf(vf[2][0]) + bitsf(vf[3][0]))) + bitsf(vf[4][0]);
      const float f1 = ((bitsf(vf[0][2]) + bitsf(vf[1][2])) + (bitsf(vf[2][2]) + bitsf(vf[3][2]))) + bitsf(vf[4][2]);
      *reinterpret_cast<f32x2*>(lds + L_GU + 2 * l48) = f32x2{bfr(f0), bfr(f1)};
    }
    WAVE_SYNC();
    if (lane < 48) lds[L_H + lane] = bfr(bfr(silu(lds[L_GU + lane])) * lds[L_GU + 48 + lane]);
    WAVE_SYNC();
    {
      float z[8];
#pragma unroll
      for (int i = 0; i < 8; ++i) z[i] = 0.f;
#pragma unroll
      for (int i = 0; i < 8; ++i) {
        const unsigned chi = fbits(lds[L_H + zr + 6 * i]), clo = chi >> 16;
#pragma unroll
        for (int k = 0; k < 4; ++k) {
          z[2 * k] = dot2(W2[i][k], clo, z[2 * k]);
          z[2 * k + 1] = dot2(W2[i][k], chi, z[2 * k + 1]);
        }
      }
      if (lane < 60) {
        float* zp = lds + L_ZP + zr * 80 + 8 * zc;
        *reinterpret_cast<f32x4*>(zp) = f32x4{z[0], z[1], z[2], z[3]};
        *reinterpret_cast<f32x4*>(zp + 4) = f32x4{z[4], z[5], z[6], z[7]};
      }
    }
    SCHED_FENCE();
  }
  const int u = (tid & 255) < 240 ? (tid & 255) : 239;
  const int rg = u / 10, ch = u - 10 * rg;
  u32x4 WO[8];
  const u32x4 rwo = mkrs(wo + (size_t)DH * h * D + 80 * sp, DH * D * 2);
  const int wofs = (rg * D + 8 * ch) * 2;
#define WOLC(i)                                                                                                            \
  asm volatile("s_cmp_lt_u32 %4, 4\n\ts_cbranch_scc1 2\n\tbuffer_load_dwordx4 %0, %1, %2, %3 offen nt"                     \
               : "=v"(WO[i]) : "v"(wofs), "s"(rwo), "s"((i) * 24 * D * 2), "s"(w) : "memory", "scc")
#define VLD(t)                                                       \
  do {                                                               \
    _Pragma("unroll") for (int j = 0; j < 3; ++j) VWL(t, j);         \
  } while (0)
#pragma unroll
  for (int t = NF; t < NF + 2; ++t)
    if (t < nt) {
#pragma unroll
      for (int j = 0; j < 3; ++j) KWL(t, j);
      VLD(t);
    }
#pragma unroll
  for (int t = 0; t < PQ; ++t) VLD(t);
  for (unsigned spin = 0; spin < (SPIN_MAX << 4); ++spin) {
    if (__hip_atomic_load(flg + 1, __ATOMIC_RELAXED, __HIP_MEMORY_SCOPE_WORKGROUP) != 0u) break;
    __builtin_amdgcn_s_sleep(1);
  }
  asm volatile("s_waitcnt lgkmcnt(0)" ::: "memory");
  SCHED_FENCE();

  u32x4 qf[3];
#pragma unroll
  for (int j = 0; j < 3; ++j) qf[j] = *reinterpret_cast<const u32x4*>(lds + L_QS + (c + 8 * j) * 4);
  float sc[NF + 2];
#pragma unroll
  for (int t = 0; t < NF + 2; ++t) {
    sc[t] = -INFINITY;
    if (t < NF || t < nt) {
      float d = 0.f;
#pragma unroll
      for (int j = 0; j < 3; ++j) {
        TIE(K[t][j]);
#pragma unroll
        for (int i = 0; i < 4; ++i) d = dot2(K[t][j][i], qf[j][i], d);
      }
      d = grp8_sum(d);
      sc[t] = bfr(bfr(d) * SCALE);
    }
    if (t == 3) {
      SCHED_FENCE();
      VLD(PQ);
    }
  }
  float mw = sc[0];
#pragma unroll
  for (int t = 1; t < NF + 2; ++t) mw = fmaxf(mw, sc[t]);
  mw = xg_max(mw);
  if (lane == 0) {
    lds[L_ML + w] = mw;
    __hip_atomic_fetch_add(flg + 3, 1u, __ATOMIC_RELAXED, __HIP_MEMORY_SCOPE_WORKGROUP);
  }
  float lw = 0.f;
#pragma unroll
  for (int t = 0; t < NF + 2; ++t) {
    sc[t] = ex2((sc[t] - mw) * LOG2E);
    lw += sc[t];
  }
  lw = xg_sum(lw);
  SCHED_FENCE();
  float Mg;
  {
    float acc[24], r3[3];
#pragma unroll
    for (int i = 0; i < 24; ++i) acc[i] = 0.f;
#define PVT(t, nw)                                                                                                          \
  do {                                                                                                                  \
    unsigned plo = bfbits(sc[t]), phi = plo << 16;                                                                      \
    _Pragma("unroll") for (int j = 0; j < 3; ++j) {                                                                     \
      if ((t) >= NF || (t) < NF - 3) asm volatile("" : "+v"(plo), "+v"(phi));                                           \
      else asm volatile("s_waitcnt vmcnt(%2)\n\ts_cmp_lt_u32 %4, 4\n\ts_cbranch_scc0 1\n\ts_waitcnt vmcnt(%3)"            \
                        : "+v"(plo), "+v"(phi)                                                                          \
                        : "n"((NF - 1 - (t)) * 3 + 2 - j + (nw)), "n"((NF - 1 - (t)) * 3 + 2 - j), "s"(w) : "memory", "scc"); \
      _Pragma("unroll") for (int i = 0; i < 4; ++i) {                                                                   \
        acc[8 * j + 2 * i] = dot2(V[t][j][i], plo, acc[8 * j + 2 * i]);                                                 \
        acc[8 * j + 2 * i + 1] = dot2(V[t][j][i], phi, acc[8 * j + 2 * i + 1]);                                         \
      }                                                                                                                 \
    }                                                                                                                   \
    _Pragma("unroll") for (int i = 0; i < 24; i += 8)                                                                   \
      asm volatile("" : "+v"(acc[i]), "+v"(acc[i + 1]), "+v"(acc[i + 2]), "+v"(acc[i + 3]), "+v"(acc[i + 4]),           \
                   "+v"(acc[i + 5]), "+v"(acc[i + 6]), "+v"(acc[i + 7]));            \
    SCHED_FENCE();                                                                                                      \
  } while (0)
#define WGMAX()                                                                                                         \
  do {                                                                                                                  \
    for (unsigned spin = 0; spin < (SPIN_MAX << 4); ++spin) {                                                           \
      if (__hip_atomic_load(flg + 3, __ATOMIC_RELAXED, __HIP_MEMORY_SCOPE_WORKGROUP) == 8u) break;                      \
      __builtin_amdgcn_s_sleep(1);                                                                                      \
    }                                                                                                                   \
    MEMBAR();                                                                                                           \
    const f32x4 ma = *reinterpret_cast<const f32x4*>(lds + L_ML), mb2 = *reinterpret_cast<const f32x4*>(lds + L_ML + 4); \
    Mg = fmaxf(fmaxf(fmaxf(ma[0], ma[1]), fmaxf(ma[2], ma[3])), fmaxf(fmaxf(mb2[0], mb2[1]), fmaxf(mb2[2], mb2[3])));   \
    ew = ex2((mw - Mg) * LOG2E);                                                                                        \
    SCHED_FENCE();                                                                                                      \
  } while (0)
    float ew;
    VLD(PQ + 1);
    if (NF < nt) PVT(NF, 0);
    VLD(PQ + 2);
    if (NF + 1 < nt) PVT(NF + 1, 0);
    PVT(0, 0);
#pragma unroll
    for (int t = 1; t < NF; ++t) {
#pragma unroll
      for (int i = 0; i < WOB; ++i)
        if ((t == NF - 1) ? (i >= t - 1) : (i == t - 1)) WOLC(i);
      if (t == NF - 2) WGMAX();
      PVT(t, t == NF - 1 ? WOB : t);
    }
    SCHED_FENCE();
    xg_scatter<3>(acc, r3, g & 1);
#pragma unroll
    for (int j = 0; j < 3; ++j) lds[L_PART + w * PS + 64 * j + 8 * c + g] = r3[j] * ew;
    if (lane == 0) lds[L_PART + w * PS + DH] = lw * ew;
  }
  __syncthreads();
#define SLOWSUM(res, base, cc)                                                                                  \
  do {                                                                                                          \
    res = 0.f;                                                                                                  \
    _Pragma("unroll 1") for (int hh = 0; hh < HQ; ++hh) {                                                       \
      u32x2 v;                                                                                                  \
      for (unsigned spin = 0; spin < SPIN_MAX; ++spin) {                                                        \
        MEMBAR();                                                                                               \
        v = __builtin_amdgcn_raw_buffer_load_b64(rws, (cc) * 8, ((base) + hh * 320) * 16, 16);                  \
        if (v[1] == tag) break;                                                                                 \
      }                                                                                                         \
      res += bitsf(v[0]);                                                                                       \
    }                                                                                                           \
  } while (0)
  if (__builtin_expect(w < 2, 1)) {
    if (tid < 97) {
      f32x2 p2[8];
#pragma unroll
      for (int ww = 0; ww < 8; ++ww) p2[ww] = *reinterpret_cast<const f32x2*>(lds + L_PART + ww * PS + 2 * tid);
      const f32x2 us = ((p2[0] + p2[1]) + (p2[2] + p2[3])) + ((p2[4] + p2[5]) + (p2[6] + p2[7]));
      gst2(G2 + rec * 104 + tid, tid < 96 ? us[0] : Mg, tid < 96 ? us[1] : us[0]);
    }
    const int t2 = tid < 96 ? tid : 95;
    const int pb = G2 + h * NSP * 104 + t2, pm = G2 + (h * NSP + c) * 104 + 96;
    u32x4 va[NSP], vml;
    if (PD1) __builtin_amdgcn_s_sleep(PD1);
    for (unsigned spin = 0;; ++spin) {
      MEMBAR();
#pragma unroll
      for (int s = 0; s < NSP; ++s) va[s] = gld2(pb, s * 104);
      vml = gld2(pm, 0);
      bool ok = gok2(vml);
#pragma unroll
      for (int s = 0; s < NSP; ++s) ok = ok && gok2(va[s]);
      if (__all(ok) || spin >= SPIN_MAX) break;
      __builtin_amdgcn_s_sleep(1);
    }
    const float ms = bitsf(vml[0]);
    float M2 = fmaxf(ms, dpp<0xB1>(ms));
    M2 = fmaxf(M2, dpp<0x4E>(M2));
    M2 = fmaxf(M2, dpp<0x141>(M2));
    const float es = ex2((ms - M2) * LOG2E);
    const float L2 = grp8_sum(es * bitsf(vml[2]));
    float o0 = 0.f, o1 = 0.f;
#pragma unroll
    for (int s = 0; s < NSP; ++s) {
      const float e = rdlane(es, s);
      o0 = fmaf(e, bitsf(va[s][0]), o0);
      o1 = fmaf(e, bitsf(va[s][2]), o1);
    }
    if (tid < 96) {
      unsigned short* po = reinterpret_cast<unsigned short*>(lds + L_O) + ((2 * tid & 31) * 4 + (tid >> 5)) * 2 + (tid >> 4 & 1);
      const float rl = __builtin_amdgcn_rcpf(L2);
      po[0] = bfbits(o0 * rl);
      po[8] = bfbits(o1 * rl);
    }
    __syncthreads();
  } else if (w == 2) {
    u64 add[2], old[2];
#pragma unroll
    for (int k = 1; k >= 0; --k) {
      const float* zp = lds + L_ZP + (k ? 64 + (lane & 15) : lane);
      add[k] = fx_enc(((zp[0] + zp[80]) + (zp[160] + zp[240])) + (zp[320] + zp[400]));
      old[k] = 0ull;
      if (k == 0 || lane < 16)
        old[k] = __hip_atomic_fetch_add(gw + 2 * GA + D + 80 * sp + 64 * k + lane, add[k], __ATOMIC_RELAXED, __HIP_MEMORY_SCOPE_AGENT);
    }
    {
      const f32x2* zp = reinterpret_cast<const f32x2*>(lds + L_ZP) + (lane < 40 ? lane : 0);
      const f32x2 zs = ((zp[0] + zp[40]) + (zp[80] + zp[120])) + (zp[160] + zp[200]);
      gst2(lane < 40 ? G3Z + h * 320 + 40 * sp + lane : GEND, zs[0], zs[1]);
    }
#pragma unroll
    for (int k = 0; k < 2; ++k)
      if ((old[k] >> 59) == 31u) {
        const int cz = 80 * sp + 64 * k + lane;
        float f = fx_dec(old[k] + add[k]);
        if (__builtin_expect(!fx_exact(old[k], add[k]), 0)) SLOWSUM(f, G3Z, cz);
        __builtin_amdgcn_raw_buffer_store_b64(u32x2{fbits(bfr(silu(bfr(f)))), tag}, rws, cz * 8, GG * 16, 16);
      }
    __syncthreads();
  } else {
    unsigned wp[24];
    if (w >= 4) {
#pragma unroll
      for (int i = 0; i < 8; ++i) {
        AWAIT(WO[i], 7 - i);
        *reinterpret_cast<u32x4*>(lds + L_WO + u * 4 + i * 960) = WO[i];
      }
      asm volatile("s_waitcnt lgkmcnt(0)" ::: "memory");
      if (lane == 0) __hip_atomic_fetch_add(flg + 2, 1u, __ATOMIC_RELAXED, __HIP_MEMORY_SCOPE_WORKGROUP);
    }
    for (unsigned spin = 0; spin < (SPIN_MAX << 4); ++spin) {
      if (__hip_atomic_load(flg + 2, __ATOMIC_RELAXED, __HIP_MEMORY_SCOPE_WORKGROUP) == 4u) break;
      __builtin_amdgcn_s_sleep(1);
    }
    asm volatile("s_waitcnt lgkmcnt(0)" ::: "memory");
    const float* wsl = lds + L_WO + (lane >> 1) * 40 + (2 * (w - 3) + (lane & 1)) * 4;
#pragma unroll
    for (int k = 0; k < 3; ++k) {
      const u32x4 A = *reinterpret_cast<const u32x4*>(wsl + 64 * k * 40), B = *reinterpret_cast<const u32x4*>(wsl + (64 * k + 32) * 40);
#pragma unroll
      for (int q = 0; q < 4; ++q) {
        wp[8 * k + 2 * q] = __builtin_amdgcn_perm(B[q], A[q], 0x05040100u);
        wp[8 * k + 2 * q + 1] = __builtin_amdgcn_perm(B[q], A[q], 0x07060302u);
      }
    }
#pragma unroll
    for (int i = 0; i < 24; i += 8)
      asm volatile("" : "+v"(wp[i]), "+v"(wp[i + 1]), "+v"(wp[i + 2]), "+v"(wp[i + 3]), "+v"(wp[i + 4]), "+v"(wp[i + 5]), "+v"(wp[i + 6]), "+v"(wp[i + 7]));
    const int cy = 80 * sp + 16 * (w - 3) + 8 * (lane & 1) + 2 * (lane >> 4) + ((lane >> 1) & 1);
    unsigned xv = reinterpret_cast<const unsigned short*>(lds + L_XS)[cy];
    gu64* pa = (gu64*)(gw + 2 * GA + cy);
    int lo4 = lane >> 1;
    asm volatile("" : "+v"(xv), "+v"(pa), "+v"(lo4));
    __syncthreads();
    const u32x4 op = reinterpret_cast<const u32x4*>(lds + L_O)[lo4];
    u32x2 gg = __builtin_amdgcn_raw_buffer_load_b64(rws, cy * 8, GG * 16, 16);
    SCHED_FENCE();
    float y[8];
#pragma unroll
    for (int e = 0; e < 8; ++e) y[e] = dot2(wp[16 + e], op[2], dot2(wp[8 + e], op[1], dot2(wp[e], op[0], 0.f)));
    float p[4];
#pragma unroll
    for (int e = 0; e < 4; ++e) p[e] = swap32_add(y[e], y[e + 4]);
    const float q0 = swap16_add(p[0], p[2]), q1 = swap16_add(p[1], p[3]);
    float q = ((lane & 2) ? q1 : q0) + dpp<0x4E>((lane & 2) ? q0 : q1);
    q += dpp<0x128>(q);
    q += dpp<0x124>(q);
    const u64 add = fx_enc(q);
    if ((lane & 12) == 0) {
      const u64 old = __hip_atomic_fetch_add(pa, add, __ATOMIC_RELAXED, __HIP_MEMORY_SCOPE_AGENT);
      __builtin_amdgcn_raw_buffer_store_b64(u32x2{fbits(q), tag}, rws, cy * 8, (G3Y + h * 320) * 16, 16);
      SCHED_FENCE();
      if ((old >> 59) == 31u) {
        float sv = fx_dec(old + add);
        if (__builtin_expect(!fx_exact(old, add), 0)) SLOWSUM(sv, G3Y, cy);
        for (unsigned spin = 0; gg[1] != tag && spin < SPIN_MAX; ++spin) {
          MEMBAR();
          gg = __builtin_amdgcn_raw_buffer_load_b64(rws, cy * 8, GG * 16, 16);
        }
        reinterpret_cast<unsigned short*>(out)[cy] = bfbits(bf_lo(xv) + bfr(bfr(sv) * bitsf(gg[0])));
        if (cy == 0)
          __hip_atomic_store(reinterpret_cast<unsigned*>(gw + 2 * GEP), tag, __ATOMIC_RELAXED, __HIP_MEMORY_SCOPE_AGENT);
      }
    }
  }
}

}

extern "C" void kernel_launch(void* const* d_in, const int* in_sizes, int n_in, void* d_out, int out_size, void* d_ws,
                              size_t ws_size, hipStream_t stream) {
  const bf16* x = (const bf16*)d_in[0];
  const bf16* kc = (const bf16*)d_in[1];
  const bf16* vc = (const bf16*)d_in[2];
  const bf16* wq = (const bf16*)d_in[3];
  const bf16* wo = (const bf16*)d_in[4];
  const bf16* w1 = (const bf16*)d_in[6];
  const bf16* w3 = (const bf16*)d_in[7];
  const bf16* w2 = (const bf16*)d_in[8];
  u64* gw = (u64*)d_ws;
  fused_decode<<<NWG, NT, 0, stream>>>(x, kc, vc, wq, wo, w1, w3, w2, gw, (bf16*)d_out);
}
```

```cpp
#include <hip/hip_runtime.h>
#include <hip/hip_bf16.h>
#include <stdint.h>

typedef __hip_bfloat16 bf16;
typedef unsigned long long u64;
typedef __attribute__((address_space(1))) unsigned long long gu64;
typedef __attribute__((address_space(1))) unsigned short gu16;
typedef unsigned u32x4 __attribute__((ext_vector_type(4)));
typedef unsigned u32x2 __attribute__((ext_vector_type(2)));
typedef float f32x4 __attribute__((ext_vector_type(4)));
typedef __bf16 bf16x2_t __attribute__((ext_vector_type(2)));
typedef __bf16 bf16x8_t __attribute__((ext_vector_type(8)));
typedef short v4i16_t __attribute__((ext_vector_type(4)));

namespace {

constexpr int D = 640, HQ = 32, DH = 192, SK = 32768, WIN = 4096, FF = 1536;
constexpr int NSP = 8;
constexpr int NWG = HQ * NSP;
constexpr int NT = 512;
constexpr float SCALE = 0.07216878364870322f;
constexpr float LOG2E = 1.4426950408889634f;
constexpr unsigned SPIN_MAX = 1u << 16;
constexpr int WND = 8;
constexpr int WOL = 7;
constexpr int PQ = 4;
constexpr int NF = 7;
constexpr int DX[8] = {2, -1, 2, -1, 1, -3, 1, -1};
constexpr unsigned dx_pack() {
  unsigned r = 0;
  for (int i = 0; i < 8; ++i) r |= (unsigned)(DX[i] + 4) << (4 * i);
  return r;
}
constexpr unsigned dx_pos() {
  unsigned r = 0, a = 0;
  for (int i = 0; i < 8; ++i) { r |= a << (4 * i); a += DX[i] > 0 ? DX[i] : 0; }
  return r;
}
constexpr unsigned long long dx_don() {
  unsigned long long r = 0;
  int k = 0;
  for (int i = 0; i < 8; ++i)
    for (int q = 0; q < -DX[i]; ++q) r |= (unsigned long long)(i * 4 + q) << (5 * k++);
  return r;
}
constexpr bool dx_ok() {
  int a = 0, p = 0;
  bool in = true;
  for (int i = 0; i < 8; ++i) { a += DX[i]; p += DX[i] > 0 ? DX[i] : 0; in = in && DX[i] >= -4 && DX[i] <= 4; }
  return a == 0 && p <= 8 && in;
}
constexpr int dx_check[dx_ok() ? 1 : -1] = {0};
constexpr int KE = 8;
constexpr int PD1 = 14;
constexpr int WOB = 8;
constexpr int ZW = 3;

constexpr int G1 = 0;
constexpr int G1F = G1 + HQ * 288;
constexpr int G2 = G1F + 8 * 960;
constexpr int G3Y = G2 + NWG * 104;
constexpr int G3Z = G3Y + 320 * 32;
constexpr int GG = G3Z + 320 * 32;
constexpr int GZF = GG + 320;
constexpr int GA = GZF + 256;
constexpr int GEP = GA + 640;
constexpr int GEND = GEP + 8;

__device__ __forceinline__ unsigned fbits(float x) { return __builtin_bit_cast(unsigned, x); }
__device__ __forceinline__ float bitsf(unsigned x) { return __builtin_bit_cast(float, x); }
#define gok2(v) ((v)[1] == tag && (v)[3] == tag)
#define gst2(unit, a, b) __builtin_amdgcn_raw_buffer_store_b128(u32x4{fbits(a), tag, fbits(b), tag}, rws, (unit) * 16, 0, 16)
#define gld2(unit, cunit) __builtin_amdgcn_raw_buffer_load_b128(rws, (unit) * 16, (cunit) * 16, 16)
#define MEMBAR() asm volatile("" ::: "memory")
typedef float f32x2 __attribute__((ext_vector_type(2)));

__device__ __forceinline__ float bf_lo(unsigned u) { return __builtin_bit_cast(float, u << 16); }
__device__ __forceinline__ float bf_hi(unsigned u) { return __builtin_bit_cast(float, u & 0xffff0000u); }
__device__ __forceinline__ float bfr(float x) { return __bfloat162float(__float2bfloat16(x)); }
__device__ __forceinline__ unsigned short bfbits(float x) {
  return __builtin_bit_cast(unsigned short, __float2bfloat16(x));
}
__device__ __forceinline__ float dot2(unsigned a, unsigned b, float c) {
  return __builtin_amdgcn_fdot2_f32_bf16(__builtin_bit_cast(bf16x2_t, a), __builtin_bit_cast(bf16x2_t, b), c, false);
}
template <int CTRL>
__device__ __forceinline__ float dpp(float x) {
  return __builtin_bit_cast(float, __builtin_amdgcn_mov_dpp(__builtin_bit_cast(int, x), CTRL, 0xf, 0xf, true));
}
__device__ __forceinline__ float grp8_sum(float x) {
  x += dpp<0xB1>(x);
  x += dpp<0x4E>(x);
  x += dpp<0x141>(x);
  return x;
}
__device__ __forceinline__ float swap16_add(float a, float b) {
  auto s = __builtin_amdgcn_permlane16_swap(fbits(a), fbits(b), false, false);
  return bitsf((unsigned)s[0]) + bitsf((unsigned)s[1]);
}
__device__ __forceinline__ float swap32_add(float a, float b) {
  auto t = __builtin_amdgcn_permlane32_swap(fbits(a), fbits(b), false, false);
  return bitsf((unsigned)t[0]) + bitsf((unsigned)t[1]);
}
__device__ __forceinline__ float xg_sum(float x) {
  x += dpp<0x128>(x);
  x = swap16_add(x, x);
  return swap32_add(x, x);
}
__device__ __forceinline__ float xg_max(float x) {
  x = fmaxf(x, dpp<0x128>(x));
  auto s = __builtin_amdgcn_permlane16_swap(fbits(x), fbits(x), false, false);
  x = fmaxf(bitsf((unsigned)s[0]), bitsf((unsigned)s[1]));
  auto t = __builtin_amdgcn_permlane32_swap(fbits(x), fbits(x), false, false);
  return fmaxf(bitsf((unsigned)t[0]), bitsf((unsigned)t[1]));
}
template <int NJ>
__device__ __forceinline__ void xg_scatter(const float (&a)[8 * NJ], float (&r)[NJ], const bool b3) {
#pragma unroll
  for (int j = 0; j < NJ; ++j) {
    float p[4];
#pragma unroll
    for (int e = 0; e < 4; ++e) p[e] = swap32_add(a[8 * j + e], a[8 * j + e + 4]);
    const float q0 = swap16_add(p[0], p[2]), q1 = swap16_add(p[1], p[3]);
    r[j] = (b3 ? q1 : q0) + dpp<0x128>(b3 ? q0 : q1);
  }
}
__device__ __forceinline__ float wave_sum(float x) { return xg_sum(grp8_sum(x)); }
__device__ __forceinline__ float ex2(float x) { return __builtin_amdgcn_exp2f(x); }
__device__ __forceinline__ float silu(float x) { return x / (1.0f + __expf(-x)); }
__device__ __forceinline__ float rdlane(float x, int l) {
  return __builtin_bit_cast(float, __builtin_amdgcn_readlane(__builtin_bit_cast(int, x), l));
}

__device__ __forceinline__ u64 fx_enc(float v) {
  const bool bad = !(fabsf(v) < 512.f), small = fabsf(v) < 0x1p-9f;
  const u64 d = __builtin_bit_cast(u64, (double)(bad ? 0.f : v) + (262144.0 + 512.0));
  const unsigned hi = (unsigned)(d >> 32) + ((1u << 27) - (0x411u << 20)) + (bad ? 1u << 22 : 0u) + (small ? 1u << 17 : 0u);
  return ((u64)hi << 32) | (unsigned)d;
}
__device__ __forceinline__ float fx_dec(u64 t) {
  const unsigned hi = ((unsigned)(t >> 32) & 0x1ffffu) | (0x411u << 20);
  return (float)(__builtin_bit_cast(double, ((u64)hi << 32) | (unsigned)t) - (262144.0 + 32 * 512.0));
}
__device__ __forceinline__ bool fx_exact(u64 old, u64 add) {
  const unsigned o = (unsigned)(old >> 49) & 0x3ffu, a = (unsigned)(add >> 49) & 0x3ffu;
  return o + a < 32u;
}
#define SCHED_FENCE() __builtin_amdgcn_sched_barrier(0)
#define ALD(dst, rs, vo, so, imm) \
  asm volatile("buffer_load_dwordx4 %0, %1, %2, %3 offen offset:%4 nt" : "=v"(dst) : "v"(vo), "s"(rs), "s"(so), "n"(imm) : "memory")
#define AWL(dst, rs, vo, so, imm)                                                                                      \
  asm volatile("s_waitcnt vmcnt(8)\n\ts_cmp_le_i32 %5, 8\n\ts_cbranch_scc0 4\n\ts_waitcnt vmcnt(7)\n\t"                 \
               "s_cmp_le_i32 %5, 7\n\ts_cbranch_scc0 1\n\ts_waitcnt vmcnt(6)\n\t"                                        \
               "buffer_load_dwordx4 %0, %1, %2, %3 offen offset:%4 nt"                                                 \
               : "=v"(dst) : "v"(vo), "s"(rs), "s"(so), "n"(imm), "s"(wl) : "memory", "scc")
#define AWAIT(v, n) asm volatile("s_waitcnt vmcnt(%1)" : "+v"(v) : "n"(n) : "memory")
#define TIE(v) asm volatile("" : "+v"(v))
__device__ __forceinline__ u32x4 mkrs(const void* p, unsigned bytes) {
  const u64 a = (u64)p;
  return u32x4{(unsigned)a, (unsigned)(a >> 32) & 0xffffu, bytes, 0x00020000u};
}
#define WAVE_SYNC()                                        \
  do {                                                     \
    __builtin_amdgcn_fence(__ATOMIC_RELEASE, "wavefront"); \
    __builtin_amdgcn_wave_barrier();                       \
    __builtin_amdgcn_fence(__ATOMIC_ACQUIRE, "wavefront"); \
  } while (0)

constexpr int L_WO = 0;
constexpr int L_XS = L_WO + DH * 40;
constexpr int PS = 256;
constexpr int L_PART = L_XS + 4 * 320;
constexpr int L_XN = L_PART + 8 * PS;
constexpr int L_ZP = L_XN + 4 * 64;
constexpr int L_QS = L_ZP + 6 * 80;
constexpr int L_ML = L_QS + 96;
constexpr int L_O = L_ML + 16;
constexpr int L_H = L_O + 128;
constexpr int L_GU = L_H + 48;
constexpr int L_GT = L_GU + 96;
constexpr int L_FLG = L_GT + 168 * 4;
constexpr int L_END = L_FLG + 4;

__global__ void __launch_bounds__(NT, 2)
fused_decode(const bf16* __restrict__ x, const bf16* __restrict__ kc, const bf16* __restrict__ vc,
             const bf16* __restrict__ wq, const bf16* __restrict__ wo, const bf16* __restrict__ w1,
             const bf16* __restrict__ w3, const bf16* __restrict__ w2, u64* __restrict__ gw, bf16* __restrict__ out) {
  __shared__ __attribute__((aligned(16))) float lds[L_END];
  const int b = blockIdx.x;
  const int xcd = b & 7, jj = b >> 3;
  const int h = xcd * 4 + (jj >> 3), sp = jj & 7;
  const int ha = jj, spa = xcd;
  const int w = __builtin_amdgcn_readfirstlane(threadIdx.x >> 6);
  const int tid = threadIdx.x, lane = tid & 63;
  const int g = lane >> 3, c = lane & 7;
  const int rec = ha * NSP + spa;
  unsigned* flg = reinterpret_cast<unsigned*>(lds + L_FLG);

  asm volatile("" ::"s"(x), "s"(kc), "s"(vc), "s"(wq), "s"(wo), "s"(w1), "s"(w3), "s"(w2), "s"(gw), "s"(out));
  const auto rws = __builtin_amdgcn_make_buffer_rsrc((void*)gw, (short)0, GEND * 16, 0x00020000);
  const size_t kvw = ((size_t)ha * SK + (SK - WIN)) * DH;
  const u32x4 rk = mkrs(kc + kvw, WIN * DH * 2), rv = mkrs(vc + kvw, WIN * DH * 2);
  const u32x4 rwa = mkrs(gw, GEND * 16);
  const int kvo = (g * DH + 8 * c) * 2;
  const int dx = (int)((dx_pack() >> (4 * xcd)) & 15u) - 4;
  const int wr = w < 4 ? (w + 3) & 3 : w;
  const int nt = 8 + ((unsigned)(wr - dx) >> 31) - ((unsigned)(7 - w + dx) >> 31);
  const int don = (int)(dx_don() >> (5 * (((dx_pos() >> (4 * xcd)) + wr) & 7u))) & 31;
  const int kvb = (spa * 8 + ((w + (ha >> 3)) & 7)) * 64 * DH * 2;
  const int ko8 = (((don >> 2) * 8 + ((7 - (don & 3) + (ha >> 3)) & 7)) * 8 + ((7 + ha) & 7)) * 8 * DH * 2;
  const int wl = __builtin_amdgcn_readfirstlane((w < 4 ? WOL : WND) + (nt - 8) + (dx < 0 ? 1 : 0));
#define KVOFF(t) ((t) == 8 ? ko8 : kvb + (((t) + ha) & 7) * 8 * DH * 2)
#define KLD(t, j) ALD(K[t][j], rk, kvo, KVOFF(t), 128 * (j))
#define KWL(t, j) AWL(K[t][j], rk, kvo, KVOFF(t), 128 * (j))
#define VWL(t, j) AWL(V[t][j], rv, kvo, KVOFF(t), 128 * (j))
#define EPLD() asm volatile("buffer_load_dword %0, off, %1, %2" : "=v"(epoch) : "s"(rwa), "s"(GEP * 16) : "memory")
  u32x4 K[NF + 2][3], V[NF + 2][3];

  const int fi = (xcd & 1) ? 19 : 8;
  const bool fidle = (unsigned)(jj - fi) < 2u;
  const int fp = jj > fi ? jj - 2 : jj;
  const int fcol = fp / 5, frp = fp - 5 * fcol;
  unsigned epoch, tag;
  if (w < 4) {
    const int fw = 4 * sp + w, ct = fw >= 20 ? 2 : fw >= 10 ? 1 : 0;
    const int r0 = 64 * (fw - 10 * ct);
    const int R = 128 * frp + 32 * w;
    const u32x4 xa = *reinterpret_cast<const u32x4*>(x + 8 * lane);
    const u32x4 xb = *reinterpret_cast<const u32x4*>(x + 512 + 8 * (lane & 15));
    u32x4 WQ[8], F[4];
    {
      const auto rq = __builtin_amdgcn_make_buffer_rsrc((void*)wq, (short)0, D * HQ * DH * 2, 0x00020000);
      const int qo = ((r0 + 8 * g) * (HQ * DH) + h * DH + 64 * ct + 8 * c) * 2;
#pragma unroll
      for (int i = 0; i < 8; ++i) WQ[i] = __builtin_amdgcn_raw_buffer_load_b128(rq, qo, i * HQ * DH * 2, 2);
      const auto rf = __builtin_amdgcn_make_buffer_rsrc((void*)(fcol >= 3 ? w3 : w1), (short)0, fidle ? 0 : D * FF * 2, 0x00020000);
      const int fo = ((R + 4 * g) * FF + 192 * xcd + 64 * (fcol >= 3 ? fcol - 3 : fcol) + 8 * c) * 2;
#pragma unroll
      for (int i = 0; i < 4; ++i) F[i] = __builtin_amdgcn_raw_buffer_load_b128(rf, fo, i * FF * 2, 2);
    }
    if (fidle && tid < 40)
      __builtin_amdgcn_raw_buffer_store_b128(u32x4{0u, 0u, 0u, 0u}, rws, (GA + (jj - fi) * 320 + 40 * xcd + lane) * 16, 0, 16);
    SCHED_FENCE();
    EPLD();
    if (tid < 4) flg[tid] = 0u;
    SCHED_FENCE();
    __syncthreads();
    SCHED_FENCE();
    unsigned short* xs = reinterpret_cast<unsigned short*>(lds + L_XS + w * 320);
    const bool lo16 = lane < 16;
    *reinterpret_cast<u32x4*>(xs + 8 * lane) = xa;
    if (lo16) *reinterpret_cast<u32x4*>(xs + 512 + 8 * lane) = xb;
    float xf[8], xg[8];
#pragma unroll
    for (int i = 0; i < 4; ++i) {
      xf[2 * i] = bf_lo(xa[i]);
      xf[2 * i + 1] = bf_hi(xa[i]);
      xg[2 * i] = bf_lo(xb[i]);
      xg[2 * i + 1] = bf_hi(xb[i]);
    }
    float sum = 0.f;
#pragma unroll
    for (int i = 0; i < 8; ++i) sum += xf[i] + (lo16 ? xg[i] : 0.f);
    sum = wave_sum(sum);
    const float mean = sum * (1.0f / D);
    float sq = 0.f;
#pragma unroll
    for (int i = 0; i < 8; ++i) {
      const float d1 = xf[i] - mean, d2 = xg[i] - mean;
      sq += d1 * d1 + (lo16 ? d2 * d2 : 0.f);
    }
    sq = wave_sum(sq);
    const float mb = bfr(mean);
    const float vb = bfr(sq * (1.0f / D));
    const float den = bfr(sqrtf(bfr(vb + 1e-5f)));
    WAVE_SYNC();
    auto xnorm = [&](int r) -> unsigned short { return bfbits(bfr(bf_lo(xs[r]) - mb) / den); };
    {
      unsigned short* xn = reinterpret_cast<unsigned short*>(lds + L_XN + w * 64);
      xn[lane] = xnorm((r0 < D ? r0 : 0) + lane);
      xn[64 + (lane & 31)] = xnorm(R + (lane & 31));
      WAVE_SYNC();
      const u32x4 XQ = *reinterpret_cast<const u32x4*>(xn + 8 * g);
      const u32x2 XF = *reinterpret_cast<const u32x2*>(xn + 64 + 4 * g);
      float acc[16], r2[2];
#pragma unroll
      for (int i = 0; i < 16; ++i) acc[i] = 0.f;
#pragma unroll
      for (int i = 0; i < 8; ++i) {
        const unsigned d = XQ[i >> 1], lo = (i & 1) ? d >> 16 : d & 0xffffu, hi = (i & 1) ? d & 0xffff0000u : d << 16;
#pragma unroll
        for (int k = 0; k < 4; ++k) {
          acc[2 * k] = dot2(WQ[i][k], lo, acc[2 * k]);
          acc[2 * k + 1] = dot2(WQ[i][k], hi, acc[2 * k + 1]);
        }
      }
#pragma unroll
      for (int i = 0; i < 4; ++i) {
        const unsigned d = XF[i >> 1], lo = (i & 1) ? d >> 16 : d & 0xffffu, hi = (i & 1) ? d & 0xffff0000u : d << 16;
#pragma unroll
        for (int k = 0; k < 4; ++k) {
          acc[8 + 2 * k] = dot2(F[i][k], lo, acc[8 + 2 * k]);
          acc[8 + 2 * k + 1] = dot2(F[i][k], hi, acc[8 + 2 * k + 1]);
        }
      }
      xg_scatter<2>(acc, r2, g & 1);
#pragma unroll
      for (int j = 0; j < 2; ++j) lds[L_PART + w * 128 + 64 * j + 8 * c + g] = r2[j];
    }
    asm volatile("s_waitcnt lgkmcnt(0)" ::: "memory");
    if (lane == 0) __hip_atomic_fetch_add(flg, 1u, __ATOMIC_RELAXED, __HIP_MEMORY_SCOPE_WORKGROUP);
    SCHED_FENCE();
  } else {
#pragma unroll
    for (int m = 0; m < KE; ++m) KLD(m / 3, m % 3);
    SCHED_FENCE();
    EPLD();
    SCHED_FENCE();
    __syncthreads();
    SCHED_FENCE();
  }
  const int lz = lane < 60 ? lane : 59;
  const int zr = lz / 10, zc = lz - 10 * zr;
  int w2o = (zr * D + 8 * zc) * 2;
  asm volatile("" : "+v"(w2o));
  if (w == 0) {
    for (unsigned spin = 0; spin < (SPIN_MAX << 4); ++spin) {
      if (__hip_atomic_load(flg, __ATOMIC_RELAXED, __HIP_MEMORY_SCOPE_WORKGROUP) == 4u) break;
      __builtin_amdgcn_s_sleep(1);
    }
    asm volatile("s_waitcnt lgkmcnt(0)" ::: "memory");
    AWAIT(epoch, 0);
    tag = __builtin_amdgcn_readfirstlane(epoch) + 1u;
    const f32x2* pp = reinterpret_cast<const f32x2*>(lds + L_PART) + lane;
    const f32x2 a01 = pp[0] + pp[64], a23 = pp[128] + pp[192];
    const bool two = sp == 2 && lane < 32;
    const f32x2 sm = two ? a01 : a01 + a23;
    const int ct0 = sp >= 5 ? 2 : sp >= 3 ? 1 : 0, slot = sp - (sp >= 5 ? 5 : sp >= 3 ? 2 : 0);
    const int uf = fidle ? GZF + 32 * xcd + 16 * (jj - fi) : G1F + xcd * 960 + fp * 32;
    const int un = lane < 32 ? G1 + h * 288 + slot * 96 + ct0 * 32 + lane : uf + lane - 32;
    if (lane < 33 || !fidle) gst2(un, sm[0], sm[1]);
    if (two) gst2(G1 + h * 288 + 32 + lane, a23[0], a23[1]);
    SCHED_FENCE();
  }
  int wop = w;
  asm volatile("" : "+s"(wop));
  if (wop != 0) {
    if (w < 4) {
#pragma unroll
      for (int m = 0; m < KE; ++m) KWL(m / 3, m % 3);
    }
#pragma unroll
    for (int m = KE; m < 3 * NF; ++m) KWL(m / 3, m % 3);
    TIE(epoch);
    tag = __builtin_amdgcn_readfirstlane(epoch) + 1u;
    SCHED_FENCE();
  } else {
    u32x4 W2[8];
    const auto rw2 = __builtin_amdgcn_make_buffer_rsrc((void*)(w2 + (size_t)48 * h * D + 80 * sp), (short)0, 48 * D * 2, 0x00020000);
#pragma unroll
    for (int i = 0; i < 8; ++i) W2[i] = __builtin_amdgcn_raw_buffer_load_b128(rw2, w2o, i * 6 * D * 2, 2);
#pragma unroll
    for (int t = 0; t < NF; ++t)
#pragma unroll
      for (int j = 0; j < 3; ++j) KLD(t, j);
    SCHED_FENCE();
    const int row = lane >> 4, l16 = lane & 15;
    const int gb = G1 + ha * 288 + lane;
    const int gc = row < 3 ? G1 + ha * 288 + row * 96 + 64 + l16 : GZF + 32 * sp;
    const int l48 = lane < 48 ? lane : 47, fm = l48 >= 24 ? 1 : 0, fpr = 24 * (h & 3) + l48 - 24 * fm;
    const int gf = G1F + xcd * 960 + (3 * fm + (fpr >> 5)) * 160 + (fpr & 31);
    u32x4 va[3], vb[2], vf[5];
    for (unsigned spin = 0;; ++spin) {
      MEMBAR();
#pragma unroll
      for (int s = 0; s < 3; ++s) va[s] = gld2(gb, s * 96);
#pragma unroll
      for (int s = 0; s < 2; ++s) vb[s] = gld2(gc, s * 16);
#pragma unroll
      for (int s = 0; s < 5; ++s) vf[s] = gld2(gf, s * 32);
      bool ok = gok2(vb[0]) && gok2(vb[1]) && gok2(vf[3]) && gok2(vf[4]);
#pragma unroll
      for (int s = 0; s < 3; ++s) ok = ok && gok2(va[s]) && gok2(vf[s]);
      if (__all(ok) || spin >= SPIN_MAX) break;
      __builtin_amdgcn_s_sleep(1);
    }
    {
      const float a0 = (bitsf(va[0][0]) + bitsf(va[1][0])) + bitsf(va[2][0]), a1 = (bitsf(va[0][2]) + bitsf(va[1][2])) + bitsf(va[2][2]);
      reinterpret_cast<unsigned*>(lds + L_QS)[lane] = (unsigned)bfbits(a0) | ((unsigned)bfbits(a1) << 16);
      const float p0 = swap16_add(row < 3 ? bitsf(vb[0][0]) : 0.f, row < 3 ? bitsf(vb[0][2]) : 0.f);
      const float p1 = swap16_add(row < 3 ? bitsf(vb[1][0]) : 0.f, row < 3 ? bitsf(vb[1][2]) : 0.f);
      const float bq = swap32_add(p0, p1);
      reinterpret_cast<unsigned short*>(lds + L_QS)[2 * (64 + 16 * (row >> 1) + l16) + (row & 1)] = bfbits(bq);
    }
    asm volatile("s_waitcnt lgkmcnt(0)" ::: "memory");
    if (lane == 0) __hip_atomic_store(flg + 1, 1u, __ATOMIC_RELAXED, __HIP_MEMORY_SCOPE_WORKGROUP);
    SCHED_FENCE();
    {
      const float f0 = ((bitsf(vf[0][0]) + bitsf(vf[1][0])) + (bitsf(vf[2][0]) + bitsf(vf[3][0]))) + bitsf(vf[4][0]);
      const float f1 = ((bitsf(vf[0][2]) + bitsf(vf[1][2])) + (bitsf(vf[2][2]) + bitsf(vf[3][2]))) + bitsf(vf[4][2]);
      *reinterpret_cast<f32x2*>(lds + L_GU + 2 * l48) = f32x2{bfr(f0), bfr(f1)};
    }
    WAVE_SYNC();
    if (lane < 48) lds[L_H + lane] = bfr(bfr(silu(lds[L_GU + lane])) * lds[L_GU + 48 + lane]);
    WAVE_SYNC();
    {
      float z[8];
#pragma unroll
      for (int i = 0; i < 8; ++i) z[i] = 0.f;
#pragma unroll
      for (int i = 0; i < 8; ++i) {
        const unsigned chi = fbits(lds[L_H + zr + 6 * i]), clo = chi >> 16;
#pragma unroll
        for (int k = 0; k < 4; ++k) {
          z[2 * k] = dot2(W2[i][k], clo, z[2 * k]);
          z[2 * k + 1] = dot2(W2[i][k], chi, z[2 * k + 1]);
        }
      }
      if (lane < 60) {
        float* zp = lds + L_ZP + zr * 80 + 8 * zc;
        *reinterpret_cast<f32x4*>(zp) = f32x4{z[0], z[1], z[2], z[3]};
        *reinterpret_cast<f32x4*>(zp + 4) = f32x4{z[4], z[5], z[6], z[7]};
      }
    }
    SCHED_FENCE();
  }
  const int u = (tid & 255) < 240 ? (tid & 255) : 239;
  const int rg = u / 10, ch = u - 10 * rg;
  u32x4 WO[8];
  const u32x4 rwo = mkrs(wo + (size_t)DH * h * D + 80 * sp, DH * D * 2);
  const int wofs = (rg * D + 8 * ch) * 2;
#define WOLC(i)                                                                                                            \
  asm volatile("s_cmp_lt_u32 %4, 4\n\ts_cbranch_scc1 2\n\tbuffer_load_dwordx4 %0, %1, %2, %3 offen nt"                     \
               : "=v"(WO[i]) : "v"(wofs), "s"(rwo), "s"((i) * 24 * D * 2), "s"(w) : "memory", "scc")
#define VLD(t)                                                       \
  do {                                                               \
    _Pragma("unroll") for (int j = 0; j < 3; ++j) VWL(t, j);         \
  } while (0)
#pragma unroll
  for (int t = NF; t < NF + 2; ++t)
    if (t < nt) {
#pragma unroll
      for (int j = 0; j < 3; ++j) KWL(t, j);
      VLD(t);
    }
#pragma unroll
  for (int t = 0; t < PQ; ++t) {
    VLD(t);
  }
  for (unsigned spin = 0; spin < (SPIN_MAX << 4); ++spin) {
    if (__hip_atomic_load(flg + 1, __ATOMIC_RELAXED, __HIP_MEMORY_SCOPE_WORKGROUP) != 0u) break;
    __builtin_amdgcn_s_sleep(1);
  }
  asm volatile("s_waitcnt lgkmcnt(0)" ::: "memory");
  SCHED_FENCE();
  u32x4 qf[3];
#pragma unroll
  for (int j = 0; j < 3; ++j) qf[j] = *reinterpret_cast<const u32x4*>(lds + L_QS + (c + 8 * j) * 4);
  float sc[NF + 2];
#pragma unroll
  for (int t = 0; t < NF + 2; ++t) {
    sc[t] = -INFINITY;
    if (t < NF || t < nt) {
      float d = 0.f;
#pragma unroll
      for (int j = 0; j < 3; ++j) {
        TIE(K[t][j]);
#pragma unroll
        for (int i = 0; i < 4; ++i) d = dot2(K[t][j][i], qf[j][i], d);
      }
      d = grp8_sum(d);
      sc[t] = bfr(bfr(d) * SCALE);
    }
    if (t == 3) {
      SCHED_FENCE();
      VLD(PQ);
    }
  }
  float mw = sc[0];
#pragma unroll
  for (int t = 1; t < NF + 2; ++t) mw = fmaxf(mw, sc[t]);
  mw = xg_max(mw);
  if (lane == 0) {
    lds[L_ML + w] = mw;
    __hip_atomic_fetch_add(flg + 3, 1u, __ATOMIC_RELAXED, __HIP_MEMORY_SCOPE_WORKGROUP);
  }
  float lw = 0.f;
#pragma unroll
  for (int t = 0; t < NF + 2; ++t) {
    sc[t] = ex2((sc[t] - mw) * LOG2E);
    lw += sc[t];
  }
  lw = xg_sum(lw);
  SCHED_FENCE();
  u64 zold[2] = {0ull, 0ull}, zadd[2] = {0ull, 0ull};
  int mro = 8 * tid, mso = tid < 97 ? (G2 + rec * 104 + tid) * 16 : GEND * 16;
  u32x4 pub = {0u, tag, 0u, tag};
  asm volatile("" : "+v"(mro), "+v"(mso), "+v"(pub));
  {
    float Mg;
    float acc[3][8];
#pragma unroll
    for (int i = 0; i < 24; ++i) acc[i >> 3][i & 7] = 0.f;
    float* pw = lds + L_PART + w * PS + 8 * c + g;
    const bool b3 = g & 1;
#define PVT(t, nw)                                                                                                          \
  do {                                                                                                                  \
    unsigned plo = bfbits(sc[t]), phi = plo << 16;                                                                      \
    _Pragma("unroll") for (int j = 0; j < 3; ++j) {                                                                     \
      if ((t) >= NF || (t) < NF - 3) asm volatile("" : "+v"(plo), "+v"(phi));                                           \
      else if ((t) >= NF - 2)                                      \
        asm volatile("s_waitcnt vmcnt(%2)\n\ts_cmp_lt_u32 %4, 4\n\ts_cbranch_scc0 4\n\ts_waitcnt vmcnt(%3 + 2)\n\t"      \
                     "s_cmp_eq_u32 %4, %5\n\ts_cbranch_scc1 1\n\ts_waitcnt vmcnt(%3)"                                    \
                     : "+v"(plo), "+v"(phi)                                                                             \
                     : "n"((NF - 1 - (t)) * 3 + 2 - j + (nw)), "n"((NF - 1 - (t)) * 3 + 2 - j), "s"(w), "n"(ZW) : "memory", "scc"); \
      else asm volatile("s_waitcnt vmcnt(%2)\n\ts_cmp_lt_u32 %4, 4\n\ts_cbranch_scc0 1\n\ts_waitcnt vmcnt(%3)"            \
                        : "+v"(plo), "+v"(phi)                                                                          \
                        : "n"((NF - 1 - (t)) * 3 + 2 - j + (nw)), "n"((NF - 1 - (t)) * 3 + 2 - j), "s"(w) : "memory", "scc"); \
      _Pragma("unroll") for (int i = 0; i < 4; ++i) {                                                                   \
        acc[j][2 * i] = dot2(V[t][j][i], plo, acc[j][2 * i]);                                                           \
        acc[j][2 * i + 1] = dot2(V[t][j][i], phi, acc[j][2 * i + 1]);                                                   \
      }                                                                                                                 \
      if ((t) == NF - 1) {                                                        \
        float r1[1];                                                                                                    \
        xg_scatter<1>(acc[j], r1, b3);                                                                                  \
        pw[64 * j] = r1[0] * ew;                                                                                        \
        SCHED_FENCE();                                                                                                  \
      }                                                                                                                 \
    }                                                                                                                   \
    if ((t) != NF - 1) {                                                                                                \
      _Pragma("unroll") for (int j = 0; j < 3; ++j)                                                                     \
        asm volatile("" : "+v"(acc[j][0]), "+v"(acc[j][1]), "+v"(acc[j][2]), "+v"(acc[j][3]), "+v"(acc[j][4]),          \
                     "+v"(acc[j][5]), "+v"(acc[j][6]), "+v"(acc[j][7]));             \
    }                                                                                                                   \
    SCHED_FENCE();                                                                                                      \
  } while (0)
#define WGMAX()                                                                                                         \
  do {                                                                                                                  \
    for (unsigned spin = 0; spin < (SPIN_MAX << 4); ++spin) {                                                           \
      if (__hip_atomic_load(flg + 3, __ATOMIC_RELAXED, __HIP_MEMORY_SCOPE_WORKGROUP) == 8u) break;                      \
      __builtin_amdgcn_s_sleep(1);                                                                                      \
    }                                                                                                                   \
    MEMBAR();                                                                                                           \
    const f32x4 ma = *reinterpret_cast<const f32x4*>(lds + L_ML), mb2 = *reinterpret_cast<const f32x4*>(lds + L_ML + 4); \
    Mg = fmaxf(fmaxf(fmaxf(ma[0], ma[1]), fmaxf(ma[2], ma[3])), fmaxf(fmaxf(mb2[0], mb2[1]), fmaxf(mb2[2], mb2[3])));   \
    ew = ex2((mw - Mg) * LOG2E);                                                                                        \
    if (lane == 0) *reinterpret_cast<f32x2*>(lds + L_PART + w * PS + DH) = f32x2{w == 0 ? Mg : 0.f, lw * ew};           \
    SCHED_FENCE();                                                                                                      \
  } while (0)
    float ew;
    VLD(PQ + 1);
    if (NF < nt) PVT(NF, 0);
    VLD(PQ + 2);
    if (NF + 1 < nt) PVT(NF + 1, 0);
    PVT(0, 0);
#pragma unroll
    for (int t = 1; t < NF; ++t) {
#pragma unroll
      for (int i = 0; i < WOB; ++i)
        if ((t == NF - 1) ? (i >= t - 1) : (i == t - 1)) WOLC(i);
      if (t == NF - 2) {
        WGMAX();
        if (w == ZW) {
#pragma unroll
          for (int k = 0; k < 2; ++k) {
            const float* zp = lds + L_ZP + (k ? 64 + (lane & 15) : lane);
            zold[k] = zadd[k] = fx_enc(((zp[0] + zp[80]) + (zp[160] + zp[240])) + (zp[320] + zp[400]));
          }
          const int za0 = (2 * GA + D + 80 * sp + lane) * 8, za1 = lane < 16 ? za0 + 512 : GEND * 16;
          asm volatile("buffer_atomic_add_x2 %0, %2, %4, 0 offen sc0\n\tbuffer_atomic_add_x2 %1, %3, %4, 0 offen sc0"
                       : "+v"(zold[0]), "+v"(zold[1]) : "v"(za0), "v"(za1), "s"(rwa) : "memory");
        }
        SCHED_FENCE();
      }
      PVT(t, t == NF - 1 ? WOB : t);
    }
  }
#define SLOWSUM(res, base, cc)                                                                                  \
  do {                                                                                                          \
    res = 0.f;                                                                                                  \
    _Pragma("unroll 1") for (int hh = 0; hh < HQ; ++hh) {                                                       \
      u32x2 v;                                                                                                  \
      for (unsigned spin = 0; spin < SPIN_MAX; ++spin) {                                                        \
        MEMBAR();                                                                                               \
        v = __builtin_amdgcn_raw_buffer_load_b64(rws, (cc) * 8, ((base) + hh * 320) * 16, 16);                  \
        if (v[1] == tag) break;                                                                                 \
      }                                                                                                         \
      res += bitsf(v[0]);                                                                                       \
    }                                                                                                           \
  } while (0)
  if (w == ZW) {
    {
      const f32x2* zp = reinterpret_cast<const f32x2*>(lds + L_ZP) + (lane < 40 ? lane : 0);
      const f32x2 zs = ((zp[0] + zp[40]) + (zp[80] + zp[120])) + (zp[160] + zp[200]);
      gst2(lane < 40 ? G3Z + h * 320 + 40 * sp + lane : GEND, zs[0], zs[1]);
    }
    asm volatile("s_waitcnt vmcnt(1)" : "+v"(zold[0]), "+v"(zold[1])::"memory");
#pragma unroll
    for (int k = 0; k < 2; ++k)
      if ((k == 0 || lane < 16) && (zold[k] >> 59) == 31u) {
        const int cz = 80 * sp + 64 * k + lane;
        float f = fx_dec(zold[k] + zadd[k]);
        if (__builtin_expect(!fx_exact(zold[k], zadd[k]), 0)) SLOWSUM(f, G3Z, cz);
        __builtin_amdgcn_raw_buffer_store_b64(u32x2{fbits(bfr(silu(bfr(f)))), tag}, rws, cz * 8, GG * 16, 16);
      }
  }
  __syncthreads();
  if (__builtin_expect(w < 2, 1)) {
    {
      f32x2 p2[8];
#pragma unroll
      for (int ww = 0; ww < 8; ++ww) p2[ww] = *reinterpret_cast<const f32x2*>(reinterpret_cast<const char*>(lds + L_PART + ww * PS) + mro);
      const f32x2 ua = (p2[0] + p2[1]) + (p2[2] + p2[3]), ub = (p2[4] + p2[5]) + (p2[6] + p2[7]);
      pub[0] = fbits(ua[0] + ub[0]);
      pub[2] = fbits(ua[1] + ub[1]);
      __builtin_amdgcn_raw_buffer_store_b128(pub, rws, mso, 0, 16);
    }
    SCHED_FENCE();
    const int t2 = tid < 96 ? tid : 95;
    const int pb = (G2 + h * NSP * 104 + t2) * 16, pm = (G2 + (h * NSP + c) * 104 + 96) * 16;
    const unsigned po = (unsigned)(size_t)(__attribute__((address_space(3))) float*)(lds + L_O) + 4 * t2;
    const int pg = lane < 20 ? (GG + 40 * sp + 20 * w + lane) * 16 : GEND * 16;
    const unsigned pl = (unsigned)(size_t)(__attribute__((address_space(3))) float*)(lds + L_GT) + 16 * (lane < 20 ? 20 * w + lane : 40 + tid);
    __builtin_amdgcn_s_sleep(PD1);
    {
      unsigned e0, e1, e2, e3, e4, e5, e6, e7, cnt;
#define DPC " row_mask:0xf bank_mask:0xf\n\t"
#define CHK(d, a, b) "v_bitop3_b32 " d ", " a ", " b ", %13 bitop3:0x7e\n\t"
#define ACC2(e, a, b) "v_fmac_f32 v44, " e ", " a "\n\tv_fmac_f32 v45, " e ", " b "\n\t"
      asm volatile(
          "s_mov_b32 %8, %16\n"
          "1:\n\t"
          "buffer_load_dwordx4 v[48:51], %18, %12, 0 offen sc1\n\t"
          "buffer_load_dwordx4 v[4:7], %9, %12, 0 offen sc1\n\t"
          "buffer_load_dwordx4 v[8:11], %10, %12, 0 offen sc1\n\t"
          "buffer_load_dwordx4 v[12:15], %10, %12, 0 offen offset:1664 sc1\n\t"
          "buffer_load_dwordx4 v[16:19], %10, %12, 0 offen offset:3328 sc1\n\t"
          "buffer_load_dwordx4 v[20:23], %10, %12, %14 offen sc1\n\t"
          "buffer_load_dwordx4 v[24:27], %10, %12, %14 offen offset:1664 sc1\n\t"
          "buffer_load_dwordx4 v[28:31], %10, %12, %14 offen offset:3328 sc1\n\t"
          "buffer_load_dwordx4 v[32:35], %10, %12, %15 offen sc1\n\t"
          "buffer_load_dwordx4 v[36:39], %10, %12, %15 offen offset:1664 sc1\n\t"
          "s_sub_u32 %8, %8, 1\n\t"
          "s_waitcnt vmcnt(8)\n\t"
          CHK("v41", "v5", "v7")
          "ds_write_b128 %19, v[48:51]\n\t"
          "v_max_f32_dpp v40, v4, v4 quad_perm:[1,0,3,2]" DPC
          "s_waitcnt vmcnt(7)\n\t"
          CHK("v46", "v9", "v11")
          "v_max_f32_dpp v40, v40, v40 quad_perm:[2,3,0,1]" DPC
          "s_waitcnt vmcnt(6)\n\t"
          CHK("v47", "v13", "v15")
          "v_max_f32_dpp v40, v40, v40 row_half_mirror" DPC
          "v_sub_f32 v42, v4, v40\n\t"
          "v_mul_f32 v42, 0x3fb8aa3b, v42\n\t"
          "v_exp_f32 v42, v42\n\t"
          "v_or3_b32 v41, v41, v46, v47\n\t"
          "v_mul_f32 v40, v6, v42\n\t"
          "s_waitcnt vmcnt(5)\n\t"
          CHK("v46", "v17", "v19")
          "v_add_f32_dpp v40, v40, v40 quad_perm:[1,0,3,2]" DPC
          "s_waitcnt vmcnt(4)\n\t"
          CHK("v47", "v21", "v23")
          "v_add_f32_dpp v40, v40, v40 quad_perm:[2,3,0,1]" DPC
          "v_or3_b32 v41, v41, v46, v47\n\t"
          "s_nop 0\n\t"
          "v_add_f32_dpp v40, v40, v40 row_half_mirror" DPC
          "v_rcp_f32 v40, v40\n\t"
          "s_nop 0\n\t"
          "v_mul_f32 v42, v42, v40\n\t"
          "s_nop 0\n\t"
          "v_readlane_b32 %0, v42, 0\n\tv_readlane_b32 %1, v42, 1\n\tv_readlane_b32 %2, v42, 2\n\tv_readlane_b32 %3, v42, 3\n\t"
          "v_readlane_b32 %4, v42, 4\n\tv_readlane_b32 %5, v42, 5\n\tv_readlane_b32 %6, v42, 6\n\tv_readlane_b32 %7, v42, 7\n\t"
          "v_mul_f32 v44, %0, v8\n\tv_mul_f32 v45, %0, v10\n\t"
          ACC2("%1", "v12", "v14") ACC2("%2", "v16", "v18") ACC2("%3", "v20", "v22")
          "s_waitcnt vmcnt(3)\n\t"
          ACC2("%4", "v24", "v26") CHK("v46", "v25", "v27")
          "s_waitcnt vmcnt(2)\n\t"
          ACC2("%5", "v28", "v30") CHK("v47", "v29", "v31")
          "v_or3_b32 v41, v41, v46, v47\n\t"
          "s_waitcnt vmcnt(1)\n\t"
          ACC2("%6", "v32", "v34") CHK("v46", "v33", "v35")
          "s_waitcnt vmcnt(0)\n\t"
          ACC2("%7", "v36", "v38")
          "v_cvt_pk_bf16_f32 v44, v44, v45\n\t"
          "ds_write_b32 %11, v44\n\t"
          CHK("v47", "v37", "v39")
          "v_or3_b32 v41, v41, v46, v47\n\t"
          "v_cmp_ne_u32 vcc, 0, v41\n\t"
          "s_cbranch_vccz 2f\n\t"
          "s_sleep %17\n\t"
          "s_cbranch_scc0 1b\n"
          "2:\n\t"
          "s_waitcnt lgkmcnt(0)"
          : "=&s"(e0), "=&s"(e1), "=&s"(e2), "=&s"(e3), "=&s"(e4), "=&s"(e5), "=&s"(e6), "=&s"(e7), "=&s"(cnt)
          : "v"(pm), "v"(pb), "v"(po), "s"(rwa), "s"(tag), "s"(3 * 1664), "s"(6 * 1664), "n"(SPIN_MAX), "n"(1), "v"(pg), "v"(pl)
          : "memory", "scc", "vcc", "v4", "v5", "v6", "v7", "v8", "v9", "v10", "v11", "v12", "v13", "v14", "v15", "v16", "v17", "v18", "v19",
            "v20", "v21", "v22", "v23", "v24", "v25", "v26", "v27", "v28", "v29", "v30", "v31", "v32", "v33", "v34", "v35", "v36", "v37",
            "v38", "v39", "v40", "v41", "v42", "v44", "v45", "v46", "v47", "v48", "v49", "v50", "v51");
    }
    __syncthreads();
  } else if (w == 2) {
    __syncthreads();
  } else {
    if (w >= 4) {
#pragma unroll
      for (int i = 0; i < 8; ++i) {
        AWAIT(WO[i], 7 - i);
        *reinterpret_cast<u32x4*>(lds + L_WO + u * 4 + i * 960) = WO[i];
      }
      asm volatile("s_waitcnt lgkmcnt(0)" ::: "memory");
      if (lane == 0) __hip_atomic_fetch_add(flg + 2, 1u, __ATOMIC_RELAXED, __HIP_MEMORY_SCOPE_WORKGROUP);
    }
    for (unsigned spin = 0; spin < (SPIN_MAX << 4); ++spin) {
      if (__hip_atomic_load(flg + 2, __ATOMIC_RELAXED, __HIP_MEMORY_SCOPE_WORKGROUP) == 4u) break;
      __builtin_amdgcn_s_sleep(1);
    }
    asm volatile("s_waitcnt lgkmcnt(0)" ::: "memory");
    u32x4 wb[6];
    {
      typedef __attribute__((address_space(3))) v4i16_t* ltr;
      const unsigned la = (unsigned)(size_t)(__attribute__((address_space(3))) float*)(lds + L_WO) + (8 * (lane >> 4) + ((lane >> 2) & 3)) * 160 + 32 * (w - 3) + 8 * (lane & 3);
#pragma unroll
      for (int k = 0; k < 6; ++k) {
        const u32x2 t0 = __builtin_bit_cast(u32x2, __builtin_amdgcn_ds_read_tr16_b64_v4i16((ltr)(size_t)(la + 32 * k * 160)));
        const u32x2 t1 = __builtin_bit_cast(u32x2, __builtin_amdgcn_ds_read_tr16_b64_v4i16((ltr)(size_t)(la + (32 * k + 4) * 160)));
        wb[k] = u32x4{t0[0], t0[1], t1[0], t1[1]};
      }
    }
    const int cy = 80 * sp + 16 * (w - 3) + (lane & 15);
    float xv = bf_lo(reinterpret_cast<const unsigned short*>(lds + L_XS)[cy]);
    gu64* pa = (gu64*)(gw + 2 * GA + cy);
    gu16* pout = (gu16*)(out + cy);
    typedef __attribute__((address_space(3))) const u32x4* la4;
    la4 pa4 = (la4)(lds + L_O) + 4 * (lane & 3) + (lane >> 4);
    int cy8 = cy * 8;
    typedef __attribute__((address_space(3))) const u32x2* la2;
    la2 pgt = (la2)(lds + L_GT) + 16 * (w - 3) + (lane & 15);
    asm volatile("" : "+v"(pgt));
    unsigned k0 = (1u << 27) - (0x411u << 20), k1 = k0 + (1u << 22), k2 = 1u << 17;
    double c0 = 262144.0 + 512.0, c1 = -(262144.0 + 32 * 512.0);
    float f0 = 512.f, f1 = 0x1p-9f;
    asm volatile("" : "+v"(wb[0]), "+v"(wb[1]), "+v"(wb[2]), "+v"(wb[3]), "+v"(wb[4]), "+v"(wb[5]));
    asm volatile("" : "+v"(xv), "+v"(pa), "+v"(pout), "+v"(pa4), "+v"(cy8), "+v"(k0), "+v"(k1), "+v"(k2), "+s"(c0), "+s"(c1), "+s"(f0), "+s"(f1));
    __syncthreads();
    const u32x4 a1 = pa4[0], a2 = pa4[16];
    SCHED_FENCE();
    u32x2 gg = *pgt;
    SCHED_FENCE();
#define MFMA(a, b, cc) __builtin_amdgcn_mfma_f32_16x16x32_bf16(__builtin_bit_cast(bf16x8_t, a), __builtin_bit_cast(bf16x8_t, b), cc, 0, 0, 0)
    const f32x4 z4 = {0.f, 0.f, 0.f, 0.f};
    const f32x4 d2 = MFMA(a1, wb[2], z4), d3 = MFMA(a1, wb[3], z4);
    SCHED_FENCE();
    f32x4 d0 = MFMA(a1, wb[0], z4), d1 = MFMA(a1, wb[1], z4);
    SCHED_FENCE();
    d0 = MFMA(a2, wb[4], d0);
    SCHED_FENCE();
    d1 = MFMA(a2, wb[5], d1);
    SCHED_FENCE();
    if (lane < 16) {
      const float q = ((d2[2] + d3[3]) + d0[0]) + d1[1];
      const bool bad = !(fabsf(q) < f0);
      const u64 db = __builtin_bit_cast(u64, (double)(bad ? 0.f : q) + c0);
      const unsigned fl = (bad ? k1 : k0) + (fabsf(q) < f1 ? k2 : 0u);
      const u64 add = ((u64)((unsigned)(db >> 32) + fl) << 32) | (unsigned)db;
      const u64 old = __hip_atomic_fetch_add(pa, add, __ATOMIC_RELAXED, __HIP_MEMORY_SCOPE_AGENT);
      __builtin_amdgcn_raw_buffer_store_b64(u32x2{fbits(q), tag}, rws, cy8, (G3Y + h * 320) * 16, 16);
      SCHED_FENCE();
      unsigned lim = (gg[1] == tag && !bad) ? (32u << 17) - (fl - k0) : 0u;
      asm volatile("" : "+v"(lim));
      SCHED_FENCE();
      float sv = (float)(__builtin_bit_cast(double, (old & ((1ull << 49) - 1ull)) + db) + c1);
      asm("" : "+v"(sv));
      unsigned res = bfbits(xv + bfr(bfr(sv) * bitsf(gg[0])));
      const unsigned rel = (unsigned)(old >> 32) - (31u << 27);
      TIE(res);
      if (rel < lim) *pout = res;
      SCHED_FENCE();
      if ((old >> 59) == 31u) {
        if (__builtin_expect(rel >= lim, 0)) {
          if (!fx_exact(old, add)) SLOWSUM(sv, G3Y, cy);
          for (unsigned spin = 0; gg[1] != tag && spin < SPIN_MAX; ++spin) {
            MEMBAR();
            gg = __builtin_amdgcn_raw_buffer_load_b64(rws, cy8, GG * 16, 16);
          }
          *pout = bfbits(xv + bfr(bfr(sv) * bitsf(gg[0])));
        }
        if (cy == 0)
          __hip_atomic_store(reinterpret_cast<unsigned*>(gw + 2 * GEP), tag, __ATOMIC_RELAXED, __HIP_MEMORY_SCOPE_AGENT);
      }
    }
  }
}

}

extern "C" void kernel_launch(void* const* d_in, const int* in_sizes, int n_in, void* d_out, int out_size, void* d_ws,
                              size_t ws_size, hipStream_t stream) {
  const bf16* x = (const bf16*)d_in[0];
  const bf16* kc = (const bf16*)d_in[1];
  const bf16* vc = (const bf16*)d_in[2];
  const bf16* wq = (const bf16*)d_in[3];
  const bf16* wo = (const bf16*)d_in[4];
  const bf16* w1 = (const bf16*)d_in[6];
  const bf16* w3 = (const bf16*)d_in[7];
  const bf16* w2 = (const bf16*)d_in[8];
  u64* gw = (u64*)d_ws;
  fused_decode<<<NWG, NT, 0, stream>>>(x, kc, vc, wq, wo, w1, w3, w2, gw, (bf16*)d_out);
}
```

```cpp
#define PROBE 0
#define PX 0
#include <hip/hip_runtime.h>
#include <hip/hip_bf16.h>
#include <stdint.h>

typedef __hip_bfloat16 bf16;
typedef unsigned long long u64;
typedef __attribute__((address_space(1))) unsigned long long gu64;
typedef __attribute__((address_space(1))) unsigned short gu16;
typedef unsigned u32x4 __attribute__((ext_vector_type(4)));
typedef unsigned u32x2 __attribute__((ext_vector_type(2)));
typedef float f32x4 __attribute__((ext_vector_type(4)));
typedef __bf16 bf16x2_t __attribute__((ext_vector_type(2)));
typedef __bf16 bf16x8_t __attribute__((ext_vector_type(8)));
typedef short v4i16_t __attribute__((ext_vector_type(4)));

namespace {

constexpr int D = 640, HQ = 32, DH = 192, SK = 32768, WIN = 4096, FF = 1536;
constexpr int NSP = 8;
constexpr int NWG = HQ * NSP;
constexpr int NT = 512;
constexpr float SCALE = 0.07216878364870322f;
constexpr float LOG2E = 1.4426950408889634f;
constexpr unsigned SPIN_MAX = 1u << 16;
constexpr int WND = 8;
constexpr int WOL = 7;
constexpr int PQ = 4;
constexpr int NF = 7;
constexpr int DX[8] = {2, -1, 2, -1, 1, -3, 1, -1};
constexpr unsigned dx_pack() {
  unsigned r = 0;
  for (int i = 0; i < 8; ++i) r |= (unsigned)(DX[i] + 4) << (4 * i);
  return r;
}
constexpr unsigned dx_pos() {
  unsigned r = 0, a = 0;
  for (int i = 0; i < 8; ++i) { r |= a << (4 * i); a += DX[i] > 0 ? DX[i] : 0; }
  return r;
}
constexpr unsigned long long dx_don() {
  unsigned long long r = 0;
  int k = 0;
  for (int i = 0; i < 8; ++i)
    for (int q = 0; q < -DX[i]; ++q) r |= (unsigned long long)(i * 4 + q) << (5 * k++);
  return r;
}
constexpr bool dx_ok() {
  int a = 0, p = 0;
  bool in = true;
  for (int i = 0; i < 8; ++i) { a += DX[i]; p += DX[i] > 0 ? DX[i] : 0; in = in && DX[i] >= -4 && DX[i] <= 4; }
  return a == 0 && p <= 8 && in;
}
constexpr int dx_check[dx_ok() ? 1 : -1] = {0};
constexpr int KE = 8;
constexpr int PD1 = 14;
constexpr int WOB = 8;
constexpr int ZW = 3;

constexpr int G1 = 0;
constexpr int G1F = G1 + HQ * 288;
constexpr int G2 = G1F + 8 * 960;
constexpr int G3Y = G2 + NWG * 104;
constexpr int G3Z = G3Y + 320 * 32;
constexpr int GG = G3Z + 320 * 32;
constexpr int GZF = GG + 320;
constexpr int GA = GZF + 256;
constexpr int GEP = GA + 640;
constexpr int GPR = GEP + 8;
constexpr int GEND = GPR + 256;

__device__ __forceinline__ unsigned fbits(float x) { return __builtin_bit_cast(unsigned, x); }
__device__ __forceinline__ float bitsf(unsigned x) { return __builtin_bit_cast(float, x); }
#define gok2(v) ((v)[1] == tag && (v)[3] == tag)
#define gst2(unit, a, b) __builtin_amdgcn_raw_buffer_store_b128(u32x4{fbits(a), tag, fbits(b), tag}, rws, (unit) * 16, 0, 16)
#define gld2(unit, cunit) __builtin_amdgcn_raw_buffer_load_b128(rws, (unit) * 16, (cunit) * 16, 16)
#define MEMBAR() asm volatile("" ::: "memory")
typedef float f32x2 __attribute__((ext_vector_type(2)));

__device__ __forceinline__ float bf_lo(unsigned u) { return __builtin_bit_cast(float, u << 16); }
__device__ __forceinline__ float bf_hi(unsigned u) { return __builtin_bit_cast(float, u & 0xffff0000u); }
__device__ __forceinline__ float bfr(float x) { return __bfloat162float(__float2bfloat16(x)); }
__device__ __forceinline__ unsigned short bfbits(float x) {
  return __builtin_bit_cast(unsigned short, __float2bfloat16(x));
}
__device__ __forceinline__ float dot2(unsigned a, unsigned b, float c) {
  return __builtin_amdgcn_fdot2_f32_bf16(__builtin_bit_cast(bf16x2_t, a), __builtin_bit_cast(bf16x2_t, b), c, false);
}
template <int CTRL>
__device__ __forceinline__ float dpp(float x) {
  return __builtin_bit_cast(float, __builtin_amdgcn_mov_dpp(__builtin_bit_cast(int, x), CTRL, 0xf, 0xf, true));
}
__device__ __forceinline__ float grp8_sum(float x) {
  x += dpp<0xB1>(x);
  x += dpp<0x4E>(x);
  x += dpp<0x141>(x);
  return x;
}
__device__ __forceinline__ float swap16_add(float a, float b) {
  auto s = __builtin_amdgcn_permlane16_swap(fbits(a), fbits(b), false, false);
  return bitsf((unsigned)s[0]) + bitsf((unsigned)s[1]);
}
__device__ __forceinline__ float swap32_add(float a, float b) {
  auto t = __builtin_amdgcn_permlane32_swap(fbits(a), fbits(b), false, false);
  return bitsf((unsigned)t[0]) + bitsf((unsigned)t[1]);
}
__device__ __forceinline__ float xg_sum(float x) {
  x += dpp<0x128>(x);
  x = swap16_add(x, x);
  return swap32_add(x, x);
}
__device__ __forceinline__ float xg_max(float x) {
  x = fmaxf(x, dpp<0x128>(x));
  auto s = __builtin_amdgcn_permlane16_swap(fbits(x), fbits(x), false, false);
  x = fmaxf(bitsf((unsigned)s[0]), bitsf((unsigned)s[1]));
  auto t = __builtin_amdgcn_permlane32_swap(fbits(x), fbits(x), false, false);
  return fmaxf(bitsf((unsigned)t[0]), bitsf((unsigned)t[1]));
}
template <int NJ>
__device__ __forceinline__ void xg_scatter(const float (&a)[8 * NJ], float (&r)[NJ], const bool b3) {
#pragma unroll
  for (int j = 0; j < NJ; ++j) {
    float p[4];
#pragma unroll
    for (int e = 0; e < 4; ++e) p[e] = swap32_add(a[8 * j + e], a[8 * j + e + 4]);
    const float q0 = swap16_add(p[0], p[2]), q1 = swap16_add(p[1], p[3]);
    r[j] = (b3 ? q1 : q0) + dpp<0x128>(b3 ? q0 : q1);
  }
}
__device__ __forceinline__ float wave_sum(float x) { return xg_sum(grp8_sum(x)); }
__device__ __forceinline__ float ex2(float x) { return __builtin_amdgcn_exp2f(x); }
__device__ __forceinline__ float silu(float x) { return x / (1.0f + __expf(-x)); }
__device__ __forceinline__ float rdlane(float x, int l) {
  return __builtin_bit_cast(float, __builtin_amdgcn_readlane(__builtin_bit_cast(int, x), l));
}

__device__ __forceinline__ u64 fx_enc(float v) {
  const bool bad = !(fabsf(v) < 512.f), small = fabsf(v) < 0x1p-9f;
  const u64 d = __builtin_bit_cast(u64, (double)(bad ? 0.f : v) + (262144.0 + 512.0));
  const unsigned hi = (unsigned)(d >> 32) + ((1u << 27) - (0x411u << 20)) + (bad ? 1u << 22 : 0u) + (small ? 1u << 17 : 0u);
  return ((u64)hi << 32) | (unsigned)d;
}
__device__ __forceinline__ float fx_dec(u64 t) {
  const unsigned hi = ((unsigned)(t >> 32) & 0x1ffffu) | (0x411u << 20);
  return (float)(__builtin_bit_cast(double, ((u64)hi << 32) | (unsigned)t) - (262144.0 + 32 * 512.0));
}
__device__ __forceinline__ bool fx_exact(u64 old, u64 add) {
  const unsigned o = (unsigned)(old >> 49) & 0x3ffu, a = (unsigned)(add >> 49) & 0x3ffu;
  return o + a < 32u;
}
#define SCHED_FENCE() __builtin_amdgcn_sched_barrier(0)
#define ALD(dst, rs, vo, so, imm) \
  asm volatile("buffer_load_dwordx4 %0, %1, %2, %3 offen offset:%4 nt" : "=v"(dst) : "v"(vo), "s"(rs), "s"(so), "n"(imm) : "memory")
#define AWL(dst, rs, vo, so, imm)                                                                                      \
  asm volatile("s_waitcnt vmcnt(8)\n\ts_cmp_le_i32 %5, 8\n\ts_cbranch_scc0 4\n\ts_waitcnt vmcnt(7)\n\t"                 \
               "s_cmp_le_i32 %5, 7\n\ts_cbranch_scc0 1\n\ts_waitcnt vmcnt(6)\n\t"                                        \
               "buffer_load_dwordx4 %0, %1, %2, %3 offen offset:%4 nt"                                                 \
               : "=v"(dst) : "v"(vo), "s"(rs), "s"(so), "n"(imm), "s"(wl) : "memory", "scc")
#define AWAIT(v, n) asm volatile("s_waitcnt vmcnt(%1)" : "+v"(v) : "n"(n) : "memory")
#define TIE(v) asm volatile("" : "+v"(v))
__device__ __forceinline__ u32x4 mkrs(const void* p, unsigned bytes) {
  const u64 a = (u64)p;
  return u32x4{(unsigned)a, (unsigned)(a >> 32) & 0xffffu, bytes, 0x00020000u};
}
#define WAVE_SYNC()                                        \
  do {                                                     \
    __builtin_amdgcn_fence(__ATOMIC_RELEASE, "wavefront"); \
    __builtin_amdgcn_wave_barrier();                       \
    __builtin_amdgcn_fence(__ATOMIC_ACQUIRE, "wavefront"); \
  } while (0)

constexpr int L_WO = 0;
constexpr int L_XS = L_WO + DH * 40;
constexpr int PS = 256;
constexpr int L_PART = L_XS + 4 * 320;
constexpr int L_XN = L_PART + 8 * PS;
constexpr int L_ZP = L_XN + 4 * 64;
constexpr int L_QS = L_ZP + 6 * 80;
constexpr int L_ML = L_QS + 96;
constexpr int L_O = L_ML + 16;
constexpr int L_H = L_O + 128;
constexpr int L_GU = L_H + 48;
constexpr int L_GT = L_GU + 96;
constexpr int L_FLG = L_GT + 168 * 4;
constexpr int L_END = L_FLG + 4;

__global__ void __launch_bounds__(NT, 2)
fused_decode(const bf16* __restrict__ x, const bf16* __restrict__ kc, const bf16* __restrict__ vc,
             const bf16* __restrict__ wq, const bf16* __restrict__ wo, const bf16* __restrict__ w1,
             const bf16* __restrict__ w3, const bf16* __restrict__ w2, u64* __restrict__ gw, bf16* __restrict__ out) {
  __shared__ __attribute__((aligned(16))) float lds[L_END];
  const u64 tstart = __builtin_amdgcn_s_memrealtime();
  SCHED_FENCE();
  const int b = blockIdx.x;
  const int xcd = b & 7, jj = b >> 3;
  const int h = xcd * 4 + (jj >> 3), sp = jj & 7;
  const int ha = jj, spa = xcd;
  const int w = __builtin_amdgcn_readfirstlane(threadIdx.x >> 6);
  const int tid = threadIdx.x, lane = tid & 63;
  const int g = lane >> 3, c = lane & 7;
  const int rec = ha * NSP + spa;
  unsigned* flg = reinterpret_cast<unsigned*>(lds + L_FLG);

  asm volatile("" ::"s"(x), "s"(kc), "s"(vc), "s"(wq), "s"(wo), "s"(w1), "s"(w3), "s"(w2), "s"(gw), "s"(out));
  const auto rws = __builtin_amdgcn_make_buffer_rsrc((void*)gw, (short)0, GEND * 16, 0x00020000);
  const size_t kvw = ((size_t)ha * SK + (SK - WIN)) * DH;
  const u32x4 rk = mkrs(kc + kvw, WIN * DH * 2), rv = mkrs(vc + kvw, WIN * DH * 2);
  const u32x4 rwa = mkrs(gw, GEND * 16);
  const int kvo = (g * DH + 8 * c) * 2;
  const int dx = (int)((dx_pack() >> (4 * xcd)) & 15u) - 4;
  const int wr = w < 4 ? (w + 3) & 3 : w;
  const int nt = 8 + ((unsigned)(wr - dx) >> 31) - ((unsigned)(7 - w + dx) >> 31);
  const int don = (int)(dx_don() >> (5 * (((dx_pos() >> (4 * xcd)) + wr) & 7u))) & 31;
  const int kvb = (spa * 8 + ((w + (ha >> 3)) & 7)) * 64 * DH * 2;
  const int ko8 = (((don >> 2) * 8 + ((7 - (don & 3) + (ha >> 3)) & 7)) * 8 + ((7 + ha) & 7)) * 8 * DH * 2;
  const int wl = __builtin_amdgcn_readfirstlane((w < 4 ? WOL : WND) + (nt - 8) + (dx < 0 ? 1 : 0));
#define KVOFF(t) ((t) == 8 ? ko8 : kvb + (((t) + ha) & 7) * 8 * DH * 2)
#define KLD(t, j) ALD(K[t][j], rk, kvo, KVOFF(t), 128 * (j))
#define KWL(t, j) AWL(K[t][j], rk, kvo, KVOFF(t), 128 * (j))
#define VWL(t, j) AWL(V[t][j], rv, kvo, KVOFF(t), 128 * (j))
#define EPLD() asm volatile("buffer_load_dword %0, off, %1, %2" : "=v"(epoch) : "s"(rwa), "s"(GEP * 16) : "memory")
  u32x4 K[NF + 2][3], V[NF + 2][3];

  const int fi = (xcd & 1) ? 19 : 8;
  const bool fidle = (unsigned)(jj - fi) < 2u;
  const int fp = jj > fi ? jj - 2 : jj;
  const int fcol = fp / 5, frp = fp - 5 * fcol;
  unsigned epoch, tag;
  if (w < 4) {
    const int fw = 4 * sp + w, ct = fw >= 20 ? 2 : fw >= 10 ? 1 : 0;
    const int r0 = 64 * (fw - 10 * ct);
    const int R = 128 * frp + 32 * w;
    const u32x4 xa = *reinterpret_cast<const u32x4*>(x + 8 * lane);
    const u32x4 xb = *reinterpret_cast<const u32x4*>(x + 512 + 8 * (lane & 15));
    u32x4 WQ[8], F[4];
    {
      const auto rq = __builtin_amdgcn_make_buffer_rsrc((void*)wq, (short)0, D * HQ * DH * 2, 0x00020000);
      const int qo = ((r0 + 8 * g) * (HQ * DH) + h * DH + 64 * ct + 8 * c) * 2;
#pragma unroll
      for (int i = 0; i < 8; ++i) WQ[i] = __builtin_amdgcn_raw_buffer_load_b128(rq, qo, i * HQ * DH * 2, 2);
      const auto rf = __builtin_amdgcn_make_buffer_rsrc((void*)(fcol >= 3 ? w3 : w1), (short)0, fidle ? 0 : D * FF * 2, 0x00020000);
      const int fo = ((R + 4 * g) * FF + 192 * xcd + 64 * (fcol >= 3 ? fcol - 3 : fcol) + 8 * c) * 2;
#pragma unroll
      for (int i = 0; i < 4; ++i) F[i] = __builtin_amdgcn_raw_buffer_load_b128(rf, fo, i * FF * 2, 2);
    }
    if (fidle && tid < 40)
      __builtin_amdgcn_raw_buffer_store_b128(u32x4{0u, 0u, 0u, 0u}, rws, (GA + (jj - fi) * 320 + 40 * xcd + lane) * 16, 0, 16);
    SCHED_FENCE();
    EPLD();
    if (tid < 4) flg[tid] = 0u;
    SCHED_FENCE();
    __syncthreads();
    SCHED_FENCE();
    unsigned short* xs = reinterpret_cast<unsigned short*>(lds + L_XS + w * 320);
    const bool lo16 = lane < 16;
    *reinterpret_cast<u32x4*>(xs + 8 * lane) = xa;
    if (lo16) *reinterpret_cast<u32x4*>(xs + 512 + 8 * lane) = xb;
    float xf[8], xg[8];
#pragma unroll
    for (int i = 0; i < 4; ++i) {
      xf[2 * i] = bf_lo(xa[i]);
      xf[2 * i + 1] = bf_hi(xa[i]);
      xg[2 * i] = bf_lo(xb[i]);
      xg[2 * i + 1] = bf_hi(xb[i]);
    }
    float sum = 0.f;
#pragma unroll
    for (int i = 0; i < 8; ++i) sum += xf[i] + (lo16 ? xg[i] : 0.f);
    sum = wave_sum(sum);
    const float mean = sum * (1.0f / D);
    float sq = 0.f;
#pragma unroll
    for (int i = 0; i < 8; ++i) {
      const float d1 = xf[i] - mean, d2 = xg[i] - mean;
      sq += d1 * d1 + (lo16 ? d2 * d2 : 0.f);
    }
    sq = wave_sum(sq);
    const float mb = bfr(mean);
    const float vb = bfr(sq * (1.0f / D));
    const float den = bfr(sqrtf(bfr(vb + 1e-5f)));
    WAVE_SYNC();
    auto xnorm = [&](int r) -> unsigned short { return bfbits(bfr(bf_lo(xs[r]) - mb) / den); };
    {
      unsigned short* xn = reinterpret_cast<unsigned short*>(lds + L_XN + w * 64);
      xn[lane] = xnorm((r0 < D ? r0 : 0) + lane);
      xn[64 + (lane & 31)] = xnorm(R + (lane & 31));
      WAVE_SYNC();
      const u32x4 XQ = *reinterpret_cast<const u32x4*>(xn + 8 * g);
      const u32x2 XF = *reinterpret_cast<const u32x2*>(xn + 64 + 4 * g);
      float acc[16], r2[2];
#pragma unroll
      for (int i = 0; i < 16; ++i) acc[i] = 0.f;
#pragma unroll
      for (int i = 0; i < 8; ++i) {
        const unsigned d = XQ[i >> 1], lo = (i & 1) ? d >> 16 : d & 0xffffu, hi = (i & 1) ? d & 0xffff0000u : d << 16;
#pragma unroll
        for (int k = 0; k < 4; ++k) {
          acc[2 * k] = dot2(WQ[i][k], lo, acc[2 * k]);
          acc[2 * k + 1] = dot2(WQ[i][k], hi, acc[2 * k + 1]);
        }
      }
#pragma unroll
      for (int i = 0; i < 4; ++i) {
        const unsigned d = XF[i >> 1], lo = (i & 1) ? d >> 16 : d & 0xffffu, hi = (i & 1) ? d & 0xffff0000u : d << 16;
#pragma unroll
        for (int k = 0; k < 4; ++k) {
          acc[8 + 2 * k] = dot2(F[i][k], lo, acc[8 + 2 * k]);
          acc[8 + 2 * k + 1] = dot2(F[i][k], hi, acc[8 + 2 * k + 1]);
        }
      }
      xg_scatter<2>(acc, r2, g & 1);
#pragma unroll
      for (int j = 0; j < 2; ++j) lds[L_PART + w * 128 + 64 * j + 8 * c + g] = r2[j];
    }
    asm volatile("s_waitcnt lgkmcnt(0)" ::: "memory");
    if (lane == 0) __hip_atomic_fetch_add(flg, 1u, __ATOMIC_RELAXED, __HIP_MEMORY_SCOPE_WORKGROUP);
    SCHED_FENCE();
  } else {
#pragma unroll
    for (int m = 0; m < KE; ++m) KLD(m / 3, m % 3);
    SCHED_FENCE();
    EPLD();
    SCHED_FENCE();
    __syncthreads();
    SCHED_FENCE();
  }
  const int lz = lane < 60 ? lane : 59;
  const int zr = lz / 10, zc = lz - 10 * zr;
  int w2o = (zr * D + 8 * zc) * 2;
  asm volatile("" : "+v"(w2o));
  if (w == 0) {
    for (unsigned spin = 0; spin < (SPIN_MAX << 4); ++spin) {
      if (__hip_atomic_load(flg, __ATOMIC_RELAXED, __HIP_MEMORY_SCOPE_WORKGROUP) == 4u) break;
      __builtin_amdgcn_s_sleep(1);
    }
    asm volatile("s_waitcnt lgkmcnt(0)" ::: "memory");
    AWAIT(epoch, 0);
    tag = __builtin_amdgcn_readfirstlane(epoch) + 1u;
    const f32x2* pp = reinterpret_cast<const f32x2*>(lds + L_PART) + lane;
    const f32x2 a01 = pp[0] + pp[64], a23 = pp[128] + pp[192];
    const bool two = sp == 2 && lane < 32;
    const f32x2 sm = two ? a01 : a01 + a23;
    const int ct0 = sp >= 5 ? 2 : sp >= 3 ? 1 : 0, slot = sp - (sp >= 5 ? 5 : sp >= 3 ? 2 : 0);
    const int uf = fidle ? GZF + 32 * xcd + 16 * (jj - fi) : G1F + xcd * 960 + fp * 32;
    const int un = lane < 32 ? G1 + h * 288 + slot * 96 + ct0 * 32 + lane : uf + lane - 32;
    if (lane < 33 || !fidle) gst2(un, sm[0], sm[1]);
    if (two) gst2(G1 + h * 288 + 32 + lane, a23[0], a23[1]);
    SCHED_FENCE();
  }
  int wop = w;
  asm volatile("" : "+s"(wop));
  if (wop != 0) {
    if (w < 4) {
#pragma unroll
      for (int m = 0; m < KE; ++m) KWL(m / 3, m % 3);
    }
#pragma unroll
    for (int m = KE; m < 3 * NF; ++m) KWL(m / 3, m % 3);
    TIE(epoch);
    tag = __builtin_amdgcn_readfirstlane(epoch) + 1u;
    SCHED_FENCE();
  } else {
    u32x4 W2[8];
    const auto rw2 = __builtin_amdgcn_make_buffer_rsrc((void*)(w2 + (size_t)48 * h * D + 80 * sp), (short)0, 48 * D * 2, 0x00020000);
#pragma unroll
    for (int i = 0; i < 8; ++i) W2[i] = __builtin_amdgcn_raw_buffer_load_b128(rw2, w2o, i * 6 * D * 2, 2);
#pragma unroll
    for (int t = 0; t < NF; ++t)
#pragma unroll
      for (int j = 0; j < 3; ++j) KLD(t, j);
    SCHED_FENCE();
    const int row = lane >> 4, l16 = lane & 15;
    const int gb = G1 + ha * 288 + lane;
    const int gc = row < 3 ? G1 + ha * 288 + row * 96 + 64 + l16 : GZF + 32 * sp;
    const int l48 = lane < 48 ? lane : 47, fm = l48 >= 24 ? 1 : 0, fpr = 24 * (h & 3) + l48 - 24 * fm;
    const int gf = G1F + xcd * 960 + (3 * fm + (fpr >> 5)) * 160 + (fpr & 31);
    u32x4 va[3], vb[2], vf[5];
    for (unsigned spin = 0;; ++spin) {
      MEMBAR();
#pragma unroll
      for (int s = 0; s < 3; ++s) va[s] = gld2(gb, s * 96);
#pragma unroll
      for (int s = 0; s < 2; ++s) vb[s] = gld2(gc, s * 16);
#pragma unroll
      for (int s = 0; s < 5; ++s) vf[s] = gld2(gf, s * 32);
      bool ok = gok2(vb[0]) && gok2(vb[1]) && gok2(vf[3]) && gok2(vf[4]);
#pragma unroll
      for (int s = 0; s < 3; ++s) ok = ok && gok2(va[s]) && gok2(vf[s]);
      if (__all(ok) || spin >= SPIN_MAX) break;
      __builtin_amdgcn_s_sleep(1);
    }
    {
      const float a0 = (bitsf(va[0][0]) + bitsf(va[1][0])) + bitsf(va[2][0]), a1 = (bitsf(va[0][2]) + bitsf(va[1][2])) + bitsf(va[2][2]);
      reinterpret_cast<unsigned*>(lds + L_QS)[lane] = (unsigned)bfbits(a0) | ((unsigned)bfbits(a1) << 16);
      const float p0 = swap16_add(row < 3 ? bitsf(vb[0][0]) : 0.f, row < 3 ? bitsf(vb[0][2]) : 0.f);
      const float p1 = swap16_add(row < 3 ? bitsf(vb[1][0]) : 0.f, row < 3 ? bitsf(vb[1][2]) : 0.f);
      const float bq = swap32_add(p0, p1);
      reinterpret_cast<unsigned short*>(lds + L_QS)[2 * (64 + 16 * (row >> 1) + l16) + (row & 1)] = bfbits(bq);
    }
    asm volatile("s_waitcnt lgkmcnt(0)" ::: "memory");
    if (lane == 0) __hip_atomic_store(flg + 1, 1u, __ATOMIC_RELAXED, __HIP_MEMORY_SCOPE_WORKGROUP);
    SCHED_FENCE();
    {
      const float f0 = ((bitsf(vf[0][0]) + bitsf(vf[1][0])) + (bitsf(vf[2][0]) + bitsf(vf[3][0]))) + bitsf(vf[4][0]);
      const float f1 = ((bitsf(vf[0][2]) + bitsf(vf[1][2])) + (bitsf(vf[2][2]) + bitsf(vf[3][2]))) + bitsf(vf[4][2]);
      *reinterpret_cast<f32x2*>(lds + L_GU + 2 * l48) = f32x2{bfr(f0), bfr(f1)};
    }
    WAVE_SYNC();
    if (lane < 48) lds[L_H + lane] = bfr(bfr(silu(lds[L_GU + lane])) * lds[L_GU + 48 + lane]);
    WAVE_SYNC();
    {
      float z[8];
#pragma unroll
      for (int i = 0; i < 8; ++i) z[i] = 0.f;
#pragma unroll
      for (int i = 0; i < 8; ++i) {
        const unsigned chi = fbits(lds[L_H + zr + 6 * i]), clo = chi >> 16;
#pragma unroll
        for (int k = 0; k < 4; ++k) {
          z[2 * k] = dot2(W2[i][k], clo, z[2 * k]);
          z[2 * k + 1] = dot2(W2[i][k], chi, z[2 * k + 1]);
        }
      }
      if (lane < 60) {
        float* zp = lds + L_ZP + zr * 80 + 8 * zc;
        *reinterpret_cast<f32x4*>(zp) = f32x4{z[0], z[1], z[2], z[3]};
        *reinterpret_cast<f32x4*>(zp + 4) = f32x4{z[4], z[5], z[6], z[7]};
      }
    }
    SCHED_FENCE();
  }
  const int u = (tid & 255) < 240 ? (tid & 255) : 239;
  const int rg = u / 10, ch = u - 10 * rg;
  u32x4 WO[8];
  const u32x4 rwo = mkrs(wo + (size_t)DH * h * D + 80 * sp, DH * D * 2);
  const int wofs = (rg * D + 8 * ch) * 2;
#define WOLC(i)                                                                                                            \
  asm volatile("s_cmp_lt_u32 %4, 4\n\ts_cbranch_scc1 2\n\tbuffer_load_dwordx4 %0, %1, %2, %3 offen nt"                     \
               : "=v"(WO[i]) : "v"(wofs), "s"(rwo), "s"((i) * 24 * D * 2), "s"(w) : "memory", "scc")
#define VLD(t)                                                       \
  do {                                                               \
    _Pragma("unroll") for (int j = 0; j < 3; ++j) VWL(t, j);         \
  } while (0)
#pragma unroll
  for (int t = NF; t < NF + 2; ++t)
    if (t < nt) {
#pragma unroll
      for (int j = 0; j < 3; ++j) KWL(t, j);
      VLD(t);
    }
#pragma unroll
  for (int t = 0; t < PQ; ++t) {
    VLD(t);
  }
  for (unsigned spin = 0; spin < (SPIN_MAX << 4); ++spin) {
    if (__hip_atomic_load(flg + 1, __ATOMIC_RELAXED, __HIP_MEMORY_SCOPE_WORKGROUP) != 0u) break;
    __builtin_amdgcn_s_sleep(1);
  }
  asm volatile("s_waitcnt lgkmcnt(0)" ::: "memory");
  SCHED_FENCE();
  u32x4 qf[3];
#pragma unroll
  for (int j = 0; j < 3; ++j) qf[j] = *reinterpret_cast<const u32x4*>(lds + L_QS + (c + 8 * j) * 4);
  float sc[NF + 2];
#pragma unroll
  for (int t = 0; t < NF + 2; ++t) {
    sc[t] = -INFINITY;
    if (t < NF || t < nt) {
      float d = 0.f;
#pragma unroll
      for (int j = 0; j < 3; ++j) {
        TIE(K[t][j]);
#pragma unroll
        for (int i = 0; i < 4; ++i) d = dot2(K[t][j][i], qf[j][i], d);
      }
      d = grp8_sum(d);
      sc[t] = bfr(bfr(d) * SCALE);
    }
    if (t == 3) {
      SCHED_FENCE();
      VLD(PQ);
    }
  }
  float mw = sc[0];
#pragma unroll
  for (int t = 1; t < NF + 2; ++t) mw = fmaxf(mw, sc[t]);
  mw = xg_max(mw);
  if (lane == 0) {
    lds[L_ML + w] = mw;
    __hip_atomic_fetch_add(flg + 3, 1u, __ATOMIC_RELAXED, __HIP_MEMORY_SCOPE_WORKGROUP);
  }
  float lw = 0.f;
#pragma unroll
  for (int t = 0; t < NF + 2; ++t) {
    sc[t] = ex2((sc[t] - mw) * LOG2E);
    lw += sc[t];
  }
  lw = xg_sum(lw);
  SCHED_FENCE();
  u64 zold[2] = {0ull, 0ull}, zadd[2] = {0ull, 0ull};
  int mro = 8 * tid, mso = tid < 97 ? (G2 + rec * 104 + tid) * 16 : GEND * 16;
  u32x4 pub = {0u, tag, 0u, tag};
  asm volatile("" : "+v"(mro), "+v"(mso), "+v"(pub));
  {
    float Mg;
    float acc[3][8];
#pragma unroll
    for (int i = 0; i < 24; ++i) acc[i >> 3][i & 7] = 0.f;
    float* pw = lds + L_PART + w * PS + 8 * c + g;
    const bool b3 = g & 1;
#define PVT(t, nw)                                                                                                          \
  do {                                                                                                                  \
    unsigned plo = bfbits(sc[t]), phi = plo << 16;                                                                      \
    _Pragma("unroll") for (int j = 0; j < 3; ++j) {                                                                     \
      if ((t) >= NF || (t) < NF - 3) asm volatile("" : "+v"(plo), "+v"(phi));                                           \
      else if ((t) >= NF - 2)                                      \
        asm volatile("s_waitcnt vmcnt(%2)\n\ts_cmp_lt_u32 %4, 4\n\ts_cbranch_scc0 4\n\ts_waitcnt vmcnt(%3 + 2)\n\t"      \
                     "s_cmp_eq_u32 %4, %5\n\ts_cbranch_scc1 1\n\ts_waitcnt vmcnt(%3)"                                    \
                     : "+v"(plo), "+v"(phi)                                                                             \
                     : "n"((NF - 1 - (t)) * 3 + 2 - j + (nw)), "n"((NF - 1 - (t)) * 3 + 2 - j), "s"(w), "n"(ZW) : "memory", "scc"); \
      else asm volatile("s_waitcnt vmcnt(%2)\n\ts_cmp_lt_u32 %4, 4\n\ts_cbranch_scc0 1\n\ts_waitcnt vmcnt(%3)"            \
                        : "+v"(plo), "+v"(phi)                                                                          \
                        : "n"((NF - 1 - (t)) * 3 + 2 - j + (nw)), "n"((NF - 1 - (t)) * 3 + 2 - j), "s"(w) : "memory", "scc"); \
      _Pragma("unroll") for (int i = 0; i < 4; ++i) {                                                                   \
        acc[j][2 * i] = dot2(V[t][j][i], plo, acc[j][2 * i]);                                                           \
        acc[j][2 * i + 1] = dot2(V[t][j][i], phi, acc[j][2 * i + 1]);                                                   \
      }                                                                                                                 \
      if ((t) == NF - 1) {                                                        \
        float r1[1];                                                                                                    \
        xg_scatter<1>(acc[j], r1, b3);                                                                                  \
        pw[64 * j] = r1[0] * ew;                                                                                        \
        SCHED_FENCE();                                                                                                  \
      }                                                                                                                 \
    }                                                                                                                   \
    if ((t) != NF - 1) {                                                                                                \
      _Pragma("unroll") for (int j = 0; j < 3; ++j)                                                                     \
        asm volatile("" : "+v"(acc[j][0]), "+v"(acc[j][1]), "+v"(acc[j][2]), "+v"(acc[j][3]), "+v"(acc[j][4]),          \
                     "+v"(acc[j][5]), "+v"(acc[j][6]), "+v"(acc[j][7]));             \
    }                                                                                                                   \
    SCHED_FENCE();                                                                                                      \
  } while (0)
#define WGMAX()                                                                                                         \
  do {                                                                                                                  \
    for (unsigned spin = 0; spin < (SPIN_MAX << 4); ++spin) {                                                           \
      if (__hip_atomic_load(flg + 3, __ATOMIC_RELAXED, __HIP_MEMORY_SCOPE_WORKGROUP) == 8u) break;                      \
      __builtin_amdgcn_s_sleep(1);                                                                                      \
    }                                                                                                                   \
    MEMBAR();                                                                                                           \
    const f32x4 ma = *reinterpret_cast<const f32x4*>(lds + L_ML), mb2 = *reinterpret_cast<const f32x4*>(lds + L_ML + 4); \
    Mg = fmaxf(fmaxf(fmaxf(ma[0], ma[1]), fmaxf(ma[2], ma[3])), fmaxf(fmaxf(mb2[0], mb2[1]), fmaxf(mb2[2], mb2[3])));   \
    ew = ex2((mw - Mg) * LOG2E);                                                                                        \
    if (lane == 0) *reinterpret_cast<f32x2*>(lds + L_PART + w * PS + DH) = f32x2{w == 0 ? Mg : 0.f, lw * ew};           \
    SCHED_FENCE();                                                                                                      \
  } while (0)
    float ew;
    VLD(PQ + 1);
    if (NF < nt) PVT(NF, 0);
    VLD(PQ + 2);
    if (NF + 1 < nt) PVT(NF + 1, 0);
    PVT(0, 0);
#pragma unroll
    for (int t = 1; t < NF; ++t) {
#pragma unroll
      for (int i = 0; i < WOB; ++i)
        if ((t == NF - 1) ? (i >= t - 1) : (i == t - 1)) WOLC(i);
      if (t == NF - 2) {
        WGMAX();
        if (w == ZW) {
#pragma unroll
          for (int k = 0; k < 2; ++k) {
            const float* zp = lds + L_ZP + (k ? 64 + (lane & 15) : lane);
            zold[k] = zadd[k] = fx_enc(((zp[0] + zp[80]) + (zp[160] + zp[240])) + (zp[320] + zp[400]));
          }
          const int za0 = (2 * GA + D + 80 * sp + lane) * 8, za1 = lane < 16 ? za0 + 512 : GEND * 16;
          asm volatile("buffer_atomic_add_x2 %0, %2, %4, 0 offen sc0\n\tbuffer_atomic_add_x2 %1, %3, %4, 0 offen sc0"
                       : "+v"(zold[0]), "+v"(zold[1]) : "v"(za0), "v"(za1), "s"(rwa) : "memory");
        }
        SCHED_FENCE();
      }
      PVT(t, t == NF - 1 ? WOB : t);
    }
  }
#define SLOWSUM(res, base, cc)                                                                                  \
  do {                                                                                                          \
    res = 0.f;                                                                                                  \
    _Pragma("unroll 1") for (int hh = 0; hh < HQ; ++hh) {                                                       \
      u32x2 v;                                                                                                  \
      for (unsigned spin = 0; spin < SPIN_MAX; ++spin) {                                                        \
        MEMBAR();                                                                                               \
        v = __builtin_amdgcn_raw_buffer_load_b64(rws, (cc) * 8, ((base) + hh * 320) * 16, 16);                  \
        if (v[1] == tag) break;                                                                                 \
      }                                                                                                         \
      res += bitsf(v[0]);                                                                                       \
    }                                                                                                           \
  } while (0)
  if (w == ZW) {
    {
      const f32x2* zp = reinterpret_cast<const f32x2*>(lds + L_ZP) + (lane < 40 ? lane : 0);
      const f32x2 zs = ((zp[0] + zp[40]) + (zp[80] + zp[120])) + (zp[160] + zp[200]);
      gst2(lane < 40 ? G3Z + h * 320 + 40 * sp + lane : GEND, zs[0], zs[1]);
    }
    asm volatile("s_waitcnt vmcnt(1)" : "+v"(zold[0]), "+v"(zold[1])::"memory");
#pragma unroll
    for (int k = 0; k < 2; ++k)
      if ((k == 0 || lane < 16) && (zold[k] >> 59) == 31u) {
        const int cz = 80 * sp + 64 * k + lane;
        float f = fx_dec(zold[k] + zadd[k]);
        if (__builtin_expect(!fx_exact(zold[k], zadd[k]), 0)) SLOWSUM(f, G3Z, cz);
        __builtin_amdgcn_raw_buffer_store_b64(u32x2{fbits(bfr(silu(bfr(f)))), tag}, rws, cz * 8, GG * 16, 16);
      }
  }
  __syncthreads();
  if (__builtin_expect(w < 2, 1)) {
    {
      f32x2 p2[8];
#pragma unroll
      for (int ww = 0; ww < 8; ++ww) p2[ww] = *reinterpret_cast<const f32x2*>(reinterpret_cast<const char*>(lds + L_PART + ww * PS) + mro);
      const f32x2 ua = (p2[0] + p2[1]) + (p2[2] + p2[3]), ub = (p2[4] + p2[5]) + (p2[6] + p2[7]);
      pub[0] = fbits(ua[0] + ub[0]);
      pub[2] = fbits(ua[1] + ub[1]);
      __builtin_amdgcn_raw_buffer_store_b128(pub, rws, mso, 0, 16);
    }
    SCHED_FENCE();
    const int t2 = tid < 96 ? tid : 95;
    const int pb = (G2 + h * NSP * 104 + t2) * 16, pm = (G2 + (h * NSP + c) * 104 + 96) * 16;
    const unsigned po = (unsigned)(size_t)(__attribute__((address_space(3))) float*)(lds + L_O) + 4 * t2;
    const int pg = lane < 20 ? (GG + 40 * sp + 20 * w + lane) * 16 : GEND * 16;
    const unsigned pl = (unsigned)(size_t)(__attribute__((address_space(3))) float*)(lds + L_GT) + 16 * (lane < 20 ? 20 * w + lane : 40 + tid);
    __builtin_amdgcn_s_sleep(PD1);
    {
      unsigned e0, e1, e2, e3, e4, e5, e6, e7, cnt;
#define DPC " row_mask:0xf bank_mask:0xf\n\t"
#define CHK(d, a, b) "v_bitop3_b32 " d ", " a ", " b ", %13 bitop3:0x7e\n\t"
#define ACC2(e, a, b) "v_fmac_f32 v44, " e ", " a "\n\tv_fmac_f32 v45, " e ", " b "\n\t"
      asm volatile(
          "s_mov_b32 %8, %16\n"
          "1:\n\t"
          "buffer_load_dwordx4 v[48:51], %18, %12, 0 offen sc1\n\t"
          "buffer_load_dwordx4 v[4:7], %9, %12, 0 offen sc1\n\t"
          "buffer_load_dwordx4 v[8:11], %10, %12, 0 offen sc1\n\t"
          "buffer_load_dwordx4 v[12:15], %10, %12, 0 offen offset:1664 sc1\n\t"
          "buffer_load_dwordx4 v[16:19], %10, %12, 0 offen offset:3328 sc1\n\t"
          "buffer_load_dwordx4 v[20:23], %10, %12, %14 offen sc1\n\t"
          "buffer_load_dwordx4 v[24:27], %10, %12, %14 offen offset:1664 sc1\n\t"
          "buffer_load_dwordx4 v[28:31], %10, %12, %14 offen offset:3328 sc1\n\t"
          "buffer_load_dwordx4 v[32:35], %10, %12, %15 offen sc1\n\t"
          "buffer_load_dwordx4 v[36:39], %10, %12, %15 offen offset:1664 sc1\n\t"
          "s_sub_u32 %8, %8, 1\n\t"
          "s_waitcnt vmcnt(8)\n\t"
          CHK("v41", "v5", "v7")
          "ds_write_b128 %19, v[48:51]\n\t"
          "v_max_f32_dpp v40, v4, v4 quad_perm:[1,0,3,2]" DPC
          "s_waitcnt vmcnt(7)\n\t"
          CHK("v46", "v9", "v11")
          "v_max_f32_dpp v40, v40, v40 quad_perm:[2,3,0,1]" DPC
          "s_waitcnt vmcnt(6)\n\t"
          CHK("v47", "v13", "v15")
          "v_max_f32_dpp v40, v40, v40 row_half_mirror" DPC
          "v_sub_f32 v42, v4, v40\n\t"
          "v_mul_f32 v42, 0x3fb8aa3b, v42\n\t"
          "v_exp_f32 v42, v42\n\t"
          "v_or3_b32 v41, v41, v46, v47\n\t"
          "v_mul_f32 v40, v6, v42\n\t"
          "s_waitcnt vmcnt(5)\n\t"
          CHK("v46", "v17", "v19")
          "v_add_f32_dpp v40, v40, v40 quad_perm:[1,0,3,2]" DPC
          "s_waitcnt vmcnt(4)\n\t"
          CHK("v47", "v21", "v23")
          "v_add_f32_dpp v40, v40, v40 quad_perm:[2,3,0,1]" DPC
          "v_or3_b32 v41, v41, v46, v47\n\t"
          "s_nop 0\n\t"
          "v_add_f32_dpp v40, v40, v40 row_half_mirror" DPC
          "v_rcp_f32 v40, v40\n\t"
          "s_nop 0\n\t"
          "v_mul_f32 v42, v42, v40\n\t"
          "s_nop 0\n\t"
          "v_readlane_b32 %0, v42, 0\n\tv_readlane_b32 %1, v42, 1\n\tv_readlane_b32 %2, v42, 2\n\tv_readlane_b32 %3, v42, 3\n\t"
          "v_readlane_b32 %4, v42, 4\n\tv_readlane_b32 %5, v42, 5\n\tv_readlane_b32 %6, v42, 6\n\tv_readlane_b32 %7, v42, 7\n\t"
          "v_mul_f32 v44, %0, v8\n\tv_mul_f32 v45, %0, v10\n\t"
          ACC2("%1", "v12", "v14") ACC2("%2", "v16", "v18") ACC2("%3", "v20", "v22")
          "s_waitcnt vmcnt(3)\n\t"
          ACC2("%4", "v24", "v26") CHK("v46", "v25", "v27")
          "s_waitcnt vmcnt(2)\n\t"
          ACC2("%5", "v28", "v30") CHK("v47", "v29", "v31")
          "v_or3_b32 v41, v41, v46, v47\n\t"
          "s_waitcnt vmcnt(1)\n\t"
          ACC2("%6", "v32", "v34") CHK("v46", "v33", "v35")
          "s_waitcnt vmcnt(0)\n\t"
          ACC2("%7", "v36", "v38")
          "v_cvt_pk_bf16_f32 v44, v44, v45\n\t"
          "ds_write_b32 %11, v44\n\t"
          CHK("v47", "v37", "v39")
          "v_or3_b32 v41, v41, v46, v47\n\t"
          "v_cmp_ne_u32 vcc, 0, v41\n\t"
          "s_cbranch_vccz 2f\n\t"
          "s_sleep %17\n\t"
          "s_cbranch_scc0 1b\n"
          "2:\n\t"
          "s_waitcnt lgkmcnt(0)"
          : "=&s"(e0), "=&s"(e1), "=&s"(e2), "=&s"(e3), "=&s"(e4), "=&s"(e5), "=&s"(e6), "=&s"(e7), "=&s"(cnt)
          : "v"(pm), "v"(pb), "v"(po), "s"(rwa), "s"(tag), "s"(3 * 1664), "s"(6 * 1664), "n"(SPIN_MAX), "n"(1), "v"(pg), "v"(pl)
          : "memory", "scc", "vcc", "v4", "v5", "v6", "v7", "v8", "v9", "v10", "v11", "v12", "v13", "v14", "v15", "v16", "v17", "v18", "v19",
            "v20", "v21", "v22", "v23", "v24", "v25", "v26", "v27", "v28", "v29", "v30", "v31", "v32", "v33", "v34", "v35", "v36", "v37",
            "v38", "v39", "v40", "v41", "v42", "v44", "v45", "v46", "v47", "v48", "v49", "v50", "v51");
    }
    __syncthreads();
  } else if (w == 2) {
    const u64 t7 = __builtin_amdgcn_s_memrealtime();
    if (lane == 0) __builtin_amdgcn_raw_buffer_store_b128(u32x4{(unsigned)tstart, tag, (unsigned)t7, tag}, rws, (GPR + b) * 16, 0, 16);
    __syncthreads();
  } else {
    if (w >= 4) {
#pragma unroll
      for (int i = 0; i < 8; ++i) {
        AWAIT(WO[i], 7 - i);
        *reinterpret_cast<u32x4*>(lds + L_WO + u * 4 + i * 960) = WO[i];
      }
      asm volatile("s_waitcnt lgkmcnt(0)" ::: "memory");
      if (lane == 0) __hip_atomic_fetch_add(flg + 2, 1u, __ATOMIC_RELAXED, __HIP_MEMORY_SCOPE_WORKGROUP);
    }
    for (unsigned spin = 0; spin < (SPIN_MAX << 4); ++spin) {
      if (__hip_atomic_load(flg + 2, __ATOMIC_RELAXED, __HIP_MEMORY_SCOPE_WORKGROUP) == 4u) break;
      __builtin_amdgcn_s_sleep(1);
    }
    asm volatile("s_waitcnt lgkmcnt(0)" ::: "memory");
    u32x4 wb[6];
    {
      typedef __attribute__((address_space(3))) v4i16_t* ltr;
      const unsigned la = (unsigned)(size_t)(__attribute__((address_space(3))) float*)(lds + L_WO) + (8 * (lane >> 4) + ((lane >> 2) & 3)) * 160 + 32 * (w - 3) + 8 * (lane & 3);
#pragma unroll
      for (int k = 0; k < 6; ++k) {
        const u32x2 t0 = __builtin_bit_cast(u32x2, __builtin_amdgcn_ds_read_tr16_b64_v4i16((ltr)(size_t)(la + 32 * k * 160)));
        const u32x2 t1 = __builtin_bit_cast(u32x2, __builtin_amdgcn_ds_read_tr16_b64_v4i16((ltr)(size_t)(la + (32 * k + 4) * 160)));
        wb[k] = u32x4{t0[0], t0[1], t1[0], t1[1]};
      }
    }
    const int cy = 80 * sp + 16 * (w - 3) + (lane & 15);
    float xv = bf_lo(reinterpret_cast<const unsigned short*>(lds + L_XS)[cy]);
    gu64* pa = (gu64*)(gw + 2 * GA + cy);
    gu16* pout = (gu16*)(out + cy);
    typedef __attribute__((address_space(3))) const u32x4* la4;
    la4 pa4 = (la4)(lds + L_O) + 4 * (lane & 3) + (lane >> 4);
    int cy8 = cy * 8;
    typedef __attribute__((address_space(3))) const u32x2* la2;
    la2 pgt = (la2)(lds + L_GT) + 16 * (w - 3) + (lane & 15);
    asm volatile("" : "+v"(pgt));
    unsigned k0 = (1u << 27) - (0x411u << 20), k1 = k0 + (1u << 22), k2 = 1u << 17;
    double c0 = 262144.0 + 512.0, c1 = -(262144.0 + 32 * 512.0);
    float f0 = 512.f, f1 = 0x1p-9f;
    asm volatile("" : "+v"(wb[0]), "+v"(wb[1]), "+v"(wb[2]), "+v"(wb[3]), "+v"(wb[4]), "+v"(wb[5]));
    asm volatile("" : "+v"(xv), "+v"(pa), "+v"(pout), "+v"(pa4), "+v"(cy8), "+v"(k0), "+v"(k1), "+v"(k2), "+s"(c0), "+s"(c1), "+s"(f0), "+s"(f1));
    __syncthreads();
    const u32x4 a1 = pa4[0], a2 = pa4[16];
    SCHED_FENCE();
    u32x2 gg = *pgt;
    SCHED_FENCE();
#define MFMA(a, b, cc) __builtin_amdgcn_mfma_f32_16x16x32_bf16(__builtin_bit_cast(bf16x8_t, a), __builtin_bit_cast(bf16x8_t, b), cc, 0, 0, 0)
    const f32x4 z4 = {0.f, 0.f, 0.f, 0.f};
    const f32x4 d2 = MFMA(a1, wb[2], z4), d3 = MFMA(a1, wb[3], z4);
    SCHED_FENCE();
    f32x4 d0 = MFMA(a1, wb[0], z4), d1 = MFMA(a1, wb[1], z4);
    SCHED_FENCE();
    d0 = MFMA(a2, wb[4], d0);
    SCHED_FENCE();
    d1 = MFMA(a2, wb[5], d1);
    SCHED_FENCE();
    if (lane < 16) {
      const float q = ((d2[2] + d3[3]) + d0[0]) + d1[1];
      const bool bad = !(fabsf(q) < f0);
      const u64 db = __builtin_bit_cast(u64, (double)(bad ? 0.f : q) + c0);
      const unsigned fl = (bad ? k1 : k0) + (fabsf(q) < f1 ? k2 : 0u);
      const u64 add = ((u64)((unsigned)(db >> 32) + fl) << 32) | (unsigned)db;
      const u64 old = __hip_atomic_fetch_add(pa, add, __ATOMIC_RELAXED, __HIP_MEMORY_SCOPE_AGENT);
      __builtin_amdgcn_raw_buffer_store_b64(u32x2{fbits(q), tag}, rws, cy8, (G3Y + h * 320) * 16, 16);
      SCHED_FENCE();
      unsigned lim = (gg[1] == tag && !bad) ? (32u << 17) - (fl - k0) : 0u;
      asm volatile("" : "+v"(lim));
      SCHED_FENCE();
      float sv = (float)(__builtin_bit_cast(double, (old & ((1ull << 49) - 1ull)) + db) + c1);
      asm("" : "+v"(sv));
      unsigned res = bfbits(xv + bfr(bfr(sv) * bitsf(gg[0])));
      const unsigned rel = (unsigned)(old >> 32) - (31u << 27);
      TIE(res);
      if (rel < lim) *pout = res;
      SCHED_FENCE();
      if ((old >> 59) == 31u) {
        if (__builtin_expect(rel >= lim, 0)) {
          if (!fx_exact(old, add)) SLOWSUM(sv, G3Y, cy);
          for (unsigned spin = 0; gg[1] != tag && spin < SPIN_MAX; ++spin) {
            MEMBAR();
            gg = __builtin_amdgcn_raw_buffer_load_b64(rws, cy8, GG * 16, 16);
          }
          *pout = bfbits(xv + bfr(bfr(sv) * bitsf(gg[0])));
        }
        if (cy == 0) {
          __hip_atomic_store(reinterpret_cast<unsigned*>(gw + 2 * GEP), tag, __ATOMIC_RELAXED, __HIP_MEMORY_SCOPE_AGENT);
          const unsigned t0 = (unsigned)__builtin_amdgcn_s_memrealtime();
          int st[8], mn = 1 << 30, mx = -(1 << 30), sm = 0;
#pragma unroll 1
          for (int i = 0; i < 8; ++i) {
            u32x4 v;
            for (unsigned spin = 0; spin < SPIN_MAX; ++spin) {
              MEMBAR();
              v = gld2(GPR + 64 + i, 0);
              if (gok2(v)) break;
            }
            const int d = (int)(v[0] - t0);
            mn = d < mn ? d : mn; mx = d > mx ? d : mx; sm += d;
            if (i == PX) st[0] = d;
          }
          const int val = PROBE == 0 ? 0 : PROBE == 1 ? mx - mn : PROBE == 2 ? sm / 8 - mn : st[0] - mn;
          const unsigned until = 600u + 10u * (unsigned)(val < 0 ? 0 : val > 400 ? 400 : val);
          for (unsigned spin = 0; spin < (1u << 20); ++spin)
            if ((unsigned)__builtin_amdgcn_s_memrealtime() - t0 >= until) break;
        }
      }
    }
  }
}

}

extern "C" void kernel_launch(void* const* d_in, const int* in_sizes, int n_in, void* d_out, int out_size, void* d_ws,
                              size_t ws_size, hipStream_t stream) {
  const bf16* x = (const bf16*)d_in[0];
  const bf16* kc = (const bf16*)d_in[1];
  const bf16* vc = (const bf16*)d_in[2];
  const bf16* wq = (const bf16*)d_in[3];
  const bf16* wo = (const bf16*)d_in[4];
  const bf16* w1 = (const bf16*)d_in[6];
  const bf16* w3 = (const bf16*)d_in[7];
  const bf16* w2 = (const bf16*)d_in[8];
  u64* gw = (u64*)d_ws;
  fused_decode<<<NWG, NT, 0, stream>>>(x, kc, vc, wq, wo, w1, w3, w2, gw, (bf16*)d_out);
}
```

```cpp
#define PD1X 24
#define PHW 28
#define TA 250
#include <hip/hip_runtime.h>
#include <hip/hip_bf16.h>
#include <stdint.h>

typedef __hip_bfloat16 bf16;
typedef unsigned long long u64;
typedef __attribute__((address_space(1))) unsigned long long gu64;
typedef __attribute__((address_space(1))) unsigned short gu16;
typedef unsigned u32x4 __attribute__((ext_vector_type(4)));
typedef unsigned u32x2 __attribute__((ext_vector_type(2)));
typedef float f32x4 __attribute__((ext_vector_type(4)));
typedef __bf16 bf16x2_t __attribute__((ext_vector_type(2)));
typedef __bf16 bf16x8_t __attribute__((ext_vector_type(8)));
typedef short v4i16_t __attribute__((ext_vector_type(4)));

namespace {

constexpr int D = 640, HQ = 32, DH = 192, SK = 32768, WIN = 4096, FF = 1536;
constexpr int NSP = 8;
constexpr int NWG = HQ * NSP;
constexpr int NT = 512;
constexpr float SCALE = 0.07216878364870322f;
constexpr float LOG2E = 1.4426950408889634f;
constexpr unsigned SPIN_MAX = 1u << 16;
constexpr int WND = 8;
constexpr int WOL = 7;
constexpr int PQ = 4;
constexpr int NF = 7;
#define DXT 2, -1, 2, -1, 1, -3, 1, -1
constexpr int DX[8] = {DXT};
constexpr unsigned dx_pack() {
  unsigned r = 0;
  for (int i = 0; i < 8; ++i) r |= (unsigned)(DX[i] + 4) << (4 * i);
  return r;
}
constexpr unsigned dx_pos() {
  unsigned r = 0, a = 0;
  for (int i = 0; i < 8; ++i) { r |= a << (4 * i); a += DX[i] > 0 ? DX[i] : 0; }
  return r;
}
constexpr unsigned long long dx_don() {
  unsigned long long r = 0;
  int k = 0;
  for (int i = 0; i < 8; ++i)
    for (int q = 0; q < -DX[i]; ++q) r |= (unsigned long long)(i * 4 + q) << (5 * k++);
  return r;
}
constexpr bool dx_ok() {
  int a = 0, p = 0;
  bool in = true;
  for (int i = 0; i < 8; ++i) { a += DX[i]; p += DX[i] > 0 ? DX[i] : 0; in = in && DX[i] >= -4 && DX[i] <= 4; }
  return a == 0 && p <= 8 && in;
}
constexpr int dx_check[dx_ok() ? 1 : -1] = {0};
constexpr int KE = 8;
#define PS1 1
constexpr int PD1 = PD1X;
#define WOP 2, 2, 2, 2, 2, 4, 6, 8, 8, 8
constexpr int WON[10] = {WOP};
#define QS 1
#define SSL 1, 2, 3, 4, 5, 6, 8, 7, 7
#define XS 9
#define ZSL 12
#define GSL 20
constexpr int SS[9] = {SSL};
constexpr bool ss_ok() {
  bool ok = true;
  for (int t = 0; t < 9; ++t) ok = ok && SS[t] >= QS && SS[t] <= XS && SS[t] >= (t < 7 ? 3 * t - 10 : 5);
  return ok && ZSL > XS && GSL > ZSL;
}
constexpr int ss_check[ss_ok() ? 1 : -1] = {0};
constexpr int PVN(int m) {
  return m < XS + 3 ? 0 : (3 * (m - XS - 2) < m - 7 ? 3 * (m - XS - 2) : m - 7);
}
#define XOT 0, 0, 0, 0, 0, 0, 0, 0
constexpr int XO[8] = {XOT};
constexpr unsigned long long xo_pack() {
  unsigned long long r = 0;
  for (int i = 0; i < 8; ++i) r |= (unsigned long long)(XO[i] + 64) << (8 * i);
  return r;
}

constexpr int G1 = 0;
constexpr int G1F = G1 + HQ * 288;
constexpr int G2 = G1F + 8 * 960;
constexpr int G3Y = G2 + NWG * 104;
constexpr int G3Z = G3Y + 320 * 32;
constexpr int GG = G3Z + 320 * 32;
constexpr int GZF = GG + 320;
constexpr int GA = GZF + 256;
constexpr int GEP = GA + 640;
constexpr int GST = GEP + 8;
constexpr int GEND = GST + 256;

__device__ __forceinline__ unsigned fbits(float x) { return __builtin_bit_cast(unsigned, x); }
__device__ __forceinline__ float bitsf(unsigned x) { return __builtin_bit_cast(float, x); }
#define gok2(v) ((v)[1] == tag && (v)[3] == tag)
#define gst2(unit, a, b) __builtin_amdgcn_raw_buffer_store_b128(u32x4{fbits(a), tag, fbits(b), tag}, rws, (unit) * 16, 0, 16)
#define gld2(unit, cunit) __builtin_amdgcn_raw_buffer_load_b128(rws, (unit) * 16, (cunit) * 16, 16)
#define MEMBAR() asm volatile("" ::: "memory")
typedef float f32x2 __attribute__((ext_vector_type(2)));

__device__ __forceinline__ float bf_lo(unsigned u) { return __builtin_bit_cast(float, u << 16); }
__device__ __forceinline__ float bf_hi(unsigned u) { return __builtin_bit_cast(float, u & 0xffff0000u); }
__device__ __forceinline__ float bfr(float x) { return __bfloat162float(__float2bfloat16(x)); }
__device__ __forceinline__ unsigned short bfbits(float x) {
  return __builtin_bit_cast(unsigned short, __float2bfloat16(x));
}
__device__ __forceinline__ float dot2(unsigned a, unsigned b, float c) {
  return __builtin_amdgcn_fdot2_f32_bf16(__builtin_bit_cast(bf16x2_t, a), __builtin_bit_cast(bf16x2_t, b), c, false);
}
template <int CTRL>
__device__ __forceinline__ float dpp(float x) {
  return __builtin_bit_cast(float, __builtin_amdgcn_mov_dpp(__builtin_bit_cast(int, x), CTRL, 0xf, 0xf, true));
}
__device__ __forceinline__ float grp8_sum(float x) {
  x += dpp<0xB1>(x);
  x += dpp<0x4E>(x);
  x += dpp<0x141>(x);
  return x;
}
__device__ __forceinline__ float swap16_add(float a, float b) {
  auto s = __builtin_amdgcn_permlane16_swap(fbits(a), fbits(b), false, false);
  return bitsf((unsigned)s[0]) + bitsf((unsigned)s[1]);
}
__device__ __forceinline__ float swap32_add(float a, float b) {
  auto t = __builtin_amdgcn_permlane32_swap(fbits(a), fbits(b), false, false);
  return bitsf((unsigned)t[0]) + bitsf((unsigned)t[1]);
}
__device__ __forceinline__ float xg_sum(float x) {
  x += dpp<0x128>(x);
  x = swap16_add(x, x);
  return swap32_add(x, x);
}
__device__ __forceinline__ float xg_max(float x) {
  x = fmaxf(x, dpp<0x128>(x));
  auto s = __builtin_amdgcn_permlane16_swap(fbits(x), fbits(x), false, false);
  x = fmaxf(bitsf((unsigned)s[0]), bitsf((unsigned)s[1]));
  auto t = __builtin_amdgcn_permlane32_swap(fbits(x), fbits(x), false, false);
  return fmaxf(bitsf((unsigned)t[0]), bitsf((unsigned)t[1]));
}
template <int NJ>
__device__ __forceinline__ void xg_scatter(const float (&a)[8 * NJ], float (&r)[NJ], const bool b3) {
#pragma unroll
  for (int j = 0; j < NJ; ++j) {
    float p[4];
#pragma unroll
    for (int e = 0; e < 4; ++e) p[e] = swap32_add(a[8 * j + e], a[8 * j + e + 4]);
    const float q0 = swap16_add(p[0], p[2]), q1 = swap16_add(p[1], p[3]);
    r[j] = (b3 ? q1 : q0) + dpp<0x128>(b3 ? q0 : q1);
  }
}
__device__ __forceinline__ float wave_sum(float x) { return xg_sum(grp8_sum(x)); }
__device__ __forceinline__ float ex2(float x) { return __builtin_amdgcn_exp2f(x); }
__device__ __forceinline__ float silu(float x) { return x / (1.0f + __expf(-x)); }
__device__ __forceinline__ float rdlane(float x, int l) {
  return __builtin_bit_cast(float, __builtin_amdgcn_readlane(__builtin_bit_cast(int, x), l));
}

__device__ __forceinline__ u64 fx_enc(float v) {
  const bool bad = !(fabsf(v) < 512.f), small = fabsf(v) < 0x1p-9f;
  const u64 d = __builtin_bit_cast(u64, (double)(bad ? 0.f : v) + (262144.0 + 512.0));
  const unsigned hi = (unsigned)(d >> 32) + ((1u << 27) - (0x411u << 20)) + (bad ? 1u << 22 : 0u) + (small ? 1u << 17 : 0u);
  return ((u64)hi << 32) | (unsigned)d;
}
__device__ __forceinline__ float fx_dec(u64 t) {
  const unsigned hi = ((unsigned)(t >> 32) & 0x1ffffu) | (0x411u << 20);
  return (float)(__builtin_bit_cast(double, ((u64)hi << 32) | (unsigned)t) - (262144.0 + 32 * 512.0));
}
__device__ __forceinline__ bool fx_exact(u64 old, u64 add) {
  const unsigned o = (unsigned)(old >> 49) & 0x3ffu, a = (unsigned)(add >> 49) & 0x3ffu;
  return o + a < 32u;
}
template <int N> struct IC { static constexpr int value = N; };
#define SCHED_FENCE() __builtin_amdgcn_sched_barrier(0)
#define ALD(dst, rs, vo, so, imm) \
  asm volatile("buffer_load_dwordx4 %0, %1, %2, %3 offen offset:%4 nt" : "=v"(dst) : "v"(vo), "s"(rs), "s"(so), "n"(imm) : "memory")
#define AWL(dst, rs, vo, so, imm)                                                                                      \
  asm volatile("s_waitcnt vmcnt(8)\n\ts_cmp_le_i32 %5, 8\n\ts_cbranch_scc0 4\n\ts_waitcnt vmcnt(7)\n\t"                 \
               "s_cmp_le_i32 %5, 7\n\ts_cbranch_scc0 1\n\ts_waitcnt vmcnt(6)\n\t"                                        \
               "buffer_load_dwordx4 %0, %1, %2, %3 offen offset:%4 nt"                                                 \
               : "=v"(dst) : "v"(vo), "s"(rs), "s"(so), "n"(imm), "s"(wl) : "memory", "scc")
#define AWT(dst, rs, vo, so, imm, tref, lim, mul, wn)                                                                  \
  asm volatile("s_memrealtime s[98:99]\n\ts_waitcnt vmcnt(%5)\n"                                                       \
               "8:\n\ts_waitcnt lgkmcnt(0)\n\ts_sub_u32 s98, s98, %6\n\ts_mul_i32 s98, s98, %8\n\ts_cmp_ge_i32 s98, %7\n\t" \
               "s_cbranch_scc1 9f\n\ts_sleep %9\n\ts_memrealtime s[98:99]\n\ts_branch 8b\n"                              \
               "9:\n\tbuffer_load_dwordx4 %0, %1, %2, %3 offen offset:%4 nt"                                            \
               : "=v"(dst) : "v"(vo), "s"(rs), "s"(so), "n"(imm), "n"((wn) - 1), "s"(tref), "n"(lim), "s"(mul), "n"(SLP) \
               : "memory", "scc", "s98", "s99")
#define WK 8
#define SLP 1
#define WALL 9
#define TB 28
#define TB2 TB
#define TE (TA + 27 * TB + 20 * TB2)
#define P0 21
#define WS 5
#define WS2 WS
#define TD 100
#define AWAIT(v, n) asm volatile("s_waitcnt vmcnt(%1)" : "+v"(v) : "n"(n) : "memory")
#define TIE(v) asm volatile("" : "+v"(v))
__device__ __forceinline__ u32x4 mkrs(const void* p, unsigned bytes) {
  const u64 a = (u64)p;
  return u32x4{(unsigned)a, (unsigned)(a >> 32) & 0xffffu, bytes, 0x00020000u};
}
#define STAMP(i)
#define STAMPT(i) STAMP(i)
#define WAVE_SYNC()                                        \
  do {                                                     \
    __builtin_amdgcn_fence(__ATOMIC_RELEASE, "wavefront"); \
    __builtin_amdgcn_wave_barrier();                       \
    __builtin_amdgcn_fence(__ATOMIC_ACQUIRE, "wavefront"); \
  } while (0)

constexpr int L_WO = 0;
constexpr int L_XS = L_WO + DH * 40;
constexpr int PS = 256;
constexpr int L_PART = L_XS + 4 * 320;
constexpr int L_XN = L_PART + 8 * PS;
constexpr int L_ZP = L_XN + 4 * 64;
constexpr int L_QS = L_ZP + 6 * 80;
constexpr int L_ML = L_QS + 96;
constexpr int L_O = L_ML + 16;
constexpr int L_H = L_O + 128;
constexpr int L_GU = L_H + 48;
constexpr int L_GT = L_GU + 96;
constexpr int L_FLG = L_GT + 168 * 4;
constexpr int L_END = L_FLG + 4;

__global__ void __launch_bounds__(NT, 2)
fused_decode(const bf16* __restrict__ x, const bf16* __restrict__ kc, const bf16* __restrict__ vc,
             const bf16* __restrict__ wq, const bf16* __restrict__ wo, const bf16* __restrict__ w1,
             const bf16* __restrict__ w3, const bf16* __restrict__ w2, u64* __restrict__ gw, bf16* __restrict__ out) {
  __shared__ __attribute__((aligned(16))) float lds[L_END];
  const int b = blockIdx.x;
  const int xcd = b & 7, jj = b >> 3;
  const int h = xcd * 4 + (jj >> 3), sp = jj & 7;
  const int ha = jj, spa = xcd;
  const int w = __builtin_amdgcn_readfirstlane(threadIdx.x >> 6);
  const int tid = threadIdx.x, lane = tid & 63;
  const int g = lane >> 3, c = lane & 7;
  const int rec = ha * NSP + spa;
  unsigned* flg = reinterpret_cast<unsigned*>(lds + L_FLG);

#define CP 1
#define CPW 4
#define CPM 7
  unsigned cpv = (jj & 7) * 4096 + lane * 64;
#define CPLD()                                                                                                              \
  asm volatile("s_cmp_lg_u32 %1, %2\n\ts_cbranch_scc1 7f\n"                                                                 \
               ".Lfd_beg:\n\ts_getpc_b64 s[96:97]\n\ts_and_b32 s97, s97, 0xffff\n\ts_mov_b32 s98, .Lfd_end-.Lfd_beg-8\n\t"   \
               "s_mov_b32 s99, 0x20000\n\tbuffer_load_dword %0, %0, s[96:99], 0 offen\n7:"                                   \
               : "+v"(cpv) : "s"(w + 8 * (jj & ~CPM)), "n"(CPW) : "memory", "scc", "s96", "s97", "s98", "s99")
#define T0 0
  u64 tstart64;
  if (T0) tstart64 = __builtin_amdgcn_s_memrealtime();
  SCHED_FENCE();
  if (CP == 1) CPLD();
  SCHED_FENCE();
  asm volatile("" ::"s"(x), "s"(kc), "s"(vc), "s"(wq), "s"(wo), "s"(w1), "s"(w3), "s"(w2), "s"(gw), "s"(out));
  SCHED_FENCE();
  if (!T0) tstart64 = __builtin_amdgcn_s_memrealtime();
  SCHED_FENCE();
  const auto rws = __builtin_amdgcn_make_buffer_rsrc((void*)gw, (short)0, GEND * 16, 0x00020000);
  const size_t kvw = ((size_t)ha * SK + (SK - WIN)) * DH;
  const u32x4 rk = mkrs(kc + kvw, WIN * DH * 2), rv = mkrs(vc + kvw, WIN * DH * 2);
  const u32x4 rwa = mkrs(gw, GEND * 16);
#define CM 0
#define JO (CM ? 1024 : 128)
  const int kvo = CM ? lane * 16 : (g * DH + 8 * c) * 2;
  const int dx = (int)((dx_pack() >> (4 * xcd)) & 15u) - 4;
  const int wr = w < 4 ? (w + 3) & 3 : w;
  const int nt = 8 + ((unsigned)(wr - dx) >> 31) - ((unsigned)(7 - w + dx) >> 31);
  const int don = (int)(dx_don() >> (5 * (((dx_pos() >> (4 * xcd)) + wr) & 7u))) & 31;
#define LM 0
#define RT 1
#define TIX(s_, b_, u_) (LM == 0 ? ((s_) * 8 + (b_)) * 8 + (u_) : LM == 1 ? (u_) * 64 + (s_) * 8 + (b_) : LM == 2 ? (u_) * 64 + (b_) * 8 + (s_) : \
                         LM == 3 ? ((s_) * 8 + (u_)) * 8 + (b_) : LM == 4 ? ((b_) * 8 + (u_)) * 8 + (s_) : ((b_) * 8 + (s_)) * 8 + (u_))
  const int hr = RT == 1 ? ha : RT == 2 ? ha >> 2 : RT == 3 ? ha >> 1 : 0;
  const int kvb = TIX(spa, (w + (ha >> 3)) & 7, 0) * 8 * DH * 2;
  const int ko8 = TIX(don >> 2, (7 - (don & 3) + (ha >> 3)) & 7, (7 + hr) & 7) * 8 * DH * 2;
  const int wl = __builtin_amdgcn_readfirstlane((w < 4 ? WOL : WND) + (nt - 8) + (dx < 0 ? 1 : 0));
#define KVOFF(t) ((t) == 8 ? ko8 : kvb + TIX(0, 0, ((t) + hr) & 7) * 8 * DH * 2)
#define KLD(t, j) ALD(K[t][j], rk, kvo, KVOFF(t), JO * (j))
#define CR 0
#define PH 0
#define RP 16
  unsigned ta, te, tk, stv, tstart;
  const int sto = (GST + ha * 8) * 16 + 4 * c;
#define STLD() asm volatile("buffer_load_dword %0, %1, %2, 0 offen sc1" : "=v"(stv) : "v"(sto), "s"(rwa) : "memory")
#define TK 760
#define SH 49
#define SL 22
  const int km = w < 4 ? SH : SL;
#define TG 0
#define JJ(j) (TG == 1 ? 2 : TG == 2 ? ((j) == 0 ? 0 : 2) : (j))
#define KWL(t, j)                                                                                         \
  do {                                                                                                    \
    if ((t) >= NF) AWT(K[t][j], rk, kvo, KVOFF(t), JO * (j), ta, (6 * (t) - 21 + JJ(j)) * TB * 8, nt, WALL); \
    else AWL(K[t][j], rk, kvo, KVOFF(t), JO * (j));                                                      \
  } while (0)
#define VWL(t, j)                                                                                         \
  do {                                                                                                    \
    if ((t) < NF) AWT(V[t][j], rv, kvo, KVOFF(t), JO * (j), te, -(20 - 3 * (t) - JJ(j)) * TB2 * 8, nt, WALL); \
    else AWT(V[t][j], rv, kvo, KVOFF(t), JO * (j), ta, (6 * (t) - 18 + JJ(j)) * TB * 8, nt, WALL);          \
  } while (0)
#define EPLD() asm volatile("buffer_load_dword %0, off, %1, %2" : "=v"(epoch) : "s"(rwa), "s"(GEP * 16) : "memory")
  u32x4 K[NF + 2][3], V[NF + 2][3];

  const int fi = (xcd & 1) ? 19 : 8;
  const bool fidle = (unsigned)(jj - fi) < 2u;
  const int fp = jj > fi ? jj - 2 : jj;
  const int fcol = fp / 5, frp = fp - 5 * fcol;
  unsigned epoch, tag;
  if (w < 4) {
    const int fw = 4 * sp + w, ct = fw >= 20 ? 2 : fw >= 10 ? 1 : 0;
    const int r0 = 64 * (fw - 10 * ct);
    const int R = 128 * frp + 32 * w;
    const u32x4 xa = *reinterpret_cast<const u32x4*>(x + 8 * lane);
    const u32x4 xb = *reinterpret_cast<const u32x4*>(x + 512 + 8 * (lane & 15));
    u32x4 WQ[8], F[4];
    {
      const auto rq = __builtin_amdgcn_make_buffer_rsrc((void*)wq, (short)0, D * HQ * DH * 2, 0x00020000);
      const int qo = ((r0 + 8 * g) * (HQ * DH) + h * DH + 64 * ct + 8 * c) * 2;
#pragma unroll
      for (int i = 0; i < 8; ++i) WQ[i] = __builtin_amdgcn_raw_buffer_load_b128(rq, qo, i * HQ * DH * 2, 2);
      const auto rf = __builtin_amdgcn_make_buffer_rsrc((void*)(fcol >= 3 ? w3 : w1), (short)0, fidle ? 0 : D * FF * 2, 0x00020000);
      const int fo = ((R + 4 * g) * FF + 192 * xcd + 64 * (fcol >= 3 ? fcol - 3 : fcol) + 8 * c) * 2;
#pragma unroll
      for (int i = 0; i < 4; ++i) F[i] = __builtin_amdgcn_raw_buffer_load_b128(rf, fo, i * FF * 2, 2);
    }
    if (fidle && tid < 40)
      __builtin_amdgcn_raw_buffer_store_b128(u32x4{0u, 0u, 0u, 0u}, rws, (GA + (jj - fi) * 320 + 40 * xcd + lane) * 16, 0, 16);
    SCHED_FENCE();
    EPLD();
    if (tid < 4) flg[tid] = 0u;
    SCHED_FENCE();
    STAMP(2);
    __syncthreads();
    SCHED_FENCE();
    STAMP(3);
    unsigned short* xs = reinterpret_cast<unsigned short*>(lds + L_XS + w * 320);
    const bool lo16 = lane < 16;
    *reinterpret_cast<u32x4*>(xs + 8 * lane) = xa;
    if (lo16) *reinterpret_cast<u32x4*>(xs + 512 + 8 * lane) = xb;
    float xf[8], xg[8];
#pragma unroll
    for (int i = 0; i < 4; ++i) {
      xf[2 * i] = bf_lo(xa[i]);
      xf[2 * i + 1] = bf_hi(xa[i]);
      xg[2 * i] = bf_lo(xb[i]);
      xg[2 * i + 1] = bf_hi(xb[i]);
    }
    float sum = 0.f;
#pragma unroll
    for (int i = 0; i < 8; ++i) sum += xf[i] + (lo16 ? xg[i] : 0.f);
    sum = wave_sum(sum);
    const float mean = sum * (1.0f / D);
    float sq = 0.f;
#pragma unroll
    for (int i = 0; i < 8; ++i) {
      const float d1 = xf[i] - mean, d2 = xg[i] - mean;
      sq += d1 * d1 + (lo16 ? d2 * d2 : 0.f);
    }
    sq = wave_sum(sq);
    const float mb = bfr(mean);
    const float vb = bfr(sq * (1.0f / D));
    const float den = bfr(sqrtf(bfr(vb + 1e-5f)));
    WAVE_SYNC();
    auto xnorm = [&](int r) -> unsigned short { return bfbits(bfr(bf_lo(xs[r]) - mb) / den); };
    {
      unsigned short* xn = reinterpret_cast<unsigned short*>(lds + L_XN + w * 64);
      xn[lane] = xnorm((r0 < D ? r0 : 0) + lane);
      xn[64 + (lane & 31)] = xnorm(R + (lane & 31));
      WAVE_SYNC();
      const u32x4 XQ = *reinterpret_cast<const u32x4*>(xn + 8 * g);
      const u32x2 XF = *reinterpret_cast<const u32x2*>(xn + 64 + 4 * g);
      float acc[16], r2[2];
#pragma unroll
      for (int i = 0; i < 16; ++i) acc[i] = 0.f;
#pragma unroll
      for (int i = 0; i < 8; ++i) {
        const unsigned d = XQ[i >> 1], lo = (i & 1) ? d >> 16 : d & 0xffffu, hi = (i & 1) ? d & 0xffff0000u : d << 16;
#pragma unroll
        for (int k = 0; k < 4; ++k) {
          acc[2 * k] = dot2(WQ[i][k], lo, acc[2 * k]);
          acc[2 * k + 1] = dot2(WQ[i][k], hi, acc[2 * k + 1]);
        }
      }
#pragma unroll
      for (int i = 0; i < 4; ++i) {
        const unsigned d = XF[i >> 1], lo = (i & 1) ? d >> 16 : d & 0xffffu, hi = (i & 1) ? d & 0xffff0000u : d << 16;
#pragma unroll
        for (int k = 0; k < 4; ++k) {
          acc[8 + 2 * k] = dot2(F[i][k], lo, acc[8 + 2 * k]);
          acc[8 + 2 * k + 1] = dot2(F[i][k], hi, acc[8 + 2 * k + 1]);
        }
      }
      xg_scatter<2>(acc, r2, g & 1);
#pragma unroll
      for (int j = 0; j < 2; ++j) lds[L_PART + w * 128 + 64 * j + 8 * c + g] = r2[j];
    }
    STAMP(5);
    asm volatile("s_waitcnt lgkmcnt(0)" ::: "memory");
    if (lane == 0) __hip_atomic_fetch_add(flg, 1u, __ATOMIC_RELAXED, __HIP_MEMORY_SCOPE_WORKGROUP);
    SCHED_FENCE();
  } else {
    STAMP(1);
#pragma unroll
    for (int m = 0; m < KE; ++m) KLD(m / 3, m % 3);
    SCHED_FENCE();
    if (CR && w == 7 && jj < 4)
      __builtin_amdgcn_raw_buffer_store_b32((unsigned)tstart64, rws, lane < 8 ? (GST + (jj * 8 + lane) * 8) * 16 + 4 * xcd : GEND * 16, 0, 16);
    SCHED_FENCE();
    EPLD();
    SCHED_FENCE();
    STAMP(2);
    __syncthreads();
    SCHED_FENCE();
    STAMP(3);
  }
  const int lz = lane < 60 ? lane : 59;
  const int zr = lz / 10, zc = lz - 10 * zr;
  int w2o = (zr * D + 8 * zc) * 2;
  asm volatile("" : "+v"(w2o));
  if (w == 0) {
    for (unsigned spin = 0; spin < (SPIN_MAX << 4); ++spin) {
      if (__hip_atomic_load(flg, __ATOMIC_RELAXED, __HIP_MEMORY_SCOPE_WORKGROUP) == 4u) break;
      __builtin_amdgcn_s_sleep(1);
    }
    asm volatile("s_waitcnt lgkmcnt(0)" ::: "memory");
    AWAIT(epoch, 0);
    tag = __builtin_amdgcn_readfirstlane(epoch) + 1u;
    const f32x2* pp = reinterpret_cast<const f32x2*>(lds + L_PART) + lane;
    const f32x2 a01 = pp[0] + pp[64], a23 = pp[128] + pp[192];
    const bool two = sp == 2 && lane < 32;
    const f32x2 sm = two ? a01 : a01 + a23;
    const int ct0 = sp >= 5 ? 2 : sp >= 3 ? 1 : 0, slot = sp - (sp >= 5 ? 5 : sp >= 3 ? 2 : 0);
    const int uf = fidle ? GZF + 32 * xcd + 16 * (jj - fi) : G1F + xcd * 960 + fp * 32;
    const int un = lane < 32 ? G1 + h * 288 + slot * 96 + ct0 * 32 + lane : uf + lane - 32;
    if (lane < 33 || !fidle) gst2(un, sm[0], sm[1]);
    if (two) gst2(G1 + h * 288 + 32 + lane, a23[0], a23[1]);
    SCHED_FENCE();
  }
  asm volatile("" : "+s"(tstart64));
#define WOF 0
  tstart = (unsigned)tstart64 + (unsigned)((xo_pack() >> (8 * xcd)) & 255u) - 64u - (w < 4 ? 0 : WOF);
  tstart -= (((w & 3) * 2 + (w >> 2)) * PHW) >> 3;
  ta = tstart + TA;
  tk = tstart + TK;
  int wop = w;
  asm volatile("" : "+s"(wop));
  if (wop != 0) {
    if (w < 4) {
#pragma unroll
      for (int m = 0; m < KE; ++m) KWL(m / 3, m % 3);
    }
#pragma unroll
    for (int m = KE; m < 3 * NF; ++m) {
      KWL(m / 3, m % 3);
      if (CR && m == RP) STLD();
    }
    STAMP(8);
    TIE(epoch);
    if (CP) TIE(cpv);
    tag = __builtin_amdgcn_readfirstlane(epoch) + 1u;
    SCHED_FENCE();
  } else {
    u32x4 W2[8];
    const auto rw2 = __builtin_amdgcn_make_buffer_rsrc((void*)(w2 + (size_t)48 * h * D + 80 * sp), (short)0, 48 * D * 2, 0x00020000);
#pragma unroll
    for (int i = 0; i < 8; ++i) W2[i] = __builtin_amdgcn_raw_buffer_load_b128(rw2, w2o, i * 6 * D * 2, 2);
#pragma unroll
    for (int t = 0; t < NF; ++t)
#pragma unroll
      for (int j = 0; j < 3; ++j) KLD(t, j);
    if (CR) STLD();
    SCHED_FENCE();
    const int row = lane >> 4, l16 = lane & 15;
    const int gb = G1 + ha * 288 + lane;
    const int gc = row < 3 ? G1 + ha * 288 + row * 96 + 64 + l16 : GZF + 32 * sp;
    const int l48 = lane < 48 ? lane : 47, fm = l48 >= 24 ? 1 : 0, fpr = 24 * (h & 3) + l48 - 24 * fm;
    const int gf = G1F + xcd * 960 + (3 * fm + (fpr >> 5)) * 160 + (fpr & 31);
    u32x4 va[3], vb[2], vf[5];
    for (unsigned spin = 0;; ++spin) {
      MEMBAR();
#pragma unroll
      for (int s = 0; s < 3; ++s) va[s] = gld2(gb, s * 96);
#pragma unroll
      for (int s = 0; s < 2; ++s) vb[s] = gld2(gc, s * 16);
#pragma unroll
      for (int s = 0; s < 5; ++s) vf[s] = gld2(gf, s * 32);
      bool ok = gok2(vb[0]) && gok2(vb[1]) && gok2(vf[3]) && gok2(vf[4]);
#pragma unroll
      for (int s = 0; s < 3; ++s) ok = ok && gok2(va[s]) && gok2(vf[s]);
      if (__all(ok) || spin >= SPIN_MAX) break;
      __builtin_amdgcn_s_sleep(1);
    }
    {
      const float a0 = (bitsf(va[0][0]) + bitsf(va[1][0])) + bitsf(va[2][0]), a1 = (bitsf(va[0][2]) + bitsf(va[1][2])) + bitsf(va[2][2]);
      reinterpret_cast<unsigned*>(lds + L_QS)[lane] = (unsigned)bfbits(a0) | ((unsigned)bfbits(a1) << 16);
      const float p0 = swap16_add(row < 3 ? bitsf(vb[0][0]) : 0.f, row < 3 ? bitsf(vb[0][2]) : 0.f);
      const float p1 = swap16_add(row < 3 ? bitsf(vb[1][0]) : 0.f, row < 3 ? bitsf(vb[1][2]) : 0.f);
      const float bq = swap32_add(p0, p1);
      reinterpret_cast<unsigned short*>(lds + L_QS)[2 * (64 + 16 * (row >> 1) + l16) + (row & 1)] = bfbits(bq);
    }
    asm volatile("s_waitcnt lgkmcnt(0)" ::: "memory");
    if (lane == 0) __hip_atomic_store(flg + 1, 1u, __ATOMIC_RELAXED, __HIP_MEMORY_SCOPE_WORKGROUP);
    SCHED_FENCE();
    {
      const float f0 = ((bitsf(vf[0][0]) + bitsf(vf[1][0])) + (bitsf(vf[2][0]) + bitsf(vf[3][0]))) + bitsf(vf[4][0]);
      const float f1 = ((bitsf(vf[0][2]) + bitsf(vf[1][2])) + (bitsf(vf[2][2]) + bitsf(vf[3][2]))) + bitsf(vf[4][2]);
      *reinterpret_cast<f32x2*>(lds + L_GU + 2 * l48) = f32x2{bfr(f0), bfr(f1)};
    }
    WAVE_SYNC();
    if (lane < 48) lds[L_H + lane] = bfr(bfr(silu(lds[L_GU + lane])) * lds[L_GU + 48 + lane]);
    WAVE_SYNC();
    {
      float z[8];
#pragma unroll
      for (int i = 0; i < 8; ++i) z[i] = 0.f;
#pragma unroll
      for (int i = 0; i < 8; ++i) {
        const unsigned chi = fbits(lds[L_H + zr + 6 * i]), clo = chi >> 16;
#pragma unroll
        for (int k = 0; k < 4; ++k) {
          z[2 * k] = dot2(W2[i][k], clo, z[2 * k]);
          z[2 * k + 1] = dot2(W2[i][k], chi, z[2 * k + 1]);
        }
      }
      if (lane < 60) {
        float* zp = lds + L_ZP + zr * 80 + 8 * zc;
        *reinterpret_cast<f32x4*>(zp) = f32x4{z[0], z[1], z[2], z[3]};
        *reinterpret_cast<f32x4*>(zp + 4) = f32x4{z[4], z[5], z[6], z[7]};
      }
    }
    SCHED_FENCE();
  }
  const int u = (tid & 255) < 240 ? (tid & 255) : 239;
  const int rg = u / 10, ch = u - 10 * rg;
  u32x4 WO[8];
  const u32x4 rwo = mkrs(wo + (size_t)DH * h * D + 80 * sp, DH * D * 2);
  const int wofs = (rg * D + 8 * ch) * 2;
#define WOLC(i)                                                                                                            \
  asm volatile("s_cmp_lt_u32 %4, 4\n\ts_cbranch_scc1 2\n\tbuffer_load_dwordx4 %0, %1, %2, %3 offen nt"                     \
               : "=v"(WO[i]) : "v"(wofs), "s"(rwo), "s"((i) * 24 * D * 2), "s"(w) : "memory", "scc")
#define VLD(t)                                                       \
  do {                                                               \
    _Pragma("unroll") for (int j = 0; j < 3; ++j) VWL(t, j);         \
  } while (0)
#pragma unroll
  for (int t = NF; t < NF + 2; ++t)
    if (t < nt) {
#pragma unroll
      for (int j = 0; j < 3; ++j) KWL(t, j);
      VLD(t);
    }
  STAMP(9);
  {
    int d = 0;
    if (CR) {
      asm volatile("s_cmp_gt_i32 %1, %2\n\ts_cbranch_scc1 1\n\ts_waitcnt vmcnt(%3)" : "+v"(stv) : "s"(nt), "n"(NF), "n"(3 * NF - 1 - RP) : "memory", "scc");
      d = (int)(tstart - stv);
      d = (unsigned)(d + 150) <= 300u ? d : 0;
#define IDPP(x, ctrl) __builtin_amdgcn_update_dpp(0, (int)(x), ctrl, 0xf, 0xf, true)
      d += IDPP(d, 0xB1);
      d += IDPP(d, 0x4E);
      d += IDPP(d, 0x141);
      d = __builtin_amdgcn_readfirstlane(d) >> 3;
    }
    te = tstart - d + TE + (PH ? ((w * 4 + (jj & 3) - 16) * TB) >> 5 : 0);
    SCHED_FENCE();
  }
#define ZW 3
#define ZT 1
#define SLOWSUM(res, base, cc)                                                                                  \
  do {                                                                                                          \
    res = 0.f;                                                                                                  \
    _Pragma("unroll 1") for (int hh = 0; hh < HQ; ++hh) {                                                       \
      u32x2 v;                                                                                                  \
      for (unsigned spin = 0; spin < SPIN_MAX; ++spin) {                                                        \
        MEMBAR();                                                                                               \
        v = __builtin_amdgcn_raw_buffer_load_b64(rws, (cc) * 8, ((base) + hh * 320) * 16, 16);                  \
        if (v[1] == tag) break;                                                                                 \
      }                                                                                                         \
      res += bitsf(v[0]);                                                                                       \
    }                                                                                                           \
  } while (0)
#define ZS 0
#define ZPUB()                                                                                      \
  do {                                                                                              \
    const f32x2* zp = reinterpret_cast<const f32x2*>(lds + L_ZP) + (lane < 40 ? lane : 0);          \
    const f32x2 zs = ((zp[0] + zp[40]) + (zp[80] + zp[120])) + (zp[160] + zp[200]);                 \
    gst2(lane < 40 ? G3Z + h * 320 + 40 * sp + lane : GEND, zs[0], zs[1]);   \
  } while (0)
  u64 zold[2] = {0ull, 0ull}, zadd[2] = {0ull, 0ull};
  int mro = 8 * tid, mso = tid < 97 ? (G2 + rec * 104 + tid) * 16 : GEND * 16;
  u32x4 pub = {0u, tag, 0u, tag};
  asm volatile("" : "+v"(mro), "+v"(mso), "+v"(pub));
  {
    float Mg;
    float acc[3][8];
#pragma unroll
    for (int i = 0; i < 24; ++i) acc[i >> 3][i & 7] = 0.f;
    float* pw = lds + L_PART + w * PS + 8 * c + g;
    const bool b3 = g & 1;
#define PV3(t, j, cl, ch)                                                                                               \
  do {                                                                                                                  \
    if ((cl) < 0) asm volatile("" : "+v"(plo), "+v"(phi));                                                              \
    else if ((cl) == (ch)) asm volatile("s_waitcnt vmcnt(%2)" : "+v"(plo), "+v"(phi) : "n"((cl) < 0 ? 0 : (cl)) : "memory");  \
    else asm volatile("s_waitcnt vmcnt(%2)\n\ts_cmp_lt_u32 %4, 4\n\ts_cbranch_scc0 1\n\ts_waitcnt vmcnt(%3)"              \
                      : "+v"(plo), "+v"(phi) : "n"(ch), "n"((cl) < 0 ? 0 : (cl)), "s"(w) : "memory", "scc");            \
    _Pragma("unroll") for (int i = 0; i < 4; ++i) {                                                                     \
      acc[j][2 * i] = dot2(V[t][j][i], plo, acc[j][2 * i]);                                                             \
      acc[j][2 * i + 1] = dot2(V[t][j][i], phi, acc[j][2 * i + 1]);                                                     \
    }                                                                                                                   \
    if ((t) == NF - 1) {                                                          \
      float r1[1];                                                                                                      \
      xg_scatter<1>(acc[j], r1, b3);                                                                                    \
      pw[64 * (j)] = r1[0] * ew;                                                                                        \
    } else {                                                                                                            \
      asm volatile("" : "+v"(acc[j][0]), "+v"(acc[j][1]), "+v"(acc[j][2]), "+v"(acc[j][3]), "+v"(acc[j][4]),            \
                   "+v"(acc[j][5]), "+v"(acc[j][6]), "+v"(acc[j][7]));                       \
    }                                                                                                                   \
    SCHED_FENCE();                                                                                                      \
  } while (0)
#define PVT(t, c)                                                                                                       \
  do {                                                                                                                  \
    unsigned plo = pb[t], phi = plo << 16;                                                                              \
    PV3(t, 0, c, c);                                                                                                    \
    PV3(t, 1, -1, -1);                                                                                                  \
    PV3(t, 2, -1, -1);                                                                                                  \
  } while (0)
#define WGMAX()                                                                                                         \
  do {                                                                                                                  \
    for (unsigned spin = 0; spin < (SPIN_MAX << 4); ++spin) {                                                           \
      if (__hip_atomic_load(flg + 3, __ATOMIC_RELAXED, __HIP_MEMORY_SCOPE_WORKGROUP) == 8u) break;                      \
      __builtin_amdgcn_s_sleep(1);                                                                                      \
    }                                                                                                                   \
    MEMBAR();                                                                                                           \
    const f32x4 ma = *reinterpret_cast<const f32x4*>(lds + L_ML), mb2 = *reinterpret_cast<const f32x4*>(lds + L_ML + 4); \
    Mg = fmaxf(fmaxf(fmaxf(ma[0], ma[1]), fmaxf(ma[2], ma[3])), fmaxf(fmaxf(mb2[0], mb2[1]), fmaxf(mb2[2], mb2[3])));   \
    ew = ex2((mw - Mg) * LOG2E);                                                                                        \
    if (lane == 0) *reinterpret_cast<f32x2*>(lds + L_PART + w * PS + DH) = f32x2{w == 0 ? Mg : 0.f, lw * ew};           \
    SCHED_FENCE();                                                                                                      \
  } while (0)
    float ew, mw, lw;
    u32x4 qf[3];
    float sc[NF + 2];
    unsigned pb[NF + 2];
#define GATE(cnt)                                                                                                       \
  do {                                                                                                                  \
    asm volatile("s_waitcnt vmcnt(%2)" : "+v"(zold[0]), "+v"(zold[1]) : "n"(cnt) : "memory");                           \
    _Pragma("unroll") for (int k = 0; k < 2; ++k)                                                                       \
      if ((k == 0 || lane < 16) && (zold[k] >> 59) == 31u) {                                                            \
        const int cz = 80 * sp + 64 * k + lane;                                                                         \
        float f = fx_dec(zold[k] + zadd[k]);                                                                            \
        if (__builtin_expect(!fx_exact(zold[k], zadd[k]), 0)) SLOWSUM(f, G3Z, cz);                                      \
        __builtin_amdgcn_raw_buffer_store_b64(u32x2{fbits(bfr(silu(bfr(f)))), tag}, rws, cz * 8, GG * 16, 16);          \
      }                                                                                                                 \
    STAMPT(12);                                                                                                          \
  } while (0)
    auto slot = [&](auto mc) __attribute__((always_inline)) {
      constexpr int m = decltype(mc)::value;
      VWL(m / 3, m % 3);
      if (m == QS) {
        for (unsigned spin = 0; spin < (SPIN_MAX << 4); ++spin) {
          if (__hip_atomic_load(flg + 1, __ATOMIC_RELAXED, __HIP_MEMORY_SCOPE_WORKGROUP) != 0u) break;
          __builtin_amdgcn_s_sleep(1);
        }
        asm volatile("s_waitcnt lgkmcnt(0)" ::: "memory");
        SCHED_FENCE();
        STAMP(6);
#pragma unroll
        for (int j = 0; j < 3; ++j) qf[j] = *reinterpret_cast<const u32x4*>(lds + L_QS + (c + 8 * j) * 4);
      }
#pragma unroll
      for (int t = 0; t < NF + 2; ++t)
        if (SS[t] == m) {
          sc[t] = -INFINITY;
          if (t < NF || t < nt) {
            float d = 0.f;
#pragma unroll
            for (int j = 0; j < 3; ++j) {
              TIE(K[t][j]);
#pragma unroll
              for (int i = 0; i < 4; ++i) d = dot2(K[t][j][i], qf[j][i], d);
            }
            d = grp8_sum(d);
            sc[t] = bfr(bfr(d) * SCALE);
          }
          TIE(sc[t]);
        }
      if (m == XS) {
        mw = sc[0];
#pragma unroll
        for (int t = 1; t < NF + 2; ++t) mw = fmaxf(mw, sc[t]);
        mw = xg_max(mw);
        if (lane == 0) {
          lds[L_ML + w] = mw;
          __hip_atomic_fetch_add(flg + 3, 1u, __ATOMIC_RELAXED, __HIP_MEMORY_SCOPE_WORKGROUP);
        }
        lw = 0.f;
#pragma unroll
        for (int t = 0; t < NF + 2; ++t) {
          const float p = ex2((sc[t] - mw) * LOG2E);
          lw += p;
          pb[t] = bfbits(p);
          TIE(pb[t]);
        }
        lw = xg_sum(lw);
        TIE(lw);
        STAMP(10);
      }
      if (m == XS + 1 && NF < nt) PVT(NF, -1);
      if (m == XS + 2 && NF + 1 < nt) PVT(NF + 1, -1);
      if (m >= XS + 3) {
        constexpr int n0 = PVN(m - 1), n1 = PVN(m);
#pragma unroll
        for (int n = n0; n < n1; ++n) {
          unsigned plo = pb[n / 3], phi = plo << 16;
          PV3(n / 3, n % 3, n == m - 8 ? 8 : -1, n == m - 8 ? 8 : -1);
        }
      }
      if (m == ZSL) {
        WGMAX();
        if (w == ZW) {
#pragma unroll
          for (int k = 0; k < 2; ++k) {
            const float* zp = lds + L_ZP + (k ? 64 + (lane & 15) : lane);
            zold[k] = zadd[k] = fx_enc(((zp[0] + zp[80]) + (zp[160] + zp[240])) + (zp[320] + zp[400]));
          }
          const int za0 = (2 * GA + D + 80 * sp + lane) * 8, za1 = lane < 16 ? za0 + 512 : GEND * 16;
          if (!ZS) ZPUB();
          asm volatile("buffer_atomic_add_x2 %0, %2, %4, 0 offen sc0\n\tbuffer_atomic_add_x2 %1, %3, %4, 0 offen sc0"
                       : "+v"(zold[0]), "+v"(zold[1]) : "v"(za0), "v"(za1), "s"(rwa) : "memory");
        }
      }
      if (m == GSL && w == ZW) GATE(GSL - ZSL);
      SCHED_FENCE();
    };
#define SL3(t) slot(IC<3 * (t)>{}); slot(IC<3 * (t) + 1>{}); slot(IC<3 * (t) + 2>{})
    SL3(0); SL3(1); SL3(2); SL3(3); SL3(4); SL3(5); SL3(6);
    STAMPT(14);
#pragma unroll
    for (int n = PVN(3 * NF - 1); n < 3 * NF; ++n) {
      const int m = n - (3 * NF - 9);
      if (m >= 0) {
#pragma unroll
        for (int i = (m && n > PVN(3 * NF - 1)) ? WON[m - 1] : 0; i < WON[m]; ++i) WOLC(i);
      }
      unsigned plo = pb[n / 3], phi = plo << 16;
      PV3(n / 3, n % 3, m < 0 ? -1 : 8 - m, m < 0 ? -1 : 8 - m + WON[m < 0 ? 0 : m]);
    }
#pragma unroll
    for (int i = WON[8]; i < WON[9]; ++i) WOLC(i);
    if (GSL >= 3 * NF && w == ZW) {
      STAMPT(15);
      if (ZS) ZPUB();
      GATE(ZS ? 1 : 0);
    }
  }
  if (!(GSL >= 3 * NF && w == ZW)) STAMPT(15);
  __syncthreads();
  STAMP(7);
  if (__builtin_expect(w < 2, 1)) {
    {
      f32x2 p2[8];
#pragma unroll
      for (int ww = 0; ww < 8; ++ww) p2[ww] = *reinterpret_cast<const f32x2*>(reinterpret_cast<const char*>(lds + L_PART + ww * PS) + mro);
      const f32x2 ua = (p2[0] + p2[1]) + (p2[2] + p2[3]), ub = (p2[4] + p2[5]) + (p2[6] + p2[7]);
      float ub1 = ub[1];
      asm("" : "+v"(ub1));
      pub[0] = fbits(ua[0] + ub[0]);
      pub[2] = fbits(ua[1] + ub1);
      __builtin_amdgcn_raw_buffer_store_b128(pub, rws, mso, 0, 16);
    }
    SCHED_FENCE();
    const int t2 = tid < 96 ? tid : 95;
    const int pb = (G2 + h * NSP * 104 + t2) * 16, pm = (G2 + (h * NSP + c) * 104 + 96) * 16;
    const unsigned po = (unsigned)(size_t)(__attribute__((address_space(3))) float*)(lds + L_O) + 4 * t2;
    const int pg = lane < 20 ? (GG + 40 * sp + 20 * w + lane) * 16 : GEND * 16;
    const unsigned pl = (unsigned)(size_t)(__attribute__((address_space(3))) float*)(lds + L_GT) + 16 * (lane < 20 ? 20 * w + lane : 40 + tid);
    if (PD1) __builtin_amdgcn_s_sleep(PD1);
    {
      unsigned e0, e1, e2, e3, e4, e5, e6, e7, cnt;
#define DPC " row_mask:0xf bank_mask:0xf\n\t"
#define CHK(d, a, b) "v_bitop3_b32 " d ", " a ", " b ", %13 bitop3:0x7e\n\t"
#define ACC2(e, a, b) "v_fmac_f32 v44, " e ", " a "\n\tv_fmac_f32 v45, " e ", " b "\n\t"
      asm volatile(
          "s_mov_b32 %8, %16\n"
          "1:\n\t"
          "buffer_load_dwordx4 v[48:51], %18, %12, 0 offen sc1\n\t"
          "buffer_load_dwordx4 v[4:7], %9, %12, 0 offen sc1\n\t"
          "buffer_load_dwordx4 v[8:11], %10, %12, 0 offen sc1\n\t"
          "buffer_load_dwordx4 v[12:15], %10, %12, 0 offen offset:1664 sc1\n\t"
          "buffer_load_dwordx4 v[16:19], %10, %12, 0 offen offset:3328 sc1\n\t"
          "buffer_load_dwordx4 v[20:23], %10, %12, %14 offen sc1\n\t"
          "buffer_load_dwordx4 v[24:27], %10, %12, %14 offen offset:1664 sc1\n\t"
          "buffer_load_dwordx4 v[28:31], %10, %12, %14 offen offset:3328 sc1\n\t"
          "buffer_load_dwordx4 v[32:35], %10, %12, %15 offen sc1\n\t"
          "buffer_load_dwordx4 v[36:39], %10, %12, %15 offen offset:1664 sc1\n\t"
          "s_sub_u32 %8, %8, 1\n\t"
          "s_waitcnt vmcnt(8)\n\t"
          CHK("v41", "v5", "v7")
          "ds_write_b128 %19, v[48:51]\n\t"
          "v_max_f32_dpp v40, v4, v4 quad_perm:[1,0,3,2]" DPC
          "s_waitcnt vmcnt(7)\n\t"
          CHK("v46", "v9", "v11")
          "v_max_f32_dpp v40, v40, v40 quad_perm:[2,3,0,1]" DPC
          "s_waitcnt vmcnt(6)\n\t"
          CHK("v47", "v13", "v15")
          "v_max_f32_dpp v40, v40, v40 row_half_mirror" DPC
          "v_sub_f32 v42, v4, v40\n\t"
          "v_mul_f32 v42, 0x3fb8aa3b, v42\n\t"
          "v_exp_f32 v42, v42\n\t"
          "v_or3_b32 v41, v41, v46, v47\n\t"
          "v_mul_f32 v40, v6, v42\n\t"
          "s_waitcnt vmcnt(5)\n\t"
          CHK("v46", "v17", "v19")
          "v_add_f32_dpp v40, v40, v40 quad_perm:[1,0,3,2]" DPC
          "s_waitcnt vmcnt(4)\n\t"
          CHK("v47", "v21", "v23")
          "v_add_f32_dpp v40, v40, v40 quad_perm:[2,3,0,1]" DPC
          "v_or3_b32 v41, v41, v46, v47\n\t"
          "s_nop 0\n\t"
          "v_add_f32_dpp v40, v40, v40 row_half_mirror" DPC
          "v_rcp_f32 v40, v40\n\t"
          "s_nop 0\n\t"
          "v_mul_f32 v42, v42, v40\n\t"
          "s_nop 0\n\t"
          "v_readlane_b32 %0, v42, 0\n\tv_readlane_b32 %1, v42, 1\n\tv_readlane_b32 %2, v42, 2\n\tv_readlane_b32 %3, v42, 3\n\t"
          "v_readlane_b32 %4, v42, 4\n\tv_readlane_b32 %5, v42, 5\n\tv_readlane_b32 %6, v42, 6\n\tv_readlane_b32 %7, v42, 7\n\t"
          "v_mul_f32 v44, %0, v8\n\tv_mul_f32 v45, %0, v10\n\t"
          ACC2("%1", "v12", "v14") ACC2("%2", "v16", "v18") ACC2("%3", "v20", "v22")
          "s_waitcnt vmcnt(3)\n\t"
          ACC2("%4", "v24", "v26") CHK("v46", "v25", "v27")
          "s_waitcnt vmcnt(2)\n\t"
          ACC2("%5", "v28", "v30") CHK("v47", "v29", "v31")
          "v_or3_b32 v41, v41, v46, v47\n\t"
          "s_waitcnt vmcnt(1)\n\t"
          ACC2("%6", "v32", "v34") CHK("v46", "v33", "v35")
          "s_waitcnt vmcnt(0)\n\t"
          ACC2("%7", "v36", "v38")
          "v_cvt_pk_bf16_f32 v44, v44, v45\n\t"
          "ds_write_b32 %11, v44\n\t"
          CHK("v47", "v37", "v39")
          "v_or3_b32 v41, v41, v46, v47\n\t"
          "v_cmp_ne_u32 vcc, 0, v41\n\t"
          "s_cbranch_vccz 2f\n\t"
          "s_sleep %17\n\t"
          "s_cbranch_scc0 1b\n"
          "2:\n\t"
          "s_waitcnt lgkmcnt(0)"
          : "=&s"(e0), "=&s"(e1), "=&s"(e2), "=&s"(e3), "=&s"(e4), "=&s"(e5), "=&s"(e6), "=&s"(e7), "=&s"(cnt)
          : "v"(pm), "v"(pb), "v"(po), "s"(rwa), "s"(tag), "s"(3 * 1664), "s"(6 * 1664), "n"(SPIN_MAX), "n"(PS1), "v"(pg), "v"(pl)
          : "memory", "scc", "vcc", "v4", "v5", "v6", "v7", "v8", "v9", "v10", "v11", "v12", "v13", "v14", "v15", "v16", "v17", "v18", "v19",
            "v20", "v21", "v22", "v23", "v24", "v25", "v26", "v27", "v28", "v29", "v30", "v31", "v32", "v33", "v34", "v35", "v36", "v37",
            "v38", "v39", "v40", "v41", "v42", "v44", "v45", "v46", "v47", "v48", "v49", "v50", "v51");
    }
    STAMPT(13);
    __syncthreads();
  } else if (w == 2) {
    __syncthreads();
  } else {
    if (w >= 4) {
#pragma unroll
      for (int i = WON[9]; i < 8; ++i) WOLC(i);
#pragma unroll
      for (int i = 0; i < 8; ++i) {
        AWAIT(WO[i], 7 - i);
        *reinterpret_cast<u32x4*>(lds + L_WO + u * 4 + i * 960) = WO[i];
      }
      asm volatile("s_waitcnt lgkmcnt(0)" ::: "memory");
      if (lane == 0) __hip_atomic_fetch_add(flg + 2, 1u, __ATOMIC_RELAXED, __HIP_MEMORY_SCOPE_WORKGROUP);
      STAMPT(12);
    }
    for (unsigned spin = 0; spin < (SPIN_MAX << 4); ++spin) {
      if (__hip_atomic_load(flg + 2, __ATOMIC_RELAXED, __HIP_MEMORY_SCOPE_WORKGROUP) == 4u) break;
      __builtin_amdgcn_s_sleep(1);
    }
    asm volatile("s_waitcnt lgkmcnt(0)" ::: "memory");
    u32x4 wb[6];
    {
      typedef __attribute__((address_space(3))) v4i16_t* ltr;
      const unsigned la = (unsigned)(size_t)(__attribute__((address_space(3))) float*)(lds + L_WO) + (8 * (lane >> 4) + ((lane >> 2) & 3)) * 160 + 32 * (w - 3) + 8 * (lane & 3);
#pragma unroll
      for (int k = 0; k < 6; ++k) {
        const u32x2 t0 = __builtin_bit_cast(u32x2, __builtin_amdgcn_ds_read_tr16_b64_v4i16((ltr)(la + 32 * k * 160)));
        const u32x2 t1 = __builtin_bit_cast(u32x2, __builtin_amdgcn_ds_read_tr16_b64_v4i16((ltr)(la + (32 * k + 4) * 160)));
        wb[k] = u32x4{t0[0], t0[1], t1[0], t1[1]};
      }
    }
    const int cy = 80 * sp + 16 * (w - 3) + (lane & 15);
    float xv = bf_lo(reinterpret_cast<const unsigned short*>(lds + L_XS)[cy]);
    gu64* pa = (gu64*)(gw + 2 * GA + cy);
    gu16* pout = (gu16*)(out + cy);
    typedef __attribute__((address_space(3))) const u32x4* la4;
    la4 pa4 = (la4)(lds + L_O) + 4 * (lane & 3) + (lane >> 4);
    int cy8 = cy * 8;
    typedef __attribute__((address_space(3))) const u32x2* la2;
    la2 pgt = (la2)(lds + L_GT) + 16 * (w - 3) + (lane & 15);
    asm volatile("" : "+v"(pgt));
    unsigned k0 = (1u << 27) - (0x411u << 20), k1 = k0 + (1u << 22), k2 = 1u << 17;
    double c0 = 262144.0 + 512.0, c1 = -(262144.0 + 32 * 512.0);
    float f0 = 512.f, f1 = 0x1p-9f;
    asm volatile("" : "+v"(wb[0]), "+v"(wb[1]), "+v"(wb[2]), "+v"(wb[3]), "+v"(wb[4]), "+v"(wb[5]));
    asm volatile("" : "+v"(xv), "+v"(pa), "+v"(pout), "+v"(pa4), "+v"(cy8), "+v"(k0), "+v"(k1), "+v"(k2), "+s"(c0), "+s"(c1), "+s"(f0), "+s"(f1));
    __syncthreads();
    STAMP(1);
    const u32x4 a1 = pa4[0], a2 = pa4[16];
    SCHED_FENCE();
    u32x2 gg = *pgt;
    SCHED_FENCE();
#define MFMA(a, b, cc) __builtin_amdgcn_mfma_f32_16x16x32_bf16(__builtin_bit_cast(bf16x8_t, a), __builtin_bit_cast(bf16x8_t, b), cc, 0, 0, 0)
    const f32x4 z4 = {0.f, 0.f, 0.f, 0.f};
    const f32x4 d2 = MFMA(a1, wb[2], z4), d3 = MFMA(a1, wb[3], z4);
    SCHED_FENCE();
    f32x4 d0 = MFMA(a1, wb[0], z4), d1 = MFMA(a1, wb[1], z4);
    SCHED_FENCE();
    d0 = MFMA(a2, wb[4], d0);
    SCHED_FENCE();
    d1 = MFMA(a2, wb[5], d1);
    SCHED_FENCE();
    if (lane < 16) {
      const float q = ((d2[2] + d3[3]) + d0[0]) + d1[1];
      const bool bad = !(fabsf(q) < f0);
      const u64 db = __builtin_bit_cast(u64, (double)(bad ? 0.f : q) + c0);
      const unsigned fl = (bad ? k1 : k0) + (fabsf(q) < f1 ? k2 : 0u);
      const u64 add = ((u64)((unsigned)(db >> 32) + fl) << 32) | (unsigned)db;
      const u64 old = __hip_atomic_fetch_add(pa, add, __ATOMIC_RELAXED, __HIP_MEMORY_SCOPE_AGENT);
      __builtin_amdgcn_raw_buffer_store_b64(u32x2{fbits(q), tag}, rws, cy8, (G3Y + h * 320) * 16, 16);
      SCHED_FENCE();
      unsigned lim = (gg[1] == tag && !bad) ? (32u << 17) - (fl - k0) : 0u;
      asm volatile("" : "+v"(lim));
      SCHED_FENCE();
      float sv = (float)(__builtin_bit_cast(double, (old & ((1ull << 49) - 1ull)) + db) + c1);
      asm("" : "+v"(sv));
      unsigned res = bfbits(xv + bfr(bfr(sv) * bitsf(gg[0])));
      const unsigned rel = (unsigned)(old >> 32) - (31u << 27);
      TIE(res);
      if (rel < lim) *pout = res;
      SCHED_FENCE();
      if (CP) asm volatile(".Lfd_end:");
      if ((old >> 59) == 31u) {
        if (__builtin_expect(rel >= lim, 0)) {
          if (!fx_exact(old, add)) SLOWSUM(sv, G3Y, cy);
          for (unsigned spin = 0; gg[1] != tag && spin < SPIN_MAX; ++spin) {
            MEMBAR();
            gg = __builtin_amdgcn_raw_buffer_load_b64(rws, cy8, GG * 16, 16);
          }
          *pout = bfbits(xv + bfr(bfr(sv) * bitsf(gg[0])));
        }
        if (cy == 0)
          __hip_atomic_store(reinterpret_cast<unsigned*>(gw + 2 * GEP), tag, __ATOMIC_RELAXED, __HIP_MEMORY_SCOPE_AGENT);
      }
    }
  }
  STAMP(11);
}

}

extern "C" void kernel_launch(void* const* d_in, const int* in_sizes, int n_in, void* d_out, int out_size, void* d_ws,
                              size_t ws_size, hipStream_t stream) {
  const bf16* x = (const bf16*)d_in[0];
  const bf16* kc = (const bf16*)d_in[1];
  const bf16* vc = (const bf16*)d_in[2];
  const bf16* wq = (const bf16*)d_in[3];
  const bf16* wo = (const bf16*)d_in[4];
  const bf16* w1 = (const bf16*)d_in[6];
  const bf16* w3 = (const bf16*)d_in[7];
  const bf16* w2 = (const bf16*)d_in[8];
  u64* gw = (u64*)d_ws;
  fused_decode<<<NWG, NT, 0, stream>>>(x, kc, vc, wq, wo, w1, w3, w2, gw, (bf16*)d_out);
}
```

```cpp
#include <hip/hip_runtime.h>
#include <hip/hip_bf16.h>
#include <stdint.h>

typedef __hip_bfloat16 bf16;
typedef unsigned long long u64;
typedef __attribute__((address_space(1))) unsigned long long gu64;
typedef __attribute__((address_space(1))) unsigned short gu16;
typedef unsigned u32x4 __attribute__((ext_vector_type(4)));
typedef unsigned u32x2 __attribute__((ext_vector_type(2)));
typedef float f32x4 __attribute__((ext_vector_type(4)));
typedef __bf16 bf16x2_t __attribute__((ext_vector_type(2)));
typedef __bf16 bf16x8_t __attribute__((ext_vector_type(8)));
typedef short v4i16_t __attribute__((ext_vector_type(4)));

namespace {

constexpr int D = 640, HQ = 32, DH = 192, SK = 32768, WIN = 4096, FF = 1536;
constexpr int NSP = 8;
constexpr int NWG = HQ * NSP;
constexpr int NT = 512;
constexpr float SCALE = 0.07216878364870322f;
constexpr float LOG2E = 1.4426950408889634f;
constexpr unsigned SPIN_MAX = 1u << 16;
constexpr int WND = 8;
constexpr int WOL = 7;
constexpr int NF = 7;
#define DXT 2, -1, 2, -1, 1, -3, 1, -1
constexpr int DX[8] = {DXT};
constexpr unsigned dx_pack() {
  unsigned r = 0;
  for (int i = 0; i < 8; ++i) r |= (unsigned)(DX[i] + 4) << (4 * i);
  return r;
}
constexpr unsigned dx_pos() {
  unsigned r = 0, a = 0;
  for (int i = 0; i < 8; ++i) { r |= a << (4 * i); a += DX[i] > 0 ? DX[i] : 0; }
  return r;
}
constexpr unsigned long long dx_don() {
  unsigned long long r = 0;
  int k = 0;
  for (int i = 0; i < 8; ++i)
    for (int q = 0; q < -DX[i]; ++q) r |= (unsigned long long)(i * 4 + q) << (5 * k++);
  return r;
}
constexpr bool dx_ok() {
  int a = 0, p = 0;
  bool in = true;
  for (int i = 0; i < 8; ++i) { a += DX[i]; p += DX[i] > 0 ? DX[i] : 0; in = in && DX[i] >= -4 && DX[i] <= 4; }
  return a == 0 && p <= 8 && in;
}
constexpr int dx_check[dx_ok() ? 1 : -1] = {0};
constexpr int KE = 8;
#define PD1X 24
#define PS1 1
constexpr int PD1 = PD1X;
#define WOP 2, 2, 2, 2, 2, 4, 6, 8, 8, 8
constexpr int WON[10] = {WOP};
#define QS 1
#define SSL 1, 2, 3, 4, 5, 6, 8, 7, 7
#define XS 9
#define ZSL 12
#define GSL 20
constexpr int SS[9] = {SSL};
constexpr bool ss_ok() {
  bool ok = true;
  for (int t = 0; t < 9; ++t) ok = ok && SS[t] >= QS && SS[t] <= XS && SS[t] >= (t < 7 ? 3 * t - 10 : 5);
  return ok && ZSL > XS && GSL > ZSL && GSL < 21;
}
constexpr int ss_check[ss_ok() ? 1 : -1] = {0};
constexpr int PVN(int m) {
  return m < XS + 3 ? 0 : (3 * (m - XS - 2) < m - 7 ? 3 * (m - XS - 2) : m - 7);
}

constexpr int G1 = 0;
constexpr int G1F = G1 + HQ * 288;
constexpr int G2 = G1F + 8 * 960;
constexpr int G3Y = G2 + NWG * 104;
constexpr int G3Z = G3Y + 320 * 32;
constexpr int GG = G3Z + 320 * 32;
constexpr int GZF = GG + 320;
constexpr int GA = GZF + 256;
constexpr int GEP = GA + 640;
constexpr int GEND = GEP + 8;

__device__ __forceinline__ unsigned fbits(float x) { return __builtin_bit_cast(unsigned, x); }
__device__ __forceinline__ float bitsf(unsigned x) { return __builtin_bit_cast(float, x); }
#define gok2(v) ((v)[1] == tag && (v)[3] == tag)
#define gst2(unit, a, b) __builtin_amdgcn_raw_buffer_store_b128(u32x4{fbits(a), tag, fbits(b), tag}, rws, (unit) * 16, 0, 16)
#define gld2(unit, cunit) __builtin_amdgcn_raw_buffer_load_b128(rws, (unit) * 16, (cunit) * 16, 16)
#define MEMBAR() asm volatile("" ::: "memory")
typedef float f32x2 __attribute__((ext_vector_type(2)));

__device__ __forceinline__ float bf_lo(unsigned u) { return __builtin_bit_cast(float, u << 16); }
__device__ __forceinline__ float bf_hi(unsigned u) { return __builtin_bit_cast(float, u & 0xffff0000u); }
__device__ __forceinline__ float bfr(float x) { return __bfloat162float(__float2bfloat16(x)); }
__device__ __forceinline__ unsigned short bfbits(float x) {
  return __builtin_bit_cast(unsigned short, __float2bfloat16(x));
}
__device__ __forceinline__ float dot2(unsigned a, unsigned b, float c) {
  return __builtin_amdgcn_fdot2_f32_bf16(__builtin_bit_cast(bf16x2_t, a), __builtin_bit_cast(bf16x2_t, b), c, false);
}
template <int CTRL>
__device__ __forceinline__ float dpp(float x) {
  return __builtin_bit_cast(float, __builtin_amdgcn_mov_dpp(__builtin_bit_cast(int, x), CTRL, 0xf, 0xf, true));
}
__device__ __forceinline__ float grp8_sum(float x) {
  x += dpp<0xB1>(x);
  x += dpp<0x4E>(x);
  x += dpp<0x141>(x);
  return x;
}
__device__ __forceinline__ float swap16_add(float a, float b) {
  auto s = __builtin_amdgcn_permlane16_swap(fbits(a), fbits(b), false, false);
  return bitsf((unsigned)s[0]) + bitsf((unsigned)s[1]);
}
__device__ __forceinline__ float swap32_add(float a, float b) {
  auto t = __builtin_amdgcn_permlane32_swap(fbits(a), fbits(b), false, false);
  return bitsf((unsigned)t[0]) + bitsf((unsigned)t[1]);
}
__device__ __forceinline__ float xg_sum(float x) {
  x += dpp<0x128>(x);
  x = swap16_add(x, x);
  return swap32_add(x, x);
}
__device__ __forceinline__ float xg_max(float x) {
  x = fmaxf(x, dpp<0x128>(x));
  auto s = __builtin_amdgcn_permlane16_swap(fbits(x), fbits(x), false, false);
  x = fmaxf(bitsf((unsigned)s[0]), bitsf((unsigned)s[1]));
  auto t = __builtin_amdgcn_permlane32_swap(fbits(x), fbits(x), false, false);
  return fmaxf(bitsf((unsigned)t[0]), bitsf((unsigned)t[1]));
}
template <int NJ>
__device__ __forceinline__ void xg_scatter(const float (&a)[8 * NJ], float (&r)[NJ], const bool b3) {
#pragma unroll
  for (int j = 0; j < NJ; ++j) {
    float p[4];
#pragma unroll
    for (int e = 0; e < 4; ++e) p[e] = swap32_add(a[8 * j + e], a[8 * j + e + 4]);
    const float q0 = swap16_add(p[0], p[2]), q1 = swap16_add(p[1], p[3]);
    r[j] = (b3 ? q1 : q0) + dpp<0x128>(b3 ? q0 : q1);
  }
}
__device__ __forceinline__ float wave_sum(float x) { return xg_sum(grp8_sum(x)); }
__device__ __forceinline__ float ex2(float x) { return __builtin_amdgcn_exp2f(x); }
__device__ __forceinline__ float silu(float x) { return x / (1.0f + __expf(-x)); }
__device__ __forceinline__ float rdlane(float x, int l) {
  return __builtin_bit_cast(float, __builtin_amdgcn_readlane(__builtin_bit_cast(int, x), l));
}

__device__ __forceinline__ u64 fx_enc(float v) {
  const bool bad = !(fabsf(v) < 512.f), small = fabsf(v) < 0x1p-9f;
  const u64 d = __builtin_bit_cast(u64, (double)(bad ? 0.f : v) + (262144.0 + 512.0));
  const unsigned hi = (unsigned)(d >> 32) + ((1u << 27) - (0x411u << 20)) + (bad ? 1u << 22 : 0u) + (small ? 1u << 17 : 0u);
  return ((u64)hi << 32) | (unsigned)d;
}
__device__ __forceinline__ float fx_dec(u64 t) {
  const unsigned hi = ((unsigned)(t >> 32) & 0x1ffffu) | (0x411u << 20);
  return (float)(__builtin_bit_cast(double, ((u64)hi << 32) | (unsigned)t) - (262144.0 + 32 * 512.0));
}
__device__ __forceinline__ bool fx_exact(u64 old, u64 add) {
  const unsigned o = (unsigned)(old >> 49) & 0x3ffu, a = (unsigned)(add >> 49) & 0x3ffu;
  return o + a < 32u;
}
template <int N> struct IC { static constexpr int value = N; };
#define SCHED_FENCE() __builtin_amdgcn_sched_barrier(0)
#define ALD(dst, rs, vo, so, imm) \
  asm volatile("buffer_load_dwordx4 %0, %1, %2, %3 offen offset:%4 nt" : "=v"(dst) : "v"(vo), "s"(rs), "s"(so), "n"(imm) : "memory")
#define AWL(dst, rs, vo, so, imm)                                                                                      \
  asm volatile("s_waitcnt vmcnt(8)\n\ts_cmp_le_i32 %5, 8\n\ts_cbranch_scc0 4\n\ts_waitcnt vmcnt(7)\n\t"                 \
               "s_cmp_le_i32 %5, 7\n\ts_cbranch_scc0 1\n\ts_waitcnt vmcnt(6)\n\t"                                        \
               "buffer_load_dwordx4 %0, %1, %2, %3 offen offset:%4 nt"                                                 \
               : "=v"(dst) : "v"(vo), "s"(rs), "s"(so), "n"(imm), "s"(wl) : "memory", "scc")
constexpr int TOFF = 1 << 14;
#define AWT(dst, rs, vo, so, imm, tref, lim)                                                                            \
  asm volatile("s_memrealtime s[98:99]\n\ts_waitcnt vmcnt(8)\n"                                                          \
               "8:\n\ts_waitcnt lgkmcnt(0)\n\ts_mul_i32 s98, s98, %7\n\ts_sub_u32 s98, s98, %5\n\ts_cmp_ge_u32 s98, %6\n\t" \
               "s_cbranch_scc1 9f\n\ts_sleep 1\n\ts_memrealtime s[98:99]\n\ts_branch 8b\n"                               \
               "9:\n\tbuffer_load_dwordx4 %0, %1, %2, %3 offen offset:%4 nt"                                            \
               : "=v"(dst) : "v"(vo), "s"(rs), "s"(so), "n"(imm), "s"(tref), "n"((lim) + TOFF), "s"(nt)                  \
               : "memory", "scc", "s98", "s99")
#define TA 262
#define TB 28
#define TE (TA + 47 * TB)
constexpr int te_check[(TE + 64) * 9 < TOFF ? 1 : -1] = {0};
#define AWAIT(v, n) asm volatile("s_waitcnt vmcnt(%1)" : "+v"(v) : "n"(n) : "memory")
#define TIE(v) asm volatile("" : "+v"(v))
__device__ __forceinline__ u32x4 mkrs(const void* p, unsigned bytes) {
  const u64 a = (u64)p;
  return u32x4{(unsigned)a, (unsigned)(a >> 32) & 0xffffu, bytes, 0x00020000u};
}
#define WAVE_SYNC()                                        \
  do {                                                     \
    __builtin_amdgcn_fence(__ATOMIC_RELEASE, "wavefront"); \
    __builtin_amdgcn_wave_barrier();                       \
    __builtin_amdgcn_fence(__ATOMIC_ACQUIRE, "wavefront"); \
  } while (0)

constexpr int L_WO = 0;
constexpr int L_XS = L_WO + DH * 40;
constexpr int PS = 256;
constexpr int L_PART = L_XS + 4 * 320;
constexpr int L_XN = L_PART + 8 * PS;
constexpr int L_ZP = L_XN + 4 * 64;
constexpr int L_QS = L_ZP + 6 * 80;
constexpr int L_ML = L_QS + 96;
constexpr int L_O = L_ML + 16;
constexpr int L_H = L_O + 128;
constexpr int L_GU = L_H + 48;
constexpr int L_GT = L_GU + 96;
constexpr int L_FLG = L_GT + 168 * 4;
constexpr int L_END = L_FLG + 4;

__global__ void __launch_bounds__(NT, 2)
fused_decode(const bf16* __restrict__ x, const bf16* __restrict__ kc, const bf16* __restrict__ vc,
             const bf16* __restrict__ wq, const bf16* __restrict__ wo, const bf16* __restrict__ w1,
             const bf16* __restrict__ w3, const bf16* __restrict__ w2, u64* __restrict__ gw, bf16* __restrict__ out) {
  __shared__ __attribute__((aligned(16))) float lds[L_END];
  const int b = blockIdx.x;
  const int xcd = b & 7, jj = b >> 3;
  const int h = xcd * 4 + (jj >> 3), sp = jj & 7;
  const int ha = jj, spa = xcd;
  const int w = __builtin_amdgcn_readfirstlane(threadIdx.x >> 6);
  const int tid = threadIdx.x, lane = tid & 63;
  const int g = lane >> 3, c = lane & 7;
  const int rec = ha * NSP + spa;
  unsigned* flg = reinterpret_cast<unsigned*>(lds + L_FLG);

  unsigned cpv = (jj & 7) * 4096 + lane * 64;
#define CPLD()                                                                                                              \
  asm volatile("s_cmp_lg_u32 %1, %2\n\ts_cbranch_scc1 7f\n"                                                                 \
               ".Lfd_beg:\n\ts_getpc_b64 s[96:97]\n\ts_and_b32 s97, s97, 0xffff\n\ts_mov_b32 s98, .Lfd_end-.Lfd_beg-8\n\t"   \
               "s_mov_b32 s99, 0x20000\n\tbuffer_load_dword %0, %0, s[96:99], 0 offen\n7:"                                   \
               : "+v"(cpv) : "s"(w + 8 * (jj & ~7)), "n"(4) : "memory", "scc", "s96", "s97", "s98", "s99")
  SCHED_FENCE();
  CPLD();
  SCHED_FENCE();
  asm volatile("" ::"s"(x), "s"(kc), "s"(vc), "s"(wq), "s"(wo), "s"(w1), "s"(w3), "s"(w2), "s"(gw), "s"(out));
  SCHED_FENCE();
  u64 tstart64 = __builtin_amdgcn_s_memrealtime();
  SCHED_FENCE();
  const auto rws = __builtin_amdgcn_make_buffer_rsrc((void*)gw, (short)0, GEND * 16, 0x00020000);
  const size_t kvw = ((size_t)ha * SK + (SK - WIN)) * DH;
  const u32x4 rk = mkrs(kc + kvw, WIN * DH * 2), rv = mkrs(vc + kvw, WIN * DH * 2);
  const u32x4 rwa = mkrs(gw, GEND * 16);
  const int kvo = (g * DH + 8 * c) * 2;
  const int dx = (int)((dx_pack() >> (4 * xcd)) & 15u) - 4;
  const int wr = w < 4 ? (w + 3) & 3 : w;
  const int nt = 8 + ((unsigned)(wr - dx) >> 31) - ((unsigned)(7 - w + dx) >> 31);
  const int don = (int)(dx_don() >> (5 * (((dx_pos() >> (4 * xcd)) + wr) & 7u))) & 31;
  const int kvb = (spa * 8 + ((w + (ha >> 3)) & 7)) * 64 * DH * 2;
  const int ko8 = (((don >> 2) * 8 + ((7 - (don & 3) + (ha >> 3)) & 7)) * 8 + ((7 + ha) & 7)) * 8 * DH * 2;
  const int wl = __builtin_amdgcn_readfirstlane((w < 4 ? WOL : WND) + (nt - 8) + (dx < 0 ? 1 : 0));
#define KVOFF(t) ((t) == 8 ? ko8 : kvb + (((t) + ha) & 7) * 8 * DH * 2)
#define KLD(t, j) ALD(K[t][j], rk, kvo, KVOFF(t), 128 * (j))
  unsigned ta, te;
#define KWL(t, j)                                                                            \
  do {                                                                                       \
    if ((t) >= NF) AWT(K[t][j], rk, kvo, KVOFF(t), 128 * (j), ta, (6 * (t) - 21 + (j)) * TB * 8); \
    else AWL(K[t][j], rk, kvo, KVOFF(t), 128 * (j));                                         \
  } while (0)
#define VWL(t, j)                                                                            \
  do {                                                                                       \
    if ((t) < NF) AWT(V[t][j], rv, kvo, KVOFF(t), 128 * (j), te, (3 * (t) + (j) - 20) * TB * 8); \
    else AWT(V[t][j], rv, kvo, KVOFF(t), 128 * (j), ta, (6 * (t) - 18 + (j)) * TB * 8);      \
  } while (0)
#define EPLD() asm volatile("buffer_load_dword %0, off, %1, %2" : "=v"(epoch) : "s"(rwa), "s"(GEP * 16) : "memory")
  u32x4 K[NF + 2][3], V[NF + 2][3];

  const int fi = (xcd & 1) ? 19 : 8;
  const bool fidle = (unsigned)(jj - fi) < 2u;
  const int fp = jj > fi ? jj - 2 : jj;
  const int fcol = fp / 5, frp = fp - 5 * fcol;
  unsigned epoch, tag;
  if (w < 4) {
    const int fw = 4 * sp + w, ct = fw >= 20 ? 2 : fw >= 10 ? 1 : 0;
    const int r0 = 64 * (fw - 10 * ct);
    const int R = 128 * frp + 32 * w;
    const u32x4 xa = *reinterpret_cast<const u32x4*>(x + 8 * lane);
    const u32x4 xb = *reinterpret_cast<const u32x4*>(x + 512 + 8 * (lane & 15));
    u32x4 WQ[8], F[4];
    {
      const auto rq = __builtin_amdgcn_make_buffer_rsrc((void*)wq, (short)0, D * HQ * DH * 2, 0x00020000);
      const int qo = ((r0 + 8 * g) * (HQ * DH) + h * DH + 64 * ct + 8 * c) * 2;
#pragma unroll
      for (int i = 0; i < 8; ++i) WQ[i] = __builtin_amdgcn_raw_buffer_load_b128(rq, qo, i * HQ * DH * 2, 2);
      const auto rf = __builtin_amdgcn_make_buffer_rsrc((void*)(fcol >= 3 ? w3 : w1), (short)0, fidle ? 0 : D * FF * 2, 0x00020000);
      const int fo = ((R + 4 * g) * FF + 192 * xcd + 64 * (fcol >= 3 ? fcol - 3 : fcol) + 8 * c) * 2;
#pragma unroll
      for (int i = 0; i < 4; ++i) F[i] = __builtin_amdgcn_raw_buffer_load_b128(rf, fo, i * FF * 2, 2);
    }
    if (fidle && tid < 40)
      __builtin_amdgcn_raw_buffer_store_b128(u32x4{0u, 0u, 0u, 0u}, rws, (GA + (jj - fi) * 320 + 40 * xcd + lane) * 16, 0, 16);
    SCHED_FENCE();
    EPLD();
    if (tid < 4) flg[tid] = 0u;
    SCHED_FENCE();
    __syncthreads();
    SCHED_FENCE();
    unsigned short* xs = reinterpret_cast<unsigned short*>(lds + L_XS + w * 320);
    const bool lo16 = lane < 16;
    *reinterpret_cast<u32x4*>(xs + 8 * lane) = xa;
    if (lo16) *reinterpret_cast<u32x4*>(xs + 512 + 8 * lane) = xb;
    float xf[8], xg[8];
#pragma unroll
    for (int i = 0; i < 4; ++i) {
      xf[2 * i] = bf_lo(xa[i]);
      xf[2 * i + 1] = bf_hi(xa[i]);
      xg[2 * i] = bf_lo(xb[i]);
      xg[2 * i + 1] = bf_hi(xb[i]);
    }
    float sum = 0.f;
#pragma unroll
    for (int i = 0; i < 8; ++i) sum += xf[i] + (lo16 ? xg[i] : 0.f);
    sum = wave_sum(sum);
    const float mean = sum * (1.0f / D);
    float sq = 0.f;
#pragma unroll
    for (int i = 0; i < 8; ++i) {
      const float d1 = xf[i] - mean, d2 = xg[i] - mean;
      sq += d1 * d1 + (lo16 ? d2 * d2 : 0.f);
    }
    sq = wave_sum(sq);
    const float mb = bfr(mean);
    const float vb = bfr(sq * (1.0f / D));
    const float den = bfr(sqrtf(bfr(vb + 1e-5f)));
    WAVE_SYNC();
    auto xnorm = [&](int r) -> unsigned short { return bfbits(bfr(bf_lo(xs[r]) - mb) / den); };
    {
      unsigned short* xn = reinterpret_cast<unsigned short*>(lds + L_XN + w * 64);
      xn[lane] = xnorm((r0 < D ? r0 : 0) + lane);
      xn[64 + (lane & 31)] = xnorm(R + (lane & 31));
      WAVE_SYNC();
      const u32x4 XQ = *reinterpret_cast<const u32x4*>(xn + 8 * g);
      const u32x2 XF = *reinterpret_cast<const u32x2*>(xn + 64 + 4 * g);
      float acc[16], r2[2];
#pragma unroll
      for (int i = 0; i < 16; ++i) acc[i] = 0.f;
#pragma unroll
      for (int i = 0; i < 8; ++i) {
        const unsigned d = XQ[i >> 1], lo = (i & 1) ? d >> 16 : d & 0xffffu, hi = (i & 1) ? d & 0xffff0000u : d << 16;
#pragma unroll
        for (int k = 0; k < 4; ++k) {
          acc[2 * k] = dot2(WQ[i][k], lo, acc[2 * k]);
          acc[2 * k + 1] = dot2(WQ[i][k], hi, acc[2 * k + 1]);
        }
      }
#pragma unroll
      for (int i = 0; i < 4; ++i) {
        const unsigned d = XF[i >> 1], lo = (i & 1) ? d >> 16 : d & 0xffffu, hi = (i & 1) ? d & 0xffff0000u : d << 16;
#pragma unroll
        for (int k = 0; k < 4; ++k) {
          acc[8 + 2 * k] = dot2(F[i][k], lo, acc[8 + 2 * k]);
          acc[8 + 2 * k + 1] = dot2(F[i][k], hi, acc[8 + 2 * k + 1]);
        }
      }
      xg_scatter<2>(acc, r2, g & 1);
#pragma unroll
      for (int j = 0; j < 2; ++j) lds[L_PART + w * 128 + 64 * j + 8 * c + g] = r2[j];
    }
    asm volatile("s_waitcnt lgkmcnt(0)" ::: "memory");
    if (lane == 0) __hip_atomic_fetch_add(flg, 1u, __ATOMIC_RELAXED, __HIP_MEMORY_SCOPE_WORKGROUP);
    SCHED_FENCE();
  } else {
#pragma unroll
    for (int m = 0; m < KE; ++m) KLD(m / 3, m % 3);
    SCHED_FENCE();
    EPLD();
    SCHED_FENCE();
    __syncthreads();
    SCHED_FENCE();
  }
  const int lz = lane < 60 ? lane : 59;
  const int zr = lz / 10, zc = lz - 10 * zr;
  int w2o = (zr * D + 8 * zc) * 2;
  asm volatile("" : "+v"(w2o));
  if (w == 0) {
    for (unsigned spin = 0; spin < (SPIN_MAX << 4); ++spin) {
      if (__hip_atomic_load(flg, __ATOMIC_RELAXED, __HIP_MEMORY_SCOPE_WORKGROUP) == 4u) break;
      __builtin_amdgcn_s_sleep(1);
    }
    asm volatile("s_waitcnt lgkmcnt(0)" ::: "memory");
    AWAIT(epoch, 0);
    tag = __builtin_amdgcn_readfirstlane(epoch) + 1u;
    const f32x2* pp = reinterpret_cast<const f32x2*>(lds + L_PART) + lane;
    const f32x2 a01 = pp[0] + pp[64], a23 = pp[128] + pp[192];
    const bool two = sp == 2 && lane < 32;
    const f32x2 sm = two ? a01 : a01 + a23;
    const int ct0 = sp >= 5 ? 2 : sp >= 3 ? 1 : 0, slot = sp - (sp >= 5 ? 5 : sp >= 3 ? 2 : 0);
    const int uf = fidle ? GZF + 32 * xcd + 16 * (jj - fi) : G1F + xcd * 960 + fp * 32;
    const int un = lane < 32 ? G1 + h * 288 + slot * 96 + ct0 * 32 + lane : uf + lane - 32;
    if (lane < 33 || !fidle) gst2(un, sm[0], sm[1]);
    if (two) gst2(G1 + h * 288 + 32 + lane, a23[0], a23[1]);
    SCHED_FENCE();
  }
  asm volatile("" : "+s"(tstart64));
#define PHW 28
  const unsigned tstart = (unsigned)tstart64 - ((((w & 3) * 2 + (w >> 2)) * PHW) >> 3);
  ta = (tstart + TA) * nt - TOFF;
  te = (tstart + TE) * nt - TOFF;
  int wop = w;
  asm volatile("" : "+s"(wop));
  if (wop != 0) {
    if (w < 4) {
#pragma unroll
      for (int m = 0; m < KE; ++m) KWL(m / 3, m % 3);
    }
#pragma unroll
    for (int m = KE; m < 3 * NF; ++m) {
      KWL(m / 3, m % 3);
    }
    TIE(epoch);
    TIE(cpv);
    tag = __builtin_amdgcn_readfirstlane(epoch) + 1u;
    SCHED_FENCE();
  } else {
    u32x4 W2[8];
    const auto rw2 = __builtin_amdgcn_make_buffer_rsrc((void*)(w2 + (size_t)48 * h * D + 80 * sp), (short)0, 48 * D * 2, 0x00020000);
#pragma unroll
    for (int i = 0; i < 8; ++i) W2[i] = __builtin_amdgcn_raw_buffer_load_b128(rw2, w2o, i * 6 * D * 2, 2);
#pragma unroll
    for (int t = 0; t < NF; ++t)
#pragma unroll
      for (int j = 0; j < 3; ++j) KLD(t, j);
    SCHED_FENCE();
    const int row = lane >> 4, l16 = lane & 15;
    const int gb = G1 + ha * 288 + lane;
    const int gc = row < 3 ? G1 + ha * 288 + row * 96 + 64 + l16 : GZF + 32 * sp;
    const int l48 = lane < 48 ? lane : 47, fm = l48 >= 24 ? 1 : 0, fpr = 24 * (h & 3) + l48 - 24 * fm;
    const int gf = G1F + xcd * 960 + (3 * fm + (fpr >> 5)) * 160 + (fpr & 31);
    u32x4 va[3], vb[2], vf[5];
    for (unsigned spin = 0;; ++spin) {
      MEMBAR();
#pragma unroll
      for (int s = 0; s < 3; ++s) va[s] = gld2(gb, s * 96);
#pragma unroll
      for (int s = 0; s < 2; ++s) vb[s] = gld2(gc, s * 16);
#pragma unroll
      for (int s = 0; s < 5; ++s) vf[s] = gld2(gf, s * 32);
      bool ok = gok2(vb[0]) && gok2(vb[1]) && gok2(vf[3]) && gok2(vf[4]);
#pragma unroll
      for (int s = 0; s < 3; ++s) ok = ok && gok2(va[s]) && gok2(vf[s]);
      if (__all(ok) || spin >= SPIN_MAX) break;
      __builtin_amdgcn_s_sleep(1);
    }
    {
      const float a0 = (bitsf(va[0][0]) + bitsf(va[1][0])) + bitsf(va[2][0]), a1 = (bitsf(va[0][2]) + bitsf(va[1][2])) + bitsf(va[2][2]);
      reinterpret_cast<unsigned*>(lds + L_QS)[lane] = (unsigned)bfbits(a0) | ((unsigned)bfbits(a1) << 16);
      const float p0 = swap16_add(row < 3 ? bitsf(vb[0][0]) : 0.f, row < 3 ? bitsf(vb[0][2]) : 0.f);
      const float p1 = swap16_add(row < 3 ? bitsf(vb[1][0]) : 0.f, row < 3 ? bitsf(vb[1][2]) : 0.f);
      const float bq = swap32_add(p0, p1);
      reinterpret_cast<unsigned short*>(lds + L_QS)[2 * (64 + 16 * (row >> 1) + l16) + (row & 1)] = bfbits(bq);
    }
    asm volatile("s_waitcnt lgkmcnt(0)" ::: "memory");
    if (lane == 0) __hip_atomic_store(flg + 1, 1u, __ATOMIC_RELAXED, __HIP_MEMORY_SCOPE_WORKGROUP);
    SCHED_FENCE();
    {
      const float f0 = ((bitsf(vf[0][0]) + bitsf(vf[1][0])) + (bitsf(vf[2][0]) + bitsf(vf[3][0]))) + bitsf(vf[4][0]);
      const float f1 = ((bitsf(vf[0][2]) + bitsf(vf[1][2])) + (bitsf(vf[2][2]) + bitsf(vf[3][2]))) + bitsf(vf[4][2]);
      *reinterpret_cast<f32x2*>(lds + L_GU + 2 * l48) = f32x2{bfr(f0), bfr(f1)};
    }
    WAVE_SYNC();
    if (lane < 48) lds[L_H + lane] = bfr(bfr(silu(lds[L_GU + lane])) * lds[L_GU + 48 + lane]);
    WAVE_SYNC();
    {
      float z[8];
#pragma unroll
      for (int i = 0; i < 8; ++i) z[i] = 0.f;
#pragma unroll
      for (int i = 0; i < 8; ++i) {
        const unsigned chi = fbits(lds[L_H + zr + 6 * i]), clo = chi >> 16;
#pragma unroll
        for (int k = 0; k < 4; ++k) {
          z[2 * k] = dot2(W2[i][k], clo, z[2 * k]);
          z[2 * k + 1] = dot2(W2[i][k], chi, z[2 * k + 1]);
        }
      }
      if (lane < 60) {
        float* zp = lds + L_ZP + zr * 80 + 8 * zc;
        *reinterpret_cast<f32x4*>(zp) = f32x4{z[0], z[1], z[2], z[3]};
        *reinterpret_cast<f32x4*>(zp + 4) = f32x4{z[4], z[5], z[6], z[7]};
      }
    }
    SCHED_FENCE();
  }
  const int u = (tid & 255) < 240 ? (tid & 255) : 239;
  const int rg = u / 10, ch = u - 10 * rg;
  u32x4 WO[8];
  const u32x4 rwo = mkrs(wo + (size_t)DH * h * D + 80 * sp, DH * D * 2);
  const int wofs = (rg * D + 8 * ch) * 2;
#define WOLC(i)                                                                                                            \
  asm volatile("s_cmp_lt_u32 %4, 4\n\ts_cbranch_scc1 2\n\tbuffer_load_dwordx4 %0, %1, %2, %3 offen nt"                     \
               : "=v"(WO[i]) : "v"(wofs), "s"(rwo), "s"((i) * 24 * D * 2), "s"(w) : "memory", "scc")
#define VLD(t)                                                       \
  do {                                                               \
    _Pragma("unroll") for (int j = 0; j < 3; ++j) VWL(t, j);         \
  } while (0)
#pragma unroll
  for (int t = NF; t < NF + 2; ++t)
    if (t < nt) {
#pragma unroll
      for (int j = 0; j < 3; ++j) KWL(t, j);
      VLD(t);
    }
#define ZW 3
#define SLOWSUM(res, base, cc)                                                                                  \
  do {                                                                                                          \
    res = 0.f;                                                                                                  \
    _Pragma("unroll 1") for (int hh = 0; hh < HQ; ++hh) {                                                       \
      u32x2 v;                                                                                                  \
      for (unsigned spin = 0; spin < SPIN_MAX; ++spin) {                                                        \
        MEMBAR();                                                                                               \
        v = __builtin_amdgcn_raw_buffer_load_b64(rws, (cc) * 8, ((base) + hh * 320) * 16, 16);                  \
        if (v[1] == tag) break;                                                                                 \
      }                                                                                                         \
      res += bitsf(v[0]);                                                                                       \
    }                                                                                                           \
  } while (0)
#define ZPUB()                                                                                      \
  do {                                                                                              \
    const f32x2* zp = reinterpret_cast<const f32x2*>(lds + L_ZP) + (lane < 40 ? lane : 0);          \
    const f32x2 zs = ((zp[0] + zp[40]) + (zp[80] + zp[120])) + (zp[160] + zp[200]);                 \
    gst2(lane < 40 ? G3Z + h * 320 + 40 * sp + lane : GEND, zs[0], zs[1]);   \
  } while (0)
  u64 zold[2] = {0ull, 0ull}, zadd[2] = {0ull, 0ull};
  int mro = 8 * tid, mso = tid < 97 ? (G2 + rec * 104 + tid) * 16 : GEND * 16;
  u32x4 pub = {0u, tag, 0u, tag};
  asm volatile("" : "+v"(mro), "+v"(mso), "+v"(pub));
  {
    float Mg;
    float acc[3][8];
#pragma unroll
    for (int i = 0; i < 24; ++i) acc[i >> 3][i & 7] = 0.f;
    float* pw = lds + L_PART + w * PS + 8 * c + g;
    const bool b3 = g & 1;
#define PV3(t, j, cl, ch)                                                                                               \
  do {                                                                                                                  \
    if ((cl) < 0) asm volatile("" : "+v"(plo), "+v"(phi));                                                              \
    else if ((cl) == (ch)) asm volatile("s_waitcnt vmcnt(%2)" : "+v"(plo), "+v"(phi) : "n"((cl) < 0 ? 0 : (cl)) : "memory");  \
    else asm volatile("s_waitcnt vmcnt(%2)\n\ts_cmp_lt_u32 %4, 4\n\ts_cbranch_scc0 1\n\ts_waitcnt vmcnt(%3)"              \
                      : "+v"(plo), "+v"(phi) : "n"(ch), "n"((cl) < 0 ? 0 : (cl)), "s"(w) : "memory", "scc");            \
    _Pragma("unroll") for (int i = 0; i < 4; ++i) {                                                                     \
      acc[j][2 * i] = dot2(V[t][j][i], plo, acc[j][2 * i]);                                                             \
      acc[j][2 * i + 1] = dot2(V[t][j][i], phi, acc[j][2 * i + 1]);                                                     \
    }                                                                                                                   \
    if ((t) == NF - 1) {                                                          \
      float r1[1];                                                                                                      \
      xg_scatter<1>(acc[j], r1, b3);                                                                                    \
      pw[64 * (j)] = r1[0] * ew;                                                                                        \
    } else {                                                                                                            \
      asm volatile("" : "+v"(acc[j][0]), "+v"(acc[j][1]), "+v"(acc[j][2]), "+v"(acc[j][3]), "+v"(acc[j][4]),            \
                   "+v"(acc[j][5]), "+v"(acc[j][6]), "+v"(acc[j][7]));                       \
    }                                                                                                                   \
    SCHED_FENCE();                                                                                                      \
  } while (0)
#define PVT(t, c)                                                                                                       \
  do {                                                                                                                  \
    unsigned plo = pb[t], phi = plo << 16;                                                                              \
    PV3(t, 0, c, c);                                                                                                    \
    PV3(t, 1, -1, -1);                                                                                                  \
    PV3(t, 2, -1, -1);                                                                                                  \
  } while (0)
#define WGMAX()                                                                                                         \
  do {                                                                                                                  \
    for (unsigned spin = 0; spin < (SPIN_MAX << 4); ++spin) {                                                           \
      if (__hip_atomic_load(flg + 3, __ATOMIC_RELAXED, __HIP_MEMORY_SCOPE_WORKGROUP) == 8u) break;                      \
      __builtin_amdgcn_s_sleep(1);                                                                                      \
    }                                                                                                                   \
    MEMBAR();                                                                                                           \
    const f32x4 ma = *reinterpret_cast<const f32x4*>(lds + L_ML), mb2 = *reinterpret_cast<const f32x4*>(lds + L_ML + 4); \
    Mg = fmaxf(fmaxf(fmaxf(ma[0], ma[1]), fmaxf(ma[2], ma[3])), fmaxf(fmaxf(mb2[0], mb2[1]), fmaxf(mb2[2], mb2[3])));   \
    ew = ex2((mw - Mg) * LOG2E);                                                                                        \
    if (lane == 0) *reinterpret_cast<f32x2*>(lds + L_PART + w * PS + DH) = f32x2{w == 0 ? Mg : 0.f, lw * ew};           \
    SCHED_FENCE();                                                                                                      \
  } while (0)
    float ew, mw, lw;
    u32x4 qf[3];
    float sc[NF + 2];
    unsigned pb[NF + 2];
#define GATE(cnt)                                                                                                       \
  do {                                                                                                                  \
    asm volatile("s_waitcnt vmcnt(%2)" : "+v"(zold[0]), "+v"(zold[1]) : "n"(cnt) : "memory");                           \
    _Pragma("unroll") for (int k = 0; k < 2; ++k)                                                                       \
      if ((k == 0 || lane < 16) && (zold[k] >> 59) == 31u) {                                                            \
        const int cz = 80 * sp + 64 * k + lane;                                                                         \
        float f = fx_dec(zold[k] + zadd[k]);                                                                            \
        if (__builtin_expect(!fx_exact(zold[k], zadd[k]), 0)) SLOWSUM(f, G3Z, cz);                                      \
        __builtin_amdgcn_raw_buffer_store_b64(u32x2{fbits(bfr(silu(bfr(f)))), tag}, rws, cz * 8, GG * 16, 16);          \
      }                                                                                                                 \
  } while (0)
    auto slot = [&](auto mc) __attribute__((always_inline)) {
      constexpr int m = decltype(mc)::value;
      VWL(m / 3, m % 3);
      if (m == QS) {
        for (unsigned spin = 0; spin < (SPIN_MAX << 4); ++spin) {
          if (__hip_atomic_load(flg + 1, __ATOMIC_RELAXED, __HIP_MEMORY_SCOPE_WORKGROUP) != 0u) break;
          __builtin_amdgcn_s_sleep(1);
        }
        asm volatile("s_waitcnt lgkmcnt(0)" ::: "memory");
        SCHED_FENCE();
#pragma unroll
        for (int j = 0; j < 3; ++j) qf[j] = *reinterpret_cast<const u32x4*>(lds + L_QS + (c + 8 * j) * 4);
      }
#pragma unroll
      for (int t = 0; t < NF + 2; ++t)
        if (SS[t] == m) {
          sc[t] = -INFINITY;
          if (t < NF || t < nt) {
            float d = 0.f;
#pragma unroll
            for (int j = 0; j < 3; ++j) {
              TIE(K[t][j]);
#pragma unroll
              for (int i = 0; i < 4; ++i) d = dot2(K[t][j][i], qf[j][i], d);
            }
            d = grp8_sum(d);
            sc[t] = bfr(bfr(d) * SCALE);
          }
          TIE(sc[t]);
        }
      if (m == XS) {
        mw = sc[0];
#pragma unroll
        for (int t = 1; t < NF + 2; ++t) mw = fmaxf(mw, sc[t]);
        mw = xg_max(mw);
        if (lane == 0) {
          lds[L_ML + w] = mw;
          __hip_atomic_fetch_add(flg + 3, 1u, __ATOMIC_RELAXED, __HIP_MEMORY_SCOPE_WORKGROUP);
        }
        lw = 0.f;
#pragma unroll
        for (int t = 0; t < NF + 2; ++t) {
          const float p = ex2((sc[t] - mw) * LOG2E);
          lw += p;
          pb[t] = bfbits(p);
          TIE(pb[t]);
        }
        lw = xg_sum(lw);
        TIE(lw);
      }
      if (m == XS + 1 && NF < nt) PVT(NF, -1);
      if (m == XS + 2 && NF + 1 < nt) PVT(NF + 1, -1);
      if (m >= XS + 3) {
        constexpr int n0 = PVN(m - 1), n1 = PVN(m);
#pragma unroll
        for (int n = n0; n < n1; ++n) {
          unsigned plo = pb[n / 3], phi = plo << 16;
          PV3(n / 3, n % 3, n == m - 8 ? 8 : -1, n == m - 8 ? 8 : -1);
        }
      }
      if (m == ZSL) {
        WGMAX();
        if (w == ZW) {
#pragma unroll
          for (int k = 0; k < 2; ++k) {
            const float* zp = lds + L_ZP + (k ? 64 + (lane & 15) : lane);
            zold[k] = zadd[k] = fx_enc(((zp[0] + zp[80]) + (zp[160] + zp[240])) + (zp[320] + zp[400]));
          }
          const int za0 = (2 * GA + D + 80 * sp + lane) * 8, za1 = lane < 16 ? za0 + 512 : GEND * 16;
          ZPUB();
          asm volatile("buffer_atomic_add_x2 %0, %2, %4, 0 offen sc0\n\tbuffer_atomic_add_x2 %1, %3, %4, 0 offen sc0"
                       : "+v"(zold[0]), "+v"(zold[1]) : "v"(za0), "v"(za1), "s"(rwa) : "memory");
        }
      }
      if (m == GSL && w == ZW) GATE(GSL - ZSL);
      SCHED_FENCE();
    };
#define SL3(t) slot(IC<3 * (t)>{}); slot(IC<3 * (t) + 1>{}); slot(IC<3 * (t) + 2>{})
    SL3(0); SL3(1); SL3(2); SL3(3); SL3(4); SL3(5); SL3(6);
#pragma unroll
    for (int n = PVN(3 * NF - 1); n < 3 * NF; ++n) {
      const int m = n - (3 * NF - 9);
      if (m >= 0) {
#pragma unroll
        for (int i = (m && n > PVN(3 * NF - 1)) ? WON[m - 1] : 0; i < WON[m]; ++i) WOLC(i);
      }
      unsigned plo = pb[n / 3], phi = plo << 16;
      PV3(n / 3, n % 3, m < 0 ? -1 : 8 - m, m < 0 ? -1 : 8 - m + WON[m < 0 ? 0 : m]);
    }
#pragma unroll
    for (int i = WON[8]; i < WON[9]; ++i) WOLC(i);
  }
  __syncthreads();
  if (__builtin_expect(w < 2, 1)) {
    {
      f32x2 p2[8];
#pragma unroll
      for (int ww = 0; ww < 8; ++ww) p2[ww] = *reinterpret_cast<const f32x2*>(reinterpret_cast<const char*>(lds + L_PART + ww * PS) + mro);
      const f32x2 ua = (p2[0] + p2[1]) + (p2[2] + p2[3]), ub = (p2[4] + p2[5]) + (p2[6] + p2[7]);
      float ub1 = ub[1];
      asm("" : "+v"(ub1));
      pub[0] = fbits(ua[0] + ub[0]);
      pub[2] = fbits(ua[1] + ub1);
      __builtin_amdgcn_raw_buffer_store_b128(pub, rws, mso, 0, 16);
    }
    SCHED_FENCE();
    const int t2 = tid < 96 ? tid : 95;
    const int pb = (G2 + h * NSP * 104 + t2) * 16, pm = (G2 + (h * NSP + c) * 104 + 96) * 16;
    const unsigned po = (unsigned)(size_t)(__attribute__((address_space(3))) float*)(lds + L_O) + 4 * t2;
    const int pg = lane < 20 ? (GG + 40 * sp + 20 * w + lane) * 16 : GEND * 16;
    const unsigned pl = (unsigned)(size_t)(__attribute__((address_space(3))) float*)(lds + L_GT) + 16 * (lane < 20 ? 20 * w + lane : 40 + tid);
    if (PD1) __builtin_amdgcn_s_sleep(PD1);
    {
      unsigned e0, e1, e2, e3, e4, e5, e6, e7, cnt;
#define DPC " row_mask:0xf bank_mask:0xf\n\t"
#define CHK(d, a, b) "v_bitop3_b32 " d ", " a ", " b ", %13 bitop3:0x7e\n\t"
#define ACC2(e, a, b) "v_fmac_f32 v44, " e ", " a "\n\tv_fmac_f32 v45, " e ", " b "\n\t"
      asm volatile(
          "s_mov_b32 %8, %16\n"
          "1:\n\t"
          "buffer_load_dwordx4 v[48:51], %18, %12, 0 offen sc1\n\t"
          "buffer_load_dwordx4 v[4:7], %9, %12, 0 offen sc1\n\t"
          "buffer_load_dwordx4 v[8:11], %10, %12, 0 offen sc1\n\t"
          "buffer_load_dwordx4 v[12:15], %10, %12, 0 offen offset:1664 sc1\n\t"
          "buffer_load_dwordx4 v[16:19], %10, %12, 0 offen offset:3328 sc1\n\t"
          "buffer_load_dwordx4 v[20:23], %10, %12, %14 offen sc1\n\t"
          "buffer_load_dwordx4 v[24:27], %10, %12, %14 offen offset:1664 sc1\n\t"
          "buffer_load_dwordx4 v[28:31], %10, %12, %14 offen offset:3328 sc1\n\t"
          "buffer_load_dwordx4 v[32:35], %10, %12, %15 offen sc1\n\t"
          "buffer_load_dwordx4 v[36:39], %10, %12, %15 offen offset:1664 sc1\n\t"
          "s_sub_u32 %8, %8, 1\n\t"
          "s_waitcnt vmcnt(8)\n\t"
          CHK("v41", "v5", "v7")
          "ds_write_b128 %19, v[48:51]\n\t"
          "v_max_f32_dpp v40, v4, v4 quad_perm:[1,0,3,2]" DPC
          "s_waitcnt vmcnt(7)\n\t"
          CHK("v46", "v9", "v11")
          "v_max_f32_dpp v40, v40, v40 quad_perm:[2,3,0,1]" DPC
          "s_waitcnt vmcnt(6)\n\t"
          CHK("v47", "v13", "v15")
          "v_max_f32_dpp v40, v40, v40 row_half_mirror" DPC
          "v_sub_f32 v42, v4, v40\n\t"
          "v_mul_f32 v42, 0x3fb8aa3b, v42\n\t"
          "v_exp_f32 v42, v42\n\t"
          "v_or3_b32 v41, v41, v46, v47\n\t"
          "v_mul_f32 v40, v6, v42\n\t"
          "s_waitcnt vmcnt(5)\n\t"
          CHK("v46", "v17", "v19")
          "v_add_f32_dpp v40, v40, v40 quad_perm:[1,0,3,2]" DPC
          "s_waitcnt vmcnt(4)\n\t"
          CHK("v47", "v21", "v23")
          "v_add_f32_dpp v40, v40, v40 quad_perm:[2,3,0,1]" DPC
          "v_or3_b32 v41, v41, v46, v47\n\t"
          "s_nop 0\n\t"
          "v_add_f32_dpp v40, v40, v40 row_half_mirror" DPC
          "v_rcp_f32 v40, v40\n\t"
          "s_nop 0\n\t"
          "v_mul_f32 v42, v42, v40\n\t"
          "s_nop 0\n\t"
          "v_readlane_b32 %0, v42, 0\n\tv_readlane_b32 %1, v42, 1\n\tv_readlane_b32 %2, v42, 2\n\tv_readlane_b32 %3, v42, 3\n\t"
          "v_readlane_b32 %4, v42, 4\n\tv_readlane_b32 %5, v42, 5\n\tv_readlane_b32 %6, v42, 6\n\tv_readlane_b32 %7, v42, 7\n\t"
          "v_mul_f32 v44, %0, v8\n\tv_mul_f32 v45, %0, v10\n\t"
          ACC2("%1", "v12", "v14") ACC2("%2", "v16", "v18") ACC2("%3", "v20", "v22")
          "s_waitcnt vmcnt(3)\n\t"
          ACC2("%4", "v24", "v26") CHK("v46", "v25", "v27")
          "s_waitcnt vmcnt(2)\n\t"
          ACC2("%5", "v28", "v30") CHK("v47", "v29", "v31")
          "v_or3_b32 v41, v41, v46, v47\n\t"
          "s_waitcnt vmcnt(1)\n\t"
          ACC2("%6", "v32", "v34") CHK("v46", "v33", "v35")
          "s_waitcnt vmcnt(0)\n\t"
          ACC2("%7", "v36", "v38")
          "v_cvt_pk_bf16_f32 v44, v44, v45\n\t"
          "ds_write_b32 %11, v44\n\t"
          CHK("v47", "v37", "v39")
          "v_or3_b32 v41, v41, v46, v47\n\t"
          "v_cmp_ne_u32 vcc, 0, v41\n\t"
          "s_cbranch_vccz 2f\n\t"
          "s_sleep %17\n\t"
          "s_cbranch_scc0 1b\n"
          "2:\n\t"
          "s_waitcnt lgkmcnt(0)"
          : "=&s"(e0), "=&s"(e1), "=&s"(e2), "=&s"(e3), "=&s"(e4), "=&s"(e5), "=&s"(e6), "=&s"(e7), "=&s"(cnt)
          : "v"(pm), "v"(pb), "v"(po), "s"(rwa), "s"(tag), "s"(3 * 1664), "s"(6 * 1664), "n"(SPIN_MAX), "n"(PS1), "v"(pg), "v"(pl)
          : "memory", "scc", "vcc", "v4", "v5", "v6", "v7", "v8", "v9", "v10", "v11", "v12", "v13", "v14", "v15", "v16", "v17", "v18", "v19",
            "v20", "v21", "v22", "v23", "v24", "v25", "v26", "v27", "v28", "v29", "v30", "v31", "v32", "v33", "v34", "v35", "v36", "v37",
            "v38", "v39", "v40", "v41", "v42", "v44", "v45", "v46", "v47", "v48", "v49", "v50", "v51");
    }
    __syncthreads();
  } else if (w == 2) {
    __syncthreads();
  } else {
    if (w >= 4) {
#pragma unroll
      for (int i = WON[9]; i < 8; ++i) WOLC(i);
#pragma unroll
      for (int i = 0; i < 8; ++i) {
        AWAIT(WO[i], 7 - i);
        *reinterpret_cast<u32x4*>(lds + L_WO + u * 4 + i * 960) = WO[i];
      }
      asm volatile("s_waitcnt lgkmcnt(0)" ::: "memory");
      if (lane == 0) __hip_atomic_fetch_add(flg + 2, 1u, __ATOMIC_RELAXED, __HIP_MEMORY_SCOPE_WORKGROUP);
    }
    for (unsigned spin = 0; spin < (SPIN_MAX << 4); ++spin) {
      if (__hip_atomic_load(flg + 2, __ATOMIC_RELAXED, __HIP_MEMORY_SCOPE_WORKGROUP) == 4u) break;
      __builtin_amdgcn_s_sleep(1);
    }
    asm volatile("s_waitcnt lgkmcnt(0)" ::: "memory");
    u32x4 wb[6];
    {
      typedef __attribute__((address_space(3))) v4i16_t* ltr;
      const unsigned la = (unsigned)(size_t)(__attribute__((address_space(3))) float*)(lds + L_WO) + (8 * (lane >> 4) + ((lane >> 2) & 3)) * 160 + 32 * (w - 3) + 8 * (lane & 3);
#pragma unroll
      for (int k = 0; k < 6; ++k) {
        const u32x2 t0 = __builtin_bit_cast(u32x2, __builtin_amdgcn_ds_read_tr16_b64_v4i16((ltr)(la + 32 * k * 160)));
        const u32x2 t1 = __builtin_bit_cast(u32x2, __builtin_amdgcn_ds_read_tr16_b64_v4i16((ltr)(la + (32 * k + 4) * 160)));
        wb[k] = u32x4{t0[0], t0[1], t1[0], t1[1]};
      }
    }
    const int cy = 80 * sp + 16 * (w - 3) + (lane & 15);
    float xv = bf_lo(reinterpret_cast<const unsigned short*>(lds + L_XS)[cy]);
    gu64* pa = (gu64*)(gw + 2 * GA + cy);
    gu16* pout = (gu16*)(out + cy);
    typedef __attribute__((address_space(3))) const u32x4* la4;
    la4 pa4 = (la4)(lds + L_O) + 4 * (lane & 3) + (lane >> 4);
    int cy8 = cy * 8;
    typedef __attribute__((address_space(3))) const u32x2* la2;
    la2 pgt = (la2)(lds + L_GT) + 16 * (w - 3) + (lane & 15);
    asm volatile("" : "+v"(pgt));
    unsigned k0 = (1u << 27) - (0x411u << 20), k1 = k0 + (1u << 22), k2 = 1u << 17;
    double c0 = 262144.0 + 512.0, c1 = -(262144.0 + 32 * 512.0);
    float f0 = 512.f, f1 = 0x1p-9f;
    asm volatile("" : "+v"(wb[0]), "+v"(wb[1]), "+v"(wb[2]), "+v"(wb[3]), "+v"(wb[4]), "+v"(wb[5]));
    asm volatile("" : "+v"(xv), "+v"(pa), "+v"(pout), "+v"(pa4), "+v"(cy8), "+v"(k0), "+v"(k1), "+v"(k2), "+s"(c0), "+s"(c1), "+s"(f0), "+s"(f1));
    __syncthreads();
    const u32x4 a1 = pa4[0], a2 = pa4[16];
    SCHED_FENCE();
    u32x2 gg = *pgt;
    SCHED_FENCE();
#define MFMA(a, b, cc) __builtin_amdgcn_mfma_f32_16x16x32_bf16(__builtin_bit_cast(bf16x8_t, a), __builtin_bit_cast(bf16x8_t, b), cc, 0, 0, 0)
    const f32x4 z4 = {0.f, 0.f, 0.f, 0.f};
    const f32x4 d2 = MFMA(a1, wb[2], z4), d3 = MFMA(a1, wb[3], z4);
    SCHED_FENCE();
    f32x4 d0 = MFMA(a1, wb[0], z4), d1 = MFMA(a1, wb[1], z4);
    SCHED_FENCE();
    d0 = MFMA(a2, wb[4], d0);
    SCHED_FENCE();
    d1 = MFMA(a2, wb[5], d1);
    SCHED_FENCE();
    if (lane < 16) {
      const float q = ((d2[2] + d3[3]) + d0[0]) + d1[1];
      const bool bad = !(fabsf(q) < f0);
      const u64 db = __builtin_bit_cast(u64, (double)(bad ? 0.f : q) + c0);
      const unsigned fl = (bad ? k1 : k0) + (fabsf(q) < f1 ? k2 : 0u);
      const u64 add = ((u64)((unsigned)(db >> 32) + fl) << 32) | (unsigned)db;
      const u64 old = __hip_atomic_fetch_add(pa, add, __ATOMIC_RELAXED, __HIP_MEMORY_SCOPE_AGENT);
      __builtin_amdgcn_raw_buffer_store_b64(u32x2{fbits(q), tag}, rws, cy8, (G3Y + h * 320) * 16, 16);
      SCHED_FENCE();
      unsigned lim = (gg[1] == tag && !bad) ? (32u << 17) - (fl - k0) : 0u;
      asm volatile("" : "+v"(lim));
      SCHED_FENCE();
      float sv = (float)(__builtin_bit_cast(double, (old & ((1ull << 49) - 1ull)) + db) + c1);
      asm("" : "+v"(sv));
      unsigned res = bfbits(xv + bfr(bfr(sv) * bitsf(gg[0])));
      const unsigned rel = (unsigned)(old >> 32) - (31u << 27);
      TIE(res);
      if (rel < lim) *pout = res;
      SCHED_FENCE();
      asm volatile(".Lfd_end:");
      if ((old >> 59) == 31u) {
        if (__builtin_expect(rel >= lim, 0)) {
          if (!fx_exact(old, add)) SLOWSUM(sv, G3Y, cy);
          for (unsigned spin = 0; gg[1] != tag && spin < SPIN_MAX; ++spin) {
            MEMBAR();
            gg = __builtin_amdgcn_raw_buffer_load_b64(rws, cy8, GG * 16, 16);
          }
          *pout = bfbits(xv + bfr(bfr(sv) * bitsf(gg[0])));
        }
        if (cy == 0)
          __hip_atomic_store(reinterpret_cast<unsigned*>(gw + 2 * GEP), tag, __ATOMIC_RELAXED, __HIP_MEMORY_SCOPE_AGENT);
      }
    }
  }
}

}

extern "C" void kernel_launch(void* const* d_in, const int* in_sizes, int n_in, void* d_out, int out_size, void* d_ws,
                              size_t ws_size, hipStream_t stream) {
  const bf16* x = (const bf16*)d_in[0];
  const bf16* kc = (const bf16*)d_in[1];
  const bf16* vc = (const bf16*)d_in[2];
  const bf16* wq = (const bf16*)d_in[3];
  const bf16* wo = (const bf16*)d_in[4];
  const bf16* w1 = (const bf16*)d_in[6];
  const bf16* w3 = (const bf16*)d_in[7];
  const bf16* w2 = (const bf16*)d_in[8];
  u64* gw = (u64*)d_ws;
  fused_decode<<<NWG, NT, 0, stream>>>(x, kc, vc, wq, wo, w1, w3, w2, gw, (bf16*)d_out);
}
```

```cpp
#define PD1X 24
#define PHW 28
#define TA 270
#define PROBE 2
#include <hip/hip_runtime.h>
#include <hip/hip_bf16.h>
#include <stdint.h>

typedef __hip_bfloat16 bf16;
typedef unsigned long long u64;
typedef __attribute__((address_space(1))) unsigned long long gu64;
typedef __attribute__((address_space(1))) unsigned short gu16;
typedef unsigned u32x4 __attribute__((ext_vector_type(4)));
typedef unsigned u32x2 __attribute__((ext_vector_type(2)));
typedef float f32x4 __attribute__((ext_vector_type(4)));
typedef __bf16 bf16x2_t __attribute__((ext_vector_type(2)));
typedef __bf16 bf16x8_t __attribute__((ext_vector_type(8)));
typedef short v4i16_t __attribute__((ext_vector_type(4)));

namespace {

constexpr int D = 640, HQ = 32, DH = 192, SK = 32768, WIN = 4096, FF = 1536;
constexpr int NSP = 8;
constexpr int NWG = HQ * NSP;
constexpr int NT = 512;
constexpr float SCALE = 0.07216878364870322f;
constexpr float LOG2E = 1.4426950408889634f;
constexpr unsigned SPIN_MAX = 1u << 16;
constexpr int WND = 8;
constexpr int WOL = 7;
constexpr int PQ = 4;
constexpr int NF = 7;
#define DXT 2, -1, 2, -1, 1, -3, 1, -1
constexpr int DX[8] = {DXT};
constexpr unsigned dx_pack() {
  unsigned r = 0;
  for (int i = 0; i < 8; ++i) r |= (unsigned)(DX[i] + 4) << (4 * i);
  return r;
}
constexpr unsigned dx_pos() {
  unsigned r = 0, a = 0;
  for (int i = 0; i < 8; ++i) { r |= a << (4 * i); a += DX[i] > 0 ? DX[i] : 0; }
  return r;
}
constexpr unsigned long long dx_don() {
  unsigned long long r = 0;
  int k = 0;
  for (int i = 0; i < 8; ++i)
    for (int q = 0; q < -DX[i]; ++q) r |= (unsigned long long)(i * 4 + q) << (5 * k++);
  return r;
}
constexpr bool dx_ok() {
  int a = 0, p = 0;
  bool in = true;
  for (int i = 0; i < 8; ++i) { a += DX[i]; p += DX[i] > 0 ? DX[i] : 0; in = in && DX[i] >= -4 && DX[i] <= 4; }
  return a == 0 && p <= 8 && in;
}
constexpr int dx_check[dx_ok() ? 1 : -1] = {0};
constexpr int KE = 8;
#define PS1 1
constexpr int PD1 = PD1X;
#define WOP 2, 2, 2, 2, 2, 4, 6, 8, 8, 8
constexpr int WON[10] = {WOP};
#define QS 1
#define SSL 1, 2, 3, 4, 5, 6, 8, 7, 7
#define XS 9
#define ZSL 12
#define GSL 20
constexpr int SS[9] = {SSL};
constexpr bool ss_ok() {
  bool ok = true;
  for (int t = 0; t < 9; ++t) ok = ok && SS[t] >= QS && SS[t] <= XS && SS[t] >= (t < 7 ? 3 * t - 10 : 5);
  return ok && ZSL > XS && GSL > ZSL;
}
constexpr int ss_check[ss_ok() ? 1 : -1] = {0};
constexpr int PVN(int m) {
  return m < XS + 3 ? 0 : (3 * (m - XS - 2) < m - 7 ? 3 * (m - XS - 2) : m - 7);
}
#define XOT 0, 0, 0, 0, 0, 0, 0, 0
constexpr int XO[8] = {XOT};
constexpr unsigned long long xo_pack() {
  unsigned long long r = 0;
  for (int i = 0; i < 8; ++i) r |= (unsigned long long)(XO[i] + 64) << (8 * i);
  return r;
}

constexpr int G1 = 0;
constexpr int G1F = G1 + HQ * 288;
constexpr int G2 = G1F + 8 * 960;
constexpr int G3Y = G2 + NWG * 104;
constexpr int G3Z = G3Y + 320 * 32;
constexpr int GG = G3Z + 320 * 32;
constexpr int GZF = GG + 320;
constexpr int GA = GZF + 256;
constexpr int GEP = GA + 640;
constexpr int GST = GEP + 8;
constexpr int GPR = GST + 256;
constexpr int GEND = GPR + 512;

__device__ __forceinline__ unsigned fbits(float x) { return __builtin_bit_cast(unsigned, x); }
__device__ __forceinline__ float bitsf(unsigned x) { return __builtin_bit_cast(float, x); }
#define gok2(v) ((v)[1] == tag && (v)[3] == tag)
#define gst2(unit, a, b) __builtin_amdgcn_raw_buffer_store_b128(u32x4{fbits(a), tag, fbits(b), tag}, rws, (unit) * 16, 0, 16)
#define gld2(unit, cunit) __builtin_amdgcn_raw_buffer_load_b128(rws, (unit) * 16, (cunit) * 16, 16)
#define MEMBAR() asm volatile("" ::: "memory")
typedef float f32x2 __attribute__((ext_vector_type(2)));

__device__ __forceinline__ float bf_lo(unsigned u) { return __builtin_bit_cast(float, u << 16); }
__device__ __forceinline__ float bf_hi(unsigned u) { return __builtin_bit_cast(float, u & 0xffff0000u); }
__device__ __forceinline__ float bfr(float x) { return __bfloat162float(__float2bfloat16(x)); }
__device__ __forceinline__ unsigned short bfbits(float x) {
  return __builtin_bit_cast(unsigned short, __float2bfloat16(x));
}
__device__ __forceinline__ float dot2(unsigned a, unsigned b, float c) {
  return __builtin_amdgcn_fdot2_f32_bf16(__builtin_bit_cast(bf16x2_t, a), __builtin_bit_cast(bf16x2_t, b), c, false);
}
template <int CTRL>
__device__ __forceinline__ float dpp(float x) {
  return __builtin_bit_cast(float, __builtin_amdgcn_mov_dpp(__builtin_bit_cast(int, x), CTRL, 0xf, 0xf, true));
}
__device__ __forceinline__ float grp8_sum(float x) {
  x += dpp<0xB1>(x);
  x += dpp<0x4E>(x);
  x += dpp<0x141>(x);
  return x;
}
__device__ __forceinline__ float swap16_add(float a, float b) {
  auto s = __builtin_amdgcn_permlane16_swap(fbits(a), fbits(b), false, false);
  return bitsf((unsigned)s[0]) + bitsf((unsigned)s[1]);
}
__device__ __forceinline__ float swap32_add(float a, float b) {
  auto t = __builtin_amdgcn_permlane32_swap(fbits(a), fbits(b), false, false);
  return bitsf((unsigned)t[0]) + bitsf((unsigned)t[1]);
}
__device__ __forceinline__ float xg_sum(float x) {
  x += dpp<0x128>(x);
  x = swap16_add(x, x);
  return swap32_add(x, x);
}
__device__ __forceinline__ float xg_max(float x) {
  x = fmaxf(x, dpp<0x128>(x));
  auto s = __builtin_amdgcn_permlane16_swap(fbits(x), fbits(x), false, false);
  x = fmaxf(bitsf((unsigned)s[0]), bitsf((unsigned)s[1]));
  auto t = __builtin_amdgcn_permlane32_swap(fbits(x), fbits(x), false, false);
  return fmaxf(bitsf((unsigned)t[0]), bitsf((unsigned)t[1]));
}
template <int NJ>
__device__ __forceinline__ void xg_scatter(const float (&a)[8 * NJ], float (&r)[NJ], const bool b3) {
#pragma unroll
  for (int j = 0; j < NJ; ++j) {
    float p[4];
#pragma unroll
    for (int e = 0; e < 4; ++e) p[e] = swap32_add(a[8 * j + e], a[8 * j + e + 4]);
    const float q0 = swap16_add(p[0], p[2]), q1 = swap16_add(p[1], p[3]);
    r[j] = (b3 ? q1 : q0) + dpp<0x128>(b3 ? q0 : q1);
  }
}
__device__ __forceinline__ float wave_sum(float x) { return xg_sum(grp8_sum(x)); }
__device__ __forceinline__ float ex2(float x) { return __builtin_amdgcn_exp2f(x); }
__device__ __forceinline__ float silu(float x) { return x / (1.0f + __expf(-x)); }
__device__ __forceinline__ float rdlane(float x, int l) {
  return __builtin_bit_cast(float, __builtin_amdgcn_readlane(__builtin_bit_cast(int, x), l));
}

__device__ __forceinline__ u64 fx_enc(float v) {
  const bool bad = !(fabsf(v) < 512.f), small = fabsf(v) < 0x1p-9f;
  const u64 d = __builtin_bit_cast(u64, (double)(bad ? 0.f : v) + (262144.0 + 512.0));
  const unsigned hi = (unsigned)(d >> 32) + ((1u << 27) - (0x411u << 20)) + (bad ? 1u << 22 : 0u) + (small ? 1u << 17 : 0u);
  return ((u64)hi << 32) | (unsigned)d;
}
__device__ __forceinline__ float fx_dec(u64 t) {
  const unsigned hi = ((unsigned)(t >> 32) & 0x1ffffu) | (0x411u << 20);
  return (float)(__builtin_bit_cast(double, ((u64)hi << 32) | (unsigned)t) - (262144.0 + 32 * 512.0));
}
__device__ __forceinline__ bool fx_exact(u64 old, u64 add) {
  const unsigned o = (unsigned)(old >> 49) & 0x3ffu, a = (unsigned)(add >> 49) & 0x3ffu;
  return o + a < 32u;
}
template <int N> struct IC { static constexpr int value = N; };
#define SCHED_FENCE() __builtin_amdgcn_sched_barrier(0)
#define ALD(dst, rs, vo, so, imm) \
  asm volatile("buffer_load_dwordx4 %0, %1, %2, %3 offen offset:%4 nt" : "=v"(dst) : "v"(vo), "s"(rs), "s"(so), "n"(imm) : "memory")
#define AWL(dst, rs, vo, so, imm)                                                                                      \
  asm volatile("s_waitcnt vmcnt(8)\n\ts_cmp_le_i32 %5, 8\n\ts_cbranch_scc0 4\n\ts_waitcnt vmcnt(7)\n\t"                 \
               "s_cmp_le_i32 %5, 7\n\ts_cbranch_scc0 1\n\ts_waitcnt vmcnt(6)\n\t"                                        \
               "buffer_load_dwordx4 %0, %1, %2, %3 offen offset:%4 nt"                                                 \
               : "=v"(dst) : "v"(vo), "s"(rs), "s"(so), "n"(imm), "s"(wl) : "memory", "scc")
#define AWT(dst, rs, vo, so, imm, tref, lim, mul, wn)                                                                  \
  asm volatile("s_memrealtime s[98:99]\n\ts_waitcnt vmcnt(%5)\n"                                                       \
               "8:\n\ts_waitcnt lgkmcnt(0)\n\ts_sub_u32 s98, s98, %6\n\ts_mul_i32 s98, s98, %8\n\ts_cmp_ge_i32 s98, %7\n\t" \
               "s_cbranch_scc1 9f\n\ts_sleep %9\n\ts_memrealtime s[98:99]\n\ts_branch 8b\n"                              \
               "9:\n\tbuffer_load_dwordx4 %0, %1, %2, %3 offen offset:%4 nt"                                            \
               : "=v"(dst) : "v"(vo), "s"(rs), "s"(so), "n"(imm), "n"((wn) - 1), "s"(tref), "n"(lim), "s"(mul), "n"(SLP) \
               : "memory", "scc", "s98", "s99")
#define WK 8
#define SLP 1
#define WALL 9
#define TB 28
#define TB2 TB
#define TE (TA + 27 * TB + 20 * TB2)
#define P0 21
#define WS 5
#define WS2 WS
#define TD 100
#define AWAIT(v, n) asm volatile("s_waitcnt vmcnt(%1)" : "+v"(v) : "n"(n) : "memory")
#define TIE(v) asm volatile("" : "+v"(v))
__device__ __forceinline__ u32x4 mkrs(const void* p, unsigned bytes) {
  const u64 a = (u64)p;
  return u32x4{(unsigned)a, (unsigned)(a >> 32) & 0xffffu, bytes, 0x00020000u};
}
#define STAMP(i)
#define STAMPT(i) STAMP(i)
#define WAVE_SYNC()                                        \
  do {                                                     \
    __builtin_amdgcn_fence(__ATOMIC_RELEASE, "wavefront"); \
    __builtin_amdgcn_wave_barrier();                       \
    __builtin_amdgcn_fence(__ATOMIC_ACQUIRE, "wavefront"); \
  } while (0)

constexpr int L_WO = 0;
constexpr int L_XS = L_WO + DH * 40;
constexpr int PS = 256;
constexpr int L_PART = L_XS + 4 * 320;
constexpr int L_XN = L_PART + 8 * PS;
constexpr int L_ZP = L_XN + 4 * 64;
constexpr int L_QS = L_ZP + 6 * 80;
constexpr int L_ML = L_QS + 96;
constexpr int L_O = L_ML + 16;
constexpr int L_H = L_O + 128;
constexpr int L_GU = L_H + 48;
constexpr int L_GT = L_GU + 96;
constexpr int L_FLG = L_GT + 168 * 4;
constexpr int L_END = L_FLG + 4;

__global__ void __launch_bounds__(NT, 2)
fused_decode(const bf16* __restrict__ x, const bf16* __restrict__ kc, const bf16* __restrict__ vc,
             const bf16* __restrict__ wq, const bf16* __restrict__ wo, const bf16* __restrict__ w1,
             const bf16* __restrict__ w3, const bf16* __restrict__ w2, u64* __restrict__ gw, bf16* __restrict__ out) {
  __shared__ __attribute__((aligned(16))) float lds[L_END];
  const unsigned pt0 = (unsigned)__builtin_amdgcn_s_memrealtime();
  unsigned pt21 = 0;
  bool pfin = false;
  SCHED_FENCE();
  const int b = blockIdx.x;
  const int xcd = b & 7, jj = b >> 3;
  const int h = xcd * 4 + (jj >> 3), sp = jj & 7;
  const int ha = jj, spa = xcd;
  const int w = __builtin_amdgcn_readfirstlane(threadIdx.x >> 6);
  const int tid = threadIdx.x, lane = tid & 63;
  const int g = lane >> 3, c = lane & 7;
  const int rec = ha * NSP + spa;
  unsigned* flg = reinterpret_cast<unsigned*>(lds + L_FLG);

#define CP 1
#define CPW 4
#define CPM 7
  unsigned cpv = (jj & 7) * 4096 + lane * 64;
#define CPLD()                                                                                                              \
  asm volatile("s_cmp_lg_u32 %1, %2\n\ts_cbranch_scc1 7f\n"                                                                 \
               ".Lfd_beg:\n\ts_getpc_b64 s[96:97]\n\ts_and_b32 s97, s97, 0xffff\n\ts_mov_b32 s98, .Lfd_end-.Lfd_beg-8\n\t"   \
               "s_mov_b32 s99, 0x20000\n\tbuffer_load_dword %0, %0, s[96:99], 0 offen\n7:"                                   \
               : "+v"(cpv) : "s"(w + 8 * (jj & ~CPM)), "n"(CPW) : "memory", "scc", "s96", "s97", "s98", "s99")
#define T0 0
  u64 tstart64;
  if (T0) tstart64 = __builtin_amdgcn_s_memrealtime();
  SCHED_FENCE();
  if (CP == 1) CPLD();
  SCHED_FENCE();
  asm volatile("" ::"s"(x), "s"(kc), "s"(vc), "s"(wq), "s"(wo), "s"(w1), "s"(w3), "s"(w2), "s"(gw), "s"(out));
  SCHED_FENCE();
  if (!T0) tstart64 = __builtin_amdgcn_s_memrealtime();
  SCHED_FENCE();
  const auto rws = __builtin_amdgcn_make_buffer_rsrc((void*)gw, (short)0, GEND * 16, 0x00020000);
  const size_t kvw = ((size_t)ha * SK + (SK - WIN)) * DH;
  const u32x4 rk = mkrs(kc + kvw, WIN * DH * 2), rv = mkrs(vc + kvw, WIN * DH * 2);
  const u32x4 rwa = mkrs(gw, GEND * 16);
#define CM 0
#define JO (CM ? 1024 : 128)
  const int kvo = CM ? lane * 16 : (g * DH + 8 * c) * 2;
  const int dx = (int)((dx_pack() >> (4 * xcd)) & 15u) - 4;
  const int wr = w < 4 ? (w + 3) & 3 : w;
  const int nt = 8 + ((unsigned)(wr - dx) >> 31) - ((unsigned)(7 - w + dx) >> 31);
  const int don = (int)(dx_don() >> (5 * (((dx_pos() >> (4 * xcd)) + wr) & 7u))) & 31;
#define LM 0
#define RT 1
#define TIX(s_, b_, u_) (LM == 0 ? ((s_) * 8 + (b_)) * 8 + (u_) : LM == 1 ? (u_) * 64 + (s_) * 8 + (b_) : LM == 2 ? (u_) * 64 + (b_) * 8 + (s_) : \
                         LM == 3 ? ((s_) * 8 + (u_)) * 8 + (b_) : LM == 4 ? ((b_) * 8 + (u_)) * 8 + (s_) : ((b_) * 8 + (s_)) * 8 + (u_))
  const int hr = RT == 1 ? ha : RT == 2 ? ha >> 2 : RT == 3 ? ha >> 1 : 0;
  const int kvb = TIX(spa, (w + (ha >> 3)) & 7, 0) * 8 * DH * 2;
  const int ko8 = TIX(don >> 2, (7 - (don & 3) + (ha >> 3)) & 7, (7 + hr) & 7) * 8 * DH * 2;
  const int wl = __builtin_amdgcn_readfirstlane((w < 4 ? WOL : WND) + (nt - 8) + (dx < 0 ? 1 : 0));
#define KVOFF(t) ((t) == 8 ? ko8 : kvb + TIX(0, 0, ((t) + hr) & 7) * 8 * DH * 2)
#define KLD(t, j) ALD(K[t][j], rk, kvo, KVOFF(t), JO * (j))
#define CR 0
#define PH 0
#define RP 16
  unsigned ta, te, tk, stv, tstart;
  const int sto = (GST + ha * 8) * 16 + 4 * c;
#define STLD() asm volatile("buffer_load_dword %0, %1, %2, 0 offen sc1" : "=v"(stv) : "v"(sto), "s"(rwa) : "memory")
#define TK 760
#define SH 49
#define SL 22
  const int km = w < 4 ? SH : SL;
#define TG 0
#define JJ(j) (TG == 1 ? 2 : TG == 2 ? ((j) == 0 ? 0 : 2) : (j))
#define KWL(t, j)                                                                                         \
  do {                                                                                                    \
    if ((t) >= NF) AWT(K[t][j], rk, kvo, KVOFF(t), JO * (j), ta, (6 * (t) - 21 + JJ(j)) * TB * 8, nt, WALL); \
    else AWL(K[t][j], rk, kvo, KVOFF(t), JO * (j));                                                      \
  } while (0)
#define VWL(t, j)                                                                                         \
  do {                                                                                                    \
    if ((t) < NF) AWT(V[t][j], rv, kvo, KVOFF(t), JO * (j), te, -(20 - 3 * (t) - JJ(j)) * TB2 * 8, nt, WALL); \
    else AWT(V[t][j], rv, kvo, KVOFF(t), JO * (j), ta, (6 * (t) - 18 + JJ(j)) * TB * 8, nt, WALL);          \
  } while (0)
#define EPLD() asm volatile("buffer_load_dword %0, off, %1, %2" : "=v"(epoch) : "s"(rwa), "s"(GEP * 16) : "memory")
  u32x4 K[NF + 2][3], V[NF + 2][3];

  const int fi = (xcd & 1) ? 19 : 8;
  const bool fidle = (unsigned)(jj - fi) < 2u;
  const int fp = jj > fi ? jj - 2 : jj;
  const int fcol = fp / 5, frp = fp - 5 * fcol;
  unsigned epoch, tag;
  if (w < 4) {
    const int fw = 4 * sp + w, ct = fw >= 20 ? 2 : fw >= 10 ? 1 : 0;
    const int r0 = 64 * (fw - 10 * ct);
    const int R = 128 * frp + 32 * w;
    const u32x4 xa = *reinterpret_cast<const u32x4*>(x + 8 * lane);
    const u32x4 xb = *reinterpret_cast<const u32x4*>(x + 512 + 8 * (lane & 15));
    u32x4 WQ[8], F[4];
    {
      const auto rq = __builtin_amdgcn_make_buffer_rsrc((void*)wq, (short)0, D * HQ * DH * 2, 0x00020000);
      const int qo = ((r0 + 8 * g) * (HQ * DH) + h * DH + 64 * ct + 8 * c) * 2;
#pragma unroll
      for (int i = 0; i < 8; ++i) WQ[i] = __builtin_amdgcn_raw_buffer_load_b128(rq, qo, i * HQ * DH * 2, 2);
      const auto rf = __builtin_amdgcn_make_buffer_rsrc((void*)(fcol >= 3 ? w3 : w1), (short)0, fidle ? 0 : D * FF * 2, 0x00020000);
      const int fo = ((R + 4 * g) * FF + 192 * xcd + 64 * (fcol >= 3 ? fcol - 3 : fcol) + 8 * c) * 2;
#pragma unroll
      for (int i = 0; i < 4; ++i) F[i] = __builtin_amdgcn_raw_buffer_load_b128(rf, fo, i * FF * 2, 2);
    }
    if (fidle && tid < 40)
      __builtin_amdgcn_raw_buffer_store_b128(u32x4{0u, 0u, 0u, 0u}, rws, (GA + (jj - fi) * 320 + 40 * xcd + lane) * 16, 0, 16);
    SCHED_FENCE();
    EPLD();
    if (tid < 4) flg[tid] = 0u;
    SCHED_FENCE();
    STAMP(2);
    __syncthreads();
    SCHED_FENCE();
    STAMP(3);
    unsigned short* xs = reinterpret_cast<unsigned short*>(lds + L_XS + w * 320);
    const bool lo16 = lane < 16;
    *reinterpret_cast<u32x4*>(xs + 8 * lane) = xa;
    if (lo16) *reinterpret_cast<u32x4*>(xs + 512 + 8 * lane) = xb;
    float xf[8], xg[8];
#pragma unroll
    for (int i = 0; i < 4; ++i) {
      xf[2 * i] = bf_lo(xa[i]);
      xf[2 * i + 1] = bf_hi(xa[i]);
      xg[2 * i] = bf_lo(xb[i]);
      xg[2 * i + 1] = bf_hi(xb[i]);
    }
    float sum = 0.f;
#pragma unroll
    for (int i = 0; i < 8; ++i) sum += xf[i] + (lo16 ? xg[i] : 0.f);
    sum = wave_sum(sum);
    const float mean = sum * (1.0f / D);
    float sq = 0.f;
#pragma unroll
    for (int i = 0; i < 8; ++i) {
      const float d1 = xf[i] - mean, d2 = xg[i] - mean;
      sq += d1 * d1 + (lo16 ? d2 * d2 : 0.f);
    }
    sq = wave_sum(sq);
    const float mb = bfr(mean);
    const float vb = bfr(sq * (1.0f / D));
    const float den = bfr(sqrtf(bfr(vb + 1e-5f)));
    WAVE_SYNC();
    auto xnorm = [&](int r) -> unsigned short { return bfbits(bfr(bf_lo(xs[r]) - mb) / den); };
    {
      unsigned short* xn = reinterpret_cast<unsigned short*>(lds + L_XN + w * 64);
      xn[lane] = xnorm((r0 < D ? r0 : 0) + lane);
      xn[64 + (lane & 31)] = xnorm(R + (lane & 31));
      WAVE_SYNC();
      const u32x4 XQ = *reinterpret_cast<const u32x4*>(xn + 8 * g);
      const u32x2 XF = *reinterpret_cast<const u32x2*>(xn + 64 + 4 * g);
      float acc[16], r2[2];
#pragma unroll
      for (int i = 0; i < 16; ++i) acc[i] = 0.f;
#pragma unroll
      for (int i = 0; i < 8; ++i) {
        const unsigned d = XQ[i >> 1], lo = (i & 1) ? d >> 16 : d & 0xffffu, hi = (i & 1) ? d & 0xffff0000u : d << 16;
#pragma unroll
        for (int k = 0; k < 4; ++k) {
          acc[2 * k] = dot2(WQ[i][k], lo, acc[2 * k]);
          acc[2 * k + 1] = dot2(WQ[i][k], hi, acc[2 * k + 1]);
        }
      }
#pragma unroll
      for (int i = 0; i < 4; ++i) {
        const unsigned d = XF[i >> 1], lo = (i & 1) ? d >> 16 : d & 0xffffu, hi = (i & 1) ? d & 0xffff0000u : d << 16;
#pragma unroll
        for (int k = 0; k < 4; ++k) {
          acc[8 + 2 * k] = dot2(F[i][k], lo, acc[8 + 2 * k]);
          acc[8 + 2 * k + 1] = dot2(F[i][k], hi, acc[8 + 2 * k + 1]);
        }
      }
      xg_scatter<2>(acc, r2, g & 1);
#pragma unroll
      for (int j = 0; j < 2; ++j) lds[L_PART + w * 128 + 64 * j + 8 * c + g] = r2[j];
    }
    STAMP(5);
    asm volatile("s_waitcnt lgkmcnt(0)" ::: "memory");
    if (lane == 0) __hip_atomic_fetch_add(flg, 1u, __ATOMIC_RELAXED, __HIP_MEMORY_SCOPE_WORKGROUP);
    SCHED_FENCE();
  } else {
    STAMP(1);
#pragma unroll
    for (int m = 0; m < KE; ++m) KLD(m / 3, m % 3);
    SCHED_FENCE();
    if (CR && w == 7 && jj < 4)
      __builtin_amdgcn_raw_buffer_store_b32((unsigned)tstart64, rws, lane < 8 ? (GST + (jj * 8 + lane) * 8) * 16 + 4 * xcd : GEND * 16, 0, 16);
    SCHED_FENCE();
    EPLD();
    SCHED_FENCE();
    STAMP(2);
    __syncthreads();
    SCHED_FENCE();
    STAMP(3);
  }
  const int lz = lane < 60 ? lane : 59;
  const int zr = lz / 10, zc = lz - 10 * zr;
  int w2o = (zr * D + 8 * zc) * 2;
  asm volatile("" : "+v"(w2o));
  if (w == 0) {
    for (unsigned spin = 0; spin < (SPIN_MAX << 4); ++spin) {
      if (__hip_atomic_load(flg, __ATOMIC_RELAXED, __HIP_MEMORY_SCOPE_WORKGROUP) == 4u) break;
      __builtin_amdgcn_s_sleep(1);
    }
    asm volatile("s_waitcnt lgkmcnt(0)" ::: "memory");
    AWAIT(epoch, 0);
    tag = __builtin_amdgcn_readfirstlane(epoch) + 1u;
    const f32x2* pp = reinterpret_cast<const f32x2*>(lds + L_PART) + lane;
    const f32x2 a01 = pp[0] + pp[64], a23 = pp[128] + pp[192];
    const bool two = sp == 2 && lane < 32;
    const f32x2 sm = two ? a01 : a01 + a23;
    const int ct0 = sp >= 5 ? 2 : sp >= 3 ? 1 : 0, slot = sp - (sp >= 5 ? 5 : sp >= 3 ? 2 : 0);
    const int uf = fidle ? GZF + 32 * xcd + 16 * (jj - fi) : G1F + xcd * 960 + fp * 32;
    const int un = lane < 32 ? G1 + h * 288 + slot * 96 + ct0 * 32 + lane : uf + lane - 32;
    if (lane < 33 || !fidle) gst2(un, sm[0], sm[1]);
    if (two) gst2(G1 + h * 288 + 32 + lane, a23[0], a23[1]);
    SCHED_FENCE();
  }
  asm volatile("" : "+s"(tstart64));
#define WOF 0
  tstart = (unsigned)tstart64 + (unsigned)((xo_pack() >> (8 * xcd)) & 255u) - 64u - (w < 4 ? 0 : WOF);
  tstart -= (((w & 3) * 2 + (w >> 2)) * PHW) >> 3;
  ta = tstart + TA;
  tk = tstart + TK;
  int wop = w;
  asm volatile("" : "+s"(wop));
  if (wop != 0) {
    if (w < 4) {
#pragma unroll
      for (int m = 0; m < KE; ++m) KWL(m / 3, m % 3);
    }
#pragma unroll
    for (int m = KE; m < 3 * NF; ++m) {
      KWL(m / 3, m % 3);
      if (CR && m == RP) STLD();
    }
    STAMP(8);
    SCHED_FENCE();
    pt21 = (unsigned)__builtin_amdgcn_s_memrealtime();
    SCHED_FENCE();
    TIE(epoch);
    if (CP) TIE(cpv);
    tag = __builtin_amdgcn_readfirstlane(epoch) + 1u;
    SCHED_FENCE();
  } else {
    u32x4 W2[8];
    const auto rw2 = __builtin_amdgcn_make_buffer_rsrc((void*)(w2 + (size_t)48 * h * D + 80 * sp), (short)0, 48 * D * 2, 0x00020000);
#pragma unroll
    for (int i = 0; i < 8; ++i) W2[i] = __builtin_amdgcn_raw_buffer_load_b128(rw2, w2o, i * 6 * D * 2, 2);
#pragma unroll
    for (int t = 0; t < NF; ++t)
#pragma unroll
      for (int j = 0; j < 3; ++j) KLD(t, j);
    if (CR) STLD();
    SCHED_FENCE();
    const int row = lane >> 4, l16 = lane & 15;
    const int gb = G1 + ha * 288 + lane;
    const int gc = row < 3 ? G1 + ha * 288 + row * 96 + 64 + l16 : GZF + 32 * sp;
    const int l48 = lane < 48 ? lane : 47, fm = l48 >= 24 ? 1 : 0, fpr = 24 * (h & 3) + l48 - 24 * fm;
    const int gf = G1F + xcd * 960 + (3 * fm + (fpr >> 5)) * 160 + (fpr & 31);
    u32x4 va[3], vb[2], vf[5];
    for (unsigned spin = 0;; ++spin) {
      MEMBAR();
#pragma unroll
      for (int s = 0; s < 3; ++s) va[s] = gld2(gb, s * 96);
#pragma unroll
      for (int s = 0; s < 2; ++s) vb[s] = gld2(gc, s * 16);
#pragma unroll
      for (int s = 0; s < 5; ++s) vf[s] = gld2(gf, s * 32);
      bool ok = gok2(vb[0]) && gok2(vb[1]) && gok2(vf[3]) && gok2(vf[4]);
#pragma unroll
      for (int s = 0; s < 3; ++s) ok = ok && gok2(va[s]) && gok2(vf[s]);
      if (__all(ok) || spin >= SPIN_MAX) break;
      __builtin_amdgcn_s_sleep(1);
    }
    {
      const float a0 = (bitsf(va[0][0]) + bitsf(va[1][0])) + bitsf(va[2][0]), a1 = (bitsf(va[0][2]) + bitsf(va[1][2])) + bitsf(va[2][2]);
      reinterpret_cast<unsigned*>(lds + L_QS)[lane] = (unsigned)bfbits(a0) | ((unsigned)bfbits(a1) << 16);
      const float p0 = swap16_add(row < 3 ? bitsf(vb[0][0]) : 0.f, row < 3 ? bitsf(vb[0][2]) : 0.f);
      const float p1 = swap16_add(row < 3 ? bitsf(vb[1][0]) : 0.f, row < 3 ? bitsf(vb[1][2]) : 0.f);
      const float bq = swap32_add(p0, p1);
      reinterpret_cast<unsigned short*>(lds + L_QS)[2 * (64 + 16 * (row >> 1) + l16) + (row & 1)] = bfbits(bq);
    }
    asm volatile("s_waitcnt lgkmcnt(0)" ::: "memory");
    if (lane == 0) __hip_atomic_store(flg + 1, 1u, __ATOMIC_RELAXED, __HIP_MEMORY_SCOPE_WORKGROUP);
    SCHED_FENCE();
    {
      const float f0 = ((bitsf(vf[0][0]) + bitsf(vf[1][0])) + (bitsf(vf[2][0]) + bitsf(vf[3][0]))) + bitsf(vf[4][0]);
      const float f1 = ((bitsf(vf[0][2]) + bitsf(vf[1][2])) + (bitsf(vf[2][2]) + bitsf(vf[3][2]))) + bitsf(vf[4][2]);
      *reinterpret_cast<f32x2*>(lds + L_GU + 2 * l48) = f32x2{bfr(f0), bfr(f1)};
    }
    WAVE_SYNC();
    if (lane < 48) lds[L_H + lane] = bfr(bfr(silu(lds[L_GU + lane])) * lds[L_GU + 48 + lane]);
    WAVE_SYNC();
    {
      float z[8];
#pragma unroll
      for (int i = 0; i < 8; ++i) z[i] = 0.f;
#pragma unroll
      for (int i = 0; i < 8; ++i) {
        const unsigned chi = fbits(lds[L_H + zr + 6 * i]), clo = chi >> 16;
#pragma unroll
        for (int k = 0; k < 4; ++k) {
          z[2 * k] = dot2(W2[i][k], clo, z[2 * k]);
          z[2 * k + 1] = dot2(W2[i][k], chi, z[2 * k + 1]);
        }
      }
      if (lane < 60) {
        float* zp = lds + L_ZP + zr * 80 + 8 * zc;
        *reinterpret_cast<f32x4*>(zp) = f32x4{z[0], z[1], z[2], z[3]};
        *reinterpret_cast<f32x4*>(zp + 4) = f32x4{z[4], z[5], z[6], z[7]};
      }
    }
    SCHED_FENCE();
  }
  const int u = (tid & 255) < 240 ? (tid & 255) : 239;
  const int rg = u / 10, ch = u - 10 * rg;
  u32x4 WO[8];
  const u32x4 rwo = mkrs(wo + (size_t)DH * h * D + 80 * sp, DH * D * 2);
  const int wofs = (rg * D + 8 * ch) * 2;
#define WOLC(i)                                                                                                            \
  asm volatile("s_cmp_lt_u32 %4, 4\n\ts_cbranch_scc1 2\n\tbuffer_load_dwordx4 %0, %1, %2, %3 offen nt"                     \
               : "=v"(WO[i]) : "v"(wofs), "s"(rwo), "s"((i) * 24 * D * 2), "s"(w) : "memory", "scc")
#define VLD(t)                                                       \
  do {                                                               \
    _Pragma("unroll") for (int j = 0; j < 3; ++j) VWL(t, j);         \
  } while (0)
#pragma unroll
  for (int t = NF; t < NF + 2; ++t)
    if (t < nt) {
#pragma unroll
      for (int j = 0; j < 3; ++j) KWL(t, j);
      VLD(t);
    }
  STAMP(9);
  {
    int d = 0;
    if (CR) {
      asm volatile("s_cmp_gt_i32 %1, %2\n\ts_cbranch_scc1 1\n\ts_waitcnt vmcnt(%3)" : "+v"(stv) : "s"(nt), "n"(NF), "n"(3 * NF - 1 - RP) : "memory", "scc");
      d = (int)(tstart - stv);
      d = (unsigned)(d + 150) <= 300u ? d : 0;
#define IDPP(x, ctrl) __builtin_amdgcn_update_dpp(0, (int)(x), ctrl, 0xf, 0xf, true)
      d += IDPP(d, 0xB1);
      d += IDPP(d, 0x4E);
      d += IDPP(d, 0x141);
      d = __builtin_amdgcn_readfirstlane(d) >> 3;
    }
    te = tstart - d + TE + (PH ? ((w * 4 + (jj & 3) - 16) * TB) >> 5 : 0);
    SCHED_FENCE();
  }
#define ZW 3
#define ZT 1
#define SLOWSUM(res, base, cc)                                                                                  \
  do {                                                                                                          \
    res = 0.f;                                                                                                  \
    _Pragma("unroll 1") for (int hh = 0; hh < HQ; ++hh) {                                                       \
      u32x2 v;                                                                                                  \
      for (unsigned spin = 0; spin < SPIN_MAX; ++spin) {                                                        \
        MEMBAR();                                                                                               \
        v = __builtin_amdgcn_raw_buffer_load_b64(rws, (cc) * 8, ((base) + hh * 320) * 16, 16);                  \
        if (v[1] == tag) break;                                                                                 \
      }                                                                                                         \
      res += bitsf(v[0]);                                                                                       \
    }                                                                                                           \
  } while (0)
#define ZS 0
#define ZPUB()                                                                                      \
  do {                                                                                              \
    const f32x2* zp = reinterpret_cast<const f32x2*>(lds + L_ZP) + (lane < 40 ? lane : 0);          \
    const f32x2 zs = ((zp[0] + zp[40]) + (zp[80] + zp[120])) + (zp[160] + zp[200]);                 \
    gst2(lane < 40 ? G3Z + h * 320 + 40 * sp + lane : GEND, zs[0], zs[1]);   \
  } while (0)
  u64 zold[2] = {0ull, 0ull}, zadd[2] = {0ull, 0ull};
  int mro = 8 * tid, mso = tid < 97 ? (G2 + rec * 104 + tid) * 16 : GEND * 16;
  u32x4 pub = {0u, tag, 0u, tag};
  asm volatile("" : "+v"(mro), "+v"(mso), "+v"(pub));
  {
    float Mg;
    float acc[3][8];
#pragma unroll
    for (int i = 0; i < 24; ++i) acc[i >> 3][i & 7] = 0.f;
    float* pw = lds + L_PART + w * PS + 8 * c + g;
    const bool b3 = g & 1;
#define PV3(t, j, cl, ch)                                                                                               \
  do {                                                                                                                  \
    if ((cl) < 0) asm volatile("" : "+v"(plo), "+v"(phi));                                                              \
    else if ((cl) == (ch)) asm volatile("s_waitcnt vmcnt(%2)" : "+v"(plo), "+v"(phi) : "n"((cl) < 0 ? 0 : (cl)) : "memory");  \
    else asm volatile("s_waitcnt vmcnt(%2)\n\ts_cmp_lt_u32 %4, 4\n\ts_cbranch_scc0 1\n\ts_waitcnt vmcnt(%3)"              \
                      : "+v"(plo), "+v"(phi) : "n"(ch), "n"((cl) < 0 ? 0 : (cl)), "s"(w) : "memory", "scc");            \
    _Pragma("unroll") for (int i = 0; i < 4; ++i) {                                                                     \
      acc[j][2 * i] = dot2(V[t][j][i], plo, acc[j][2 * i]);                                                             \
      acc[j][2 * i + 1] = dot2(V[t][j][i], phi, acc[j][2 * i + 1]);                                                     \
    }                                                                                                                   \
    if ((t) == NF - 1) {                                                          \
      float r1[1];                                                                                                      \
      xg_scatter<1>(acc[j], r1, b3);                                                                                    \
      pw[64 * (j)] = r1[0] * ew;                                                                                        \
    } else {                                                                                                            \
      asm volatile("" : "+v"(acc[j][0]), "+v"(acc[j][1]), "+v"(acc[j][2]), "+v"(acc[j][3]), "+v"(acc[j][4]),            \
                   "+v"(acc[j][5]), "+v"(acc[j][6]), "+v"(acc[j][7]));                       \
    }                                                                                                                   \
    SCHED_FENCE();                                                                                                      \
  } while (0)
#define PVT(t, c)                                                                                                       \
  do {                                                                                                                  \
    unsigned plo = pb[t], phi = plo << 16;                                                                              \
    PV3(t, 0, c, c);                                                                                                    \
    PV3(t, 1, -1, -1);                                                                                                  \
    PV3(t, 2, -1, -1);                                                                                                  \
  } while (0)
#define WGMAX()                                                                                                         \
  do {                                                                                                                  \
    for (unsigned spin = 0; spin < (SPIN_MAX << 4); ++spin) {                                                           \
      if (__hip_atomic_load(flg + 3, __ATOMIC_RELAXED, __HIP_MEMORY_SCOPE_WORKGROUP) == 8u) break;                      \
      __builtin_amdgcn_s_sleep(1);                                                                                      \
    }                                                                                                                   \
    MEMBAR();                                                                                                           \
    const f32x4 ma = *reinterpret_cast<const f32x4*>(lds + L_ML), mb2 = *reinterpret_cast<const f32x4*>(lds + L_ML + 4); \
    Mg = fmaxf(fmaxf(fmaxf(ma[0], ma[1]), fmaxf(ma[2], ma[3])), fmaxf(fmaxf(mb2[0], mb2[1]), fmaxf(mb2[2], mb2[3])));   \
    ew = ex2((mw - Mg) * LOG2E);                                                                                        \
    if (lane == 0) *reinterpret_cast<f32x2*>(lds + L_PART + w * PS + DH) = f32x2{w == 0 ? Mg : 0.f, lw * ew};           \
    SCHED_FENCE();                                                                                                      \
  } while (0)
    float ew, mw, lw;
    u32x4 qf[3];
    float sc[NF + 2];
    unsigned pb[NF + 2];
#define GATE(cnt)                                                                                                       \
  do {                                                                                                                  \
    asm volatile("s_waitcnt vmcnt(%2)" : "+v"(zold[0]), "+v"(zold[1]) : "n"(cnt) : "memory");                           \
    _Pragma("unroll") for (int k = 0; k < 2; ++k)                                                                       \
      if ((k == 0 || lane < 16) && (zold[k] >> 59) == 31u) {                                                            \
        const int cz = 80 * sp + 64 * k + lane;                                                                         \
        float f = fx_dec(zold[k] + zadd[k]);                                                                            \
        if (__builtin_expect(!fx_exact(zold[k], zadd[k]), 0)) SLOWSUM(f, G3Z, cz);                                      \
        __builtin_amdgcn_raw_buffer_store_b64(u32x2{fbits(bfr(silu(bfr(f)))), tag}, rws, cz * 8, GG * 16, 16);          \
      }                                                                                                                 \
    STAMPT(12);                                                                                                          \
  } while (0)
    auto slot = [&](auto mc) __attribute__((always_inline)) {
      constexpr int m = decltype(mc)::value;
      VWL(m / 3, m % 3);
      if (m == QS) {
        for (unsigned spin = 0; spin < (SPIN_MAX << 4); ++spin) {
          if (__hip_atomic_load(flg + 1, __ATOMIC_RELAXED, __HIP_MEMORY_SCOPE_WORKGROUP) != 0u) break;
          __builtin_amdgcn_s_sleep(1);
        }
        asm volatile("s_waitcnt lgkmcnt(0)" ::: "memory");
        SCHED_FENCE();
        STAMP(6);
#pragma unroll
        for (int j = 0; j < 3; ++j) qf[j] = *reinterpret_cast<const u32x4*>(lds + L_QS + (c + 8 * j) * 4);
      }
#pragma unroll
      for (int t = 0; t < NF + 2; ++t)
        if (SS[t] == m) {
          sc[t] = -INFINITY;
          if (t < NF || t < nt) {
            float d = 0.f;
#pragma unroll
            for (int j = 0; j < 3; ++j) {
              TIE(K[t][j]);
#pragma unroll
              for (int i = 0; i < 4; ++i) d = dot2(K[t][j][i], qf[j][i], d);
            }
            d = grp8_sum(d);
            sc[t] = bfr(bfr(d) * SCALE);
          }
          TIE(sc[t]);
        }
      if (m == XS) {
        mw = sc[0];
#pragma unroll
        for (int t = 1; t < NF + 2; ++t) mw = fmaxf(mw, sc[t]);
        mw = xg_max(mw);
        if (lane == 0) {
          lds[L_ML + w] = mw;
          __hip_atomic_fetch_add(flg + 3, 1u, __ATOMIC_RELAXED, __HIP_MEMORY_SCOPE_WORKGROUP);
        }
        lw = 0.f;
#pragma unroll
        for (int t = 0; t < NF + 2; ++t) {
          const float p = ex2((sc[t] - mw) * LOG2E);
          lw += p;
          pb[t] = bfbits(p);
          TIE(pb[t]);
        }
        lw = xg_sum(lw);
        TIE(lw);
        STAMP(10);
      }
      if (m == XS + 1 && NF < nt) PVT(NF, -1);
      if (m == XS + 2 && NF + 1 < nt) PVT(NF + 1, -1);
      if (m >= XS + 3) {
        constexpr int n0 = PVN(m - 1), n1 = PVN(m);
#pragma unroll
        for (int n = n0; n < n1; ++n) {
          unsigned plo = pb[n / 3], phi = plo << 16;
          PV3(n / 3, n % 3, n == m - 8 ? 8 : -1, n == m - 8 ? 8 : -1);
        }
      }
      if (m == ZSL) {
        WGMAX();
        if (w == ZW) {
#pragma unroll
          for (int k = 0; k < 2; ++k) {
            const float* zp = lds + L_ZP + (k ? 64 + (lane & 15) : lane);
            zold[k] = zadd[k] = fx_enc(((zp[0] + zp[80]) + (zp[160] + zp[240])) + (zp[320] + zp[400]));
          }
          const int za0 = (2 * GA + D + 80 * sp + lane) * 8, za1 = lane < 16 ? za0 + 512 : GEND * 16;
          if (!ZS) ZPUB();
          asm volatile("buffer_atomic_add_x2 %0, %2, %4, 0 offen sc0\n\tbuffer_atomic_add_x2 %1, %3, %4, 0 offen sc0"
                       : "+v"(zold[0]), "+v"(zold[1]) : "v"(za0), "v"(za1), "s"(rwa) : "memory");
        }
      }
      if (m == GSL && w == ZW) GATE(GSL - ZSL);
      SCHED_FENCE();
    };
#define SL3(t) slot(IC<3 * (t)>{}); slot(IC<3 * (t) + 1>{}); slot(IC<3 * (t) + 2>{})
    SL3(0); SL3(1); SL3(2); SL3(3); SL3(4); SL3(5); SL3(6);
    STAMPT(14);
#pragma unroll
    for (int n = PVN(3 * NF - 1); n < 3 * NF; ++n) {
      const int m = n - (3 * NF - 9);
      if (m >= 0) {
#pragma unroll
        for (int i = (m && n > PVN(3 * NF - 1)) ? WON[m - 1] : 0; i < WON[m]; ++i) WOLC(i);
      }
      unsigned plo = pb[n / 3], phi = plo << 16;
      PV3(n / 3, n % 3, m < 0 ? -1 : 8 - m, m < 0 ? -1 : 8 - m + WON[m < 0 ? 0 : m]);
    }
#pragma unroll
    for (int i = WON[8]; i < WON[9]; ++i) WOLC(i);
    if (GSL >= 3 * NF && w == ZW) {
      STAMPT(15);
      if (ZS) ZPUB();
      GATE(ZS ? 1 : 0);
    }
  }
  if (!(GSL >= 3 * NF && w == ZW)) STAMPT(15);
  __syncthreads();
  STAMP(7);
  if (__builtin_expect(w < 2, 1)) {
    {
      f32x2 p2[8];
#pragma unroll
      for (int ww = 0; ww < 8; ++ww) p2[ww] = *reinterpret_cast<const f32x2*>(reinterpret_cast<const char*>(lds + L_PART + ww * PS) + mro);
      const f32x2 ua = (p2[0] + p2[1]) + (p2[2] + p2[3]), ub = (p2[4] + p2[5]) + (p2[6] + p2[7]);
      float ub1 = ub[1];
      asm("" : "+v"(ub1));
      pub[0] = fbits(ua[0] + ub[0]);
      pub[2] = fbits(ua[1] + ub1);
      __builtin_amdgcn_raw_buffer_store_b128(pub, rws, mso, 0, 16);
    }
    SCHED_FENCE();
    const int t2 = tid < 96 ? tid : 95;
    const int pb = (G2 + h * NSP * 104 + t2) * 16, pm = (G2 + (h * NSP + c) * 104 + 96) * 16;
    const unsigned po = (unsigned)(size_t)(__attribute__((address_space(3))) float*)(lds + L_O) + 4 * t2;
    const int pg = lane < 20 ? (GG + 40 * sp + 20 * w + lane) * 16 : GEND * 16;
    const unsigned pl = (unsigned)(size_t)(__attribute__((address_space(3))) float*)(lds + L_GT) + 16 * (lane < 20 ? 20 * w + lane : 40 + tid);
    if (PD1) __builtin_amdgcn_s_sleep(PD1);
    {
      unsigned e0, e1, e2, e3, e4, e5, e6, e7, cnt;
#define DPC " row_mask:0xf bank_mask:0xf\n\t"
#define CHK(d, a, b) "v_bitop3_b32 " d ", " a ", " b ", %13 bitop3:0x7e\n\t"
#define ACC2(e, a, b) "v_fmac_f32 v44, " e ", " a "\n\tv_fmac_f32 v45, " e ", " b "\n\t"
      asm volatile(
          "s_mov_b32 %8, %16\n"
          "1:\n\t"
          "buffer_load_dwordx4 v[48:51], %18, %12, 0 offen sc1\n\t"
          "buffer_load_dwordx4 v[4:7], %9, %12, 0 offen sc1\n\t"
          "buffer_load_dwordx4 v[8:11], %10, %12, 0 offen sc1\n\t"
          "buffer_load_dwordx4 v[12:15], %10, %12, 0 offen offset:1664 sc1\n\t"
          "buffer_load_dwordx4 v[16:19], %10, %12, 0 offen offset:3328 sc1\n\t"
          "buffer_load_dwordx4 v[20:23], %10, %12, %14 offen sc1\n\t"
          "buffer_load_dwordx4 v[24:27], %10, %12, %14 offen offset:1664 sc1\n\t"
          "buffer_load_dwordx4 v[28:31], %10, %12, %14 offen offset:3328 sc1\n\t"
          "buffer_load_dwordx4 v[32:35], %10, %12, %15 offen sc1\n\t"
          "buffer_load_dwordx4 v[36:39], %10, %12, %15 offen offset:1664 sc1\n\t"
          "s_sub_u32 %8, %8, 1\n\t"
          "s_waitcnt vmcnt(8)\n\t"
          CHK("v41", "v5", "v7")
          "ds_write_b128 %19, v[48:51]\n\t"
          "v_max_f32_dpp v40, v4, v4 quad_perm:[1,0,3,2]" DPC
          "s_waitcnt vmcnt(7)\n\t"
          CHK("v46", "v9", "v11")
          "v_max_f32_dpp v40, v40, v40 quad_perm:[2,3,0,1]" DPC
          "s_waitcnt vmcnt(6)\n\t"
          CHK("v47", "v13", "v15")
          "v_max_f32_dpp v40, v40, v40 row_half_mirror" DPC
          "v_sub_f32 v42, v4, v40\n\t"
          "v_mul_f32 v42, 0x3fb8aa3b, v42\n\t"
          "v_exp_f32 v42, v42\n\t"
          "v_or3_b32 v41, v41, v46, v47\n\t"
          "v_mul_f32 v40, v6, v42\n\t"
          "s_waitcnt vmcnt(5)\n\t"
          CHK("v46", "v17", "v19")
          "v_add_f32_dpp v40, v40, v40 quad_perm:[1,0,3,2]" DPC
          "s_waitcnt vmcnt(4)\n\t"
          CHK("v47", "v21", "v23")
          "v_add_f32_dpp v40, v40, v40 quad_perm:[2,3,0,1]" DPC
          "v_or3_b32 v41, v41, v46, v47\n\t"
          "s_nop 0\n\t"
          "v_add_f32_dpp v40, v40, v40 row_half_mirror" DPC
          "v_rcp_f32 v40, v40\n\t"
          "s_nop 0\n\t"
          "v_mul_f32 v42, v42, v40\n\t"
          "s_nop 0\n\t"
          "v_readlane_b32 %0, v42, 0\n\tv_readlane_b32 %1, v42, 1\n\tv_readlane_b32 %2, v42, 2\n\tv_readlane_b32 %3, v42, 3\n\t"
          "v_readlane_b32 %4, v42, 4\n\tv_readlane_b32 %5, v42, 5\n\tv_readlane_b32 %6, v42, 6\n\tv_readlane_b32 %7, v42, 7\n\t"
          "v_mul_f32 v44, %0, v8\n\tv_mul_f32 v45, %0, v10\n\t"
          ACC2("%1", "v12", "v14") ACC2("%2", "v16", "v18") ACC2("%3", "v20", "v22")
          "s_waitcnt vmcnt(3)\n\t"
          ACC2("%4", "v24", "v26") CHK("v46", "v25", "v27")
          "s_waitcnt vmcnt(2)\n\t"
          ACC2("%5", "v28", "v30") CHK("v47", "v29", "v31")
          "v_or3_b32 v41, v41, v46, v47\n\t"
          "s_waitcnt vmcnt(1)\n\t"
          ACC2("%6", "v32", "v34") CHK("v46", "v33", "v35")
          "s_waitcnt vmcnt(0)\n\t"
          ACC2("%7", "v36", "v38")
          "v_cvt_pk_bf16_f32 v44, v44, v45\n\t"
          "ds_write_b32 %11, v44\n\t"
          CHK("v47", "v37", "v39")
          "v_or3_b32 v41, v41, v46, v47\n\t"
          "v_cmp_ne_u32 vcc, 0, v41\n\t"
          "s_cbranch_vccz 2f\n\t"
          "s_sleep %17\n\t"
          "s_cbranch_scc0 1b\n"
          "2:\n\t"
          "s_waitcnt lgkmcnt(0)"
          : "=&s"(e0), "=&s"(e1), "=&s"(e2), "=&s"(e3), "=&s"(e4), "=&s"(e5), "=&s"(e6), "=&s"(e7), "=&s"(cnt)
          : "v"(pm), "v"(pb), "v"(po), "s"(rwa), "s"(tag), "s"(3 * 1664), "s"(6 * 1664), "n"(SPIN_MAX), "n"(PS1), "v"(pg), "v"(pl)
          : "memory", "scc", "vcc", "v4", "v5", "v6", "v7", "v8", "v9", "v10", "v11", "v12", "v13", "v14", "v15", "v16", "v17", "v18", "v19",
            "v20", "v21", "v22", "v23", "v24", "v25", "v26", "v27", "v28", "v29", "v30", "v31", "v32", "v33", "v34", "v35", "v36", "v37",
            "v38", "v39", "v40", "v41", "v42", "v44", "v45", "v46", "v47", "v48", "v49", "v50", "v51");
    }
    STAMPT(13);
    __syncthreads();
  } else if (w == 2) {
    const unsigned pt7 = (unsigned)__builtin_amdgcn_s_memrealtime();
    if (lane < 2) __builtin_amdgcn_raw_buffer_store_b128(u32x4{lane ? pt21 : pt0, tag, lane ? (unsigned)tstart64 : pt7, tag}, rws, (GPR + 2 * b + lane) * 16, 0, 16);
    __syncthreads();
  } else {
    if (w >= 4) {
#pragma unroll
      for (int i = WON[9]; i < 8; ++i) WOLC(i);
#pragma unroll
      for (int i = 0; i < 8; ++i) {
        AWAIT(WO[i], 7 - i);
        *reinterpret_cast<u32x4*>(lds + L_WO + u * 4 + i * 960) = WO[i];
      }
      asm volatile("s_waitcnt lgkmcnt(0)" ::: "memory");
      if (lane == 0) __hip_atomic_fetch_add(flg + 2, 1u, __ATOMIC_RELAXED, __HIP_MEMORY_SCOPE_WORKGROUP);
      STAMPT(12);
    }
    for (unsigned spin = 0; spin < (SPIN_MAX << 4); ++spin) {
      if (__hip_atomic_load(flg + 2, __ATOMIC_RELAXED, __HIP_MEMORY_SCOPE_WORKGROUP) == 4u) break;
      __builtin_amdgcn_s_sleep(1);
    }
    asm volatile("s_waitcnt lgkmcnt(0)" ::: "memory");
    u32x4 wb[6];
    {
      typedef __attribute__((address_space(3))) v4i16_t* ltr;
      const unsigned la = (unsigned)(size_t)(__attribute__((address_space(3))) float*)(lds + L_WO) + (8 * (lane >> 4) + ((lane >> 2) & 3)) * 160 + 32 * (w - 3) + 8 * (lane & 3);
#pragma unroll
      for (int k = 0; k < 6; ++k) {
        const u32x2 t0 = __builtin_bit_cast(u32x2, __builtin_amdgcn_ds_read_tr16_b64_v4i16((ltr)(la + 32 * k * 160)));
        const u32x2 t1 = __builtin_bit_cast(u32x2, __builtin_amdgcn_ds_read_tr16_b64_v4i16((ltr)(la + (32 * k + 4) * 160)));
        wb[k] = u32x4{t0[0], t0[1], t1[0], t1[1]};
      }
    }
    const int cy = 80 * sp + 16 * (w - 3) + (lane & 15);
    float xv = bf_lo(reinterpret_cast<const unsigned short*>(lds + L_XS)[cy]);
    gu64* pa = (gu64*)(gw + 2 * GA + cy);
    gu16* pout = (gu16*)(out + cy);
    typedef __attribute__((address_space(3))) const u32x4* la4;
    la4 pa4 = (la4)(lds + L_O) + 4 * (lane & 3) + (lane >> 4);
    int cy8 = cy * 8;
    typedef __attribute__((address_space(3))) const u32x2* la2;
    la2 pgt = (la2)(lds + L_GT) + 16 * (w - 3) + (lane & 15);
    asm volatile("" : "+v"(pgt));
    unsigned k0 = (1u << 27) - (0x411u << 20), k1 = k0 + (1u << 22), k2 = 1u << 17;
    double c0 = 262144.0 + 512.0, c1 = -(262144.0 + 32 * 512.0);
    float f0 = 512.f, f1 = 0x1p-9f;
    asm volatile("" : "+v"(wb[0]), "+v"(wb[1]), "+v"(wb[2]), "+v"(wb[3]), "+v"(wb[4]), "+v"(wb[5]));
    asm volatile("" : "+v"(xv), "+v"(pa), "+v"(pout), "+v"(pa4), "+v"(cy8), "+v"(k0), "+v"(k1), "+v"(k2), "+s"(c0), "+s"(c1), "+s"(f0), "+s"(f1));
    __syncthreads();
    STAMP(1);
    const u32x4 a1 = pa4[0], a2 = pa4[16];
    SCHED_FENCE();
    u32x2 gg = *pgt;
    SCHED_FENCE();
#define MFMA(a, b, cc) __builtin_amdgcn_mfma_f32_16x16x32_bf16(__builtin_bit_cast(bf16x8_t, a), __builtin_bit_cast(bf16x8_t, b), cc, 0, 0, 0)
    const f32x4 z4 = {0.f, 0.f, 0.f, 0.f};
    const f32x4 d2 = MFMA(a1, wb[2], z4), d3 = MFMA(a1, wb[3], z4);
    SCHED_FENCE();
    f32x4 d0 = MFMA(a1, wb[0], z4), d1 = MFMA(a1, wb[1], z4);
    SCHED_FENCE();
    d0 = MFMA(a2, wb[4], d0);
    SCHED_FENCE();
    d1 = MFMA(a2, wb[5], d1);
    SCHED_FENCE();
    if (lane < 16) {
      const float q = ((d2[2] + d3[3]) + d0[0]) + d1[1];
      const bool bad = !(fabsf(q) < f0);
      const u64 db = __builtin_bit_cast(u64, (double)(bad ? 0.f : q) + c0);
      const unsigned fl = (bad ? k1 : k0) + (fabsf(q) < f1 ? k2 : 0u);
      const u64 add = ((u64)((unsigned)(db >> 32) + fl) << 32) | (unsigned)db;
      const u64 old = __hip_atomic_fetch_add(pa, add, __ATOMIC_RELAXED, __HIP_MEMORY_SCOPE_AGENT);
      __builtin_amdgcn_raw_buffer_store_b64(u32x2{fbits(q), tag}, rws, cy8, (G3Y + h * 320) * 16, 16);
      SCHED_FENCE();
      unsigned lim = (gg[1] == tag && !bad) ? (32u << 17) - (fl - k0) : 0u;
      asm volatile("" : "+v"(lim));
      SCHED_FENCE();
      float sv = (float)(__builtin_bit_cast(double, (old & ((1ull << 49) - 1ull)) + db) + c1);
      asm("" : "+v"(sv));
      unsigned res = bfbits(xv + bfr(bfr(sv) * bitsf(gg[0])));
      const unsigned rel = (unsigned)(old >> 32) - (31u << 27);
      TIE(res);
      if (rel < lim) *pout = res;
      SCHED_FENCE();
      if (CP) asm volatile(".Lfd_end:");
      if ((old >> 59) == 31u) {
        if (__builtin_expect(rel >= lim, 0)) {
          if (!fx_exact(old, add)) SLOWSUM(sv, G3Y, cy);
          for (unsigned spin = 0; gg[1] != tag && spin < SPIN_MAX; ++spin) {
            MEMBAR();
            gg = __builtin_amdgcn_raw_buffer_load_b64(rws, cy8, GG * 16, 16);
          }
          *pout = bfbits(xv + bfr(bfr(sv) * bitsf(gg[0])));
        }
        if (cy == 0)
          __hip_atomic_store(reinterpret_cast<unsigned*>(gw + 2 * GEP), tag, __ATOMIC_RELAXED, __HIP_MEMORY_SCOPE_AGENT);
        if (cy == 0) pfin = true;
      }
    }
    if (__any(pfin)) {
      const unsigned t0 = (unsigned)__builtin_amdgcn_s_memrealtime();
      int mn0 = 1 << 30, mx0 = -(1 << 30), sm0 = 0, mx7 = -(1 << 30), sm7 = 0, smk = 0, sma = 0;
#pragma unroll 1
      for (int i = 0; i < 4; ++i) {
        u32x4 va, vb;
        for (unsigned spin = 0; spin < SPIN_MAX; ++spin) {
          MEMBAR();
          va = gld2(GPR + 2 * (lane * 4 + i), 0);
          vb = gld2(GPR + 2 * (lane * 4 + i) + 1, 0);
          if (gok2(va) && gok2(vb)) break;
        }
        const int d0 = (int)(va[0] - t0), d7 = (int)(va[2] - t0);
        mn0 = min(mn0, d0); mx0 = max(mx0, d0); sm0 += d0; mx7 = max(mx7, d7); sm7 += d7; smk += (int)(vb[0] - vb[2]); sma += (int)(vb[2] - va[0]);
      }
      for (int o = 32; o; o >>= 1) {
        mn0 = min(mn0, __shfl_xor(mn0, o)); mx0 = max(mx0, __shfl_xor(mx0, o)); sm0 += __shfl_xor(sm0, o); mx7 = max(mx7, __shfl_xor(mx7, o));
        sm7 += __shfl_xor(sm7, o); smk += __shfl_xor(smk, o); sma += __shfl_xor(sma, o);
      }
      const int val = PROBE == 0 ? 0 : PROBE == 1 ? mx0 - mn0 : PROBE == 2 ? sm0 / 256 - mn0 : PROBE == 3 ? -mn0 - 1700 : PROBE == 4 ? smk / 256 - 600
                    : PROBE == 5 ? mx7 - mn0 - 1500 : PROBE == 6 ? (sm7 - sm0) / 256 - 1500 : PROBE == 7 ? sma / 256 : -mx7;
      const unsigned until = 1000u + 10u * (unsigned)(val < 0 ? 0 : val > 800 ? 800 : val);
      for (unsigned spin = 0; spin < (1u << 20); ++spin)
        if ((unsigned)__builtin_amdgcn_s_memrealtime() - t0 >= until) break;
    }
  }
  STAMP(11);
}

}

extern "C" void kernel_launch(void* const* d_in, const int* in_sizes, int n_in, void* d_out, int out_size, void* d_ws,
                              size_t ws_size, hipStream_t stream) {
  const bf16* x = (const bf16*)d_in[0];
  const bf16* kc = (const bf16*)d_in[1];
  const bf16* vc = (const bf16*)d_in[2];
  const bf16* wq = (const bf16*)d_in[3];
  const bf16* wo = (const bf16*)d_in[4];
  const bf16* w1 = (const bf16*)d_in[6];
  const bf16* w3 = (const bf16*)d_in[7];
  const bf16* w2 = (const bf16*)d_in[8];
  u64* gw = (u64*)d_ws;
  fused_decode<<<NWG, NT, 0, stream>>>(x, kc, vc, wq, wo, w1, w3, w2, gw, (bf16*)d_out);
}
```

```cpp
#include <hip/hip_runtime.h>
#include <hip/hip_bf16.h>
#include <stdint.h>

typedef __hip_bfloat16 bf16;
typedef unsigned long long u64;
typedef __attribute__((address_space(1))) unsigned long long gu64;
typedef __attribute__((address_space(1))) unsigned short gu16;
typedef unsigned u32x4 __attribute__((ext_vector_type(4)));
typedef unsigned u32x2 __attribute__((ext_vector_type(2)));
typedef float f32x4 __attribute__((ext_vector_type(4)));
typedef __bf16 bf16x2_t __attribute__((ext_vector_type(2)));
typedef __bf16 bf16x8_t __attribute__((ext_vector_type(8)));
typedef short v4i16_t __attribute__((ext_vector_type(4)));

namespace {

constexpr int D = 640, HQ = 32, DH = 192, SK = 32768, WIN = 4096, FF = 1536;
constexpr int NSP = 8;
constexpr int NWG = HQ * NSP;
constexpr int NT = 512;
constexpr float SCALE = 0.07216878364870322f;
constexpr float LOG2E = 1.4426950408889634f;
constexpr unsigned SPIN_MAX = 1u << 16;
constexpr int WND = 8;
constexpr int WOL = 7;
constexpr int NF = 7;
#define DXT 3,0,3,0,0,-4,0,-2
constexpr int DX[8] = {DXT};
constexpr unsigned dx_pack() {
  unsigned r = 0;
  for (int i = 0; i < 8; ++i) r |= (unsigned)(DX[i] + 4) << (4 * i);
  return r;
}
constexpr unsigned dx_pos() {
  unsigned r = 0, a = 0;
  for (int i = 0; i < 8; ++i) { r |= a << (4 * i); a += DX[i] > 0 ? DX[i] : 0; }
  return r;
}
constexpr unsigned long long dx_don() {
  unsigned long long r = 0;
  int k = 0;
  for (int i = 0; i < 8; ++i)
    for (int q = 0; q < -DX[i]; ++q) r |= (unsigned long long)(i * 4 + q) << (5 * k++);
  return r;
}
constexpr bool dx_ok() {
  int a = 0, p = 0;
  bool in = true;
  for (int i = 0; i < 8; ++i) { a += DX[i]; p += DX[i] > 0 ? DX[i] : 0; in = in && DX[i] >= -4 && DX[i] <= 4; }
  return a == 0 && p <= 8 && in;
}
constexpr int dx_check[dx_ok() ? 1 : -1] = {0};
constexpr int KE = 8;
#define PD1X 24
#define PS1 1
constexpr int PD1 = PD1X;
#define WOP 2, 2, 2, 2, 2, 4, 6, 8, 8, 8
constexpr int WON[10] = {WOP};
#define QS 1
#define SSL 1, 2, 3, 4, 5, 6, 8, 7, 7
#define XS 9
#define ZSL 12
#define GSL 20
constexpr int SS[9] = {SSL};
constexpr bool ss_ok() {
  bool ok = true;
  for (int t = 0; t < 9; ++t) ok = ok && SS[t] >= QS && SS[t] <= XS && SS[t] >= (t < 7 ? 3 * t - 10 : 5);
  return ok && ZSL > XS && GSL > ZSL && GSL < 21;
}
constexpr int ss_check[ss_ok() ? 1 : -1] = {0};
constexpr int PVN(int m) {
  return m < XS + 3 ? 0 : (3 * (m - XS - 2) < m - 7 ? 3 * (m - XS - 2) : m - 7);
}
#define XOT 20,20,20,20,-30,-30,-30,-30
constexpr int XO[8] = {XOT};
constexpr unsigned long long xo_pack() {
  unsigned long long r = 0;
  for (int i = 0; i < 8; ++i) r |= (unsigned long long)(XO[i] + 64) << (8 * i);
  return r;
}

constexpr int G1 = 0;
constexpr int G1F = G1 + HQ * 288;
constexpr int G2 = G1F + 8 * 960;
constexpr int G3Y = G2 + NWG * 104;
constexpr int G3Z = G3Y + 320 * 32;
constexpr int GG = G3Z + 320 * 32;
constexpr int GZF = GG + 320;
constexpr int GA = GZF + 256;
constexpr int GEP = GA + 640;
constexpr int GEND = GEP + 8;

__device__ __forceinline__ unsigned fbits(float x) { return __builtin_bit_cast(unsigned, x); }
__device__ __forceinline__ float bitsf(unsigned x) { return __builtin_bit_cast(float, x); }
#define gok2(v) ((v)[1] == tag && (v)[3] == tag)
#define gst2(unit, a, b) __builtin_amdgcn_raw_buffer_store_b128(u32x4{fbits(a), tag, fbits(b), tag}, rws, (unit) * 16, 0, 16)
#define gld2(unit, cunit) __builtin_amdgcn_raw_buffer_load_b128(rws, (unit) * 16, (cunit) * 16, 16)
#define MEMBAR() asm volatile("" ::: "memory")
typedef float f32x2 __attribute__((ext_vector_type(2)));

__device__ __forceinline__ float bf_lo(unsigned u) { return __builtin_bit_cast(float, u << 16); }
__device__ __forceinline__ float bf_hi(unsigned u) { return __builtin_bit_cast(float, u & 0xffff0000u); }
__device__ __forceinline__ float bfr(float x) { return __bfloat162float(__float2bfloat16(x)); }
__device__ __forceinline__ unsigned short bfbits(float x) {
  return __builtin_bit_cast(unsigned short, __float2bfloat16(x));
}
__device__ __forceinline__ float dot2(unsigned a, unsigned b, float c) {
  return __builtin_amdgcn_fdot2_f32_bf16(__builtin_bit_cast(bf16x2_t, a), __builtin_bit_cast(bf16x2_t, b), c, false);
}
template <int CTRL>
__device__ __forceinline__ float dpp(float x) {
  return __builtin_bit_cast(float, __builtin_amdgcn_mov_dpp(__builtin_bit_cast(int, x), CTRL, 0xf, 0xf, true));
}
__device__ __forceinline__ float grp8_sum(float x) {
  x += dpp<0xB1>(x);
  x += dpp<0x4E>(x);
  x += dpp<0x141>(x);
  return x;
}
__device__ __forceinline__ float swap16_add(float a, float b) {
  auto s = __builtin_amdgcn_permlane16_swap(fbits(a), fbits(b), false, false);
  return bitsf((unsigned)s[0]) + bitsf((unsigned)s[1]);
}
__device__ __forceinline__ float swap32_add(float a, float b) {
  auto t = __builtin_amdgcn_permlane32_swap(fbits(a), fbits(b), false, false);
  return bitsf((unsigned)t[0]) + bitsf((unsigned)t[1]);
}
__device__ __forceinline__ float xg_sum(float x) {
  x += dpp<0x128>(x);
  x = swap16_add(x, x);
  return swap32_add(x, x);
}
__device__ __forceinline__ float xg_max(float x) {
  x = fmaxf(x, dpp<0x128>(x));
  auto s = __builtin_amdgcn_permlane16_swap(fbits(x), fbits(x), false, false);
  x = fmaxf(bitsf((unsigned)s[0]), bitsf((unsigned)s[1]));
  auto t = __builtin_amdgcn_permlane32_swap(fbits(x), fbits(x), false, false);
  return fmaxf(bitsf((unsigned)t[0]), bitsf((unsigned)t[1]));
}
template <int NJ>
__device__ __forceinline__ void xg_scatter(const float (&a)[8 * NJ], float (&r)[NJ], const bool b3) {
#pragma unroll
  for (int j = 0; j < NJ; ++j) {
    float p[4];
#pragma unroll
    for (int e = 0; e < 4; ++e) p[e] = swap32_add(a[8 * j + e], a[8 * j + e + 4]);
    const float q0 = swap16_add(p[0], p[2]), q1 = swap16_add(p[1], p[3]);
    r[j] = (b3 ? q1 : q0) + dpp<0x128>(b3 ? q0 : q1);
  }
}
__device__ __forceinline__ float wave_sum(float x) { return xg_sum(grp8_sum(x)); }
__device__ __forceinline__ float ex2(float x) { return __builtin_amdgcn_exp2f(x); }
__device__ __forceinline__ float silu(float x) { return x / (1.0f + __expf(-x)); }
__device__ __forceinline__ float rdlane(float x, int l) {
  return __builtin_bit_cast(float, __builtin_amdgcn_readlane(__builtin_bit_cast(int, x), l));
}

__device__ __forceinline__ u64 fx_enc(float v) {
  const bool bad = !(fabsf(v) < 512.f), small = fabsf(v) < 0x1p-9f;
  const u64 d = __builtin_bit_cast(u64, (double)(bad ? 0.f : v) + (262144.0 + 512.0));
  const unsigned hi = (unsigned)(d >> 32) + ((1u << 27) - (0x411u << 20)) + (bad ? 1u << 22 : 0u) + (small ? 1u << 17 : 0u);
  return ((u64)hi << 32) | (unsigned)d;
}
__device__ __forceinline__ float fx_dec(u64 t) {
  const unsigned hi = ((unsigned)(t >> 32) & 0x1ffffu) | (0x411u << 20);
  return (float)(__builtin_bit_cast(double, ((u64)hi << 32) | (unsigned)t) - (262144.0 + 32 * 512.0));
}
__device__ __forceinline__ bool fx_exact(u64 old, u64 add) {
  const unsigned o = (unsigned)(old >> 49) & 0x3ffu, a = (unsigned)(add >> 49) & 0x3ffu;
  return o + a < 32u;
}
template <int N> struct IC { static constexpr int value = N; };
#define SCHED_FENCE() __builtin_amdgcn_sched_barrier(0)
#define ALD(dst, rs, vo, so, imm) \
  asm volatile("buffer_load_dwordx4 %0, %1, %2, %3 offen offset:%4 nt" : "=v"(dst) : "v"(vo), "s"(rs), "s"(so), "n"(imm) : "memory")
#define AWL(dst, rs, vo, so, imm)                                                                                      \
  asm volatile("s_waitcnt vmcnt(8)\n\ts_cmp_le_i32 %5, 8\n\ts_cbranch_scc0 4\n\ts_waitcnt vmcnt(7)\n\t"                 \
               "s_cmp_le_i32 %5, 7\n\ts_cbranch_scc0 1\n\ts_waitcnt vmcnt(6)\n\t"                                        \
               "buffer_load_dwordx4 %0, %1, %2, %3 offen offset:%4 nt"                                                 \
               : "=v"(dst) : "v"(vo), "s"(rs), "s"(so), "n"(imm), "s"(wl) : "memory", "scc")
constexpr int TOFF = 1 << 14;
#define AWT(dst, rs, vo, so, imm, tref, lim)                                                                            \
  asm volatile("s_memrealtime s[98:99]\n\ts_waitcnt vmcnt(8)\n"                                                          \
               "8:\n\ts_waitcnt lgkmcnt(0)\n\ts_mul_i32 s98, s98, %7\n\ts_sub_u32 s98, s98, %5\n\ts_cmp_ge_u32 s98, %6\n\t" \
               "s_cbranch_scc1 9f\n\ts_sleep 1\n\ts_memrealtime s[98:99]\n\ts_branch 8b\n"                               \
               "9:\n\tbuffer_load_dwordx4 %0, %1, %2, %3 offen offset:%4 nt"                                            \
               : "=v"(dst) : "v"(vo), "s"(rs), "s"(so), "n"(imm), "s"(tref), "n"((lim) + TOFF), "s"(nt)                  \
               : "memory", "scc", "s98", "s99")
#define TA 262
#define TB 28
#define TE (TA + 47 * TB)
constexpr int te_check[(TE + 64) * 9 < TOFF ? 1 : -1] = {0};
#define AWAIT(v, n) asm volatile("s_waitcnt vmcnt(%1)" : "+v"(v) : "n"(n) : "memory")
#define TIE(v) asm volatile("" : "+v"(v))
__device__ __forceinline__ u32x4 mkrs(const void* p, unsigned bytes) {
  const u64 a = (u64)p;
  return u32x4{(unsigned)a, (unsigned)(a >> 32) & 0xffffu, bytes, 0x00020000u};
}
#define WAVE_SYNC()                                        \
  do {                                                     \
    __builtin_amdgcn_fence(__ATOMIC_RELEASE, "wavefront"); \
    __builtin_amdgcn_wave_barrier();                       \
    __builtin_amdgcn_fence(__ATOMIC_ACQUIRE, "wavefront"); \
  } while (0)

constexpr int L_WO = 0;
constexpr int L_XS = L_WO + DH * 40;
constexpr int PS = 256;
constexpr int L_PART = L_XS + 4 * 320;
constexpr int L_XN = L_PART + 8 * PS;
constexpr int L_ZP = L_XN + 4 * 64;
constexpr int L_QS = L_ZP + 6 * 80;
constexpr int L_ML = L_QS + 96;
constexpr int L_O = L_ML + 16;
constexpr int L_H = L_O + 128;
constexpr int L_GU = L_H + 48;
constexpr int L_GT = L_GU + 96;
constexpr int L_FLG = L_GT + 168 * 4;
constexpr int L_END = L_FLG + 4;

__global__ void __launch_bounds__(NT, 2)
fused_decode(const bf16* __restrict__ x, const bf16* __restrict__ kc, const bf16* __restrict__ vc,
             const bf16* __restrict__ wq, const bf16* __restrict__ wo, const bf16* __restrict__ w1,
             const bf16* __restrict__ w3, const bf16* __restrict__ w2, u64* __restrict__ gw, bf16* __restrict__ out) {
  __shared__ __attribute__((aligned(16))) float lds[L_END];
  const int b = blockIdx.x;
  const int xcd = b & 7, jj = b >> 3;
  const int h = xcd * 4 + (jj >> 3), sp = jj & 7;
  const int ha = jj, spa = xcd;
  const int w = __builtin_amdgcn_readfirstlane(threadIdx.x >> 6);
  const int tid = threadIdx.x, lane = tid & 63;
  const int g = lane >> 3, c = lane & 7;
  const int rec = ha * NSP + spa;
  unsigned* flg = reinterpret_cast<unsigned*>(lds + L_FLG);

  unsigned cpv = (jj & 7) * 4096 + lane * 64;
#define CPLD()                                                                                                              \
  asm volatile("s_cmp_lg_u32 %1, %2\n\ts_cbranch_scc1 7f\n"                                                                 \
               ".Lfd_beg:\n\ts_getpc_b64 s[96:97]\n\ts_and_b32 s97, s97, 0xffff\n\ts_mov_b32 s98, .Lfd_end-.Lfd_beg-8\n\t"   \
               "s_mov_b32 s99, 0x20000\n\tbuffer_load_dword %0, %0, s[96:99], 0 offen\n7:"                                   \
               : "+v"(cpv) : "s"(w + 8 * (jj & ~7)), "n"(4) : "memory", "scc", "s96", "s97", "s98", "s99")
  SCHED_FENCE();
  CPLD();
  SCHED_FENCE();
  asm volatile("" ::"s"(x), "s"(kc), "s"(vc), "s"(wq), "s"(wo), "s"(w1), "s"(w3), "s"(w2), "s"(gw), "s"(out));
  SCHED_FENCE();
  u64 tstart64 = __builtin_amdgcn_s_memrealtime();
  SCHED_FENCE();
  const auto rws = __builtin_amdgcn_make_buffer_rsrc((void*)gw, (short)0, GEND * 16, 0x00020000);
  const size_t kvw = ((size_t)ha * SK + (SK - WIN)) * DH;
  const u32x4 rk = mkrs(kc + kvw, WIN * DH * 2), rv = mkrs(vc + kvw, WIN * DH * 2);
  const u32x4 rwa = mkrs(gw, GEND * 16);
  const int kvo = (g * DH + 8 * c) * 2;
  const int dx = (int)((dx_pack() >> (4 * xcd)) & 15u) - 4;
  const int wr = w < 4 ? (w + 3) & 3 : w;
  const int nt = 8 + ((unsigned)(wr - dx) >> 31) - ((unsigned)(7 - w + dx) >> 31);
  const int don = (int)(dx_don() >> (5 * (((dx_pos() >> (4 * xcd)) + wr) & 7u))) & 31;
  const int kvb = (spa * 8 + ((w + (ha >> 3)) & 7)) * 64 * DH * 2;
  const int ko8 = (((don >> 2) * 8 + ((7 - (don & 3) + (ha >> 3)) & 7)) * 8 + ((7 + ha) & 7)) * 8 * DH * 2;
  const int wl = __builtin_amdgcn_readfirstlane((w < 4 ? WOL : WND) + (nt - 8) + (dx < 0 ? 1 : 0));
#define KVOFF(t) ((t) == 8 ? ko8 : kvb + (((t) + ha) & 7) * 8 * DH * 2)
#define KLD(t, j) ALD(K[t][j], rk, kvo, KVOFF(t), 128 * (j))
  unsigned ta, te;
#define KWL(t, j)                                                                            \
  do {                                                                                       \
    if ((t) >= NF) AWT(K[t][j], rk, kvo, KVOFF(t), 128 * (j), ta, (6 * (t) - 21 + (j)) * TB * 8); \
    else AWL(K[t][j], rk, kvo, KVOFF(t), 128 * (j));                                         \
  } while (0)
#define VWL(t, j)                                                                            \
  do {                                                                                       \
    if ((t) < NF) AWT(V[t][j], rv, kvo, KVOFF(t), 128 * (j), te, (3 * (t) + (j) - 20) * TB * 8); \
    else AWT(V[t][j], rv, kvo, KVOFF(t), 128 * (j), ta, (6 * (t) - 18 + (j)) * TB * 8);      \
  } while (0)
#define EPLD() asm volatile("buffer_load_dword %0, off, %1, %2" : "=v"(epoch) : "s"(rwa), "s"(GEP * 16) : "memory")
  u32x4 K[NF + 2][3], V[NF + 2][3];

  const int fi = (xcd & 1) ? 19 : 8;
  const bool fidle = (unsigned)(jj - fi) < 2u;
  const int fp = jj > fi ? jj - 2 : jj;
  const int fcol = fp / 5, frp = fp - 5 * fcol;
  unsigned epoch, tag;
  if (w < 4) {
    const int fw = 4 * sp + w, ct = fw >= 20 ? 2 : fw >= 10 ? 1 : 0;
    const int r0 = 64 * (fw - 10 * ct);
    const int R = 128 * frp + 32 * w;
    const u32x4 xa = *reinterpret_cast<const u32x4*>(x + 8 * lane);
    const u32x4 xb = *reinterpret_cast<const u32x4*>(x + 512 + 8 * (lane & 15));
    u32x4 WQ[8], F[4];
    {
      const auto rq = __builtin_amdgcn_make_buffer_rsrc((void*)wq, (short)0, D * HQ * DH * 2, 0x00020000);
      const int qo = ((r0 + 8 * g) * (HQ * DH) + h * DH + 64 * ct + 8 * c) * 2;
#pragma unroll
      for (int i = 0; i < 8; ++i) WQ[i] = __builtin_amdgcn_raw_buffer_load_b128(rq, qo, i * HQ * DH * 2, 2);
      const auto rf = __builtin_amdgcn_make_buffer_rsrc((void*)(fcol >= 3 ? w3 : w1), (short)0, fidle ? 0 : D * FF * 2, 0x00020000);
      const int fo = ((R + 4 * g) * FF + 192 * xcd + 64 * (fcol >= 3 ? fcol - 3 : fcol) + 8 * c) * 2;
#pragma unroll
      for (int i = 0; i < 4; ++i) F[i] = __builtin_amdgcn_raw_buffer_load_b128(rf, fo, i * FF * 2, 2);
    }
    if (fidle && tid < 40)
      __builtin_amdgcn_raw_buffer_store_b128(u32x4{0u, 0u, 0u, 0u}, rws, (GA + (jj - fi) * 320 + 40 * xcd + lane) * 16, 0, 16);
    SCHED_FENCE();
    EPLD();
    if (tid < 4) flg[tid] = 0u;
    SCHED_FENCE();
    __syncthreads();
    SCHED_FENCE();
    unsigned short* xs = reinterpret_cast<unsigned short*>(lds + L_XS + w * 320);
    const bool lo16 = lane < 16;
    *reinterpret_cast<u32x4*>(xs + 8 * lane) = xa;
    if (lo16) *reinterpret_cast<u32x4*>(xs + 512 + 8 * lane) = xb;
    float xf[8], xg[8];
#pragma unroll
    for (int i = 0; i < 4; ++i) {
      xf[2 * i] = bf_lo(xa[i]);
      xf[2 * i + 1] = bf_hi(xa[i]);
      xg[2 * i] = bf_lo(xb[i]);
      xg[2 * i + 1] = bf_hi(xb[i]);
    }
    float sum = 0.f;
#pragma unroll
    for (int i = 0; i < 8; ++i) sum += xf[i] + (lo16 ? xg[i] : 0.f);
    sum = wave_sum(sum);
    const float mean = sum * (1.0f / D);
    float sq = 0.f;
#pragma unroll
    for (int i = 0; i < 8; ++i) {
      const float d1 = xf[i] - mean, d2 = xg[i] - mean;
      sq += d1 * d1 + (lo16 ? d2 * d2 : 0.f);
    }
    sq = wave_sum(sq);
    const float mb = bfr(mean);
    const float vb = bfr(sq * (1.0f / D));
    const float den = bfr(sqrtf(bfr(vb + 1e-5f)));
    WAVE_SYNC();
    auto xnorm = [&](int r) -> unsigned short { return bfbits(bfr(bf_lo(xs[r]) - mb) / den); };
    {
      unsigned short* xn = reinterpret_cast<unsigned short*>(lds + L_XN + w * 64);
      xn[lane] = xnorm((r0 < D ? r0 : 0) + lane);
      xn[64 + (lane & 31)] = xnorm(R + (lane & 31));
      WAVE_SYNC();
      const u32x4 XQ = *reinterpret_cast<const u32x4*>(xn + 8 * g);
      const u32x2 XF = *reinterpret_cast<const u32x2*>(xn + 64 + 4 * g);
      float acc[16], r2[2];
#pragma unroll
      for (int i = 0; i < 16; ++i) acc[i] = 0.f;
#pragma unroll
      for (int i = 0; i < 8; ++i) {
        const unsigned d = XQ[i >> 1], lo = (i & 1) ? d >> 16 : d & 0xffffu, hi = (i & 1) ? d & 0xffff0000u : d << 16;
#pragma unroll
        for (int k = 0; k < 4; ++k) {
          acc[2 * k] = dot2(WQ[i][k], lo, acc[2 * k]);
          acc[2 * k + 1] = dot2(WQ[i][k], hi, acc[2 * k + 1]);
        }
      }
#pragma unroll
      for (int i = 0; i < 4; ++i) {
        const unsigned d = XF[i >> 1], lo = (i & 1) ? d >> 16 : d & 0xffffu, hi = (i & 1) ? d & 0xffff0000u : d << 16;
#pragma unroll
        for (int k = 0; k < 4; ++k) {
          acc[8 + 2 * k] = dot2(F[i][k], lo, acc[8 + 2 * k]);
          acc[8 + 2 * k + 1] = dot2(F[i][k], hi, acc[8 + 2 * k + 1]);
        }
      }
      xg_scatter<2>(acc, r2, g & 1);
#pragma unroll
      for (int j = 0; j < 2; ++j) lds[L_PART + w * 128 + 64 * j + 8 * c + g] = r2[j];
    }
    asm volatile("s_waitcnt lgkmcnt(0)" ::: "memory");
    if (lane == 0) __hip_atomic_fetch_add(flg, 1u, __ATOMIC_RELAXED, __HIP_MEMORY_SCOPE_WORKGROUP);
    SCHED_FENCE();
  } else {
#pragma unroll
    for (int m = 0; m < KE; ++m) KLD(m / 3, m % 3);
    SCHED_FENCE();
    EPLD();
    SCHED_FENCE();
    __syncthreads();
    SCHED_FENCE();
  }
  const int lz = lane < 60 ? lane : 59;
  const int zr = lz / 10, zc = lz - 10 * zr;
  int w2o = (zr * D + 8 * zc) * 2;
  asm volatile("" : "+v"(w2o));
  if (w == 0) {
    for (unsigned spin = 0; spin < (SPIN_MAX << 4); ++spin) {
      if (__hip_atomic_load(flg, __ATOMIC_RELAXED, __HIP_MEMORY_SCOPE_WORKGROUP) == 4u) break;
      __builtin_amdgcn_s_sleep(1);
    }
    asm volatile("s_waitcnt lgkmcnt(0)" ::: "memory");
    AWAIT(epoch, 0);
    tag = __builtin_amdgcn_readfirstlane(epoch) + 1u;
    const f32x2* pp = reinterpret_cast<const f32x2*>(lds + L_PART) + lane;
    const f32x2 a01 = pp[0] + pp[64], a23 = pp[128] + pp[192];
    const bool two = sp == 2 && lane < 32;
    const f32x2 sm = two ? a01 : a01 + a23;
    const int ct0 = sp >= 5 ? 2 : sp >= 3 ? 1 : 0, slot = sp - (sp >= 5 ? 5 : sp >= 3 ? 2 : 0);
    const int uf = fidle ? GZF + 32 * xcd + 16 * (jj - fi) : G1F + xcd * 960 + fp * 32;
    const int un = lane < 32 ? G1 + h * 288 + slot * 96 + ct0 * 32 + lane : uf + lane - 32;
    if (lane < 33 || !fidle) gst2(un, sm[0], sm[1]);
    if (two) gst2(G1 + h * 288 + 32 + lane, a23[0], a23[1]);
    SCHED_FENCE();
  }
  asm volatile("" : "+s"(tstart64));
#define PHW 28
  const unsigned tstart = (unsigned)tstart64 + (unsigned)((xo_pack() >> (8 * xcd)) & 255u) - 64u - ((((w & 3) * 2 + (w >> 2)) * PHW) >> 3);
  ta = (tstart + TA) * nt - TOFF;
  te = (tstart + TE) * nt - TOFF;
  int wop = w;
  asm volatile("" : "+s"(wop));
  if (wop != 0) {
    if (w < 4) {
#pragma unroll
      for (int m = 0; m < KE; ++m) KWL(m / 3, m % 3);
    }
#pragma unroll
    for (int m = KE; m < 3 * NF; ++m) {
      KWL(m / 3, m % 3);
    }
    TIE(epoch);
    TIE(cpv);
    tag = __builtin_amdgcn_readfirstlane(epoch) + 1u;
    SCHED_FENCE();
  } else {
    u32x4 W2[8];
    const auto rw2 = __builtin_amdgcn_make_buffer_rsrc((void*)(w2 + (size_t)48 * h * D + 80 * sp), (short)0, 48 * D * 2, 0x00020000);
#pragma unroll
    for (int i = 0; i < 8; ++i) W2[i] = __builtin_amdgcn_raw_buffer_load_b128(rw2, w2o, i * 6 * D * 2, 2);
#pragma unroll
    for (int t = 0; t < NF; ++t)
#pragma unroll
      for (int j = 0; j < 3; ++j) KLD(t, j);
    SCHED_FENCE();
    const int row = lane >> 4, l16 = lane & 15;
    const int gb = G1 + ha * 288 + lane;
    const int gc = row < 3 ? G1 + ha * 288 + row * 96 + 64 + l16 : GZF + 32 * sp;
    const int l48 = lane < 48 ? lane : 47, fm = l48 >= 24 ? 1 : 0, fpr = 24 * (h & 3) + l48 - 24 * fm;
    const int gf = G1F + xcd * 960 + (3 * fm + (fpr >> 5)) * 160 + (fpr & 31);
    u32x4 va[3], vb[2], vf[5];
    for (unsigned spin = 0;; ++spin) {
      MEMBAR();
#pragma unroll
      for (int s = 0; s < 3; ++s) va[s] = gld2(gb, s * 96);
#pragma unroll
      for (int s = 0; s < 2; ++s) vb[s] = gld2(gc, s * 16);
#pragma unroll
      for (int s = 0; s < 5; ++s) vf[s] = gld2(gf, s * 32);
      bool ok = gok2(vb[0]) && gok2(vb[1]) && gok2(vf[3]) && gok2(vf[4]);
#pragma unroll
      for (int s = 0; s < 3; ++s) ok = ok && gok2(va[s]) && gok2(vf[s]);
      if (__all(ok) || spin >= SPIN_MAX) break;
      __builtin_amdgcn_s_sleep(1);
    }
    {
      const float a0 = (bitsf(va[0][0]) + bitsf(va[1][0])) + bitsf(va[2][0]), a1 = (bitsf(va[0][2]) + bitsf(va[1][2])) + bitsf(va[2][2]);
      reinterpret_cast<unsigned*>(lds + L_QS)[lane] = (unsigned)bfbits(a0) | ((unsigned)bfbits(a1) << 16);
      const float p0 = swap16_add(row < 3 ? bitsf(vb[0][0]) : 0.f, row < 3 ? bitsf(vb[0][2]) : 0.f);
      const float p1 = swap16_add(row < 3 ? bitsf(vb[1][0]) : 0.f, row < 3 ? bitsf(vb[1][2]) : 0.f);
      const float bq = swap32_add(p0, p1);
      reinterpret_cast<unsigned short*>(lds + L_QS)[2 * (64 + 16 * (row >> 1) + l16) + (row & 1)] = bfbits(bq);
    }
    asm volatile("s_waitcnt lgkmcnt(0)" ::: "memory");
    if (lane == 0) __hip_atomic_store(flg + 1, 1u, __ATOMIC_RELAXED, __HIP_MEMORY_SCOPE_WORKGROUP);
    SCHED_FENCE();
    {
      const float f0 = ((bitsf(vf[0][0]) + bitsf(vf[1][0])) + (bitsf(vf[2][0]) + bitsf(vf[3][0]))) + bitsf(vf[4][0]);
      const float f1 = ((bitsf(vf[0][2]) + bitsf(vf[1][2])) + (bitsf(vf[2][2]) + bitsf(vf[3][2]))) + bitsf(vf[4][2]);
      *reinterpret_cast<f32x2*>(lds + L_GU + 2 * l48) = f32x2{bfr(f0), bfr(f1)};
    }
    WAVE_SYNC();
    if (lane < 48) lds[L_H + lane] = bfr(bfr(silu(lds[L_GU + lane])) * lds[L_GU + 48 + lane]);
    WAVE_SYNC();
    {
      float z[8];
#pragma unroll
      for (int i = 0; i < 8; ++i) z[i] = 0.f;
#pragma unroll
      for (int i = 0; i < 8; ++i) {
        const unsigned chi = fbits(lds[L_H + zr + 6 * i]), clo = chi >> 16;
#pragma unroll
        for (int k = 0; k < 4; ++k) {
          z[2 * k] = dot2(W2[i][k], clo, z[2 * k]);
          z[2 * k + 1] = dot2(W2[i][k], chi, z[2 * k + 1]);
        }
      }
      if (lane < 60) {
        float* zp = lds + L_ZP + zr * 80 + 8 * zc;
        *reinterpret_cast<f32x4*>(zp) = f32x4{z[0], z[1], z[2], z[3]};
        *reinterpret_cast<f32x4*>(zp + 4) = f32x4{z[4], z[5], z[6], z[7]};
      }
    }
    SCHED_FENCE();
  }
  const int u = (tid & 255) < 240 ? (tid & 255) : 239;
  const int rg = u / 10, ch = u - 10 * rg;
  u32x4 WO[8];
  const u32x4 rwo = mkrs(wo + (size_t)DH * h * D + 80 * sp, DH * D * 2);
  const int wofs = (rg * D + 8 * ch) * 2;
#define WOLC(i)                                                                                                            \
  asm volatile("s_cmp_lt_u32 %4, 4\n\ts_cbranch_scc1 2\n\tbuffer_load_dwordx4 %0, %1, %2, %3 offen nt"                     \
               : "=v"(WO[i]) : "v"(wofs), "s"(rwo), "s"((i) * 24 * D * 2), "s"(w) : "memory", "scc")
#define VLD(t)                                                       \
  do {                                                               \
    _Pragma("unroll") for (int j = 0; j < 3; ++j) VWL(t, j);         \
  } while (0)
#pragma unroll
  for (int t = NF; t < NF + 2; ++t)
    if (t < nt) {
#pragma unroll
      for (int j = 0; j < 3; ++j) KWL(t, j);
      VLD(t);
    }
#define ZW 3
#define SLOWSUM(res, base, cc)                                                                                  \
  do {                                                                                                          \
    res = 0.f;                                                                                                  \
    _Pragma("unroll 1") for (int hh = 0; hh < HQ; ++hh) {                                                       \
      u32x2 v;                                                                                                  \
      for (unsigned spin = 0; spin < SPIN_MAX; ++spin) {                                                        \
        MEMBAR();                                                                                               \
        v = __builtin_amdgcn_raw_buffer_load_b64(rws, (cc) * 8, ((base) + hh * 320) * 16, 16);                  \
        if (v[1] == tag) break;                                                                                 \
      }                                                                                                         \
      res += bitsf(v[0]);                                                                                       \
    }                                                                                                           \
  } while (0)
#define ZPUB()                                                                                      \
  do {                                                                                              \
    const f32x2* zp = reinterpret_cast<const f32x2*>(lds + L_ZP) + (lane < 40 ? lane : 0);          \
    const f32x2 zs = ((zp[0] + zp[40]) + (zp[80] + zp[120])) + (zp[160] + zp[200]);                 \
    gst2(lane < 40 ? G3Z + h * 320 + 40 * sp + lane : GEND, zs[0], zs[1]);   \
  } while (0)
  u64 zold[2] = {0ull, 0ull}, zadd[2] = {0ull, 0ull};
  int mro = 8 * tid, mso = tid < 97 ? (G2 + rec * 104 + tid) * 16 : GEND * 16;
  u32x4 pub = {0u, tag, 0u, tag};
  asm volatile("" : "+v"(mro), "+v"(mso), "+v"(pub));
  {
    float Mg;
    float acc[3][8];
#pragma unroll
    for (int i = 0; i < 24; ++i) acc[i >> 3][i & 7] = 0.f;
    float* pw = lds + L_PART + w * PS + 8 * c + g;
    const bool b3 = g & 1;
#define PV3(t, j, cl, ch)                                                                                               \
  do {                                                                                                                  \
    if ((cl) < 0) asm volatile("" : "+v"(plo), "+v"(phi));                                                              \
    else if ((cl) == (ch)) asm volatile("s_waitcnt vmcnt(%2)" : "+v"(plo), "+v"(phi) : "n"((cl) < 0 ? 0 : (cl)) : "memory");  \
    else asm volatile("s_waitcnt vmcnt(%2)\n\ts_cmp_lt_u32 %4, 4\n\ts_cbranch_scc0 1\n\ts_waitcnt vmcnt(%3)"              \
                      : "+v"(plo), "+v"(phi) : "n"(ch), "n"((cl) < 0 ? 0 : (cl)), "s"(w) : "memory", "scc");            \
    _Pragma("unroll") for (int i = 0; i < 4; ++i) {                                                                     \
      acc[j][2 * i] = dot2(V[t][j][i], plo, acc[j][2 * i]);                                                             \
      acc[j][2 * i + 1] = dot2(V[t][j][i], phi, acc[j][2 * i + 1]);                                                     \
    }                                                                                                                   \
    if ((t) == NF - 1) {                                                          \
      float r1[1];                                                                                                      \
      xg_scatter<1>(acc[j], r1, b3);                                                                                    \
      pw[64 * (j)] = r1[0] * ew;                                                                                        \
    } else {                                                                                                            \
      asm volatile("" : "+v"(acc[j][0]), "+v"(acc[j][1]), "+v"(acc[j][2]), "+v"(acc[j][3]), "+v"(acc[j][4]),            \
                   "+v"(acc[j][5]), "+v"(acc[j][6]), "+v"(acc[j][7]));                       \
    }                                                                                                                   \
    SCHED_FENCE();                                                                                                      \
  } while (0)
#define PVT(t, c)                                                                                                       \
  do {                                                                                                                  \
    unsigned plo = pb[t], phi = plo << 16;                                                                              \
    PV3(t, 0, c, c);                                                                                                    \
    PV3(t, 1, -1, -1);                                                                                                  \
    PV3(t, 2, -1, -1);                                                                                                  \
  } while (0)
#define WGMAX()                                                                                                         \
  do {                                                                                                                  \
    for (unsigned spin = 0; spin < (SPIN_MAX << 4); ++spin) {                                                           \
      if (__hip_atomic_load(flg + 3, __ATOMIC_RELAXED, __HIP_MEMORY_SCOPE_WORKGROUP) == 8u) break;                      \
      __builtin_amdgcn_s_sleep(1);                                                                                      \
    }                                                                                                                   \
    MEMBAR();                                                                                                           \
    const f32x4 ma = *reinterpret_cast<const f32x4*>(lds + L_ML), mb2 = *reinterpret_cast<const f32x4*>(lds + L_ML + 4); \
    Mg = fmaxf(fmaxf(fmaxf(ma[0], ma[1]), fmaxf(ma[2], ma[3])), fmaxf(fmaxf(mb2[0], mb2[1]), fmaxf(mb2[2], mb2[3])));   \
    ew = ex2((mw - Mg) * LOG2E);                                                                                        \
    if (lane == 0) *reinterpret_cast<f32x2*>(lds + L_PART + w * PS + DH) = f32x2{w == 0 ? Mg : 0.f, lw * ew};           \
    SCHED_FENCE();                                                                                                      \
  } while (0)
    float ew, mw, lw;
    u32x4 qf[3];
    float sc[NF + 2];
    unsigned pb[NF + 2];
#define GATE(cnt)                                                                                                       \
  do {                                                                                                                  \
    asm volatile("s_waitcnt vmcnt(%2)" : "+v"(zold[0]), "+v"(zold[1]) : "n"(cnt) : "memory");                           \
    _Pragma("unroll") for (int k = 0; k < 2; ++k)                                                                       \
      if ((k == 0 || lane < 16) && (zold[k] >> 59) == 31u) {                                                            \
        const int cz = 80 * sp + 64 * k + lane;                                                                         \
        float f = fx_dec(zold[k] + zadd[k]);                                                                            \
        if (__builtin_expect(!fx_exact(zold[k], zadd[k]), 0)) SLOWSUM(f, G3Z, cz);                                      \
        __builtin_amdgcn_raw_buffer_store_b64(u32x2{fbits(bfr(silu(bfr(f)))), tag}, rws, cz * 8, GG * 16, 16);          \
      }                                                                                                                 \
  } while (0)
    auto slot = [&](auto mc) __attribute__((always_inline)) {
      constexpr int m = decltype(mc)::value;
      VWL(m / 3, m % 3);
      if (m == QS) {
        for (unsigned spin = 0; spin < (SPIN_MAX << 4); ++spin) {
          if (__hip_atomic_load(flg + 1, __ATOMIC_RELAXED, __HIP_MEMORY_SCOPE_WORKGROUP) != 0u) break;
          __builtin_amdgcn_s_sleep(1);
        }
        asm volatile("s_waitcnt lgkmcnt(0)" ::: "memory");
        SCHED_FENCE();
#pragma unroll
        for (int j = 0; j < 3; ++j) qf[j] = *reinterpret_cast<const u32x4*>(lds + L_QS + (c + 8 * j) * 4);
      }
#pragma unroll
      for (int t = 0; t < NF + 2; ++t)
        if (SS[t] == m) {
          sc[t] = -INFINITY;
          if (t < NF || t < nt) {
            float d = 0.f;
#pragma unroll
            for (int j = 0; j < 3; ++j) {
              TIE(K[t][j]);
#pragma unroll
              for (int i = 0; i < 4; ++i) d = dot2(K[t][j][i], qf[j][i], d);
            }
            d = grp8_sum(d);
            sc[t] = bfr(bfr(d) * SCALE);
          }
          TIE(sc[t]);
        }
      if (m == XS) {
        mw = sc[0];
#pragma unroll
        for (int t = 1; t < NF + 2; ++t) mw = fmaxf(mw, sc[t]);
        mw = xg_max(mw);
        if (lane == 0) {
          lds[L_ML + w] = mw;
          __hip_atomic_fetch_add(flg + 3, 1u, __ATOMIC_RELAXED, __HIP_MEMORY_SCOPE_WORKGROUP);
        }
        lw = 0.f;
#pragma unroll
        for (int t = 0; t < NF + 2; ++t) {
          const float p = ex2((sc[t] - mw) * LOG2E);
          lw += p;
          pb[t] = bfbits(p);
          TIE(pb[t]);
        }
        lw = xg_sum(lw);
        TIE(lw);
      }
      if (m == XS + 1 && NF < nt) PVT(NF, -1);
      if (m == XS + 2 && NF + 1 < nt) PVT(NF + 1, -1);
      if (m >= XS + 3) {
        constexpr int n0 = PVN(m - 1), n1 = PVN(m);
#pragma unroll
        for (int n = n0; n < n1; ++n) {
          unsigned plo = pb[n / 3], phi = plo << 16;
          PV3(n / 3, n % 3, n == m - 8 ? 8 : -1, n == m - 8 ? 8 : -1);
        }
      }
      if (m == ZSL) {
        WGMAX();
        if (w == ZW) {
#pragma unroll
          for (int k = 0; k < 2; ++k) {
            const float* zp = lds + L_ZP + (k ? 64 + (lane & 15) : lane);
            zold[k] = zadd[k] = fx_enc(((zp[0] + zp[80]) + (zp[160] + zp[240])) + (zp[320] + zp[400]));
          }
          const int za0 = (2 * GA + D + 80 * sp + lane) * 8, za1 = lane < 16 ? za0 + 512 : GEND * 16;
          ZPUB();
          asm volatile("buffer_atomic_add_x2 %0, %2, %4, 0 offen sc0\n\tbuffer_atomic_add_x2 %1, %3, %4, 0 offen sc0"
                       : "+v"(zold[0]), "+v"(zold[1]) : "v"(za0), "v"(za1), "s"(rwa) : "memory");
        }
      }
      if (m == GSL && w == ZW) GATE(GSL - ZSL);
      SCHED_FENCE();
    };
#define SL3(t) slot(IC<3 * (t)>{}); slot(IC<3 * (t) + 1>{}); slot(IC<3 * (t) + 2>{})
    SL3(0); SL3(1); SL3(2); SL3(3); SL3(4); SL3(5); SL3(6);
#pragma unroll
    for (int n = PVN(3 * NF - 1); n < 3 * NF; ++n) {
      const int m = n - (3 * NF - 9);
      if (m >= 0) {
#pragma unroll
        for (int i = (m && n > PVN(3 * NF - 1)) ? WON[m - 1] : 0; i < WON[m]; ++i) WOLC(i);
      }
      unsigned plo = pb[n / 3], phi = plo << 16;
      PV3(n / 3, n % 3, m < 0 ? -1 : 8 - m, m < 0 ? -1 : 8 - m + WON[m < 0 ? 0 : m]);
    }
#pragma unroll
    for (int i = WON[8]; i < WON[9]; ++i) WOLC(i);
  }
  __syncthreads();
  if (__builtin_expect(w < 2, 1)) {
    {
      f32x2 p2[8];
#pragma unroll
      for (int ww = 0; ww < 8; ++ww) p2[ww] = *reinterpret_cast<const f32x2*>(reinterpret_cast<const char*>(lds + L_PART + ww * PS) + mro);
      const f32x2 ua = (p2[0] + p2[1]) + (p2[2] + p2[3]), ub = (p2[4] + p2[5]) + (p2[6] + p2[7]);
      float ub1 = ub[1];
      asm("" : "+v"(ub1));
      pub[0] = fbits(ua[0] + ub[0]);
      pub[2] = fbits(ua[1] + ub1);
      __builtin_amdgcn_raw_buffer_store_b128(pub, rws, mso, 0, 16);
    }
    SCHED_FENCE();
    const int t2 = tid < 96 ? tid : 95;
    const int pb = (G2 + h * NSP * 104 + t2) * 16, pm = (G2 + (h * NSP + c) * 104 + 96) * 16;
    const unsigned po = (unsigned)(size_t)(__attribute__((address_space(3))) float*)(lds + L_O) + 4 * t2;
    const int pg = lane < 20 ? (GG + 40 * sp + 20 * w + lane) * 16 : GEND * 16;
    const unsigned pl = (unsigned)(size_t)(__attribute__((address_space(3))) float*)(lds + L_GT) + 16 * (lane < 20 ? 20 * w + lane : 40 + tid);
    if (PD1) __builtin_amdgcn_s_sleep(PD1);
    {
      unsigned e0, e1, e2, e3, e4, e5, e6, e7, cnt;
#define DPC " row_mask:0xf bank_mask:0xf\n\t"
#define CHK(d, a, b) "v_bitop3_b32 " d ", " a ", " b ", %13 bitop3:0x7e\n\t"
#define ACC2(e, a, b) "v_fmac_f32 v44, " e ", " a "\n\tv_fmac_f32 v45, " e ", " b "\n\t"
      asm volatile(
          "s_mov_b32 %8, %16\n"
          "1:\n\t"
          "buffer_load_dwordx4 v[48:51], %18, %12, 0 offen sc1\n\t"
          "buffer_load_dwordx4 v[4:7], %9, %12, 0 offen sc1\n\t"
          "buffer_load_dwordx4 v[8:11], %10, %12, 0 offen sc1\n\t"
          "buffer_load_dwordx4 v[12:15], %10, %12, 0 offen offset:1664 sc1\n\t"
          "buffer_load_dwordx4 v[16:19], %10, %12, 0 offen offset:3328 sc1\n\t"
          "buffer_load_dwordx4 v[20:23], %10, %12, %14 offen sc1\n\t"
          "buffer_load_dwordx4 v[24:27], %10, %12, %14 offen offset:1664 sc1\n\t"
          "buffer_load_dwordx4 v[28:31], %10, %12, %14 offen offset:3328 sc1\n\t"
          "buffer_load_dwordx4 v[32:35], %10, %12, %15 offen sc1\n\t"
          "buffer_load_dwordx4 v[36:39], %10, %12, %15 offen offset:1664 sc1\n\t"
          "s_sub_u32 %8, %8, 1\n\t"
          "s_waitcnt vmcnt(8)\n\t"
          CHK("v41", "v5", "v7")
          "ds_write_b128 %19, v[48:51]\n\t"
          "v_max_f32_dpp v40, v4, v4 quad_perm:[1,0,3,2]" DPC
          "s_waitcnt vmcnt(7)\n\t"
          CHK("v46", "v9", "v11")
          "v_max_f32_dpp v40, v40, v40 quad_perm:[2,3,0,1]" DPC
          "s_waitcnt vmcnt(6)\n\t"
          CHK("v47", "v13", "v15")
          "v_max_f32_dpp v40, v40, v40 row_half_mirror" DPC
          "v_sub_f32 v42, v4, v40\n\t"
          "v_mul_f32 v42, 0x3fb8aa3b, v42\n\t"
          "v_exp_f32 v42, v42\n\t"
          "v_or3_b32 v41, v41, v46, v47\n\t"
          "v_mul_f32 v40, v6, v42\n\t"
          "s_waitcnt vmcnt(5)\n\t"
          CHK("v46", "v17", "v19")
          "v_add_f32_dpp v40, v40, v40 quad_perm:[1,0,3,2]" DPC
          "s_waitcnt vmcnt(4)\n\t"
          CHK("v47", "v21", "v23")
          "v_add_f32_dpp v40, v40, v40 quad_perm:[2,3,0,1]" DPC
          "v_or3_b32 v41, v41, v46, v47\n\t"
          "s_nop 0\n\t"
          "v_add_f32_dpp v40, v40, v40 row_half_mirror" DPC
          "v_rcp_f32 v40, v40\n\t"
          "s_nop 0\n\t"
          "v_mul_f32 v42, v42, v40\n\t"
          "s_nop 0\n\t"
          "v_readlane_b32 %0, v42, 0\n\tv_readlane_b32 %1, v42, 1\n\tv_readlane_b32 %2, v42, 2\n\tv_readlane_b32 %3, v42, 3\n\t"
          "v_readlane_b32 %4, v42, 4\n\tv_readlane_b32 %5, v42, 5\n\tv_readlane_b32 %6, v42, 6\n\tv_readlane_b32 %7, v42, 7\n\t"
          "v_mul_f32 v44, %0, v8\n\tv_mul_f32 v45, %0, v10\n\t"
          ACC2("%1", "v12", "v14") ACC2("%2", "v16", "v18") ACC2("%3", "v20", "v22")
          "s_waitcnt vmcnt(3)\n\t"
          ACC2("%4", "v24", "v26") CHK("v46", "v25", "v27")
          "s_waitcnt vmcnt(2)\n\t"
          ACC2("%5", "v28", "v30") CHK("v47", "v29", "v31")
          "v_or3_b32 v41, v41, v46, v47\n\t"
          "s_waitcnt vmcnt(1)\n\t"
          ACC2("%6", "v32", "v34") CHK("v46", "v33", "v35")
          "s_waitcnt vmcnt(0)\n\t"
          ACC2("%7", "v36", "v38")
          "v_cvt_pk_bf16_f32 v44, v44, v45\n\t"
          "ds_write_b32 %11, v44\n\t"
          CHK("v47", "v37", "v39")
          "v_or3_b32 v41, v41, v46, v47\n\t"
          "v_cmp_ne_u32 vcc, 0, v41\n\t"
          "s_cbranch_vccz 2f\n\t"
          "s_sleep %17\n\t"
          "s_cbranch_scc0 1b\n"
          "2:\n\t"
          "s_waitcnt lgkmcnt(0)"
          : "=&s"(e0), "=&s"(e1), "=&s"(e2), "=&s"(e3), "=&s"(e4), "=&s"(e5), "=&s"(e6), "=&s"(e7), "=&s"(cnt)
          : "v"(pm), "v"(pb), "v"(po), "s"(rwa), "s"(tag), "s"(3 * 1664), "s"(6 * 1664), "n"(SPIN_MAX), "n"(PS1), "v"(pg), "v"(pl)
          : "memory", "scc", "vcc", "v4", "v5", "v6", "v7", "v8", "v9", "v10", "v11", "v12", "v13", "v14", "v15", "v16", "v17", "v18", "v19",
            "v20", "v21", "v22", "v23", "v24", "v25", "v26", "v27", "v28", "v29", "v30", "v31", "v32", "v33", "v34", "v35", "v36", "v37",
            "v38", "v39", "v40", "v41", "v42", "v44", "v45", "v46", "v47", "v48", "v49", "v50", "v51");
    }
    __syncthreads();
  } else if (w == 2) {
    __syncthreads();
  } else {
    if (w >= 4) {
#pragma unroll
      for (int i = WON[9]; i < 8; ++i) WOLC(i);
#pragma unroll
      for (int i = 0; i < 8; ++i) {
        AWAIT(WO[i], 7 - i);
        *reinterpret_cast<u32x4*>(lds + L_WO + u * 4 + i * 960) = WO[i];
      }
      asm volatile("s_waitcnt lgkmcnt(0)" ::: "memory");
      if (lane == 0) __hip_atomic_fetch_add(flg + 2, 1u, __ATOMIC_RELAXED, __HIP_MEMORY_SCOPE_WORKGROUP);
    }
    for (unsigned spin = 0; spin < (SPIN_MAX << 4); ++spin) {
      if (__hip_atomic_load(flg + 2, __ATOMIC_RELAXED, __HIP_MEMORY_SCOPE_WORKGROUP) == 4u) break;
      __builtin_amdgcn_s_sleep(1);
    }
    asm volatile("s_waitcnt lgkmcnt(0)" ::: "memory");
    u32x4 wb[6];
    {
      typedef __attribute__((address_space(3))) v4i16_t* ltr;
      const unsigned la = (unsigned)(size_t)(__attribute__((address_space(3))) float*)(lds + L_WO) + (8 * (lane >> 4) + ((lane >> 2) & 3)) * 160 + 32 * (w - 3) + 8 * (lane & 3);
#pragma unroll
      for (int k = 0; k < 6; ++k) {
        const u32x2 t0 = __builtin_bit_cast(u32x2, __builtin_amdgcn_ds_read_tr16_b64_v4i16((ltr)(la + 32 * k * 160)));
        const u32x2 t1 = __builtin_bit_cast(u32x2, __builtin_amdgcn_ds_read_tr16_b64_v4i16((ltr)(la + (32 * k + 4) * 160)));
        wb[k] = u32x4{t0[0], t0[1], t1[0], t1[1]};
      }
    }
    const int cy = 80 * sp + 16 * (w - 3) + (lane & 15);
    float xv = bf_lo(reinterpret_cast<const unsigned short*>(lds + L_XS)[cy]);
    gu64* pa = (gu64*)(gw + 2 * GA + cy);
    gu16* pout = (gu16*)(out + cy);
    typedef __attribute__((address_space(3))) const u32x4* la4;
    la4 pa4 = (la4)(lds + L_O) + 4 * (lane & 3) + (lane >> 4);
    int cy8 = cy * 8;
    typedef __attribute__((address_space(3))) const u32x2* la2;
    la2 pgt = (la2)(lds + L_GT) + 16 * (w - 3) + (lane & 15);
    asm volatile("" : "+v"(pgt));
    unsigned k0 = (1u << 27) - (0x411u << 20), k1 = k0 + (1u << 22), k2 = 1u << 17;
    double c0 = 262144.0 + 512.0, c1 = -(262144.0 + 32 * 512.0);
    float f0 = 512.f, f1 = 0x1p-9f;
    asm volatile("" : "+v"(wb[0]), "+v"(wb[1]), "+v"(wb[2]), "+v"(wb[3]), "+v"(wb[4]), "+v"(wb[5]));
    asm volatile("" : "+v"(xv), "+v"(pa), "+v"(pout), "+v"(pa4), "+v"(cy8), "+v"(k0), "+v"(k1), "+v"(k2), "+s"(c0), "+s"(c1), "+s"(f0), "+s"(f1));
    __syncthreads();
    const u32x4 a1 = pa4[0], a2 = pa4[16];
    SCHED_FENCE();
    u32x2 gg = *pgt;
    SCHED_FENCE();
#define MFMA(a, b, cc) __builtin_amdgcn_mfma_f32_16x16x32_bf16(__builtin_bit_cast(bf16x8_t, a), __builtin_bit_cast(bf16x8_t, b), cc, 0, 0, 0)
    const f32x4 z4 = {0.f, 0.f, 0.f, 0.f};
    const f32x4 d2 = MFMA(a1, wb[2], z4), d3 = MFMA(a1, wb[3], z4);
    SCHED_FENCE();
    f32x4 d0 = MFMA(a1, wb[0], z4), d1 = MFMA(a1, wb[1], z4);
    SCHED_FENCE();
    d0 = MFMA(a2, wb[4], d0);
    SCHED_FENCE();
    d1 = MFMA(a2, wb[5], d1);
    SCHED_FENCE();
    if (lane < 16) {
      const float q = ((d2[2] + d3[3]) + d0[0]) + d1[1];
      const bool bad = !(fabsf(q) < f0);
      const u64 db = __builtin_bit_cast(u64, (double)(bad ? 0.f : q) + c0);
      const unsigned fl = (bad ? k1 : k0) + (fabsf(q) < f1 ? k2 : 0u);
      const u64 add = ((u64)((unsigned)(db >> 32) + fl) << 32) | (unsigned)db;
      const u64 old = __hip_atomic_fetch_add(pa, add, __ATOMIC_RELAXED, __HIP_MEMORY_SCOPE_AGENT);
      __builtin_amdgcn_raw_buffer_store_b64(u32x2{fbits(q), tag}, rws, cy8, (G3Y + h * 320) * 16, 16);
      SCHED_FENCE();
      unsigned lim = (gg[1] == tag && !bad) ? (32u << 17) - (fl - k0) : 0u;
      asm volatile("" : "+v"(lim));
      SCHED_FENCE();
      float sv = (float)(__builtin_bit_cast(double, (old & ((1ull << 49) - 1ull)) + db) + c1);
      asm("" : "+v"(sv));
      unsigned res = bfbits(xv + bfr(bfr(sv) * bitsf(gg[0])));
      const unsigned rel = (unsigned)(old >> 32) - (31u << 27);
      TIE(res);
      if (rel < lim) *pout = res;
      SCHED_FENCE();
      asm volatile(".Lfd_end:");
      if ((old >> 59) == 31u) {
        if (__builtin_expect(rel >= lim, 0)) {
          if (!fx_exact(old, add)) SLOWSUM(sv, G3Y, cy);
          for (unsigned spin = 0; gg[1] != tag && spin < SPIN_MAX; ++spin) {
            MEMBAR();
            gg = __builtin_amdgcn_raw_buffer_load_b64(rws, cy8, GG * 16, 16);
          }
          *pout = bfbits(xv + bfr(bfr(sv) * bitsf(gg[0])));
        }
        if (cy == 0)
          __hip_atomic_store(reinterpret_cast<unsigned*>(gw + 2 * GEP), tag, __ATOMIC_RELAXED, __HIP_MEMORY_SCOPE_AGENT);
      }
    }
  }
}

}

extern "C" void kernel_launch(void* const* d_in, const int* in_sizes, int n_in, void* d_out, int out_size, void* d_ws,
                              size_t ws_size, hipStream_t stream) {
  const bf16* x = (const bf16*)d_in[0];
  const bf16* kc = (const bf16*)d_in[1];
  const bf16* vc = (const bf16*)d_in[2];
  const bf16* wq = (const bf16*)d_in[3];
  const bf16* wo = (const bf16*)d_in[4];
  const bf16* w1 = (const bf16*)d_in[6];
  const bf16* w3 = (const bf16*)d_in[7];
  const bf16* w2 = (const bf16*)d_in[8];
  u64* gw = (u64*)d_ws;
  fused_decode<<<NWG, NT, 0, stream>>>(x, kc, vc, wq, wo, w1, w3, w2, gw, (bf16*)d_out);
}
```

```cpp
#include <hip/hip_runtime.h>
#include <hip/hip_bf16.h>
#include <stdint.h>

typedef __hip_bfloat16 bf16;
typedef unsigned long long u64;
typedef __attribute__((address_space(1))) unsigned long long gu64;
typedef __attribute__((address_space(1))) unsigned short gu16;
typedef unsigned u32x4 __attribute__((ext_vector_type(4)));
typedef unsigned u32x2 __attribute__((ext_vector_type(2)));
typedef float f32x4 __attribute__((ext_vector_type(4)));
typedef __bf16 bf16x2_t __attribute__((ext_vector_type(2)));
typedef __bf16 bf16x8_t __attribute__((ext_vector_type(8)));
typedef short v4i16_t __attribute__((ext_vector_type(4)));
typedef unsigned long long u64x8 __attribute__((ext_vector_type(8)));
typedef unsigned long long u64x2 __attribute__((ext_vector_type(2)));
typedef __attribute__((address_space(1))) const u32x4 gu32x4;

namespace {

constexpr int D = 640, HQ = 32, DH = 192, SK = 32768, WIN = 4096, FF = 1536;
constexpr int NSP = 8;
constexpr int NWG = HQ * NSP;
constexpr int NT = 512;
constexpr float SCALE = 0.07216878364870322f;
constexpr float LOG2E = 1.4426950408889634f;
constexpr unsigned SPIN_MAX = 1u << 16;
constexpr int WND = 8;
constexpr int WOL = 7;
constexpr int NF = 7;
#define DXT 2, -1, 2, -1, 1, -3, 1, -1
constexpr int DX[8] = {DXT};
constexpr unsigned dx_pack() {
  unsigned r = 0;
  for (int i = 0; i < 8; ++i) r |= (unsigned)(DX[i] + 4) << (4 * i);
  return r;
}
constexpr unsigned dx_pos() {
  unsigned r = 0, a = 0;
  for (int i = 0; i < 8; ++i) { r |= a << (4 * i); a += DX[i] > 0 ? DX[i] : 0; }
  return r;
}
constexpr unsigned long long dx_don() {
  unsigned long long r = 0;
  int k = 0;
  for (int i = 0; i < 8; ++i)
    for (int q = 0; q < -DX[i]; ++q) r |= (unsigned long long)(i * 4 + q) << (5 * k++);
  return r;
}
constexpr bool dx_ok() {
  int a = 0, p = 0;
  bool in = true;
  for (int i = 0; i < 8; ++i) { a += DX[i]; p += DX[i] > 0 ? DX[i] : 0; in = in && DX[i] >= -4 && DX[i] <= 4; }
  return a == 0 && p <= 8 && in;
}
constexpr int dx_check[dx_ok() ? 1 : -1] = {0};
constexpr int KE = 8;
#define PD1X 24
#define PS1 1
constexpr int PD1 = PD1X;
#define WOP 2, 2, 2, 2, 2, 4, 6, 8, 8, 8
constexpr int WON[10] = {WOP};
#define QS 1
#define SSL 1, 2, 3, 4, 5, 6, 8, 7, 7
#define XS 9
#define ZSL 12
#define GSL 20
constexpr int SS[9] = {SSL};
constexpr bool ss_ok() {
  bool ok = true;
  for (int t = 0; t < 9; ++t) ok = ok && SS[t] >= QS && SS[t] <= XS && SS[t] >= (t < 7 ? 3 * t - 10 : 5);
  return ok && ZSL > XS && GSL > ZSL && GSL < 21;
}
constexpr int ss_check[ss_ok() ? 1 : -1] = {0};
constexpr int PVN(int m) {
  return m < XS + 3 ? 0 : (3 * (m - XS - 2) < m - 7 ? 3 * (m - XS - 2) : m - 7);
}

constexpr int G1 = 0;
constexpr int G1F = G1 + HQ * 288;
constexpr int G2 = G1F + 8 * 960;
constexpr int G3Y = G2 + NWG * 104;
constexpr int G3Z = G3Y + 320 * 32;
constexpr int GG = G3Z + 320 * 32;
constexpr int GZF = GG + 320;
constexpr int GA = GZF + 256;
constexpr int GEP = GA + 640;
constexpr int GEND = GEP + 8;

__device__ __forceinline__ unsigned fbits(float x) { return __builtin_bit_cast(unsigned, x); }
__device__ __forceinline__ float bitsf(unsigned x) { return __builtin_bit_cast(float, x); }
#define gok2(v) ((v)[1] == tag && (v)[3] == tag)
#define gst2(unit, a, b) __builtin_amdgcn_raw_buffer_store_b128(u32x4{fbits(a), tag, fbits(b), tag}, rws, (unit) * 16, 0, 16)
#define gld2(unit, cunit) __builtin_amdgcn_raw_buffer_load_b128(rws, (unit) * 16, (cunit) * 16, 16)
#define MEMBAR() asm volatile("" ::: "memory")
typedef float f32x2 __attribute__((ext_vector_type(2)));

__device__ __forceinline__ float bf_lo(unsigned u) { return __builtin_bit_cast(float, u << 16); }
__device__ __forceinline__ float bf_hi(unsigned u) { return __builtin_bit_cast(float, u & 0xffff0000u); }
__device__ __forceinline__ float bfr(float x) { return __bfloat162float(__float2bfloat16(x)); }
__device__ __forceinline__ unsigned short bfbits(float x) {
  return __builtin_bit_cast(unsigned short, __float2bfloat16(x));
}
__device__ __forceinline__ float dot2(unsigned a, unsigned b, float c) {
  return __builtin_amdgcn_fdot2_f32_bf16(__builtin_bit_cast(bf16x2_t, a), __builtin_bit_cast(bf16x2_t, b), c, false);
}
template <int CTRL>
__device__ __forceinline__ float dpp(float x) {
  return __builtin_bit_cast(float, __builtin_amdgcn_mov_dpp(__builtin_bit_cast(int, x), CTRL, 0xf, 0xf, true));
}
__device__ __forceinline__ float grp8_sum(float x) {
  x += dpp<0xB1>(x);
  x += dpp<0x4E>(x);
  x += dpp<0x141>(x);
  return x;
}
__device__ __forceinline__ float swap16_add(float a, float b) {
  auto s = __builtin_amdgcn_permlane16_swap(fbits(a), fbits(b), false, false);
  return bitsf((unsigned)s[0]) + bitsf((unsigned)s[1]);
}
__device__ __forceinline__ float swap32_add(float a, float b) {
  auto t = __builtin_amdgcn_permlane32_swap(fbits(a), fbits(b), false, false);
  return bitsf((unsigned)t[0]) + bitsf((unsigned)t[1]);
}
__device__ __forceinline__ float xg_sum(float x) {
  x += dpp<0x128>(x);
  x = swap16_add(x, x);
  return swap32_add(x, x);
}
__device__ __forceinline__ float xg_max(float x) {
  x = fmaxf(x, dpp<0x128>(x));
  auto s = __builtin_amdgcn_permlane16_swap(fbits(x), fbits(x), false, false);
  x = fmaxf(bitsf((unsigned)s[0]), bitsf((unsigned)s[1]));
  auto t = __builtin_amdgcn_permlane32_swap(fbits(x), fbits(x), false, false);
  return fmaxf(bitsf((unsigned)t[0]), bitsf((unsigned)t[1]));
}
template <int NJ>
__device__ __forceinline__ void xg_scatter(const float (&a)[8 * NJ], float (&r)[NJ], const bool b3) {
#pragma unroll
  for (int j = 0; j < NJ; ++j) {
    float p[4];
#pragma unroll
    for (int e = 0; e < 4; ++e) p[e] = swap32_add(a[8 * j + e], a[8 * j + e + 4]);
    const float q0 = swap16_add(p[0], p[2]), q1 = swap16_add(p[1], p[3]);
    r[j] = (b3 ? q1 : q0) + dpp<0x128>(b3 ? q0 : q1);
  }
}
__device__ __forceinline__ float wave_sum(float x) { return xg_sum(grp8_sum(x)); }
__device__ __forceinline__ float ex2(float x) { return __builtin_amdgcn_exp2f(x); }
__device__ __forceinline__ float silu(float x) { return x / (1.0f + __expf(-x)); }
__device__ __forceinline__ float rdlane(float x, int l) {
  return __builtin_bit_cast(float, __builtin_amdgcn_readlane(__builtin_bit_cast(int, x), l));
}

__device__ __forceinline__ u64 fx_enc(float v) {
  const bool bad = !(fabsf(v) < 512.f), small = fabsf(v) < 0x1p-9f;
  const u64 d = __builtin_bit_cast(u64, (double)(bad ? 0.f : v) + (262144.0 + 512.0));
  const unsigned hi = (unsigned)(d >> 32) + ((1u << 27) - (0x411u << 20)) + (bad ? 1u << 22 : 0u) + (small ? 1u << 17 : 0u);
  return ((u64)hi << 32) | (unsigned)d;
}
__device__ __forceinline__ float fx_dec(u64 t) {
  const unsigned hi = ((unsigned)(t >> 32) & 0x1ffffu) | (0x411u << 20);
  return (float)(__builtin_bit_cast(double, ((u64)hi << 32) | (unsigned)t) - (262144.0 + 32 * 512.0));
}
__device__ __forceinline__ bool fx_exact(u64 old, u64 add) {
  const unsigned o = (unsigned)(old >> 49) & 0x3ffu, a = (unsigned)(add >> 49) & 0x3ffu;
  return o + a < 32u;
}
template <int N> struct IC { static constexpr int value = N; };
#define SCHED_FENCE() __builtin_amdgcn_sched_barrier(0)
#define ALD(dst, rs, vo, so, imm) \
  asm volatile("buffer_load_dwordx4 %0, %1, %2, %3 offen offset:%4 nt" : "=v"(dst) : "v"(vo), "s"(rs), "s"(so), "n"(imm) : "memory")
#define AWL(dst, rs, vo, so, imm)                                                                                      \
  asm volatile("s_waitcnt vmcnt(8)\n\ts_cmp_le_i32 %5, 8\n\ts_cbranch_scc0 4\n\ts_waitcnt vmcnt(7)\n\t"                 \
               "s_cmp_le_i32 %5, 7\n\ts_cbranch_scc0 1\n\ts_waitcnt vmcnt(6)\n\t"                                        \
               "buffer_load_dwordx4 %0, %1, %2, %3 offen offset:%4 nt"                                                 \
               : "=v"(dst) : "v"(vo), "s"(rs), "s"(so), "n"(imm), "s"(wl) : "memory", "scc")
constexpr int TOFF = 1 << 14;
#define AWT(dst, rs, vo, so, imm, tref, lim)                                                                            \
  asm volatile("s_memrealtime s[98:99]\n\ts_waitcnt vmcnt(8)\n"                                                          \
               "8:\n\ts_waitcnt lgkmcnt(0)\n\ts_mul_i32 s98, s98, %7\n\ts_sub_u32 s98, s98, %5\n\ts_cmp_ge_u32 s98, %6\n\t" \
               "s_cbranch_scc1 9f\n\ts_sleep 1\n\ts_memrealtime s[98:99]\n\ts_branch 8b\n"                               \
               "9:\n\tbuffer_load_dwordx4 %0, %1, %2, %3 offen offset:%4 nt"                                            \
               : "=v"(dst) : "v"(vo), "s"(rs), "s"(so), "n"(imm), "s"(tref), "n"((lim) + TOFF), "s"(nt)                  \
               : "memory", "scc", "s98", "s99")
#define TA 238
#define TB 28
#define TE (TA + 47 * TB)
constexpr int te_check[(TE + 64) * 9 < TOFF ? 1 : -1] = {0};
#define AWAIT(v, n) asm volatile("s_waitcnt vmcnt(%1)" : "+v"(v) : "n"(n) : "memory")
#define TIE(v) asm volatile("" : "+v"(v))
__device__ __forceinline__ u32x4 mkrs(const void* p, unsigned bytes) {
  const u64 a = (u64)p;
  return u32x4{(unsigned)a, (unsigned)(a >> 32) & 0xffffu, bytes, 0x00020000u};
}
#define WAVE_SYNC()                                        \
  do {                                                     \
    __builtin_amdgcn_fence(__ATOMIC_RELEASE, "wavefront"); \
    __builtin_amdgcn_wave_barrier();                       \
    __builtin_amdgcn_fence(__ATOMIC_ACQUIRE, "wavefront"); \
  } while (0)

constexpr int L_WO = 0;
constexpr int L_XS = L_WO + DH * 40;
constexpr int PS = 256;
constexpr int L_PART = L_XS + 4 * 320;
constexpr int L_XN = L_PART + 8 * PS;
constexpr int L_ZP = L_XN + 4 * 64;
constexpr int L_QS = L_ZP + 6 * 80;
constexpr int L_ML = L_QS + 96;
constexpr int L_O = L_ML + 16;
constexpr int L_H = L_O + 128;
constexpr int L_GU = L_H + 48;
constexpr int L_GT = L_GU + 96;
constexpr int L_FLG = L_GT + 168 * 4;
constexpr int L_END = L_FLG + 4;

__global__ void __launch_bounds__(NT, 2)
fused_decode(const bf16* x_, const bf16* kc_, const bf16* vc_, const bf16* wq_, const bf16* wo_, const bf16* w1_, const bf16* w3_,
             const bf16* w2_, u64* gw_, bf16* out_) {
  __shared__ __attribute__((aligned(16))) float lds[L_END];
  u64x8 ka;
  u64x2 kb;
  asm volatile("s_load_dwordx16 %0, %2, 0\n\ts_load_dwordx4 %1, %2, 0x40" : "=&s"(ka), "=&s"(kb) : "s"(__builtin_amdgcn_kernarg_segment_ptr()));
  SCHED_FENCE();
  const int b = blockIdx.x;
  const int xcd = b & 7, jj = b >> 3;
  const int h = xcd * 4 + (jj >> 3), sp = jj & 7;
  const int ha = jj, spa = xcd;
  const int w = __builtin_amdgcn_readfirstlane(threadIdx.x >> 6);
  const int tid = threadIdx.x, lane = tid & 63;
  const int g = lane >> 3, c = lane & 7;
  const int rec = ha * NSP + spa;
  unsigned* flg = reinterpret_cast<unsigned*>(lds + L_FLG);

  unsigned cpv = (jj & 7) * 4096 + lane * 64;
#define CPLD()                                                                                                              \
  asm volatile("s_cmp_lg_u32 %1, %2\n\ts_cbranch_scc1 7f\n"                                                                 \
               ".Lfd_beg:\n\ts_getpc_b64 s[96:97]\n\ts_and_b32 s97, s97, 0xffff\n\ts_mov_b32 s98, .Lfd_end-.Lfd_beg+184\n\t"   \
               "s_mov_b32 s99, 0x20000\n\tbuffer_load_dword %0, %0, s[96:99], 0 offen\n7:"                                   \
               : "+v"(cpv) : "s"(w + 8 * (jj & ~7)), "n"(4) : "memory", "scc", "s96", "s97", "s98", "s99")
  int kvo, kvb;
#define KVADR()                                                                  \
  do {                                                                           \
    int bo = b;                                                                  \
    asm volatile("" : "+s"(bo));                             \
    kvo = (((bo >> 3) * SK + (SK - WIN) + g) * DH + 8 * c) * 2;                  \
    kvb = ((bo & 7) * 8 + ((w + (bo >> 6)) & 7)) * 64 * DH * 2;                  \
  } while (0)
#define KVO(t) (kvb + (((t) + ha) & 7) * 8 * DH * 2)
#define KVOFF(t) ((t) == 8 ? ko8 : KVO(t))
#define KLD(t, j) ALD(K[t][j], rk, kvo, KVOFF(t), 128 * (j))
  unsigned ta, te;
#define KWL(t, j)                                                                            \
  do {                                                                                       \
    if ((t) >= NF) AWT(K[t][j], rk, kvo, KVOFF(t), 128 * (j), ta, (6 * (t) - 21 + (j)) * TB * 8); \
    else AWL(K[t][j], rk, kvo, KVOFF(t), 128 * (j));                                         \
  } while (0)
#define VWL(t, j)                                                                            \
  do {                                                                                       \
    if ((t) < NF) AWT(V[t][j], rv, kvo, KVOFF(t), 128 * (j), te, (3 * (t) + (j) - 20) * TB * 8); \
    else AWT(V[t][j], rv, kvo, KVOFF(t), 128 * (j), ta, (6 * (t) - 18 + (j)) * TB * 8);      \
  } while (0)
#define EPLD() asm volatile("buffer_load_dword %0, off, %1, %2" : "=v"(epoch) : "s"(mkrs(gw0, GEND * 16)), "s"(GEP * 16) : "memory")
  u32x4 K[NF + 2][3], V[NF + 2][3];

  int fjj, fp;
  bool fidle;
#define FPART()                                                                  \
  do {                                                                           \
    int bo = b;                                                                  \
    asm volatile("" : "+s"(bo));                             \
    fjj = (bo >> 3) - ((bo & 1) ? 19 : 8);                                       \
    fidle = (unsigned)fjj < 2u;                                                  \
    fp = (bo >> 3) - (fjj > 0 ? 2 : 0);              \
  } while (0)
  unsigned epoch, tag, ptv = 0;
  u64 tstart64;
#define ARGS(...)                                                                                                   \
  do {                                                                                                              \
    const unsigned fa = (unsigned)(size_t)(__attribute__((address_space(3))) float*)(lds + L_FLG);                  \
    asm volatile("ds_write_b128 %0, %1\n\ts_waitcnt lgkmcnt(0)" ::"v"(fa), "v"(u32x4{0u, 0u, 0u, 0u}), __VA_ARGS__ : "memory"); \
    tstart64 = __builtin_amdgcn_s_memrealtime();                                                                    \
    SCHED_FENCE();                                                                                                  \
  } while (0)
  auto arg = [](u64 a) __attribute__((always_inline)) {
    u64 r;
    asm volatile("s_mov_b64 %0, %1" : "=s"(r) : "s"(a));
    return r;
  };
  if (w < 4) {
    const int fw = 4 * sp + w, ct = (fw * 13) >> 7;
    const int r0 = 64 * (fw - 10 * (ct - (ct >> 1 & ct)));
    const int qo = ((r0 + 8 * g) * (HQ * DH) + h * DH + 64 * ct + 8 * c) * 2;
    const int xo = 16 * lane, xo2 = 16 * (lane & 15) + 1024;
    ARGS("v"(qo), "v"(xo), "v"(xo2));
    const u64 x0 = arg(ka[0]);
    const u32x4 xa = *(gu32x4*)(x0 + (unsigned)xo);
    const u32x4 xb = *(gu32x4*)(x0 + (unsigned)xo2);
    u32x4 WQ[8], F[4];
    int R;
    {
      const auto rq = __builtin_amdgcn_make_buffer_rsrc((void*)arg(ka[3]), (short)0, D * HQ * DH * 2, 0x00020000);
#pragma unroll
      for (int i = 0; i < 8; ++i) WQ[i] = __builtin_amdgcn_raw_buffer_load_b128(rq, qo, i * HQ * DH * 2, 2);
      SCHED_FENCE();
      FPART();
      const int fcol = (fp * 13) >> 6, frp = fp - 5 * fcol;
      R = 128 * frp + 32 * w;
      const int fo = ((R + 4 * g + (fidle ? D : 0)) * FF + 192 * xcd + 64 * (fcol >= 3 ? fcol - 3 : fcol) + 8 * c) * 2;
      const u64 pw1 = arg(ka[5]), pw3 = arg(ka[6]);
      const auto rf = __builtin_amdgcn_make_buffer_rsrc((void*)(fcol >= 3 ? pw3 : pw1), (short)0, D * FF * 2, 0x00020000);
#pragma unroll
      for (int i = 0; i < 4; ++i) F[i] = __builtin_amdgcn_raw_buffer_load_b128(rf, fo, i * FF * 2, 2);
    }
    void* gw0 = (void*)arg(kb[0]);
    if (fidle && tid < 40)
      __builtin_amdgcn_raw_buffer_store_b128(u32x4{0u, 0u, 0u, 0u}, __builtin_amdgcn_make_buffer_rsrc(gw0, (short)0, GEND * 16, 0x00020000), (GA + fjj * 320 + 40 * xcd + lane) * 16, 0, 16);
    SCHED_FENCE();
    EPLD();
    SCHED_FENCE();
    asm volatile("s_barrier" ::: "memory");
    SCHED_FENCE();
    unsigned short* xs = reinterpret_cast<unsigned short*>(lds + L_XS + w * 320);
    const bool lo16 = lane < 16;
    *reinterpret_cast<u32x4*>(xs + 8 * lane) = xa;
    if (lo16) *reinterpret_cast<u32x4*>(xs + 512 + 8 * lane) = xb;
    float xf[8], xg[8];
#pragma unroll
    for (int i = 0; i < 4; ++i) {
      xf[2 * i] = bf_lo(xa[i]);
      xf[2 * i + 1] = bf_hi(xa[i]);
      xg[2 * i] = bf_lo(xb[i]);
      xg[2 * i + 1] = bf_hi(xb[i]);
    }
    float sum = 0.f;
#pragma unroll
    for (int i = 0; i < 8; ++i) sum += xf[i] + (lo16 ? xg[i] : 0.f);
    sum = wave_sum(sum);
    const float mean = sum * (1.0f / D);
    float sq = 0.f;
#pragma unroll
    for (int i = 0; i < 8; ++i) {
      const float d1 = xf[i] - mean, d2 = xg[i] - mean;
      sq += d1 * d1 + (lo16 ? d2 * d2 : 0.f);
    }
    sq = wave_sum(sq);
    const float mb = bfr(mean);
    const float vb = bfr(sq * (1.0f / D));
    const float den = bfr(sqrtf(bfr(vb + 1e-5f)));
    WAVE_SYNC();
    auto xnorm = [&](int r) -> unsigned short { return bfbits(bfr(bf_lo(xs[r]) - mb) / den); };
    {
      unsigned short* xn = reinterpret_cast<unsigned short*>(lds + L_XN + w * 64);
      xn[lane] = xnorm((r0 < D ? r0 : 0) + lane);
      xn[64 + (lane & 31)] = xnorm(R + (lane & 31));
      WAVE_SYNC();
      const u32x4 XQ = *reinterpret_cast<const u32x4*>(xn + 8 * g);
      const u32x2 XF = *reinterpret_cast<const u32x2*>(xn + 64 + 4 * g);
      float acc[16], r2[2];
#pragma unroll
      for (int i = 0; i < 16; ++i) acc[i] = 0.f;
#pragma unroll
      for (int i = 0; i < 8; ++i) {
        const unsigned d = XQ[i >> 1], lo = (i & 1) ? d >> 16 : d & 0xffffu, hi = (i & 1) ? d & 0xffff0000u : d << 16;
#pragma unroll
        for (int k = 0; k < 4; ++k) {
          acc[2 * k] = dot2(WQ[i][k], lo, acc[2 * k]);
          acc[2 * k + 1] = dot2(WQ[i][k], hi, acc[2 * k + 1]);
        }
      }
#pragma unroll
      for (int i = 0; i < 4; ++i) {
        const unsigned d = XF[i >> 1], lo = (i & 1) ? d >> 16 : d & 0xffffu, hi = (i & 1) ? d & 0xffff0000u : d << 16;
#pragma unroll
        for (int k = 0; k < 4; ++k) {
          acc[8 + 2 * k] = dot2(F[i][k], lo, acc[8 + 2 * k]);
          acc[8 + 2 * k + 1] = dot2(F[i][k], hi, acc[8 + 2 * k + 1]);
        }
      }
      xg_scatter<2>(acc, r2, g & 1);
#pragma unroll
      for (int j = 0; j < 2; ++j) lds[L_PART + w * 128 + 64 * j + 8 * c + g] = r2[j];
    }
    asm volatile("s_waitcnt lgkmcnt(0)" ::: "memory");
    if (lane == 0) __hip_atomic_fetch_add(flg, 1u, __ATOMIC_RELAXED, __HIP_MEMORY_SCOPE_WORKGROUP);
    SCHED_FENCE();
  } else {
    CPLD();
    SCHED_FENCE();
    KVADR();
    const int ks0 = KVO(0), ks1 = KVO(1), ks2 = KVO(2);
    ARGS("v"(kvo), "s"(ks0), "s"(ks1), "s"(ks2));
    const u32x4 rk0 = mkrs((void*)arg(ka[1]), HQ * SK * DH * 2);
#pragma unroll
    for (int m = 0; m < KE; ++m) {
      ALD(K[m / 3][m % 3], rk0, kvo, m < 3 ? ks0 : m < 6 ? ks1 : ks2, 128 * (m % 3));
    }
    SCHED_FENCE();
    void* gw0 = (void*)arg(kb[0]);
    EPLD();
    SCHED_FENCE();
#define PT 0
    if (PT) {
      const u64 pwo = arg(ka[4]), pou = arg(kb[1]);
      const bool fo = w == 6;
      ptv = lane == 0 ? (fo ? 160 * sp : h * DH * D * 2) : (lane == 1 && !fo) ? (h * DH + DH - 1) * D * 2 : 0x7fffffff;
      if (((PT & 1) && w == 5) || ((PT & 2) && w == 6))
        asm volatile("buffer_load_dword %0, %0, %1, 0 offen" : "+v"(ptv) : "s"(mkrs((void*)(fo ? pou : pwo), fo ? D * 2 : HQ * DH * D * 2)) : "memory");
    }
    SCHED_FENCE();
    asm volatile("s_barrier" ::: "memory");
    SCHED_FENCE();
  }
  asm volatile("" : "+s"(ka), "+s"(kb));
  const bf16 *kc = (const bf16*)ka[1], *vc = (const bf16*)ka[2], *wo = (const bf16*)ka[4], *w2 = (const bf16*)ka[7];
  u64* gw = (u64*)kb[0];
  bf16* out = (bf16*)kb[1];
  const auto rws = __builtin_amdgcn_make_buffer_rsrc((void*)gw, (short)0, GEND * 16, 0x00020000);
  const u32x4 rk = mkrs(kc, HQ * SK * DH * 2), rv = mkrs(vc, HQ * SK * DH * 2);
  const u32x4 rwa = mkrs(gw, GEND * 16);
  KVADR();
  FPART();
  const int dx = (int)((dx_pack() >> (4 * xcd)) & 15u) - 4;
  const int wr = w < 4 ? (w + 3) & 3 : w;
  const int nt = __builtin_amdgcn_readfirstlane(8 + ((unsigned)(wr - dx) >> 31) - ((unsigned)(7 - w + dx) >> 31));
  const int don = (int)(dx_don() >> (5 * (((dx_pos() >> (4 * xcd)) + wr) & 7u))) & 31;
  const int ko8 = (((don >> 2) * 8 + ((7 - (don & 3) + (ha >> 3)) & 7)) * 8 + ((7 + ha) & 7)) * 8 * DH * 2;
  const int wl = __builtin_amdgcn_readfirstlane((w < 4 ? WOL : WND) + (nt - 8) + (dx < 0 ? 1 : 0));
  const int lz = lane < 60 ? lane : 59;
  const int zr = lz / 10, zc = lz - 10 * zr;
  int w2o = (zr * D + 8 * zc) * 2;
  asm volatile("" : "+v"(w2o));
  if (w == 0) {
    for (unsigned spin = 0; spin < (SPIN_MAX << 4); ++spin) {
      if (__hip_atomic_load(flg, __ATOMIC_RELAXED, __HIP_MEMORY_SCOPE_WORKGROUP) == 4u) break;
      __builtin_amdgcn_s_sleep(1);
    }
    asm volatile("s_waitcnt lgkmcnt(0)" ::: "memory");
    AWAIT(epoch, 0);
    tag = __builtin_amdgcn_readfirstlane(epoch) + 1u;
    const f32x2* pp = reinterpret_cast<const f32x2*>(lds + L_PART) + lane;
    const f32x2 a01 = pp[0] + pp[64], a23 = pp[128] + pp[192];
    const bool two = sp == 2 && lane < 32;
    const f32x2 sm = two ? a01 : a01 + a23;
    const int ct0 = sp >= 5 ? 2 : sp >= 3 ? 1 : 0, slot = sp - (sp >= 5 ? 5 : sp >= 3 ? 2 : 0);
    const int uf = fidle ? GZF + 32 * xcd + 16 * fjj : G1F + xcd * 960 + fp * 32;
    const int un = lane < 32 ? G1 + h * 288 + slot * 96 + ct0 * 32 + lane : uf + lane - 32;
    if (lane < 33 || !fidle) gst2(un, sm[0], sm[1]);
    if (two) gst2(G1 + h * 288 + 32 + lane, a23[0], a23[1]);
    SCHED_FENCE();
  }
  asm volatile("" : "+s"(tstart64));
#define PHW 28
  const unsigned tstart = (unsigned)tstart64 - ((((w & 3) * 2 + (w >> 2)) * PHW) >> 3);
  ta = (tstart + TA) * nt - TOFF;
  te = (tstart + TE) * nt - TOFF;
  int wop = w;
  asm volatile("" : "+s"(wop));
  if (wop != 0) {
    if (w < 4) {
#pragma unroll
      for (int m = 0; m < KE; ++m) KWL(m / 3, m % 3);
    }
#pragma unroll
    for (int m = KE; m < 3 * NF; ++m) {
      KWL(m / 3, m % 3);
    }
    TIE(epoch);
    TIE(cpv);
    TIE(ptv);
    tag = __builtin_amdgcn_readfirstlane(epoch) + 1u;
    SCHED_FENCE();
  } else {
    u32x4 W2[8];
    const auto rw2 = __builtin_amdgcn_make_buffer_rsrc((void*)(w2 + (size_t)48 * h * D + 80 * sp), (short)0, 48 * D * 2, 0x00020000);
#pragma unroll
    for (int i = 0; i < 8; ++i) W2[i] = __builtin_amdgcn_raw_buffer_load_b128(rw2, w2o, i * 6 * D * 2, 2);
#pragma unroll
    for (int t = 0; t < NF; ++t)
#pragma unroll
      for (int j = 0; j < 3; ++j) KLD(t, j);
    SCHED_FENCE();
    const int row = lane >> 4, l16 = lane & 15;
    const int gb = G1 + ha * 288 + lane;
    const int gc = row < 3 ? G1 + ha * 288 + row * 96 + 64 + l16 : GZF + 32 * sp;
    const int l48 = lane < 48 ? lane : 47, fm = l48 >= 24 ? 1 : 0, fpr = 24 * (h & 3) + l48 - 24 * fm;
    const int gf = G1F + xcd * 960 + (3 * fm + (fpr >> 5)) * 160 + (fpr & 31);
    u32x4 va[3], vb[2], vf[5];
    for (unsigned spin = 0;; ++spin) {
      MEMBAR();
#pragma unroll
      for (int s = 0; s < 3; ++s) va[s] = gld2(gb, s * 96);
#pragma unroll
      for (int s = 0; s < 2; ++s) vb[s] = gld2(gc, s * 16);
#pragma unroll
      for (int s = 0; s < 5; ++s) vf[s] = gld2(gf, s * 32);
      bool ok = gok2(vb[0]) && gok2(vb[1]) && gok2(vf[3]) && gok2(vf[4]);
#pragma unroll
      for (int s = 0; s < 3; ++s) ok = ok && gok2(va[s]) && gok2(vf[s]);
      if (__all(ok) || spin >= SPIN_MAX) break;
      __builtin_amdgcn_s_sleep(1);
    }
    {
      const float a0 = (bitsf(va[0][0]) + bitsf(va[1][0])) + bitsf(va[2][0]), a1 = (bitsf(va[0][2]) + bitsf(va[1][2])) + bitsf(va[2][2]);
      reinterpret_cast<unsigned*>(lds + L_QS)[lane] = (unsigned)bfbits(a0) | ((unsigned)bfbits(a1) << 16);
      const float p0 = swap16_add(row < 3 ? bitsf(vb[0][0]) : 0.f, row < 3 ? bitsf(vb[0][2]) : 0.f);
      const float p1 = swap16_add(row < 3 ? bitsf(vb[1][0]) : 0.f, row < 3 ? bitsf(vb[1][2]) : 0.f);
      const float bq = swap32_add(p0, p1);
      reinterpret_cast<unsigned short*>(lds + L_QS)[2 * (64 + 16 * (row >> 1) + l16) + (row & 1)] = bfbits(bq);
    }
    asm volatile("s_waitcnt lgkmcnt(0)" ::: "memory");
    if (lane == 0) __hip_atomic_store(flg + 1, 1u, __ATOMIC_RELAXED, __HIP_MEMORY_SCOPE_WORKGROUP);
    SCHED_FENCE();
    {
      const float f0 = ((bitsf(vf[0][0]) + bitsf(vf[1][0])) + (bitsf(vf[2][0]) + bitsf(vf[3][0]))) + bitsf(vf[4][0]);
      const float f1 = ((bitsf(vf[0][2]) + bitsf(vf[1][2])) + (bitsf(vf[2][2]) + bitsf(vf[3][2]))) + bitsf(vf[4][2]);
      *reinterpret_cast<f32x2*>(lds + L_GU + 2 * l48) = f32x2{bfr(f0), bfr(f1)};
    }
    WAVE_SYNC();
    if (lane < 48) lds[L_H + lane] = bfr(bfr(silu(lds[L_GU + lane])) * lds[L_GU + 48 + lane]);
    WAVE_SYNC();
    {
      float z[8];
#pragma unroll
      for (int i = 0; i < 8; ++i) z[i] = 0.f;
#pragma unroll
      for (int i = 0; i < 8; ++i) {
        const unsigned chi = fbits(lds[L_H + zr + 6 * i]), clo = chi >> 16;
#pragma unroll
        for (int k = 0; k < 4; ++k) {
          z[2 * k] = dot2(W2[i][k], clo, z[2 * k]);
          z[2 * k + 1] = dot2(W2[i][k], chi, z[2 * k + 1]);
        }
      }
      if (lane < 60) {
        float* zp = lds + L_ZP + zr * 80 + 8 * zc;
        *reinterpret_cast<f32x4*>(zp) = f32x4{z[0], z[1], z[2], z[3]};
        *reinterpret_cast<f32x4*>(zp + 4) = f32x4{z[4], z[5], z[6], z[7]};
      }
    }
    SCHED_FENCE();
  }
  const int u = (tid & 255) < 240 ? (tid & 255) : 239;
  const int rg = u / 10, ch = u - 10 * rg;
  u32x4 WO[8];
  const u32x4 rwo = mkrs(wo + (size_t)DH * h * D + 80 * sp, DH * D * 2);
  const int wofs = (rg * D + 8 * ch) * 2;
#define WOLC(i)                                                                                                            \
  asm volatile("s_cmp_lt_u32 %4, 4\n\ts_cbranch_scc1 2\n\tbuffer_load_dwordx4 %0, %1, %2, %3 offen nt"                     \
               : "=v"(WO[i]) : "v"(wofs), "s"(rwo), "s"((i) * 24 * D * 2), "s"(w) : "memory", "scc")
#define VLD(t)                                                       \
  do {                                                               \
    _Pragma("unroll") for (int j = 0; j < 3; ++j) VWL(t, j);         \
  } while (0)
#pragma unroll
  for (int t = NF; t < NF + 2; ++t)
    if (t < nt) {
#pragma unroll
      for (int j = 0; j < 3; ++j) KWL(t, j);
      VLD(t);
    }
#define ZW 3
#define SLOWSUM(res, base, cc)                                                                                  \
  do {                                                                                                          \
    res = 0.f;                                                                                                  \
    _Pragma("unroll 1") for (int hh = 0; hh < HQ; ++hh) {                                                       \
      u32x2 v;                                                                                                  \
      for (unsigned spin = 0; spin < SPIN_MAX; ++spin) {                                                        \
        MEMBAR();                                                                                               \
        v = __builtin_amdgcn_raw_buffer_load_b64(rws, (cc) * 8, ((base) + hh * 320) * 16, 16);                  \
        if (v[1] == tag) break;                                                                                 \
      }                                                                                                         \
      res += bitsf(v[0]);                                                                                       \
    }                                                                                                           \
  } while (0)
#define ZPUB()                                                                                      \
  do {                                                                                              \
    const f32x2* zp = reinterpret_cast<const f32x2*>(lds + L_ZP) + (lane < 40 ? lane : 0);          \
    const f32x2 zs = ((zp[0] + zp[40]) + (zp[80] + zp[120])) + (zp[160] + zp[200]);                 \
    gst2(lane < 40 ? G3Z + h * 320 + 40 * sp + lane : GEND, zs[0], zs[1]);   \
  } while (0)
  u64 zold[2] = {0ull, 0ull}, zadd[2] = {0ull, 0ull};
  int mro = 8 * tid, mso = tid < 97 ? (G2 + rec * 104 + tid) * 16 : GEND * 16;
  u32x4 pub = {0u, tag, 0u, tag};
  asm volatile("" : "+v"(mro), "+v"(mso), "+v"(pub));
  {
    float Mg;
    float acc[3][8];
#pragma unroll
    for (int i = 0; i < 24; ++i) acc[i >> 3][i & 7] = 0.f;
    float* pw = lds + L_PART + w * PS + 8 * c + g;
    const bool b3 = g & 1;
#define PV3(t, j, cl, ch)                                                                                               \
  do {                                                                                                                  \
    if ((cl) < 0) asm volatile("" : "+v"(plo), "+v"(phi));                                                              \
    else if ((cl) == (ch)) asm volatile("s_waitcnt vmcnt(%2)" : "+v"(plo), "+v"(phi) : "n"((cl) < 0 ? 0 : (cl)) : "memory");  \
    else asm volatile("s_waitcnt vmcnt(%2)\n\ts_cmp_lt_u32 %4, 4\n\ts_cbranch_scc0 1\n\ts_waitcnt vmcnt(%3)"              \
                      : "+v"(plo), "+v"(phi) : "n"(ch), "n"((cl) < 0 ? 0 : (cl)), "s"(w) : "memory", "scc");            \
    _Pragma("unroll") for (int i = 0; i < 4; ++i) {                                                                     \
      acc[j][2 * i] = dot2(V[t][j][i], plo, acc[j][2 * i]);                                                             \
      acc[j][2 * i + 1] = dot2(V[t][j][i], phi, acc[j][2 * i + 1]);                                                     \
    }                                                                                                                   \
    if ((t) == NF - 1) {                                                          \
      float r1[1];                                                                                                      \
      xg_scatter<1>(acc[j], r1, b3);                                                                                    \
      pw[64 * (j)] = r1[0] * ew;                                                                                        \
    } else {                                                                                                            \
      asm volatile("" : "+v"(acc[j][0]), "+v"(acc[j][1]), "+v"(acc[j][2]), "+v"(acc[j][3]), "+v"(acc[j][4]),            \
                   "+v"(acc[j][5]), "+v"(acc[j][6]), "+v"(acc[j][7]));                       \
    }                                                                                                                   \
    SCHED_FENCE();                                                                                                      \
  } while (0)
#define PVT(t, c)                                                                                                       \
  do {                                                                                                                  \
    unsigned plo = pb[t], phi = plo << 16;                                                                              \
    PV3(t, 0, c, c);                                                                                                    \
    PV3(t, 1, -1, -1);                                                                                                  \
    PV3(t, 2, -1, -1);                                                                                                  \
  } while (0)
#define WGMAX()                                                                                                         \
  do {                                                                                                                  \
    for (unsigned spin = 0; spin < (SPIN_MAX << 4); ++spin) {                                                           \
      if (__hip_atomic_load(flg + 3, __ATOMIC_RELAXED, __HIP_MEMORY_SCOPE_WORKGROUP) == 8u) break;                      \
      __builtin_amdgcn_s_sleep(1);                                                                                      \
    }                                                                                                                   \
    MEMBAR();                                                                                                           \
    const f32x4 ma = *reinterpret_cast<const f32x4*>(lds + L_ML), mb2 = *reinterpret_cast<const f32x4*>(lds + L_ML + 4); \
    Mg = fmaxf(fmaxf(fmaxf(ma[0], ma[1]), fmaxf(ma[2], ma[3])), fmaxf(fmaxf(mb2[0], mb2[1]), fmaxf(mb2[2], mb2[3])));   \
    ew = ex2((mw - Mg) * LOG2E);                                                                                        \
    if (lane == 0) *reinterpret_cast<f32x2*>(lds + L_PART + w * PS + DH) = f32x2{w == 0 ? Mg : 0.f, lw * ew};           \
    SCHED_FENCE();                                                                                                      \
  } while (0)
    float ew, mw, lw;
    u32x4 qf[3];
    float sc[NF + 2];
    unsigned pb[NF + 2];
#define GATE(cnt)                                                                                                       \
  do {                                                                                                                  \
    asm volatile("s_waitcnt vmcnt(%2)" : "+v"(zold[0]), "+v"(zold[1]) : "n"(cnt) : "memory");                           \
    _Pragma("unroll") for (int k = 0; k < 2; ++k)                                                                       \
      if ((k == 0 || lane < 16) && (zold[k] >> 59) == 31u) {                                                            \
        const int cz = 80 * sp + 64 * k + lane;                                                                         \
        float f = fx_dec(zold[k] + zadd[k]);                                                                            \
        if (__builtin_expect(!fx_exact(zold[k], zadd[k]), 0)) SLOWSUM(f, G3Z, cz);                                      \
        __builtin_amdgcn_raw_buffer_store_b64(u32x2{fbits(bfr(silu(bfr(f)))), tag}, rws, cz * 8, GG * 16, 16);          \
      }                                                                                                                 \
  } while (0)
    auto slot = [&](auto mc) __attribute__((always_inline)) {
      constexpr int m = decltype(mc)::value;
      VWL(m / 3, m % 3);
      if (m == QS) {
        for (unsigned spin = 0; spin < (SPIN_MAX << 4); ++spin) {
          if (__hip_atomic_load(flg + 1, __ATOMIC_RELAXED, __HIP_MEMORY_SCOPE_WORKGROUP) != 0u) break;
          __builtin_amdgcn_s_sleep(1);
        }
        asm volatile("s_waitcnt lgkmcnt(0)" ::: "memory");
        SCHED_FENCE();
#pragma unroll
        for (int j = 0; j < 3; ++j) qf[j] = *reinterpret_cast<const u32x4*>(lds + L_QS + (c + 8 * j) * 4);
      }
#pragma unroll
      for (int t = 0; t < NF + 2; ++t)
        if (SS[t] == m) {
          sc[t] = -INFINITY;
          if (t < NF || t < nt) {
            float d = 0.f;
#pragma unroll
            for (int j = 0; j < 3; ++j) {
              TIE(K[t][j]);
#pragma unroll
              for (int i = 0; i < 4; ++i) d = dot2(K[t][j][i], qf[j][i], d);
            }
            d = grp8_sum(d);
            sc[t] = bfr(bfr(d) * SCALE);
          }
          TIE(sc[t]);
        }
      if (m == XS) {
        mw = sc[0];
#pragma unroll
        for (int t = 1; t < NF + 2; ++t) mw = fmaxf(mw, sc[t]);
        mw = xg_max(mw);
        if (lane == 0) {
          lds[L_ML + w] = mw;
          __hip_atomic_fetch_add(flg + 3, 1u, __ATOMIC_RELAXED, __HIP_MEMORY_SCOPE_WORKGROUP);
        }
        lw = 0.f;
#pragma unroll
        for (int t = 0; t < NF + 2; ++t) {
          const float p = ex2((sc[t] - mw) * LOG2E);
          lw += p;
          pb[t] = bfbits(p);
          TIE(pb[t]);
        }
        lw = xg_sum(lw);
        TIE(lw);
      }
      if (m == XS + 1 && NF < nt) PVT(NF, -1);
      if (m == XS + 2 && NF + 1 < nt) PVT(NF + 1, -1);
      if (m >= XS + 3) {
        constexpr int n0 = PVN(m - 1), n1 = PVN(m);
#pragma unroll
        for (int n = n0; n < n1; ++n) {
          unsigned plo = pb[n / 3], phi = plo << 16;
          PV3(n / 3, n % 3, n == m - 8 ? 8 : -1, n == m - 8 ? 8 : -1);
        }
      }
      if (m == ZSL) {
        WGMAX();
        if (w == ZW) {
#pragma unroll
          for (int k = 0; k < 2; ++k) {
            const float* zp = lds + L_ZP + (k ? 64 + (lane & 15) : lane);
            zold[k] = zadd[k] = fx_enc(((zp[0] + zp[80]) + (zp[160] + zp[240])) + (zp[320] + zp[400]));
          }
          const int za0 = (2 * GA + D + 80 * sp + lane) * 8, za1 = lane < 16 ? za0 + 512 : GEND * 16;
          ZPUB();
          asm volatile("buffer_atomic_add_x2 %0, %2, %4, 0 offen sc0\n\tbuffer_atomic_add_x2 %1, %3, %4, 0 offen sc0"
                       : "+v"(zold[0]), "+v"(zold[1]) : "v"(za0), "v"(za1), "s"(rwa) : "memory");
        }
      }
      if (m == GSL && w == ZW) GATE(GSL - ZSL);
      SCHED_FENCE();
    };
#define SL3(t) slot(IC<3 * (t)>{}); slot(IC<3 * (t) + 1>{}); slot(IC<3 * (t) + 2>{})
    SL3(0); SL3(1); SL3(2); SL3(3); SL3(4); SL3(5); SL3(6);
#pragma unroll
    for (int n = PVN(3 * NF - 1); n < 3 * NF; ++n) {
      const int m = n - (3 * NF - 9);
      if (m >= 0) {
#pragma unroll
        for (int i = (m && n > PVN(3 * NF - 1)) ? WON[m - 1] : 0; i < WON[m]; ++i) WOLC(i);
      }
      unsigned plo = pb[n / 3], phi = plo << 16;
      PV3(n / 3, n % 3, m < 0 ? -1 : 8 - m, m < 0 ? -1 : 8 - m + WON[m < 0 ? 0 : m]);
    }
#pragma unroll
    for (int i = WON[8]; i < WON[9]; ++i) WOLC(i);
  }
  __syncthreads();
  if (__builtin_expect(w < 2, 1)) {
    {
      f32x2 p2[8];
#pragma unroll
      for (int ww = 0; ww < 8; ++ww) p2[ww] = *reinterpret_cast<const f32x2*>(reinterpret_cast<const char*>(lds + L_PART + ww * PS) + mro);
      const f32x2 ua = (p2[0] + p2[1]) + (p2[2] + p2[3]), ub = (p2[4] + p2[5]) + (p2[6] + p2[7]);
      float ub1 = ub[1];
      asm("" : "+v"(ub1));
      pub[0] = fbits(ua[0] + ub[0]);
      pub[2] = fbits(ua[1] + ub1);
      __builtin_amdgcn_raw_buffer_store_b128(pub, rws, mso, 0, 16);
    }
    SCHED_FENCE();
    const int t2 = tid < 96 ? tid : 95;
    const int pb = (G2 + h * NSP * 104 + t2) * 16, pm = (G2 + (h * NSP + c) * 104 + 96) * 16;
    const unsigned po = (unsigned)(size_t)(__attribute__((address_space(3))) float*)(lds + L_O) + 4 * t2;
    const int pg = lane < 20 ? (GG + 40 * sp + 20 * w + lane) * 16 : GEND * 16;
    const unsigned pl = (unsigned)(size_t)(__attribute__((address_space(3))) float*)(lds + L_GT) + 16 * (lane < 20 ? 20 * w + lane : 40 + tid);
    if (PD1) __builtin_amdgcn_s_sleep(PD1);
    {
      unsigned e0, e1, e2, e3, e4, e5, e6, e7, cnt;
#define DPC " row_mask:0xf bank_mask:0xf\n\t"
#define CHK(d, a, b) "v_bitop3_b32 " d ", " a ", " b ", %13 bitop3:0x7e\n\t"
#define ACC2(e, a, b) "v_fmac_f32 v44, " e ", " a "\n\tv_fmac_f32 v45, " e ", " b "\n\t"
      asm volatile(
          "s_mov_b32 %8, %16\n"
          "1:\n\t"
          "buffer_load_dwordx4 v[48:51], %18, %12, 0 offen sc1\n\t"
          "buffer_load_dwordx4 v[4:7], %9, %12, 0 offen sc1\n\t"
          "buffer_load_dwordx4 v[8:11], %10, %12, 0 offen sc1\n\t"
          "buffer_load_dwordx4 v[12:15], %10, %12, 0 offen offset:1664 sc1\n\t"
          "buffer_load_dwordx4 v[16:19], %10, %12, 0 offen offset:3328 sc1\n\t"
          "buffer_load_dwordx4 v[20:23], %10, %12, %14 offen sc1\n\t"
          "buffer_load_dwordx4 v[24:27], %10, %12, %14 offen offset:1664 sc1\n\t"
          "buffer_load_dwordx4 v[28:31], %10, %12, %14 offen offset:3328 sc1\n\t"
          "buffer_load_dwordx4 v[32:35], %10, %12, %15 offen sc1\n\t"
          "buffer_load_dwordx4 v[36:39], %10, %12, %15 offen offset:1664 sc1\n\t"
          "s_sub_u32 %8, %8, 1\n\t"
          "s_waitcnt vmcnt(8)\n\t"
          CHK("v41", "v5", "v7")
          "ds_write_b128 %19, v[48:51]\n\t"
          "v_max_f32_dpp v40, v4, v4 quad_perm:[1,0,3,2]" DPC
          "s_waitcnt vmcnt(7)\n\t"
          CHK("v46", "v9", "v11")
          "v_max_f32_dpp v40, v40, v40 quad_perm:[2,3,0,1]" DPC
          "s_waitcnt vmcnt(6)\n\t"
          CHK("v47", "v13", "v15")
          "v_max_f32_dpp v40, v40, v40 row_half_mirror" DPC
          "v_sub_f32 v42, v4, v40\n\t"
          "v_mul_f32 v42, 0x3fb8aa3b, v42\n\t"
          "v_exp_f32 v42, v42\n\t"
          "v_or3_b32 v41, v41, v46, v47\n\t"
          "v_mul_f32 v40, v6, v42\n\t"
          "s_waitcnt vmcnt(5)\n\t"
          CHK("v46", "v17", "v19")
          "v_add_f32_dpp v40, v40, v40 quad_perm:[1,0,3,2]" DPC
          "s_waitcnt vmcnt(4)\n\t"
          CHK("v47", "v21", "v23")
          "v_add_f32_dpp v40, v40, v40 quad_perm:[2,3,0,1]" DPC
          "v_or3_b32 v41, v41, v46, v47\n\t"
          "s_nop 0\n\t"
          "v_add_f32_dpp v40, v40, v40 row_half_mirror" DPC
          "v_rcp_f32 v40, v40\n\t"
          "s_nop 0\n\t"
          "v_mul_f32 v42, v42, v40\n\t"
          "s_nop 0\n\t"
          "v_readlane_b32 %0, v42, 0\n\tv_readlane_b32 %1, v42, 1\n\tv_readlane_b32 %2, v42, 2\n\tv_readlane_b32 %3, v42, 3\n\t"
          "v_readlane_b32 %4, v42, 4\n\tv_readlane_b32 %5, v42, 5\n\tv_readlane_b32 %6, v42, 6\n\tv_readlane_b32 %7, v42, 7\n\t"
          "v_mul_f32 v44, %0, v8\n\tv_mul_f32 v45, %0, v10\n\t"
          ACC2("%1", "v12", "v14") ACC2("%2", "v16", "v18") ACC2("%3", "v20", "v22")
          "s_waitcnt vmcnt(3)\n\t"
          ACC2("%4", "v24", "v26") CHK("v46", "v25", "v27")
          "s_waitcnt vmcnt(2)\n\t"
          ACC2("%5", "v28", "v30") CHK("v47", "v29", "v31")
          "v_or3_b32 v41, v41, v46, v47\n\t"
          "s_waitcnt vmcnt(1)\n\t"
          ACC2("%6", "v32", "v34") CHK("v46", "v33", "v35")
          "s_waitcnt vmcnt(0)\n\t"
          ACC2("%7", "v36", "v38")
          "v_cvt_pk_bf16_f32 v44, v44, v45\n\t"
          "ds_write_b32 %11, v44\n\t"
          CHK("v47", "v37", "v39")
          "v_or3_b32 v41, v41, v46, v47\n\t"
          "v_cmp_ne_u32 vcc, 0, v41\n\t"
          "s_cbranch_vccz 2f\n\t"
          "s_sleep %17\n\t"
          "s_cbranch_scc0 1b\n"
          "2:\n\t"
          "s_waitcnt lgkmcnt(0)"
          : "=&s"(e0), "=&s"(e1), "=&s"(e2), "=&s"(e3), "=&s"(e4), "=&s"(e5), "=&s"(e6), "=&s"(e7), "=&s"(cnt)
          : "v"(pm), "v"(pb), "v"(po), "s"(rwa), "s"(tag), "s"(3 * 1664), "s"(6 * 1664), "n"(SPIN_MAX), "n"(PS1), "v"(pg), "v"(pl)
          : "memory", "scc", "vcc", "v4", "v5", "v6", "v7", "v8", "v9", "v10", "v11", "v12", "v13", "v14", "v15", "v16", "v17", "v18", "v19",
            "v20", "v21", "v22", "v23", "v24", "v25", "v26", "v27", "v28", "v29", "v30", "v31", "v32", "v33", "v34", "v35", "v36", "v37",
            "v38", "v39", "v40", "v41", "v42", "v44", "v45", "v46", "v47", "v48", "v49", "v50", "v51");
    }
    __syncthreads();
  } else if (w == 2) {
    __syncthreads();
  } else {
    if (w >= 4) {
#pragma unroll
      for (int i = WON[9]; i < 8; ++i) WOLC(i);
#pragma unroll
      for (int i = 0; i < 8; ++i) {
        AWAIT(WO[i], 7 - i);
        *reinterpret_cast<u32x4*>(lds + L_WO + u * 4 + i * 960) = WO[i];
      }
      asm volatile("s_waitcnt lgkmcnt(0)" ::: "memory");
      if (lane == 0) __hip_atomic_fetch_add(flg + 2, 1u, __ATOMIC_RELAXED, __HIP_MEMORY_SCOPE_WORKGROUP);
    }
    for (unsigned spin = 0; spin < (SPIN_MAX << 4); ++spin) {
      if (__hip_atomic_load(flg + 2, __ATOMIC_RELAXED, __HIP_MEMORY_SCOPE_WORKGROUP) == 4u) break;
      __builtin_amdgcn_s_sleep(1);
    }
    asm volatile("s_waitcnt lgkmcnt(0)" ::: "memory");
    u32x4 wb[6];
    {
      typedef __attribute__((address_space(3))) v4i16_t* ltr;
      const unsigned la = (unsigned)(size_t)(__attribute__((address_space(3))) float*)(lds + L_WO) + (8 * (lane >> 4) + ((lane >> 2) & 3)) * 160 + 32 * (w - 3) + 8 * (lane & 3);
#pragma unroll
      for (int k = 0; k < 6; ++k) {
        const u32x2 t0 = __builtin_bit_cast(u32x2, __builtin_amdgcn_ds_read_tr16_b64_v4i16((ltr)(la + 32 * k * 160)));
        const u32x2 t1 = __builtin_bit_cast(u32x2, __builtin_amdgcn_ds_read_tr16_b64_v4i16((ltr)(la + (32 * k + 4) * 160)));
        wb[k] = u32x4{t0[0], t0[1], t1[0], t1[1]};
      }
    }
    const int cy = 80 * sp + 16 * (w - 3) + (lane & 15);
    float xv = bf_lo(reinterpret_cast<const unsigned short*>(lds + L_XS)[cy]);
    gu64* pa = (gu64*)(gw + 2 * GA + cy);
    gu16* pout = (gu16*)(out + cy);
    typedef __attribute__((address_space(3))) const u32x4* la4;
    la4 pa4 = (la4)(lds + L_O) + 4 * (lane & 3) + (lane >> 4);
    int cy8 = cy * 8;
    typedef __attribute__((address_space(3))) const u32x2* la2;
    la2 pgt = (la2)(lds + L_GT) + 16 * (w - 3) + (lane & 15);
    asm volatile("" : "+v"(pgt));
    unsigned k0 = (1u << 27) - (0x411u << 20), k1 = k0 + (1u << 22), k2 = 1u << 17;
    double c0 = 262144.0 + 512.0, c1 = -(262144.0 + 32 * 512.0);
    float f0 = 512.f, f1 = 0x1p-9f;
    asm volatile("" : "+v"(wb[0]), "+v"(wb[1]), "+v"(wb[2]), "+v"(wb[3]), "+v"(wb[4]), "+v"(wb[5]));
    asm volatile("" : "+v"(xv), "+v"(pa), "+v"(pout), "+v"(pa4), "+v"(cy8), "+v"(k0), "+v"(k1), "+v"(k2), "+s"(c0), "+s"(c1), "+s"(f0), "+s"(f1));
    __syncthreads();
    const u32x4 a1 = pa4[0], a2 = pa4[16];
    SCHED_FENCE();
    u32x2 gg = *pgt;
    SCHED_FENCE();
#define MFMA(a, b, cc) __builtin_amdgcn_mfma_f32_16x16x32_bf16(__builtin_bit_cast(bf16x8_t, a), __builtin_bit_cast(bf16x8_t, b), cc, 0, 0, 0)
    const f32x4 z4 = {0.f, 0.f, 0.f, 0.f};
    const f32x4 d2 = MFMA(a1, wb[2], z4), d3 = MFMA(a1, wb[3], z4);
    SCHED_FENCE();
    f32x4 d0 = MFMA(a1, wb[0], z4), d1 = MFMA(a1, wb[1], z4);
    SCHED_FENCE();
    d0 = MFMA(a2, wb[4], d0);
    SCHED_FENCE();
    d1 = MFMA(a2, wb[5], d1);
    SCHED_FENCE();
    if (lane < 16) {
      const float q = ((d2[2] + d3[3]) + d0[0]) + d1[1];
      const bool bad = !(fabsf(q) < f0);
      const u64 db = __builtin_bit_cast(u64, (double)(bad ? 0.f : q) + c0);
      const unsigned fl = (bad ? k1 : k0) + (fabsf(q) < f1 ? k2 : 0u);
      const u64 add = ((u64)((unsigned)(db >> 32) + fl) << 32) | (unsigned)db;
      const u64 old = __hip_atomic_fetch_add(pa, add, __ATOMIC_RELAXED, __HIP_MEMORY_SCOPE_AGENT);
      __builtin_amdgcn_raw_buffer_store_b64(u32x2{fbits(q), tag}, rws, cy8, (G3Y + h * 320) * 16, 16);
      SCHED_FENCE();
      unsigned lim = (gg[1] == tag && !bad) ? (32u << 17) - (fl - k0) : 0u;
      asm volatile("" : "+v"(lim));
      SCHED_FENCE();
      float sv = (float)(__builtin_bit_cast(double, (old & ((1ull << 49) - 1ull)) + db) + c1);
      asm("" : "+v"(sv));
      unsigned res = bfbits(xv + bfr(bfr(sv) * bitsf(gg[0])));
      const unsigned rel = (unsigned)(old >> 32) - (31u << 27);
      TIE(res);
      if (rel < lim) *pout = res;
      SCHED_FENCE();
      asm volatile(".Lfd_end:");
      if ((old >> 59) == 31u) {
        if (__builtin_expect(rel >= lim, 0)) {
          if (!fx_exact(old, add)) SLOWSUM(sv, G3Y, cy);
          for (unsigned spin = 0; gg[1] != tag && spin < SPIN_MAX; ++spin) {
            MEMBAR();
            gg = __builtin_amdgcn_raw_buffer_load_b64(rws, cy8, GG * 16, 16);
          }
          *pout = bfbits(xv + bfr(bfr(sv) * bitsf(gg[0])));
        }
        if (cy == 0)
          __hip_atomic_store((__attribute__((address_space(1))) unsigned*)(gw + 2 * GEP), tag, __ATOMIC_RELAXED, __HIP_MEMORY_SCOPE_AGENT);
      }
    }
  }
}

}

extern "C" void kernel_launch(void* const* d_in, const int* in_sizes, int n_in, void* d_out, int out_size, void* d_ws,
                              size_t ws_size, hipStream_t stream) {
  const bf16* x = (const bf16*)d_in[0];
  const bf16* kc = (const bf16*)d_in[1];
  const bf16* vc = (const bf16*)d_in[2];
  const bf16* wq = (const bf16*)d_in[3];
  const bf16* wo = (const bf16*)d_in[4];
  const bf16* w1 = (const bf16*)d_in[6];
  const bf16* w3 = (const bf16*)d_in[7];
  const bf16* w2 = (const bf16*)d_in[8];
  u64* gw = (u64*)d_ws;
  fused_decode<<<NWG, NT, 0, stream>>>(x, kc, vc, wq, wo, w1, w3, w2, gw, (bf16*)d_out);
}
```

```cpp
#include <hip/hip_runtime.h>
#include <hip/hip_bf16.h>
#include <stdint.h>

typedef __hip_bfloat16 bf16;
typedef unsigned long long u64;
typedef __attribute__((address_space(1))) unsigned long long gu64;
typedef __attribute__((address_space(1))) unsigned short gu16;
typedef unsigned u32x4 __attribute__((ext_vector_type(4)));
typedef unsigned u32x2 __attribute__((ext_vector_type(2)));
typedef float f32x4 __attribute__((ext_vector_type(4)));
typedef __bf16 bf16x2_t __attribute__((ext_vector_type(2)));
typedef __bf16 bf16x8_t __attribute__((ext_vector_type(8)));
typedef short v4i16_t __attribute__((ext_vector_type(4)));
typedef unsigned long long u64x8 __attribute__((ext_vector_type(8)));
typedef unsigned long long u64x2 __attribute__((ext_vector_type(2)));
typedef __attribute__((address_space(1))) const u32x4 gu32x4;

namespace {

constexpr int D = 640, HQ = 32, DH = 192, SK = 32768, WIN = 4096, FF = 1536;
constexpr int NSP = 8;
constexpr int NWG = HQ * NSP;
constexpr int NT = 512;
constexpr float SCALE = 0.07216878364870322f;
constexpr float LOG2E = 1.4426950408889634f;
constexpr unsigned SPIN_MAX = 1u << 16;
constexpr int WND = 8;
constexpr int WOL = 7;
constexpr int NF = 7;
#define DXT 2, -1, 2, -1, 1, -3, 1, -1
constexpr int DX[8] = {DXT};
constexpr unsigned dx_pack() {
  unsigned r = 0;
  for (int i = 0; i < 8; ++i) r |= (unsigned)(DX[i] + 4) << (4 * i);
  return r;
}
constexpr unsigned dx_pos() {
  unsigned r = 0, a = 0;
  for (int i = 0; i < 8; ++i) { r |= a << (4 * i); a += DX[i] > 0 ? DX[i] : 0; }
  return r;
}
constexpr unsigned long long dx_don() {
  unsigned long long r = 0;
  int k = 0;
  for (int i = 0; i < 8; ++i)
    for (int q = 0; q < -DX[i]; ++q) r |= (unsigned long long)(i * 4 + q) << (5 * k++);
  return r;
}
constexpr bool dx_ok() {
  int a = 0, p = 0;
  bool in = true;
  for (int i = 0; i < 8; ++i) { a += DX[i]; p += DX[i] > 0 ? DX[i] : 0; in = in && DX[i] >= -4 && DX[i] <= 4; }
  return a == 0 && p <= 8 && in;
}
constexpr int dx_check[dx_ok() ? 1 : -1] = {0};
constexpr int KE = 8;
#define PD1X 24
#define PS1 1
constexpr int PD1 = PD1X;
#define WOP 2, 2, 2, 2, 2, 4, 6, 8, 8, 8
constexpr int WON[10] = {WOP};
#define QS 1
#define SSL 1, 2, 3, 4, 5, 6, 8, 7, 7
#define XS 9
#define ZSL 12
#define GSL 20
constexpr int SS[9] = {SSL};
constexpr bool ss_ok() {
  bool ok = true;
  for (int t = 0; t < 9; ++t) ok = ok && SS[t] >= QS && SS[t] <= XS && SS[t] >= (t < 7 ? 3 * t - 10 : 5);
  return ok && ZSL > XS && GSL > ZSL && GSL < 21;
}
constexpr int ss_check[ss_ok() ? 1 : -1] = {0};
constexpr int PVN(int m) {
  return m < XS + 3 ? 0 : (3 * (m - XS - 2) < m - 7 ? 3 * (m - XS - 2) : m - 7);
}

constexpr int G1 = 0;
constexpr int G1F = G1 + HQ * 288;
constexpr int G2 = G1F + 8 * 960;
constexpr int G3Y = G2 + NWG * 104;
constexpr int G3Z = G3Y + 320 * 32;
constexpr int GG = G3Z + 320 * 32;
constexpr int GZF = GG + 320;
constexpr int GA = GZF + 256;
constexpr int GEP = GA + 640;
constexpr int GEND = GEP + 8;

__device__ __forceinline__ unsigned fbits(float x) { return __builtin_bit_cast(unsigned, x); }
__device__ __forceinline__ float bitsf(unsigned x) { return __builtin_bit_cast(float, x); }
#define gok2(v) ((v)[1] == tag && (v)[3] == tag)
#define gst2(unit, a, b) __builtin_amdgcn_raw_buffer_store_b128(u32x4{fbits(a), tag, fbits(b), tag}, rws, (unit) * 16, 0, 16)
#define gld2(unit, cunit) __builtin_amdgcn_raw_buffer_load_b128(rws, (unit) * 16, (cunit) * 16, 16)
#define MEMBAR() asm volatile("" ::: "memory")
typedef float f32x2 __attribute__((ext_vector_type(2)));

__device__ __forceinline__ float bf_lo(unsigned u) { return __builtin_bit_cast(float, u << 16); }
__device__ __forceinline__ float bf_hi(unsigned u) { return __builtin_bit_cast(float, u & 0xffff0000u); }
__device__ __forceinline__ float bfr(float x) { return __bfloat162float(__float2bfloat16(x)); }
__device__ __forceinline__ unsigned short bfbits(float x) {
  return __builtin_bit_cast(unsigned short, __float2bfloat16(x));
}
__device__ __forceinline__ float dot2(unsigned a, unsigned b, float c) {
  return __builtin_amdgcn_fdot2_f32_bf16(__builtin_bit_cast(bf16x2_t, a), __builtin_bit_cast(bf16x2_t, b), c, false);
}
template <int CTRL>
__device__ __forceinline__ float dpp(float x) {
  return __builtin_bit_cast(float, __builtin_amdgcn_mov_dpp(__builtin_bit_cast(int, x), CTRL, 0xf, 0xf, true));
}
__device__ __forceinline__ float grp8_sum(float x) {
  x += dpp<0xB1>(x);
  x += dpp<0x4E>(x);
  x += dpp<0x141>(x);
  return x;
}
__device__ __forceinline__ float swap16_add(float a, float b) {
  auto s = __builtin_amdgcn_permlane16_swap(fbits(a), fbits(b), false, false);
  return bitsf((unsigned)s[0]) + bitsf((unsigned)s[1]);
}
__device__ __forceinline__ float swap32_add(float a, float b) {
  auto t = __builtin_amdgcn_permlane32_swap(fbits(a), fbits(b), false, false);
  return bitsf((unsigned)t[0]) + bitsf((unsigned)t[1]);
}
__device__ __forceinline__ float xg_sum(float x) {
  x += dpp<0x128>(x);
  x = swap16_add(x, x);
  return swap32_add(x, x);
}
__device__ __forceinline__ float xg_max(float x) {
  x = fmaxf(x, dpp<0x128>(x));
  auto s = __builtin_amdgcn_permlane16_swap(fbits(x), fbits(x), false, false);
  x = fmaxf(bitsf((unsigned)s[0]), bitsf((unsigned)s[1]));
  auto t = __builtin_amdgcn_permlane32_swap(fbits(x), fbits(x), false, false);
  return fmaxf(bitsf((unsigned)t[0]), bitsf((unsigned)t[1]));
}
template <int NJ>
__device__ __forceinline__ void xg_scatter(const float (&a)[8 * NJ], float (&r)[NJ], const bool b3) {
#pragma unroll
  for (int j = 0; j < NJ; ++j) {
    float p[4];
#pragma unroll
    for (int e = 0; e < 4; ++e) p[e] = swap32_add(a[8 * j + e], a[8 * j + e + 4]);
    const float q0 = swap16_add(p[0], p[2]), q1 = swap16_add(p[1], p[3]);
    r[j] = (b3 ? q1 : q0) + dpp<0x128>(b3 ? q0 : q1);
  }
}
__device__ __forceinline__ float wave_sum(float x) { return xg_sum(grp8_sum(x)); }
__device__ __forceinline__ float ex2(float x) { return __builtin_amdgcn_exp2f(x); }
__device__ __forceinline__ float silu(float x) { return x / (1.0f + __expf(-x)); }
__device__ __forceinline__ float rdlane(float x, int l) {
  return __builtin_bit_cast(float, __builtin_amdgcn_readlane(__builtin_bit_cast(int, x), l));
}

__device__ __forceinline__ u64 fx_enc(float v) {
  const bool bad = !(fabsf(v) < 512.f), small = fabsf(v) < 0x1p-9f;
  const u64 d = __builtin_bit_cast(u64, (double)(bad ? 0.f : v) + (262144.0 + 512.0));
  const unsigned hi = (unsigned)(d >> 32) + ((1u << 27) - (0x411u << 20)) + (bad ? 1u << 22 : 0u) + (small ? 1u << 17 : 0u);
  return ((u64)hi << 32) | (unsigned)d;
}
__device__ __forceinline__ float fx_dec(u64 t) {
  const unsigned hi = ((unsigned)(t >> 32) & 0x1ffffu) | (0x411u << 20);
  return (float)(__builtin_bit_cast(double, ((u64)hi << 32) | (unsigned)t) - (262144.0 + 32 * 512.0));
}
__device__ __forceinline__ bool fx_exact(u64 old, u64 add) {
  const unsigned o = (unsigned)(old >> 49) & 0x3ffu, a = (unsigned)(add >> 49) & 0x3ffu;
  return o + a < 32u;
}
template <int N> struct IC { static constexpr int value = N; };
#define SCHED_FENCE() __builtin_amdgcn_sched_barrier(0)
#define ALD(dst, rs, vo, so, imm) \
  asm volatile("buffer_load_dwordx4 %0, %1, %2, %3 offen offset:%4 nt" : "=v"(dst) : "v"(vo), "s"(rs), "s"(so), "n"(imm) : "memory")
#define AWL(dst, rs, vo, so, imm)                                                                                      \
  asm volatile("s_waitcnt vmcnt(8)\n\ts_cmp_le_i32 %5, 8\n\ts_cbranch_scc0 4\n\ts_waitcnt vmcnt(7)\n\t"                 \
               "s_cmp_le_i32 %5, 7\n\ts_cbranch_scc0 1\n\ts_waitcnt vmcnt(6)\n\t"                                        \
               "buffer_load_dwordx4 %0, %1, %2, %3 offen offset:%4 nt"                                                 \
               : "=v"(dst) : "v"(vo), "s"(rs), "s"(so), "n"(imm), "s"(wl) : "memory", "scc")
constexpr int TOFF = 1 << 14;
#define AWT(dst, rs, vo, so, imm, tref, lim)                                                                            \
  asm volatile("s_memrealtime s[98:99]\n\ts_waitcnt vmcnt(8)\n"                                                          \
               "8:\n\ts_waitcnt lgkmcnt(0)\n\ts_mul_i32 s98, s98, %7\n\ts_sub_u32 s98, s98, %5\n\ts_cmp_ge_u32 s98, %6\n\t" \
               "s_cbranch_scc1 9f\n\ts_sleep 1\n\ts_memrealtime s[98:99]\n\ts_branch 8b\n"                               \
               "9:\n\tbuffer_load_dwordx4 %0, %1, %2, %3 offen offset:%4 nt"                                            \
               : "=v"(dst) : "v"(vo), "s"(rs), "s"(so), "n"(imm), "s"(tref), "n"((lim) + TOFF), "s"(nt)                  \
               : "memory", "scc", "s98", "s99")
#define TA 245
#define TB 28
#define TE (TA + 47 * TB)
constexpr int te_check[(TE + 64) * 9 < TOFF ? 1 : -1] = {0};
#define AWAIT(v, n) asm volatile("s_waitcnt vmcnt(%1)" : "+v"(v) : "n"(n) : "memory")
#define TIE(v) asm volatile("" : "+v"(v))
__device__ __forceinline__ u32x4 mkrs(const void* p, unsigned bytes) {
  const u64 a = (u64)p;
  return u32x4{(unsigned)a, (unsigned)(a >> 32) & 0xffffu, bytes, 0x00020000u};
}
#define WAVE_SYNC()                                        \
  do {                                                     \
    __builtin_amdgcn_fence(__ATOMIC_RELEASE, "wavefront"); \
    __builtin_amdgcn_wave_barrier();                       \
    __builtin_amdgcn_fence(__ATOMIC_ACQUIRE, "wavefront"); \
  } while (0)

constexpr int L_WO = 0;
constexpr int L_XS = L_WO + DH * 40;
constexpr int PS = 256;
constexpr int L_PART = L_XS + 4 * 320;
constexpr int L_XN = L_PART + 8 * PS;
constexpr int L_ZP = L_XN + 4 * 64;
constexpr int L_QS = L_ZP + 6 * 80;
constexpr int L_ML = L_QS + 96;
constexpr int L_O = L_ML + 16;
constexpr int L_H = L_O + 128;
constexpr int L_GU = L_H + 48;
constexpr int L_GT = L_GU + 96;
constexpr int L_FLG = L_GT + 168 * 4;
constexpr int L_END = L_FLG + 4;

__global__ void __launch_bounds__(NT, 2)
fused_decode(const bf16* x_, const bf16* kc_, const bf16* vc_, const bf16* wq_, const bf16* wo_, const bf16* w1_, const bf16* w3_,
             const bf16* w2_, u64* gw_, bf16* out_) {
  __shared__ __attribute__((aligned(16))) float lds[L_END];
  u64x8 ka;
  u64x2 kb;
  asm volatile("s_load_dwordx16 %0, %2, 0\n\ts_load_dwordx4 %1, %2, 0x40" : "=&s"(ka), "=&s"(kb) : "s"(__builtin_amdgcn_kernarg_segment_ptr()));
  SCHED_FENCE();
  const int b = blockIdx.x;
  const int xcd = b & 7, jj = b >> 3;
  const int h = xcd * 4 + (jj >> 3), sp = jj & 7;
  const int ha = jj, spa = xcd;
  const int w = __builtin_amdgcn_readfirstlane(threadIdx.x >> 6);
  const int tid = threadIdx.x, lane = tid & 63;
  const int g = lane >> 3, c = lane & 7;
  const int rec = ha * NSP + spa;
  unsigned* flg = reinterpret_cast<unsigned*>(lds + L_FLG);

  unsigned cpv = (jj & 7) * 4096 + lane * 64;
#define CPLD()                                                                                                              \
  asm volatile("s_cmp_lg_u32 %1, %2\n\ts_cbranch_scc1 7f\n"                                                                 \
               ".Lfd_beg:\n\ts_getpc_b64 s[96:97]\n\ts_and_b32 s97, s97, 0xffff\n\ts_mov_b32 s98, .Lfd_end-.Lfd_beg+184\n\t"   \
               "s_mov_b32 s99, 0x20000\n\tbuffer_load_dword %0, %0, s[96:99], 0 offen\n7:"                                   \
               : "+v"(cpv) : "s"(w + 8 * (jj & ~7)), "n"(4) : "memory", "scc", "s96", "s97", "s98", "s99")
  int kvo, kvb;
#define KVADR()                                                                  \
  do {                                                                           \
    int bo = b;                                                                  \
    asm volatile("" : "+s"(bo));                             \
    kvo = (((bo >> 3) * SK + (SK - WIN) + g) * DH + 8 * c) * 2;                  \
    kvb = ((bo & 7) * 8 + ((w + (bo >> 6)) & 7)) * 64 * DH * 2;                  \
  } while (0)
#define KVO(t) (kvb + (((t) + ha) & 7) * 8 * DH * 2)
#define KVOFF(t) ((t) == 8 ? ko8 : KVO(t))
#define KLD(t, j) ALD(K[t][j], rk, kvo, KVOFF(t), 128 * (j))
  unsigned ta, te;
#define KWL(t, j)                                                                            \
  do {                                                                                       \
    if ((t) >= NF) AWT(K[t][j], rk, kvo, KVOFF(t), 128 * (j), ta, (6 * (t) - 21 + (j)) * TB * 8); \
    else AWL(K[t][j], rk, kvo, KVOFF(t), 128 * (j));                                         \
  } while (0)
#define VWL(t, j)                                                                            \
  do {                                                                                       \
    if ((t) < NF) AWT(V[t][j], rv, kvo, KVOFF(t), 128 * (j), te, (3 * (t) + (j) - 20) * TB * 8); \
    else AWT(V[t][j], rv, kvo, KVOFF(t), 128 * (j), ta, (6 * (t) - 18 + (j)) * TB * 8);      \
  } while (0)
#define EPLD() asm volatile("buffer_load_dword %0, off, %1, %2" : "=v"(epoch) : "s"(mkrs(gw0, GEND * 16)), "s"(GEP * 16) : "memory")
  u32x4 K[NF + 2][3], V[NF + 2][3];

  int fjj, fp;
  bool fidle;
#define FPART()                                                                  \
  do {                                                                           \
    int bo = b;                                                                  \
    asm volatile("" : "+s"(bo));                             \
    fjj = (bo >> 3) - ((bo & 1) ? 19 : 8);                                       \
    fidle = (unsigned)fjj < 2u;                                                  \
    fp = (bo >> 3) - (fjj > 0 ? 2 : 0);              \
  } while (0)
  unsigned epoch, tag;
  u64 tstart64;
#define ARGS(...)                                                                                                   \
  do {                                                                                                              \
    const unsigned fa = (unsigned)(size_t)(__attribute__((address_space(3))) float*)(lds + L_FLG);                  \
    asm volatile("ds_write_b128 %0, %1\n\ts_waitcnt lgkmcnt(0)" ::"v"(fa), "v"(u32x4{0u, 0u, 0u, 0u}), __VA_ARGS__ : "memory"); \
    tstart64 = __builtin_amdgcn_s_memrealtime();                                                                    \
    SCHED_FENCE();                                                                                                  \
  } while (0)
  auto arg = [](u64 a) __attribute__((always_inline)) {
    u64 r;
    asm volatile("s_mov_b64 %0, %1" : "=s"(r) : "s"(a));
    return r;
  };
  if (w < 4) {
    const int fw = 4 * sp + w, ct = (fw * 13) >> 7;
    const int r0 = 64 * (fw - 10 * (ct - (ct >> 1 & ct)));
    const int qo = ((r0 + 8 * g) * (HQ * DH) + h * DH + 64 * ct + 8 * c) * 2;
    const int xo = 16 * lane, xo2 = 16 * (lane & 15) + 1024;
    ARGS("v"(qo), "v"(xo), "v"(xo2));
    const u64 x0 = arg(ka[0]);
    const u32x4 xa = *(gu32x4*)(x0 + (unsigned)xo);
    const u32x4 xb = *(gu32x4*)(x0 + (unsigned)xo2);
    u32x4 WQ[8], F[4];
    int R;
    {
      const auto rq = __builtin_amdgcn_make_buffer_rsrc((void*)arg(ka[3]), (short)0, D * HQ * DH * 2, 0x00020000);
#pragma unroll
      for (int i = 0; i < 8; ++i) WQ[i] = __builtin_amdgcn_raw_buffer_load_b128(rq, qo, i * HQ * DH * 2, 2);
      SCHED_FENCE();
      FPART();
      const int fcol = (fp * 13) >> 6, frp = fp - 5 * fcol;
      R = 128 * frp + 32 * w;
      const int fo = ((R + 4 * g + (fidle ? D : 0)) * FF + 192 * xcd + 64 * (fcol >= 3 ? fcol - 3 : fcol) + 8 * c) * 2;
      const u64 pw1 = arg(ka[5]), pw3 = arg(ka[6]);
      const auto rf = __builtin_amdgcn_make_buffer_rsrc((void*)(fcol >= 3 ? pw3 : pw1), (short)0, D * FF * 2, 0x00020000);
#pragma unroll
      for (int i = 0; i < 4; ++i) F[i] = __builtin_amdgcn_raw_buffer_load_b128(rf, fo, i * FF * 2, 2);
    }
    void* gw0 = (void*)arg(kb[0]);
    if (fidle && tid < 40)
      __builtin_amdgcn_raw_buffer_store_b128(u32x4{0u, 0u, 0u, 0u}, __builtin_amdgcn_make_buffer_rsrc(gw0, (short)0, GEND * 16, 0x00020000), (GA + fjj * 320 + 40 * xcd + lane) * 16, 0, 16);
    SCHED_FENCE();
    EPLD();
    SCHED_FENCE();
    asm volatile("s_barrier" ::: "memory");
    SCHED_FENCE();
    unsigned short* xs = reinterpret_cast<unsigned short*>(lds + L_XS + w * 320);
    const bool lo16 = lane < 16;
    *reinterpret_cast<u32x4*>(xs + 8 * lane) = xa;
    if (lo16) *reinterpret_cast<u32x4*>(xs + 512 + 8 * lane) = xb;
    float xf[8], xg[8];
#pragma unroll
    for (int i = 0; i < 4; ++i) {
      xf[2 * i] = bf_lo(xa[i]);
      xf[2 * i + 1] = bf_hi(xa[i]);
      xg[2 * i] = bf_lo(xb[i]);
      xg[2 * i + 1] = bf_hi(xb[i]);
    }
    float sum = 0.f;
#pragma unroll
    for (int i = 0; i < 8; ++i) sum += xf[i] + (lo16 ? xg[i] : 0.f);
    sum = wave_sum(sum);
    const float mean = sum * (1.0f / D);
    float sq = 0.f;
#pragma unroll
    for (int i = 0; i < 8; ++i) {
      const float d1 = xf[i] - mean, d2 = xg[i] - mean;
      sq += d1 * d1 + (lo16 ? d2 * d2 : 0.f);
    }
    sq = wave_sum(sq);
    const float mb = bfr(mean);
    const float vb = bfr(sq * (1.0f / D));
    const float den = bfr(sqrtf(bfr(vb + 1e-5f)));
    WAVE_SYNC();
    auto xnorm = [&](int r) -> unsigned short { return bfbits(bfr(bf_lo(xs[r]) - mb) / den); };
    {
      unsigned short* xn = reinterpret_cast<unsigned short*>(lds + L_XN + w * 64);
      xn[lane] = xnorm((r0 < D ? r0 : 0) + lane);
      xn[64 + (lane & 31)] = xnorm(R + (lane & 31));
      WAVE_SYNC();
      const u32x4 XQ = *reinterpret_cast<const u32x4*>(xn + 8 * g);
      const u32x2 XF = *reinterpret_cast<const u32x2*>(xn + 64 + 4 * g);
      float acc[16], r2[2];
#pragma unroll
      for (int i = 0; i < 16; ++i) acc[i] = 0.f;
#pragma unroll
      for (int i = 0; i < 8; ++i) {
        const unsigned d = XQ[i >> 1], lo = (i & 1) ? d >> 16 : d & 0xffffu, hi = (i & 1) ? d & 0xffff0000u : d << 16;
#pragma unroll
        for (int k = 0; k < 4; ++k) {
          acc[2 * k] = dot2(WQ[i][k], lo, acc[2 * k]);
          acc[2 * k + 1] = dot2(WQ[i][k], hi, acc[2 * k + 1]);
        }
      }
#pragma unroll
      for (int i = 0; i < 4; ++i) {
        const unsigned d = XF[i >> 1], lo = (i & 1) ? d >> 16 : d & 0xffffu, hi = (i & 1) ? d & 0xffff0000u : d << 16;
#pragma unroll
        for (int k = 0; k < 4; ++k) {
          acc[8 + 2 * k] = dot2(F[i][k], lo, acc[8 + 2 * k]);
          acc[8 + 2 * k + 1] = dot2(F[i][k], hi, acc[8 + 2 * k + 1]);
        }
      }
      xg_scatter<2>(acc, r2, g & 1);
#pragma unroll
      for (int j = 0; j < 2; ++j) lds[L_PART + w * 128 + 64 * j + 8 * c + g] = r2[j];
    }
    asm volatile("s_waitcnt lgkmcnt(0)" ::: "memory");
    if (lane == 0) __hip_atomic_fetch_add(flg, 1u, __ATOMIC_RELAXED, __HIP_MEMORY_SCOPE_WORKGROUP);
    SCHED_FENCE();
  } else {
    CPLD();
    SCHED_FENCE();
    KVADR();
    const int ks0 = KVO(0), ks1 = KVO(1), ks2 = KVO(2);
    ARGS("v"(kvo), "s"(ks0), "s"(ks1), "s"(ks2));
    const u32x4 rk0 = mkrs((void*)arg(ka[1]), HQ * SK * DH * 2);
#pragma unroll
    for (int m = 0; m < KE; ++m) {
      ALD(K[m / 3][m % 3], rk0, kvo, m < 3 ? ks0 : m < 6 ? ks1 : ks2, 128 * (m % 3));
    }
    SCHED_FENCE();
    void* gw0 = (void*)arg(kb[0]);
    EPLD();
    SCHED_FENCE();
    asm volatile("s_barrier" ::: "memory");
    SCHED_FENCE();
  }
  asm volatile("" : "+s"(ka), "+s"(kb));
  const bf16 *kc = (const bf16*)ka[1], *vc = (const bf16*)ka[2], *wo = (const bf16*)ka[4], *w2 = (const bf16*)ka[7];
  u64* gw = (u64*)kb[0];
  bf16* out = (bf16*)kb[1];
  const auto rws = __builtin_amdgcn_make_buffer_rsrc((void*)gw, (short)0, GEND * 16, 0x00020000);
  const u32x4 rk = mkrs(kc, HQ * SK * DH * 2), rv = mkrs(vc, HQ * SK * DH * 2);
  const u32x4 rwa = mkrs(gw, GEND * 16);
  KVADR();
  FPART();
  const int dx = (int)((dx_pack() >> (4 * xcd)) & 15u) - 4;
  const int wr = w < 4 ? (w + 3) & 3 : w;
  const int nt = __builtin_amdgcn_readfirstlane(8 + ((unsigned)(wr - dx) >> 31) - ((unsigned)(7 - w + dx) >> 31));
  const int don = (int)(dx_don() >> (5 * (((dx_pos() >> (4 * xcd)) + wr) & 7u))) & 31;
  const int ko8 = (((don >> 2) * 8 + ((7 - (don & 3) + (ha >> 3)) & 7)) * 8 + ((7 + ha) & 7)) * 8 * DH * 2;
  const int wl = __builtin_amdgcn_readfirstlane((w < 4 ? WOL : WND) + (nt - 8) + (dx < 0 ? 1 : 0));
  const int lz = lane < 60 ? lane : 59;
  const int zr = lz / 10, zc = lz - 10 * zr;
  int w2o = (zr * D + 8 * zc) * 2;
  asm volatile("" : "+v"(w2o));
  if (w == 0) {
    for (unsigned spin = 0; spin < (SPIN_MAX << 4); ++spin) {
      if (__hip_atomic_load(flg, __ATOMIC_RELAXED, __HIP_MEMORY_SCOPE_WORKGROUP) == 4u) break;
      __builtin_amdgcn_s_sleep(1);
    }
    asm volatile("s_waitcnt lgkmcnt(0)" ::: "memory");
    AWAIT(epoch, 0);
    tag = __builtin_amdgcn_readfirstlane(epoch) + 1u;
    const f32x2* pp = reinterpret_cast<const f32x2*>(lds + L_PART) + lane;
    const f32x2 a01 = pp[0] + pp[64], a23 = pp[128] + pp[192];
    const bool two = sp == 2 && lane < 32;
    const f32x2 sm = two ? a01 : a01 + a23;
    const int ct0 = sp >= 5 ? 2 : sp >= 3 ? 1 : 0, slot = sp - (sp >= 5 ? 5 : sp >= 3 ? 2 : 0);
    const int uf = fidle ? GZF + 32 * xcd + 16 * fjj : G1F + xcd * 960 + fp * 32;
    const int un = lane < 32 ? G1 + h * 288 + slot * 96 + ct0 * 32 + lane : uf + lane - 32;
    if (lane < 33 || !fidle) gst2(un, sm[0], sm[1]);
    if (two) gst2(G1 + h * 288 + 32 + lane, a23[0], a23[1]);
    SCHED_FENCE();
  }
  asm volatile("" : "+s"(tstart64));
#define PHW 28
  const unsigned tstart = (unsigned)tstart64 - ((((w & 3) * 2 + (w >> 2)) * PHW) >> 3);
  ta = (tstart + TA) * nt - TOFF;
  te = (tstart + TE) * nt - TOFF;
  int wop = w;
  asm volatile("" : "+s"(wop));
  if (wop != 0) {
    if (w < 4) {
#pragma unroll
      for (int m = 0; m < KE; ++m) KWL(m / 3, m % 3);
    }
#pragma unroll
    for (int m = KE; m < 3 * NF; ++m) {
      KWL(m / 3, m % 3);
    }
    TIE(epoch);
    TIE(cpv);
    tag = __builtin_amdgcn_readfirstlane(epoch) + 1u;
    SCHED_FENCE();
  } else {
    u32x4 W2[8];
    const auto rw2 = __builtin_amdgcn_make_buffer_rsrc((void*)(w2 + (size_t)48 * h * D + 80 * sp), (short)0, 48 * D * 2, 0x00020000);
#pragma unroll
    for (int i = 0; i < 8; ++i) W2[i] = __builtin_amdgcn_raw_buffer_load_b128(rw2, w2o, i * 6 * D * 2, 2);
#pragma unroll
    for (int t = 0; t < NF; ++t)
#pragma unroll
      for (int j = 0; j < 3; ++j) KLD(t, j);
    SCHED_FENCE();
    const int row = lane >> 4, l16 = lane & 15;
    const int gb = G1 + ha * 288 + lane;
    const int gc = row < 3 ? G1 + ha * 288 + row * 96 + 64 + l16 : GZF + 32 * sp;
    const int l48 = lane < 48 ? lane : 47, fm = l48 >= 24 ? 1 : 0, fpr = 24 * (h & 3) + l48 - 24 * fm;
    const int gf = G1F + xcd * 960 + (3 * fm + (fpr >> 5)) * 160 + (fpr & 31);
    u32x4 va[3], vb[2], vf[5];
    for (unsigned spin = 0;; ++spin) {
      MEMBAR();
#pragma unroll
      for (int s = 0; s < 3; ++s) va[s] = gld2(gb, s * 96);
#pragma unroll
      for (int s = 0; s < 2; ++s) vb[s] = gld2(gc, s * 16);
#pragma unroll
      for (int s = 0; s < 5; ++s) vf[s] = gld2(gf, s * 32);
      bool ok = gok2(vb[0]) && gok2(vb[1]) && gok2(vf[3]) && gok2(vf[4]);
#pragma unroll
      for (int s = 0; s < 3; ++s) ok = ok && gok2(va[s]) && gok2(vf[s]);
      if (__all(ok) || spin >= SPIN_MAX) break;
      __builtin_amdgcn_s_sleep(1);
    }
    {
      const float a0 = (bitsf(va[0][0]) + bitsf(va[1][0])) + bitsf(va[2][0]), a1 = (bitsf(va[0][2]) + bitsf(va[1][2])) + bitsf(va[2][2]);
      reinterpret_cast<unsigned*>(lds + L_QS)[lane] = (unsigned)bfbits(a0) | ((unsigned)bfbits(a1) << 16);
      const float p0 = swap16_add(row < 3 ? bitsf(vb[0][0]) : 0.f, row < 3 ? bitsf(vb[0][2]) : 0.f);
      const float p1 = swap16_add(row < 3 ? bitsf(vb[1][0]) : 0.f, row < 3 ? bitsf(vb[1][2]) : 0.f);
      const float bq = swap32_add(p0, p1);
      reinterpret_cast<unsigned short*>(lds + L_QS)[2 * (64 + 16 * (row >> 1) + l16) + (row & 1)] = bfbits(bq);
    }
    asm volatile("s_waitcnt lgkmcnt(0)" ::: "memory");
    if (lane == 0) __hip_atomic_store(flg + 1, 1u, __ATOMIC_RELAXED, __HIP_MEMORY_SCOPE_WORKGROUP);
    SCHED_FENCE();
    {
      const float f0 = ((bitsf(vf[0][0]) + bitsf(vf[1][0])) + (bitsf(vf[2][0]) + bitsf(vf[3][0]))) + bitsf(vf[4][0]);
      const float f1 = ((bitsf(vf[0][2]) + bitsf(vf[1][2])) + (bitsf(vf[2][2]) + bitsf(vf[3][2]))) + bitsf(vf[4][2]);
      *reinterpret_cast<f32x2*>(lds + L_GU + 2 * l48) = f32x2{bfr(f0), bfr(f1)};
    }
    WAVE_SYNC();
    if (lane < 48) lds[L_H + lane] = bfr(bfr(silu(lds[L_GU + lane])) * lds[L_GU + 48 + lane]);
    WAVE_SYNC();
    {
      float z[8];
#pragma unroll
      for (int i = 0; i < 8; ++i) z[i] = 0.f;
#pragma unroll
      for (int i = 0; i < 8; ++i) {
        const unsigned chi = fbits(lds[L_H + zr + 6 * i]), clo = chi >> 16;
#pragma unroll
        for (int k = 0; k < 4; ++k) {
          z[2 * k] = dot2(W2[i][k], clo, z[2 * k]);
          z[2 * k + 1] = dot2(W2[i][k], chi, z[2 * k + 1]);
        }
      }
      if (lane < 60) {
        float* zp = lds + L_ZP + zr * 80 + 8 * zc;
        *reinterpret_cast<f32x4*>(zp) = f32x4{z[0], z[1], z[2], z[3]};
        *reinterpret_cast<f32x4*>(zp + 4) = f32x4{z[4], z[5], z[6], z[7]};
      }
    }
    SCHED_FENCE();
  }
  const int u = (tid & 255) < 240 ? (tid & 255) : 239;
  const int rg = u / 10, ch = u - 10 * rg;
  u32x4 WO[8];
  const u32x4 rwo = mkrs(wo + (size_t)DH * h * D + 80 * sp, DH * D * 2);
  const int wofs = (rg * D + 8 * ch) * 2;
#define WOLC(i)                                                                                                            \
  asm volatile("s_cmp_lt_u32 %4, 4\n\ts_cbranch_scc1 2\n\tbuffer_load_dwordx4 %0, %1, %2, %3 offen nt"                     \
               : "=v"(WO[i]) : "v"(wofs), "s"(rwo), "s"((i) * 24 * D * 2), "s"(w) : "memory", "scc")
#define VLD(t)                                                       \
  do {                                                               \
    _Pragma("unroll") for (int j = 0; j < 3; ++j) VWL(t, j);         \
  } while (0)
#pragma unroll
  for (int t = NF; t < NF + 2; ++t)
    if (t < nt) {
#pragma unroll
      for (int j = 0; j < 3; ++j) KWL(t, j);
      VLD(t);
    }
#define ZW 3
#define SLOWSUM(res, base, cc)                                                                                  \
  do {                                                                                                          \
    res = 0.f;                                                                                                  \
    _Pragma("unroll 1") for (int hh = 0; hh < HQ; ++hh) {                                                       \
      u32x2 v;                                                                                                  \
      for (unsigned spin = 0; spin < SPIN_MAX; ++spin) {                                                        \
        MEMBAR();                                                                                               \
        v = __builtin_amdgcn_raw_buffer_load_b64(rws, (cc) * 8, ((base) + hh * 320) * 16, 16);                  \
        if (v[1] == tag) break;                                                                                 \
      }                                                                                                         \
      res += bitsf(v[0]);                                                                                       \
    }                                                                                                           \
  } while (0)
#define ZPUB()                                                                                      \
  do {                                                                                              \
    const f32x2* zp = reinterpret_cast<const f32x2*>(lds + L_ZP) + (lane < 40 ? lane : 0);          \
    const f32x2 zs = ((zp[0] + zp[40]) + (zp[80] + zp[120])) + (zp[160] + zp[200]);                 \
    gst2(lane < 40 ? G3Z + h * 320 + 40 * sp + lane : GEND, zs[0], zs[1]);   \
  } while (0)
  u64 zold[2] = {0ull, 0ull}, zadd[2] = {0ull, 0ull};
  int mro = 8 * tid, mso = tid < 97 ? (G2 + rec * 104 + tid) * 16 : GEND * 16;
  u32x4 pub = {0u, tag, 0u, tag};
  asm volatile("" : "+v"(mro), "+v"(mso), "+v"(pub));
  {
    float Mg;
    float acc[3][8];
#pragma unroll
    for (int i = 0; i < 24; ++i) acc[i >> 3][i & 7] = 0.f;
    float* pw = lds + L_PART + w * PS + 8 * c + g;
    const bool b3 = g & 1;
#define PV3(t, j, cl, ch)                                                                                               \
  do {                                                                                                                  \
    if ((cl) < 0) asm volatile("" : "+v"(plo), "+v"(phi));                                                              \
    else if ((cl) == (ch)) asm volatile("s_waitcnt vmcnt(%2)" : "+v"(plo), "+v"(phi) : "n"((cl) < 0 ? 0 : (cl)) : "memory");  \
    else asm volatile("s_waitcnt vmcnt(%2)\n\ts_cmp_lt_u32 %4, 4\n\ts_cbranch_scc0 1\n\ts_waitcnt vmcnt(%3)"              \
                      : "+v"(plo), "+v"(phi) : "n"(ch), "n"((cl) < 0 ? 0 : (cl)), "s"(w) : "memory", "scc");            \
    _Pragma("unroll") for (int i = 0; i < 4; ++i) {                                                                     \
      acc[j][2 * i] = dot2(V[t][j][i], plo, acc[j][2 * i]);                                                             \
      acc[j][2 * i + 1] = dot2(V[t][j][i], phi, acc[j][2 * i + 1]);                                                     \
    }                                                                                                                   \
    if ((t) == NF - 1) {                                                          \
      float r1[1];                                                                                                      \
      xg_scatter<1>(acc[j], r1, b3);                                                                                    \
      pw[64 * (j)] = r1[0] * ew;                                                                                        \
    } else {                                                                                                            \
      asm volatile("" : "+v"(acc[j][0]), "+v"(acc[j][1]), "+v"(acc[j][2]), "+v"(acc[j][3]), "+v"(acc[j][4]),            \
                   "+v"(acc[j][5]), "+v"(acc[j][6]), "+v"(acc[j][7]));                       \
    }                                                                                                                   \
    SCHED_FENCE();                                                                                                      \
  } while (0)
#define PVT(t, c)                                                                                                       \
  do {                                                                                                                  \
    unsigned plo = pb[t], phi = plo << 16;                                                                              \
    PV3(t, 0, c, c);                                                                                                    \
    PV3(t, 1, -1, -1);                                                                                                  \
    PV3(t, 2, -1, -1);                                                                                                  \
  } while (0)
#define WGMAX()                                                                                                         \
  do {                                                                                                                  \
    for (unsigned spin = 0; spin < (SPIN_MAX << 4); ++spin) {                                                           \
      if (__hip_atomic_load(flg + 3, __ATOMIC_RELAXED, __HIP_MEMORY_SCOPE_WORKGROUP) == 8u) break;                      \
      __builtin_amdgcn_s_sleep(1);                                                                                      \
    }                                                                                                                   \
    MEMBAR();                                                                                                           \
    const f32x4 ma = *reinterpret_cast<const f32x4*>(lds + L_ML), mb2 = *reinterpret_cast<const f32x4*>(lds + L_ML + 4); \
    Mg = fmaxf(fmaxf(fmaxf(ma[0], ma[1]), fmaxf(ma[2], ma[3])), fmaxf(fmaxf(mb2[0], mb2[1]), fmaxf(mb2[2], mb2[3])));   \
    ew = ex2((mw - Mg) * LOG2E);                                                                                        \
    if (lane == 0) *reinterpret_cast<f32x2*>(lds + L_PART + w * PS + DH) = f32x2{w == 0 ? Mg : 0.f, lw * ew};           \
    SCHED_FENCE();                                                                                                      \
  } while (0)
    float ew, mw, lw;
    u32x4 qf[3];
    float sc[NF + 2];
    unsigned pb[NF + 2];
#define GATE(cnt)                                                                                                       \
  do {                                                                                                                  \
    asm volatile("s_waitcnt vmcnt(%2)" : "+v"(zold[0]), "+v"(zold[1]) : "n"(cnt) : "memory");                           \
    _Pragma("unroll") for (int k = 0; k < 2; ++k)                                                                       \
      if ((k == 0 || lane < 16) && (zold[k] >> 59) == 31u) {                                                            \
        const int cz = 80 * sp + 64 * k + lane;                                                                         \
        float f = fx_dec(zold[k] + zadd[k]);                                                                            \
        if (__builtin_expect(!fx_exact(zold[k], zadd[k]), 0)) SLOWSUM(f, G3Z, cz);                                      \
        __builtin_amdgcn_raw_buffer_store_b64(u32x2{fbits(bfr(silu(bfr(f)))), tag}, rws, cz * 8, GG * 16, 16);          \
      }                                                                                                                 \
  } while (0)
    auto slot = [&](auto mc) __attribute__((always_inline)) {
      constexpr int m = decltype(mc)::value;
      VWL(m / 3, m % 3);
      if (m == QS) {
        for (unsigned spin = 0; spin < (SPIN_MAX << 4); ++spin) {
          if (__hip_atomic_load(flg + 1, __ATOMIC_RELAXED, __HIP_MEMORY_SCOPE_WORKGROUP) != 0u) break;
          __builtin_amdgcn_s_sleep(1);
        }
        asm volatile("s_waitcnt lgkmcnt(0)" ::: "memory");
        SCHED_FENCE();
#pragma unroll
        for (int j = 0; j < 3; ++j) qf[j] = *reinterpret_cast<const u32x4*>(lds + L_QS + (c + 8 * j) * 4);
      }
#pragma unroll
      for (int t = 0; t < NF + 2; ++t)
        if (SS[t] == m) {
          sc[t] = -INFINITY;
          if (t < NF || t < nt) {
            float d = 0.f;
#pragma unroll
            for (int j = 0; j < 3; ++j) {
              TIE(K[t][j]);
#pragma unroll
              for (int i = 0; i < 4; ++i) d = dot2(K[t][j][i], qf[j][i], d);
            }
            d = grp8_sum(d);
            sc[t] = bfr(bfr(d) * SCALE);
          }
          TIE(sc[t]);
        }
      if (m == XS) {
        mw = sc[0];
#pragma unroll
        for (int t = 1; t < NF + 2; ++t) mw = fmaxf(mw, sc[t]);
        mw = xg_max(mw);
        if (lane == 0) {
          lds[L_ML + w] = mw;
          __hip_atomic_fetch_add(flg + 3, 1u, __ATOMIC_RELAXED, __HIP_MEMORY_SCOPE_WORKGROUP);
        }
        lw = 0.f;
#pragma unroll
        for (int t = 0; t < NF + 2; ++t) {
          const float p = ex2((sc[t] - mw) * LOG2E);
          lw += p;
          pb[t] = bfbits(p);
          TIE(pb[t]);
        }
        lw = xg_sum(lw);
        TIE(lw);
      }
      if (m == XS + 1 && NF < nt) PVT(NF, -1);
      if (m == XS + 2 && NF + 1 < nt) PVT(NF + 1, -1);
      if (m >= XS + 3) {
        constexpr int n0 = PVN(m - 1), n1 = PVN(m);
#pragma unroll
        for (int n = n0; n < n1; ++n) {
          unsigned plo = pb[n / 3], phi = plo << 16;
          PV3(n / 3, n % 3, n == m - 8 ? 8 : -1, n == m - 8 ? 8 : -1);
        }
      }
      if (m == ZSL) {
        WGMAX();
        if (w == ZW) {
#pragma unroll
          for (int k = 0; k < 2; ++k) {
            const float* zp = lds + L_ZP + (k ? 64 + (lane & 15) : lane);
            zold[k] = zadd[k] = fx_enc(((zp[0] + zp[80]) + (zp[160] + zp[240])) + (zp[320] + zp[400]));
          }
          const int za0 = (2 * GA + D + 80 * sp + lane) * 8, za1 = lane < 16 ? za0 + 512 : GEND * 16;
          ZPUB();
          asm volatile("buffer_atomic_add_x2 %0, %2, %4, 0 offen sc0\n\tbuffer_atomic_add_x2 %1, %3, %4, 0 offen sc0"
                       : "+v"(zold[0]), "+v"(zold[1]) : "v"(za0), "v"(za1), "s"(rwa) : "memory");
        }
      }
      if (m == GSL && w == ZW) GATE(GSL - ZSL);
      SCHED_FENCE();
    };
#define SL3(t) slot(IC<3 * (t)>{}); slot(IC<3 * (t) + 1>{}); slot(IC<3 * (t) + 2>{})
    SL3(0); SL3(1); SL3(2); SL3(3); SL3(4); SL3(5); SL3(6);
#pragma unroll
    for (int n = PVN(3 * NF - 1); n < 3 * NF; ++n) {
      const int m = n - (3 * NF - 9);
      if (m >= 0) {
#pragma unroll
        for (int i = (m && n > PVN(3 * NF - 1)) ? WON[m - 1] : 0; i < WON[m]; ++i) WOLC(i);
      }
      unsigned plo = pb[n / 3], phi = plo << 16;
      PV3(n / 3, n % 3, m < 0 ? -1 : 8 - m, m < 0 ? -1 : 8 - m + WON[m < 0 ? 0 : m]);
    }
#pragma unroll
    for (int i = WON[8]; i < WON[9]; ++i) WOLC(i);
  }
  __syncthreads();
  if (__builtin_expect(w < 2, 1)) {
    {
      f32x2 p2[8];
#pragma unroll
      for (int ww = 0; ww < 8; ++ww) p2[ww] = *reinterpret_cast<const f32x2*>(reinterpret_cast<const char*>(lds + L_PART + ww * PS) + mro);
      const f32x2 ua = (p2[0] + p2[1]) + (p2[2] + p2[3]), ub = (p2[4] + p2[5]) + (p2[6] + p2[7]);
      float ub1 = ub[1];
      asm("" : "+v"(ub1));
      pub[0] = fbits(ua[0] + ub[0]);
      pub[2] = fbits(ua[1] + ub1);
      __builtin_amdgcn_raw_buffer_store_b128(pub, rws, mso, 0, 16);
    }
    SCHED_FENCE();
    const int t2 = tid < 96 ? tid : 95;
    const int pb = (G2 + h * NSP * 104 + t2) * 16, pm = (G2 + (h * NSP + c) * 104 + 96) * 16;
    const unsigned po = (unsigned)(size_t)(__attribute__((address_space(3))) float*)(lds + L_O) + 4 * t2;
    const int pg = lane < 20 ? (GG + 40 * sp + 20 * w + lane) * 16 : GEND * 16;
    const unsigned pl = (unsigned)(size_t)(__attribute__((address_space(3))) float*)(lds + L_GT) + 16 * (lane < 20 ? 20 * w + lane : 40 + tid);
    if (PD1) __builtin_amdgcn_s_sleep(PD1);
    {
      unsigned e0, e1, e2, e3, e4, e5, e6, e7, cnt;
#define DPC " row_mask:0xf bank_mask:0xf\n\t"
#define CHK(d, a, b) "v_bitop3_b32 " d ", " a ", " b ", %13 bitop3:0x7e\n\t"
#define ACC2(e, a, b) "v_fmac_f32 v44, " e ", " a "\n\tv_fmac_f32 v45, " e ", " b "\n\t"
      asm volatile(
          "s_mov_b32 %8, %16\n"
          "1:\n\t"
          "buffer_load_dwordx4 v[48:51], %18, %12, 0 offen sc1\n\t"
          "buffer_load_dwordx4 v[4:7], %9, %12, 0 offen sc1\n\t"
          "buffer_load_dwordx4 v[8:11], %10, %12, 0 offen sc1\n\t"
          "buffer_load_dwordx4 v[12:15], %10, %12, 0 offen offset:1664 sc1\n\t"
          "buffer_load_dwordx4 v[16:19], %10, %12, 0 offen offset:3328 sc1\n\t"
          "buffer_load_dwordx4 v[20:23], %10, %12, %14 offen sc1\n\t"
          "buffer_load_dwordx4 v[24:27], %10, %12, %14 offen offset:1664 sc1\n\t"
          "buffer_load_dwordx4 v[28:31], %10, %12, %14 offen offset:3328 sc1\n\t"
          "buffer_load_dwordx4 v[32:35], %10, %12, %15 offen sc1\n\t"
          "buffer_load_dwordx4 v[36:39], %10, %12, %15 offen offset:1664 sc1\n\t"
          "s_sub_u32 %8, %8, 1\n\t"
          "s_waitcnt vmcnt(8)\n\t"
          CHK("v41", "v5", "v7")
          "ds_write_b128 %19, v[48:51]\n\t"
          "v_max_f32_dpp v40, v4, v4 quad_perm:[1,0,3,2]" DPC
          "s_waitcnt vmcnt(7)\n\t"
          CHK("v46", "v9", "v11")
          "v_max_f32_dpp v40, v40, v40 quad_perm:[2,3,0,1]" DPC
          "s_waitcnt vmcnt(6)\n\t"
          CHK("v47", "v13", "v15")
          "v_max_f32_dpp v40, v40, v40 row_half_mirror" DPC
          "v_sub_f32 v42, v4, v40\n\t"
          "v_mul_f32 v42, 0x3fb8aa3b, v42\n\t"
          "v_exp_f32 v42, v42\n\t"
          "v_or3_b32 v41, v41, v46, v47\n\t"
          "v_mul_f32 v40, v6, v42\n\t"
          "s_waitcnt vmcnt(5)\n\t"
          CHK("v46", "v17", "v19")
          "v_add_f32_dpp v40, v40, v40 quad_perm:[1,0,3,2]" DPC
          "s_waitcnt vmcnt(4)\n\t"
          CHK("v47", "v21", "v23")
          "v_add_f32_dpp v40, v40, v40 quad_perm:[2,3,0,1]" DPC
          "v_or3_b32 v41, v41, v46, v47\n\t"
          "s_nop 0\n\t"
          "v_add_f32_dpp v40, v40, v40 row_half_mirror" DPC
          "v_rcp_f32 v40, v40\n\t"
          "s_nop 0\n\t"
          "v_mul_f32 v42, v42, v40\n\t"
          "s_nop 0\n\t"
          "v_readlane_b32 %0, v42, 0\n\tv_readlane_b32 %1, v42, 1\n\tv_readlane_b32 %2, v42, 2\n\tv_readlane_b32 %3, v42, 3\n\t"
          "v_readlane_b32 %4, v42, 4\n\tv_readlane_b32 %5, v42, 5\n\tv_readlane_b32 %6, v42, 6\n\tv_readlane_b32 %7, v42, 7\n\t"
          "v_mul_f32 v44, %0, v8\n\tv_mul_f32 v45, %0, v10\n\t"
          ACC2("%1", "v12", "v14") ACC2("%2", "v16", "v18") ACC2("%3", "v20", "v22")
          "s_waitcnt vmcnt(3)\n\t"
          ACC2("%4", "v24", "v26") CHK("v46", "v25", "v27")
          "s_waitcnt vmcnt(2)\n\t"
          ACC2("%5", "v28", "v30") CHK("v47", "v29", "v31")
          "v_or3_b32 v41, v41, v46, v47\n\t"
          "s_waitcnt vmcnt(1)\n\t"
          ACC2("%6", "v32", "v34") CHK("v46", "v33", "v35")
          "s_waitcnt vmcnt(0)\n\t"
          ACC2("%7", "v36", "v38")
          "v_cvt_pk_bf16_f32 v44, v44, v45\n\t"
          "ds_write_b32 %11, v44\n\t"
          CHK("v47", "v37", "v39")
          "v_or3_b32 v41, v41, v46, v47\n\t"
          "v_cmp_ne_u32 vcc, 0, v41\n\t"
          "s_cbranch_vccz 2f\n\t"
          "s_sleep %17\n\t"
          "s_cbranch_scc0 1b\n"
          "2:\n\t"
          "s_waitcnt lgkmcnt(0)"
          : "=&s"(e0), "=&s"(e1), "=&s"(e2), "=&s"(e3), "=&s"(e4), "=&s"(e5), "=&s"(e6), "=&s"(e7), "=&s"(cnt)
          : "v"(pm), "v"(pb), "v"(po), "s"(rwa), "s"(tag), "s"(3 * 1664), "s"(6 * 1664), "n"(SPIN_MAX), "n"(PS1), "v"(pg), "v"(pl)
          : "memory", "scc", "vcc", "v4", "v5", "v6", "v7", "v8", "v9", "v10", "v11", "v12", "v13", "v14", "v15", "v16", "v17", "v18", "v19",
            "v20", "v21", "v22", "v23", "v24", "v25", "v26", "v27", "v28", "v29", "v30", "v31", "v32", "v33", "v34", "v35", "v36", "v37",
            "v38", "v39", "v40", "v41", "v42", "v44", "v45", "v46", "v47", "v48", "v49", "v50", "v51");
    }
    __syncthreads();
  } else if (w == 2) {
    __syncthreads();
  } else {
    if (w >= 4) {
#pragma unroll
      for (int i = WON[9]; i < 8; ++i) WOLC(i);
#pragma unroll
      for (int i = 0; i < 8; ++i) {
        AWAIT(WO[i], 7 - i);
        *reinterpret_cast<u32x4*>(lds + L_WO + u * 4 + i * 960) = WO[i];
      }
      asm volatile("s_waitcnt lgkmcnt(0)" ::: "memory");
      if (lane == 0) __hip_atomic_fetch_add(flg + 2, 1u, __ATOMIC_RELAXED, __HIP_MEMORY_SCOPE_WORKGROUP);
    }
    for (unsigned spin = 0; spin < (SPIN_MAX << 4); ++spin) {
      if (__hip_atomic_load(flg + 2, __ATOMIC_RELAXED, __HIP_MEMORY_SCOPE_WORKGROUP) == 4u) break;
      __builtin_amdgcn_s_sleep(1);
    }
    asm volatile("s_waitcnt lgkmcnt(0)" ::: "memory");
    u32x4 wb[6];
    {
      typedef __attribute__((address_space(3))) v4i16_t* ltr;
      const unsigned la = (unsigned)(size_t)(__attribute__((address_space(3))) float*)(lds + L_WO) + (8 * (lane >> 4) + ((lane >> 2) & 3)) * 160 + 32 * (w - 3) + 8 * (lane & 3);
#pragma unroll
      for (int k = 0; k < 6; ++k) {
        const u32x2 t0 = __builtin_bit_cast(u32x2, __builtin_amdgcn_ds_read_tr16_b64_v4i16((ltr)(la + 32 * k * 160)));
        const u32x2 t1 = __builtin_bit_cast(u32x2, __builtin_amdgcn_ds_read_tr16_b64_v4i16((ltr)(la + (32 * k + 4) * 160)));
        wb[k] = u32x4{t0[0], t0[1], t1[0], t1[1]};
      }
    }
    const int cy = 80 * sp + 16 * (w - 3) + (lane & 15);
    float xv = bf_lo(reinterpret_cast<const unsigned short*>(lds + L_XS)[cy]);
    gu64* pa = (gu64*)(gw + 2 * GA + cy);
    gu16* pout = (gu16*)(out + cy);
    typedef __attribute__((address_space(3))) const u32x4* la4;
    la4 pa4 = (la4)(lds + L_O) + 4 * (lane & 3) + (lane >> 4);
    int cy8 = cy * 8;
    typedef __attribute__((address_space(3))) const u32x2* la2;
    la2 pgt = (la2)(lds + L_GT) + 16 * (w - 3) + (lane & 15);
    asm volatile("" : "+v"(pgt));
    unsigned k0 = (1u << 27) - (0x411u << 20), k1 = k0 + (1u << 22), k2 = 1u << 17;
    double c0 = 262144.0 + 512.0, c1 = -(262144.0 + 32 * 512.0);
    float f0 = 512.f, f1 = 0x1p-9f;
    asm volatile("" : "+v"(wb[0]), "+v"(wb[1]), "+v"(wb[2]), "+v"(wb[3]), "+v"(wb[4]), "+v"(wb[5]));
    asm volatile("" : "+v"(xv), "+v"(pa), "+v"(pout), "+v"(pa4), "+v"(cy8), "+v"(k0), "+v"(k1), "+v"(k2), "+s"(c0), "+s"(c1), "+s"(f0), "+s"(f1));
    __syncthreads();
    const u32x4 a1 = pa4[0], a2 = pa4[16];
    SCHED_FENCE();
    u32x2 gg = *pgt;
    SCHED_FENCE();
#define MFMA(a, b, cc) __builtin_amdgcn_mfma_f32_16x16x32_bf16(__builtin_bit_cast(bf16x8_t, a), __builtin_bit_cast(bf16x8_t, b), cc, 0, 0, 0)
    const f32x4 z4 = {0.f, 0.f, 0.f, 0.f};
    const f32x4 d2 = MFMA(a1, wb[2], z4), d3 = MFMA(a1, wb[3], z4);
    SCHED_FENCE();
    f32x4 d0 = MFMA(a1, wb[0], z4), d1 = MFMA(a1, wb[1], z4);
    SCHED_FENCE();
    d0 = MFMA(a2, wb[4], d0);
    SCHED_FENCE();
    d1 = MFMA(a2, wb[5], d1);
    SCHED_FENCE();
    if (lane < 16) {
      const float q = ((d2[2] + d3[3]) + d0[0]) + d1[1];
      const bool bad = !(fabsf(q) < f0);
      const u64 db = __builtin_bit_cast(u64, (double)(bad ? 0.f : q) + c0);
      const unsigned fl = (bad ? k1 : k0) + (fabsf(q) < f1 ? k2 : 0u);
      const u64 add = ((u64)((unsigned)(db >> 32) + fl) << 32) | (unsigned)db;
      const u64 old = __hip_atomic_fetch_add(pa, add, __ATOMIC_RELAXED, __HIP_MEMORY_SCOPE_AGENT);
      __builtin_amdgcn_raw_buffer_store_b64(u32x2{fbits(q), tag}, rws, cy8, (G3Y + h * 320) * 16, 16);
      SCHED_FENCE();
      unsigned lim = (gg[1] == tag && !bad) ? (32u << 17) - (fl - k0) : 0u;
      asm volatile("" : "+v"(lim));
      SCHED_FENCE();
      float sv = (float)(__builtin_bit_cast(double, (old & ((1ull << 49) - 1ull)) + db) + c1);
      asm("" : "+v"(sv));
      unsigned res = bfbits(xv + bfr(bfr(sv) * bitsf(gg[0])));
      const unsigned rel = (unsigned)(old >> 32) - (31u << 27);
      TIE(res);
      if (rel < lim) *pout = res;
      SCHED_FENCE();
      asm volatile(".Lfd_end:");
      if ((old >> 59) == 31u) {
        if (__builtin_expect(rel >= lim, 0)) {
          if (!fx_exact(old, add)) SLOWSUM(sv, G3Y, cy);
          for (unsigned spin = 0; gg[1] != tag && spin < SPIN_MAX; ++spin) {
            MEMBAR();
            gg = __builtin_amdgcn_raw_buffer_load_b64(rws, cy8, GG * 16, 16);
          }
          *pout = bfbits(xv + bfr(bfr(sv) * bitsf(gg[0])));
        }
        if (cy == 0)
          __hip_atomic_store((__attribute__((address_space(1))) unsigned*)(gw + 2 * GEP), tag, __ATOMIC_RELAXED, __HIP_MEMORY_SCOPE_AGENT);
      }
    }
  }
}

}

extern "C" void kernel_launch(void* const* d_in, const int* in_sizes, int n_in, void* d_out, int out_size, void* d_ws,
                              size_t ws_size, hipStream_t stream) {
  const bf16* x = (const bf16*)d_in[0];
  const bf16* kc = (const bf16*)d_in[1];
  const bf16* vc = (const bf16*)d_in[2];
  const bf16* wq = (const bf16*)d_in[3];
  const bf16* wo = (const bf16*)d_in[4];
  const bf16* w1 = (const bf16*)d_in[6];
  const bf16* w3 = (const bf16*)d_in[7];
  const bf16* w2 = (const bf16*)d_in[8];
  u64* gw = (u64*)d_ws;
  fused_decode<<<NWG, NT, 0, stream>>>(x, kc, vc, wq, wo, w1, w3, w2, gw, (bf16*)d_out);
}
```
